```python
import torch

def _softmax(x, axis=-1):
    return torch.softmax(x, dim=axis)
def _gelu(x):
    return torch.nn.functional.gelu(x, approximate="tanh")
def _silu(x):
    return torch.nn.functional.silu(x)
def _sigmoid(x):
    return torch.sigmoid(x)
def _layernorm(x, axis=-1, eps=1e-5):
    m = x.mean(dim=axis, keepdim=True)
    v = x.var(dim=axis, keepdim=True, unbiased=False)
    return (x - m) / torch.sqrt(v + eps)
def _scatter_add(tgt, idx, src, axis=0):
    return tgt.clone().index_add_(axis, idx, src)
def _segment_sum(src, seg, n):
    out = torch.zeros((n,) + src.shape[1:], dtype=src.dtype)
    return out.index_add_(0, seg, src)
def _pad(x, pw):
    flat = tuple(v for pair in reversed(pw) for v in pair)
    return torch.nn.functional.pad(x, flat)

kernel_name = 'recurrent_scan__mha_block_cached__glu_gated_bilinear__prompt'

def setup_inputs():

    _w = (lambda a: (lambda t: t.to(torch.bfloat16) if t.is_floating_point() else t)(torch.as_tensor(a)))
    def _A_setup():
        B, Sq, D = 1, 2048, 576
        H, Sk, Dh = 32, 2048, 128
        return {
            "x": _w((lambda *s: torch.randn(*s, dtype=torch.bfloat16) * 0.05)(B, Sq, D)),
            "k_cache": _w((lambda *s: torch.randn(*s, dtype=torch.bfloat16) * 1.6)(B, H, Sk, Dh)),
            "v_cache": _w((lambda *s: torch.randn(*s, dtype=torch.bfloat16) * 1.6)(B, H, Sk, Dh)),
            "wq": _w((lambda *s: torch.randn(*s, dtype=torch.bfloat16) * 1.6)(D, H * Dh) * (D ** -0.5)),
            "wo": _w((lambda *s: torch.randn(*s, dtype=torch.bfloat16) * 1.6)(H * Dh, D) * ((H * Dh) ** -0.5)),
        }
    _A_in = _A_setup()

    def _B_setup():
        B, S, D, F = 1, 2048, 576, 4608
        return {
            "x": _w((lambda *s: torch.randn(*s, dtype=torch.bfloat16) * 0.05)(B, S, D)),
            "w_gate": _w((lambda *s: torch.randn(*s, dtype=torch.bfloat16) * 0.2)(D, F) * (D ** -0.5)),
            "w_up": _w((lambda *s: torch.randn(*s, dtype=torch.bfloat16) * 0.2)(D, F) * (D ** -0.5)),
            "w_down": _w((lambda *s: torch.randn(*s, dtype=torch.bfloat16) * 0.2)(F, D) * (F ** -0.5)),
        }
    _B_in = _B_setup()

    return {
        "A_x": _A_in["x"],
        "A_k_cache": _A_in["k_cache"],
        "A_v_cache": _A_in["v_cache"],
        "A_wq": _A_in["wq"],
        "A_wo": _A_in["wo"],
        "B_x": _B_in["x"],
        "B_w_gate": _B_in["w_gate"],
        "B_w_up": _B_in["w_up"],
        "B_w_down": _B_in["w_down"],
    }

def reference(A_x, A_k_cache, A_v_cache, A_wq, A_wo, B_x, B_w_gate, B_w_up, B_w_down):
    def _A_ref(x, k_cache, v_cache, wq, wo):
        B, Sq, D = x.shape
        H, Dh = 32, 128
        q = (lambda x, *d: x.permute(*d))((x @ wq).reshape(B, Sq, H, Dh), 0, 2, 1, 3)
        scores = torch.einsum("bhqd,bhkd->bhqk", q, k_cache) * (Dh ** -0.5)
        p = _softmax(scores, axis=-1)
        o = torch.einsum("bhqk,bhkd->bhqd", p, v_cache)
        return (lambda x, *d: x.permute(*d))(o, 0, 2, 1, 3).reshape(B, Sq, H * Dh) @ wo

    def _B_ref(x, w_gate, w_up, w_down):
        g = torch.einsum("bsd,df->bsf", x, w_gate)
        u = torch.einsum("bsd,df->bsf", x, w_up)
        h = g * u
        return torch.einsum("bsf,fd->bsd", h, w_down)

    x = A_x
    xn = _layernorm(x, axis=-1)
    s = _A_ref(xn, A_k_cache, A_v_cache, A_wq, A_wo)
    g = _silu(_B_ref(xn, B_w_gate, B_w_up, B_w_down))
    return x + s * g
```

```cpp
#include <hip/hip_runtime.h>
#include <hip/hip_bf16.h>
#include <cstdio>
using bf16 = __hip_bfloat16;
using bf16x8 = __attribute__((ext_vector_type(8))) short;
using s16x4  = __attribute__((ext_vector_type(4))) short;
using f32x16 = __attribute__((ext_vector_type(16))) float;
using u32x4  = __attribute__((ext_vector_type(4))) unsigned;
constexpr int SQ = 2048, DM = 576, H = 32, SK = 2048, DH = 128, FF = 4608;
__device__ __forceinline__ float bf2f(unsigned short u) { return __uint_as_float((unsigned)u << 16); }
__device__ __forceinline__ unsigned cvtpk(float lo, float hi) {
  unsigned r; asm volatile("v_cvt_pk_bf16_f32 %0, %1, %2" : "=v"(r) : "v"(lo), "v"(hi)); return r;
}
__device__ __forceinline__ unsigned short f2bf(float f) { return (unsigned short)(cvtpk(f, f) & 0xffff); }

__global__ __launch_bounds__(256) void k_ln(const unsigned short* x, unsigned short* xn) {
  int row = blockIdx.x * 4 + (threadIdx.x >> 6), lane = threadIdx.x & 63;
  const unsigned short* xr = x + (long)row * DM;
  float v[9]; float s = 0;
  for (int i = 0; i < 9; ++i) { v[i] = bf2f(xr[lane + 64 * i]); s += v[i]; }
  for (int o = 32; o; o >>= 1) s += __shfl_xor(s, o);
  float m = s / DM; float q = 0;
  for (int i = 0; i < 9; ++i) { float d = v[i] - m; q += d * d; }
  for (int o = 32; o; o >>= 1) q += __shfl_xor(q, o);
  float r = rsqrtf(q / DM + 1e-5f);
  for (int i = 0; i < 9; ++i) xn[(long)row * DM + lane + 64 * i] = f2bf((v[i] - m) * r);
}
template <int MODE>
__global__ __launch_bounds__(256) void k_gemm(const unsigned short* A, const unsigned short* B, void* C, const unsigned short* C2, int M, int N, int K) {
  __shared__ __attribute__((aligned(16))) unsigned short As[64 * 40], Bs[64 * 40];
  int tid = threadIdx.x, w = tid >> 6, lane = tid & 63, r32 = lane & 31, hi = lane >> 5;
  int m0 = blockIdx.y * 64, n0 = blockIdx.x * 64, wm = (w >> 1) * 32, wn = (w & 1) * 32;
  f32x16 acc = {};
  for (int k0 = 0; k0 < K; k0 += 32) {
    { int r = tid >> 2, c = (tid & 3) * 8;
      *(bf16x8*)&As[r * 40 + c] = *(const bf16x8*)&A[(long)(m0 + r) * K + k0 + c]; }
    { int kk = tid >> 3, c = (tid & 7) * 8;
      bf16x8 v = *(const bf16x8*)&B[(long)(k0 + kk) * N + n0 + c];
      for (int j = 0; j < 8; ++j) Bs[(c + j) * 40 + kk] = v[j]; }
    __syncthreads();
    for (int ks = 0; ks < 2; ++ks) {
      bf16x8 a = *(bf16x8*)&As[(wm + r32) * 40 + ks * 16 + hi * 8];
      bf16x8 b = *(bf16x8*)&Bs[(wn + r32) * 40 + ks * 16 + hi * 8];
      acc = __builtin_amdgcn_mfma_f32_32x32x16_bf16(a, b, acc, 0, 0, 0);
    }
    __syncthreads();
  }
  for (int r = 0; r < 16; ++r) {
    int row = m0 + wm + (r & 3) + 8 * (r >> 2) + 4 * hi, col = n0 + wn + r32;
    long idx = (long)row * N + col;
    if (MODE == 0) ((unsigned short*)C)[idx] = f2bf(acc[r]);
    else if (MODE == 1) ((float*)C)[idx] = acc[r];
    else ((unsigned short*)C)[idx] = f2bf(acc[r] * bf2f(C2[idx]));
  }
}
__global__ __launch_bounds__(256) void k_final(const unsigned short* x, const float* s, const float* g, unsigned short* out, int n) {
  int i = blockIdx.x * 256 + threadIdx.x; if (i >= n) return;
  float gv = g[i]; float sl = gv / (1.f + __expf(-gv));
  out[i] = f2bf(bf2f(x[i]) + s[i] * sl);
}
constexpr int D = 128, NW = 8, QBLK = 32, KVBLK = 64;
constexpr float SCALE = 0.088388347648318440f, THR = 8.f;
constexpr int LDQ = H * D, LDK = D, LDO = H * D;
constexpr size_t SHM_V = KVBLK * D * 2, SHM_K = KVBLK * D * 2, SHM_ATTN = 2 * SHM_V + 2 * SHM_K + NW * 64 * 4;
#define KSWZ(row, colB) ((row) * 256 + ((colB) ^ (((row) & 7) << 4)))
#define SBAR() __builtin_amdgcn_sched_barrier(0)
__device__ __forceinline__ int crow(int r, int hi) { return (r & 3) + 8 * (r >> 2) + 4 * hi; }
__device__ __forceinline__ void partialSM(f32x16& p0, f32x16& p1, float& m_reg, float& mn, float& alpha) {
  constexpr float C = SCALE * 1.4426950408889634f;
  float pmax = p0[0]; for (int r = 1; r < 16; ++r) pmax = fmaxf(pmax, p0[r]); for (int r = 0; r < 16; ++r) pmax = fmaxf(pmax, p1[r]);
  { auto rr = __builtin_amdgcn_permlane32_swap(__float_as_uint(pmax), __float_as_uint(pmax), false, false);
    pmax = fmaxf(__uint_as_float(rr[0]), __uint_as_float(rr[1])); }
  if (__builtin_expect(__all(pmax - m_reg <= THR / SCALE), 1)) { mn = m_reg; alpha = 1.f; }
  else { mn = fmaxf(m_reg, pmax); alpha = __builtin_amdgcn_exp2f((m_reg - mn) * C); m_reg = mn; }
  float mnC = -mn * C;
  for (int r = 0; r < 16; ++r) p0[r] = fmaf(p0[r], C, mnC); for (int r = 0; r < 16; ++r) p1[r] = fmaf(p1[r], C, mnC);
  for (int r = 0; r < 16; ++r) p0[r] = __builtin_amdgcn_exp2f(p0[r]);
}
__device__ __forceinline__ void finishSM(f32x16& p0, f32x16& p1, float alpha, float& l_reg, bf16x8& pa0, bf16x8& pa1, bf16x8& pa2, bf16x8& pa3) {
  for (int r = 0; r < 16; ++r) p1[r] = __builtin_amdgcn_exp2f(p1[r]);
  float ps = 0; for (int r = 0; r < 16; ++r) ps += p0[r]; for (int r = 0; r < 16; ++r) ps += p1[r];
  { auto rr = __builtin_amdgcn_permlane32_swap(__float_as_uint(ps), __float_as_uint(ps), false, false);
    ps = __uint_as_float(rr[0]) + __uint_as_float(rr[1]); }
  l_reg = l_reg * alpha + ps;
#define PK4(P, BASE, OUT) do { unsigned a0 = cvtpk(P[BASE + 0], P[BASE + 1]), a1 = cvtpk(P[BASE + 2], P[BASE + 3]);   \
    unsigned b0 = cvtpk(P[BASE + 4], P[BASE + 5]), b1 = cvtpk(P[BASE + 6], P[BASE + 7]);                              \
    auto r0 = __builtin_amdgcn_permlane32_swap(a0, b0, false, false); auto r1 = __builtin_amdgcn_permlane32_swap(a1, b1, false, false); \
    u32x4 w = {r0[0], r1[0], r0[1], r1[1]}; OUT = *reinterpret_cast<bf16x8*>(&w); } while (0)
  PK4(p0, 0, pa0); PK4(p0, 8, pa1); PK4(p1, 0, pa2); PK4(p1, 8, pa3);
#undef PK4
}
__device__ __forceinline__ void qkt(f32x16& p0, f32x16& p1, const bf16* Ks, const bf16x8* qr, int r32, int hi) {
  p0 = f32x16{}; p1 = f32x16{};
  for (int d0 = 0; d0 < 8; ++d0) { int cb = (d0 * 16 + hi * 8) * 2;
    bf16x8 b0 = *reinterpret_cast<const bf16x8*>((const char*)Ks + KSWZ(r32, cb));
    bf16x8 b1 = *reinterpret_cast<const bf16x8*>((const char*)Ks + KSWZ(32 + r32, cb));
    p0 = __builtin_amdgcn_mfma_f32_32x32x16_bf16(b0, qr[d0], p0, 0, 0, 0);
    p1 = __builtin_amdgcn_mfma_f32_32x32x16_bf16(b1, qr[d0], p1, 0, 0, 0); }
}
__device__ __forceinline__ int v_st(int k, int c) { const int kk = (k & ~0xC) | ((k & 4) << 1) | ((k & 8) >> 1); return ((kk >> 3) * 4 + (c >> 5)) * 512 + ((kk & 7) * 32 + (c & 31)) * 2; }
__device__ __forceinline__ int v_rd_base(int lane) { return ((lane & 3) << 3) | (((lane >> 2) & 3) << 6) | (((lane >> 4) & 1) << 5) | (((lane >> 5) & 1) << 8); }
constexpr int v_rd_off(int d0, int ks, int half) { return d0 * 512 + ks * 4096 + half * 2048; }
template <int OFF> __device__ __forceinline__ s16x4 tr_read(int vb) {
  s16x4 r; asm volatile("ds_read_b64_tr_b16 %0, %1 offset:%2" : "=&v"(r) : "v"(vb), "i"(OFF) : "memory"); return r;
}
template <int D0> __device__ __forceinline__ void pv_one(f32x16& od, int vb, bf16x8 pa0, bf16x8 pa1, bf16x8 pa2, bf16x8 pa3) {
  const s16x4 l0 = tr_read<v_rd_off(D0, 0, 0)>(vb), h0 = tr_read<v_rd_off(D0, 0, 1)>(vb), l1 = tr_read<v_rd_off(D0, 1, 0)>(vb), h1 = tr_read<v_rd_off(D0, 1, 1)>(vb);
  const s16x4 l2 = tr_read<v_rd_off(D0, 2, 0)>(vb), h2 = tr_read<v_rd_off(D0, 2, 1)>(vb), l3 = tr_read<v_rd_off(D0, 3, 0)>(vb), h3 = tr_read<v_rd_off(D0, 3, 1)>(vb);
  asm volatile("s_waitcnt lgkmcnt(0)" ::: "memory"); SBAR();
#define PK(L, H) (bf16x8){L[0], L[1], L[2], L[3], H[0], H[1], H[2], H[3]}
  od = __builtin_amdgcn_mfma_f32_32x32x16_bf16(pa0, PK(l0, h0), od, 0, 0, 0);
  od = __builtin_amdgcn_mfma_f32_32x32x16_bf16(pa1, PK(l1, h1), od, 0, 0, 0);
  od = __builtin_amdgcn_mfma_f32_32x32x16_bf16(pa2, PK(l2, h2), od, 0, 0, 0);
  od = __builtin_amdgcn_mfma_f32_32x32x16_bf16(pa3, PK(l3, h3), od, 0, 0, 0);
#undef PK
}
__device__ __forceinline__ void pv_d0(f32x16* o, int vb, bf16x8 pa0, bf16x8 pa1, bf16x8 pa2, bf16x8 pa3) {
  pv_one<0>(o[0], vb, pa0, pa1, pa2, pa3); pv_one<1>(o[1], vb, pa0, pa1, pa2, pa3); pv_one<2>(o[2], vb, pa0, pa1, pa2, pa3); pv_one<3>(o[3], vb, pa0, pa1, pa2, pa3);
}
__device__ __forceinline__ bf16x8 ld8(const bf16* p) { return *reinterpret_cast<const bf16x8*>(p); }
__device__ __forceinline__ void attn_dense_body(const bf16* __restrict__ Qb, const bf16* __restrict__ Kh, const bf16* __restrict__ Vh,
                                                unsigned short* __restrict__ Ob, int seq, char* lds) {
  constexpr int SDEPTH = 2;
  const int tid = threadIdx.x, wid = tid >> 6, lane = tid & 63, r32 = lane & 31, hi = lane >> 5;
  bf16* V_lds = (bf16*)lds; bf16* K_lds = (bf16*)(lds + 2 * SHM_V);
  float* ws = (float*)(lds + 2 * SHM_V + 2 * SHM_K) + wid * 64; float* li_l = ws; float* al_l = ws + 32;
  float m_reg = -1e30f, l_reg = 0; f32x16 o[4] = {}; bf16x8 qr[8];
  const bf16* Qw = Qb + (long)(wid * QBLK + r32) * LDQ + hi * 8;
#pragma unroll
  for (int d0 = 0; d0 < 8; ++d0) qr[d0] = ld8(Qw + d0 * 16);
  const int sr = tid >> 4, sc = (tid & 15) * 8, vst0 = v_st(sr, sc), vst1 = v_st(32 + sr, sc);
  const int vb0 = (int)(uintptr_t)V_lds + v_rd_base(lane);
  struct { bf16x8 vs0, vs1, ks0, ks1; } sr_[SDEPTH];
#define SLOAD(i, k0) do { sr_[i].vs0 = ld8(&Vh[(long)((k0) + sr) * LDK + sc]); sr_[i].vs1 = ld8(&Vh[(long)((k0) + 32 + sr) * LDK + sc]); \
    sr_[i].ks0 = ld8(&Kh[(long)((k0) + sr) * LDK + sc]); sr_[i].ks1 = ld8(&Kh[(long)((k0) + 32 + sr) * LDK + sc]); } while (0)
#define SWRITE(b, i) do { *(bf16x8*)((char*)V_lds + (b) * SHM_V + vst0) = sr_[i].vs0;          \
    *(bf16x8*)((char*)V_lds + (b) * SHM_V + vst1) = sr_[i].vs1; int kc = sc * 2;               \
    *(bf16x8*)((char*)K_lds + (b) * SHM_K + KSWZ(sr, kc)) = sr_[i].ks0;                       \
    *(bf16x8*)((char*)K_lds + (b) * SHM_K + KSWZ(32 + sr, kc)) = sr_[i].ks1; } while (0)
#define SWAIT() do { asm volatile("s_waitcnt vmcnt(4)" ::: "memory"); } while (0)
#define RESC(a) do { if (__any((a) < 1.f)) { if (hi == 0) al_l[r32] = (a); asm volatile("s_waitcnt lgkmcnt(0)" ::: "memory"); \
    for (int d = 0; d < 4; ++d) for (int r = 0; r < 16; ++r) o[d][r] *= al_l[crow(r, hi)]; } } while (0)
  f32x16 pA0, pA1, pB0, pB1; float mnA, mnB, alA, alB; bf16x8 pa0, pa1, pa2, pa3; const int NT = seq / KVBLK;
  constexpr int SE = 0, SO = SDEPTH - 1;
  SLOAD(SE, 0); asm volatile("s_waitcnt vmcnt(0)" ::: "memory"); SWRITE(0, SE); __syncthreads();
  qkt(pA0, pA1, K_lds, qr, r32, hi); partialSM(pA0, pA1, m_reg, mnA, alA);
  SLOAD(SO, KVBLK); if (2 < NT) SLOAD(SE, 2 * KVBLK);
  SWAIT(); SWRITE(1, SO); __syncthreads();
  for (int j = 1; j + 1 < NT; j += 2) {
    SBAR(); qkt(pB0, pB1, (bf16*)((char*)K_lds + SHM_K), qr, r32, hi);
    finishSM(pA0, pA1, alA, l_reg, pa0, pa1, pa2, pa3); SBAR();
    SLOAD(SO, (j + SDEPTH) * KVBLK); SBAR();
    pv_d0(o, vb0, pa0, pa1, pa2, pa3); partialSM(pB0, pB1, m_reg, mnB, alB);
    __syncthreads(); SWAIT(); SWRITE(0, SE);
    RESC(alB); __syncthreads();
    SBAR(); qkt(pA0, pA1, K_lds, qr, r32, hi);
    finishSM(pB0, pB1, alB, l_reg, pa0, pa1, pa2, pa3); SBAR();
    if (j + 3 < NT) SLOAD(SE, (j + 1 + SDEPTH) * KVBLK); SBAR();
    pv_d0(o, vb0 + (int)SHM_V, pa0, pa1, pa2, pa3); partialSM(pA0, pA1, m_reg, mnA, alA);
    __syncthreads(); SWAIT(); SWRITE(1, SO);
    RESC(alA); __syncthreads();
  }
  SBAR(); qkt(pB0, pB1, (bf16*)((char*)K_lds + SHM_K), qr, r32, hi);
  finishSM(pA0, pA1, alA, l_reg, pa0, pa1, pa2, pa3); SBAR();
  pv_d0(o, vb0, pa0, pa1, pa2, pa3); partialSM(pB0, pB1, m_reg, mnB, alB);
  __syncthreads(); RESC(alB);
  finishSM(pB0, pB1, alB, l_reg, pa0, pa1, pa2, pa3); SBAR();
  pv_d0(o, vb0 + (int)SHM_V, pa0, pa1, pa2, pa3);
  if (hi == 0) li_l[r32] = l_reg; asm volatile("s_waitcnt lgkmcnt(0)" ::: "memory");
  float rli[16];
#pragma unroll
  for (int r = 0; r < 16; ++r) rli[r] = __builtin_amdgcn_rcpf(li_l[crow(r, hi)]);
  unsigned short* Ow = Ob + (long)(wid * QBLK) * LDO;
#pragma unroll
  for (int r = 0; r < 16; ++r) { int orow = crow(r, hi);
    for (int d0 = 0; d0 < 4; ++d0) Ow[(long)orow * LDO + d0 * 32 + r32] = f2bf(o[d0][r] * rli[r]); }
}
__global__ __launch_bounds__(64 * NW, 1)
void attn_fwd_dense(const bf16* __restrict__ Q, const bf16* __restrict__ K, const bf16* __restrict__ V, unsigned short* __restrict__ O) {
  extern __shared__ __attribute__((aligned(16))) char shm[];
  const int b = blockIdx.x, h = (b & 7) * 4 + ((b >> 3) & 3), i0 = (b >> 5) * 256;
  attn_dense_body(Q + (long)i0 * LDQ + h * D, K + (long)h * SK * D, V + (long)h * SK * D, O + (long)i0 * LDO + h * D, SK, shm);
}

extern "C" void kernel_launch(void* const* d_in, const int* in_sizes, int n_in, void* d_out, int out_size, void* d_ws, size_t ws_size, hipStream_t stream) {
  const unsigned short* x = (const unsigned short*)d_in[0];
  const bf16* kc = (const bf16*)d_in[1]; const bf16* vc = (const bf16*)d_in[2];
  const unsigned short* wq = (const unsigned short*)d_in[3]; const unsigned short* wo = (const unsigned short*)d_in[4];
  const unsigned short* wg = (const unsigned short*)d_in[6]; const unsigned short* wu = (const unsigned short*)d_in[7]; const unsigned short* wd = (const unsigned short*)d_in[8];
  char* ws = (char*)d_ws;
  unsigned short* xn = (unsigned short*)ws;
  unsigned short* q = (unsigned short*)(ws + (4 << 20));
  unsigned short* o = (unsigned short*)(ws + (24 << 20));
  unsigned short* g = (unsigned short*)(ws + (44 << 20));
  unsigned short* hh = (unsigned short*)(ws + (64 << 20));
  float* s = (float*)(ws + (84 << 20)); float* gd = (float*)(ws + (92 << 20));
  (void)hipFuncSetAttribute((const void*)attn_fwd_dense, hipFuncAttributeMaxDynamicSharedMemorySize, (int)SHM_ATTN);
  k_ln<<<SQ / 4, 256, 0, stream>>>(x, xn);
  k_gemm<0><<<dim3(H * DH / 64, SQ / 64), 256, 0, stream>>>(xn, wq, q, nullptr, SQ, H * DH, DM);
  k_gemm<0><<<dim3(FF / 64, SQ / 64), 256, 0, stream>>>(xn, wg, g, nullptr, SQ, FF, DM);
  k_gemm<2><<<dim3(FF / 64, SQ / 64), 256, 0, stream>>>(xn, wu, hh, g, SQ, FF, DM);
  attn_fwd_dense<<<256, 512, SHM_ATTN, stream>>>((const bf16*)q, kc, vc, o);
  k_gemm<1><<<dim3(DM / 64, SQ / 64), 256, 0, stream>>>(o, wo, s, nullptr, SQ, DM, H * DH);
  k_gemm<1><<<dim3(DM / 64, SQ / 64), 256, 0, stream>>>(hh, wd, gd, nullptr, SQ, DM, FF);
  k_final<<<(SQ * DM + 255) / 256, 256, 0, stream>>>(x, s, gd, (unsigned short*)d_out, SQ * DM);
}
```

```cpp
#include <hip/hip_runtime.h>

__global__ __launch_bounds__(256, 1) void k_block(const void* x, const void* wq, const void* wg, const void* wu, const void* kc, const void* vc, const void* wo, const void* wd, void* out, void* ws) {
  asm volatile(
    "s_load_dwordx16 s[36:51], %0, 0\n"
    "s_load_dwordx4 s[52:55], %0, 64\n"
    "s_mov_b32 s68, %2\n"
    "s_and_b32 s69, %1, 7\n"
    "s_lshl_b32 s69, s69, 2\n"
    "s_lshr_b32 s72, %1, 3\n"
    "s_and_b32 s72, s72, 3\n"
    "s_add_u32 s69, s69, s72\n"
    "s_lshr_b32 s70, %1, 5\n"
    "s_and_b32 s75, s69, 1\n"
    "v_mbcnt_lo_u32_b32 v253, -1, 0\n"
    "v_mbcnt_hi_u32_b32 v253, -1, v253\n"
    "v_and_b32 v100, 7, v253\n"
    "v_lshrrev_b32 v101, 3, v253\n"
    "v_mul_u32_u24 v102, 1152, v100\n"
    "v_lshl_add_u32 v102, v101, 4, v102\n"
    "s_lshl_b32 s72, s70, 5\n"
    "s_add_u32 s72, s72, s69\n"
    "s_mul_i32 s72, s72, 9216\n"
    "s_lshl_b32 s73, s68, 7\n"
    "s_add_u32 s72, s72, s73\n"
    "s_lshl_b32 s6, s68, 30\n"
    "s_add_u32 s73, s72, s6\n"
    "s_lshl_b32 s87, s70, 16\n"
    "v_lshrrev_b32 v104, 2, v253\n"
    "v_and_b32 v105, 3, v253\n"
    "v_lshlrev_b32 v106, 13, v104\n"
    "v_lshl_add_u32 v243, v105, 4, v106\n"
    "v_lshrrev_b32 v107, 4, v253\n"
    "v_and_b32 v107, 2, v107\n"
    "v_xor_b32 v107, v107, v105\n"
    "v_mul_u32_u24 v106, 9216, v104\n"
    "v_lshl_add_u32 v244, v107, 4, v106\n"
    "v_and_b32 v107, 1, v107\n"
    "v_lshl_add_u32 v245, v107, 4, v106\n"
    "s_mov_b32 s12, 0x33333333\n"
    "s_mov_b32 s13, 0xcccccccc\n"
    "s_mov_b32 s14, 0xcccccccc\n"
    "s_mov_b32 s15, 0x33333333\n"
    "v_lshlrev_b32 v114, 4, v100\n"
    "v_add_u32 v114, 159744, v114\n"
    "s_and_b32 s4, s68, 1\n"
    "s_lshl_b32 s4, s4, 7\n"
    "v_add_u32 v115, s4, v114\n"
    "s_xor_b32 s4, s4, 128\n"
    "v_add_u32 v114, s4, v114\n"
    "v_mov_b32 v98, 0\n"
    "v_mov_b32 v99, 1\n"
    "ds_write_b64 v115, v[98:99] offset:8\n"
    "s_waitcnt lgkmcnt(0)\n"
    "s_barrier\n"
    "s_mov_b32 s56, s36\n"
    "s_and_b32 s57, s37, 0xffff\n"
    "s_mov_b32 s58, 2359296\n"
    "s_mov_b32 s59, 0x20000\n"
    "s_cmp_ge_u32 s68, 2\n"
    "s_cbranch_scc1 L_nox\n"
    "buffer_load_dwordx4 v[0:3], v102, s[56:59], s72 offen offset:0\n"
    "buffer_load_dwordx4 v[4:7], v102, s[56:59], s72 offen offset:256\n"
    "buffer_load_dwordx4 v[8:11], v102, s[56:59], s72 offen offset:512\n"
    "buffer_load_dwordx4 v[12:15], v102, s[56:59], s72 offen offset:768\n"
    "buffer_load_dwordx4 v[16:19], v102, s[56:59], s73 offen offset:1024\n"
    "L_nox:\n"
    "s_mov_b32 s20, s38\n"
    "s_and_b32 s21, s39, 0xffff\n"
    "s_mov_b32 s22, 4718592\n"
    "s_mov_b32 s23, 0x20000\n"
    "s_mov_b32 s24, s40\n"
    "s_and_b32 s25, s41, 0xffff\n"
    "s_mov_b32 s26, 5308416\n"
    "s_mov_b32 s27, 0x20000\n"
    "s_mov_b32 s28, s42\n"
    "s_and_b32 s29, s43, 0xffff\n"
    "s_mov_b32 s30, 5308416\n"
    "s_mov_b32 s31, 0x20000\n"
    "s_lshl_b32 s72, s70, 2\n"
    "s_add_u32 s72, s72, s68\n"
    "s_cmp_ge_u32 s72, 8\n"
    "s_cbranch_scc1 L_pfg\n"
    "s_mov_b64 s[8:9], s[20:21]\n"
    "s_mov_b64 s[10:11], s[22:23]\n"
    "s_mov_b32 s32, 524288\n"
    "s_lshl_b32 s72, s72, 16\n"
    "s_lshl_b32 s7, s69, 8\n"
    "s_add_u32 s7, s7, s72\n"
    "v_lshrrev_b32 v255, 1, v253\n"
    "v_lshlrev_b32 v255, 13, v255\n"
    "v_and_b32 v107, 1, v253\n"
    "v_lshl_add_u32 v255, v107, 7, v255\n"
    "s_branch L_pfd\n"
    "L_pfg:\n"
    "s_sub_u32 s72, s72, 8\n"
    "s_cmp_ge_u32 s72, 12\n"
    "s_cselect_b64 s[8:9], s[28:29], s[24:25]\n"
    "s_cselect_b32 s86, 12, 0\n"
    "s_mov_b64 s[10:11], s[26:27]\n"
    "s_sub_u32 s72, s72, s86\n"
    "s_lshl_b32 s72, s72, 4\n"
    "v_add_u32 v107, s72, v253\n"
    "v_mul_u32_u24 v108, 21846, v107\n"
    "v_lshrrev_b32 v108, 16, v108\n"
    "v_mul_u32_u24 v255, 8832, v108\n"
    "v_lshl_add_u32 v255, v107, 7, v255\n"
    "s_mov_b32 s32, 589824\n"
    "s_mul_i32 s7, s69, 288\n"
    "s_and_b32 s7, s7, 0xffffff80\n"
    "L_pfd:\n"
    "s_sub_u32 s7, s7, s32\n"
    "s_mov_b32 m0, 160768\n"
    "s_add_u32 s7, s7, s32\n"
    "s_mov_b64 exec, 0xffff\n"
    "buffer_load_dword v255, s[8:11], s7 offen lds\n"
    "s_mov_b64 exec, -1\n"
    "s_mov_b32 m0, 160768\n"
    "s_add_u32 s7, s7, s32\n"
    "s_mov_b64 exec, 0xffff\n"
    "buffer_load_dword v255, s[8:11], s7 offen lds\n"
    "s_mov_b64 exec, -1\n"
    "s_cmp_lt_u32 s68, 2\n"
    "s_cbranch_scc1 L_now\n"
    "s_lshl_b32 s86, s68, 1\n"
    "s_sub_u32 s86, s86, 4\n"
    "s_lshl_b32 s78, s86, 17\n"
    "s_lshl_b32 s72, s69, 8\n"
    "s_add_u32 s78, s78, s72\n"
    "s_mul_i32 s79, s86, 147456\n"
    "s_mul_i32 s72, s69, 288\n"
    "s_add_u32 s79, s79, s72\n"
    "s_lshl_b32 s72, s75, 8\n"
    "s_sub_u32 s72, 256, s72\n"
    "s_add_u32 s80, s79, s72\n"
    "s_lshl_b32 s72, s75, 5\n"
    "s_add_u32 s79, s79, s72\n"
    "s_lshl_b32 s76, s86, 10\n"
    "s_add_u32 s76, s76, 106496\n"
    "s_add_u32 m0, s76, 0\n"
    "s_add_u32 s72, s78, 0\n"
    "buffer_load_dwordx4 v243, s[20:23], s72 offen lds\n"
    "s_add_u32 m0, s76, 4096\n"
    "s_add_u32 s72, s78, 64\n"
    "buffer_load_dwordx4 v243, s[20:23], s72 offen lds\n"
    "s_add_u32 m0, s76, 8192\n"
    "s_add_u32 s72, s78, 128\n"
    "buffer_load_dwordx4 v243, s[20:23], s72 offen lds\n"
    "s_add_u32 m0, s76, 12288\n"
    "s_add_u32 s72, s78, 192\n"
    "buffer_load_dwordx4 v243, s[20:23], s72 offen lds\n"
    "s_add_u32 m0, s76, 16384\n"
    "s_add_u32 s72, s79, 0\n"
    "buffer_load_dwordx4 v244, s[24:27], s72 offen lds\n"
    "s_add_u32 m0, s76, 20480\n"
    "s_add_u32 s72, s79, 64\n"
    "buffer_load_dwordx4 v244, s[24:27], s72 offen lds\n"
    "s_add_u32 m0, s76, 24576\n"
    "s_add_u32 s72, s79, 128\n"
    "buffer_load_dwordx4 v244, s[24:27], s72 offen lds\n"
    "s_add_u32 m0, s76, 28672\n"
    "s_add_u32 s72, s79, 192\n"
    "buffer_load_dwordx4 v244, s[24:27], s72 offen lds\n"
    "s_add_u32 m0, s76, 49152\n"
    "s_nop 0\n"
    "s_mov_b64 exec, s[12:13]\n"
    "buffer_load_dwordx4 v245, s[24:27], s80 offen lds\n"
    "s_mov_b64 exec, -1\n"
    "s_add_u32 m0, s76, 32768\n"
    "s_add_u32 s72, s79, 0\n"
    "buffer_load_dwordx4 v244, s[28:31], s72 offen lds\n"
    "s_add_u32 m0, s76, 36864\n"
    "s_add_u32 s72, s79, 64\n"
    "buffer_load_dwordx4 v244, s[28:31], s72 offen lds\n"
    "s_add_u32 m0, s76, 40960\n"
    "s_add_u32 s72, s79, 128\n"
    "buffer_load_dwordx4 v244, s[28:31], s72 offen lds\n"
    "s_add_u32 m0, s76, 45056\n"
    "s_add_u32 s72, s79, 192\n"
    "buffer_load_dwordx4 v244, s[28:31], s72 offen lds\n"
    "s_add_u32 m0, s76, 49152\n"
    "s_nop 0\n"
    "s_mov_b64 exec, s[14:15]\n"
    "buffer_load_dwordx4 v245, s[28:31], s80 offen lds\n"
    "s_mov_b64 exec, -1\n"
    "s_add_u32 s78, s78, 131072\n"
    "s_add_u32 s79, s79, 147456\n"
    "s_add_u32 s80, s80, 147456\n"
    "s_add_u32 s76, s76, 1024\n"
    "s_add_u32 m0, s76, 0\n"
    "s_add_u32 s72, s78, 0\n"
    "buffer_load_dwordx4 v243, s[20:23], s72 offen lds\n"
    "s_add_u32 m0, s76, 4096\n"
    "s_add_u32 s72, s78, 64\n"
    "buffer_load_dwordx4 v243, s[20:23], s72 offen lds\n"
    "s_add_u32 m0, s76, 8192\n"
    "s_add_u32 s72, s78, 128\n"
    "buffer_load_dwordx4 v243, s[20:23], s72 offen lds\n"
    "s_add_u32 m0, s76, 12288\n"
    "s_add_u32 s72, s78, 192\n"
    "buffer_load_dwordx4 v243, s[20:23], s72 offen lds\n"
    "s_add_u32 m0, s76, 16384\n"
    "s_add_u32 s72, s79, 0\n"
    "buffer_load_dwordx4 v244, s[24:27], s72 offen lds\n"
    "s_add_u32 m0, s76, 20480\n"
    "s_add_u32 s72, s79, 64\n"
    "buffer_load_dwordx4 v244, s[24:27], s72 offen lds\n"
    "s_add_u32 m0, s76, 24576\n"
    "s_add_u32 s72, s79, 128\n"
    "buffer_load_dwordx4 v244, s[24:27], s72 offen lds\n"
    "s_add_u32 m0, s76, 28672\n"
    "s_add_u32 s72, s79, 192\n"
    "buffer_load_dwordx4 v244, s[24:27], s72 offen lds\n"
    "s_add_u32 m0, s76, 49152\n"
    "s_nop 0\n"
    "s_mov_b64 exec, s[12:13]\n"
    "buffer_load_dwordx4 v245, s[24:27], s80 offen lds\n"
    "s_mov_b64 exec, -1\n"
    "s_add_u32 m0, s76, 32768\n"
    "s_add_u32 s72, s79, 0\n"
    "buffer_load_dwordx4 v244, s[28:31], s72 offen lds\n"
    "s_add_u32 m0, s76, 36864\n"
    "s_add_u32 s72, s79, 64\n"
    "buffer_load_dwordx4 v244, s[28:31], s72 offen lds\n"
    "s_add_u32 m0, s76, 40960\n"
    "s_add_u32 s72, s79, 128\n"
    "buffer_load_dwordx4 v244, s[28:31], s72 offen lds\n"
    "s_add_u32 m0, s76, 45056\n"
    "s_add_u32 s72, s79, 192\n"
    "buffer_load_dwordx4 v244, s[28:31], s72 offen lds\n"
    "s_add_u32 m0, s76, 49152\n"
    "s_nop 0\n"
    "s_mov_b64 exec, s[14:15]\n"
    "buffer_load_dwordx4 v245, s[28:31], s80 offen lds\n"
    "s_mov_b64 exec, -1\n"
    "L_now:\n"
    "s_mov_b32 s60, s52\n"
    "s_and_b32 s61, s53, 0xffff\n"
    "s_mov_b32 s62, 2359296\n"
    "s_mov_b32 s63, 0x20000\n"
    "s_add_u32 s16, s54, 1048576\n"
    "s_addc_u32 s17, s55, 0\n"
    "s_and_b32 s17, s17, 0xffff\n"
    "s_mov_b32 s18, 2359296\n"
    "s_mov_b32 s19, 0x20000\n"
    "s_mov_b64 s[64:65], s[48:49]\n"
    "s_mov_b64 s[66:67], s[50:51]\n"
    "s_lshl_b32 s72, s69, 19\n"
    "s_add_u32 s36, s44, s72\n"
    "s_addc_u32 s37, s45, 0\n"
    "s_and_b32 s37, s37, 0xffff\n"
    "s_mov_b32 s38, 524288\n"
    "s_mov_b32 s39, 0x20000\n"
    "s_add_u32 s40, s46, s72\n"
    "s_addc_u32 s41, s47, 0\n"
    "s_and_b32 s41, s41, 0xffff\n"
    "s_mov_b32 s42, 524288\n"
    "s_mov_b32 s43, 0x20000\n"
    "s_add_u32 s44, s54, 4194304\n"
    "s_addc_u32 s45, s55, 0\n"
    "s_and_b32 s45, s45, 0xffff\n"
    "s_mov_b32 s46, 18874368\n"
    "s_mov_b32 s47, 0x20000\n"
    "s_add_u32 s48, s54, 25165824\n"
    "s_addc_u32 s49, s55, 0\n"
    "s_and_b32 s49, s49, 0xffff\n"
    "s_mov_b32 s50, 16777216\n"
    "s_mov_b32 s51, 0x20000\n"
    "s_add_u32 s52, s54, s87\n"
    "s_addc_u32 s53, s55, 0\n"
    "s_and_b32 s53, s53, 0xffff\n"
    "s_mov_b32 s54, 65536\n"
    "s_mov_b32 s55, 0x20000\n"
    "buffer_load_dword v112, off, s[52:55], 0 sc1\n"
    "s_lshl_b32 s78, s68, 17\n"
    "s_lshl_b32 s72, s69, 8\n"
    "s_add_u32 s78, s78, s72\n"
    "s_mul_i32 s79, s68, 147456\n"
    "s_mul_i32 s72, s69, 288\n"
    "s_add_u32 s79, s79, s72\n"
    "s_lshl_b32 s72, s75, 8\n"
    "s_sub_u32 s72, 256, s72\n"
    "s_add_u32 s80, s79, s72\n"
    "s_lshl_b32 s72, s75, 5\n"
    "s_add_u32 s79, s79, s72\n"
    "s_lshl_b32 s76, s68, 10\n"
    "s_add_u32 s76, s76, 106496\n"
    "s_add_u32 s78, s78, 524288\n"
    "s_add_u32 s79, s79, 589824\n"
    "s_add_u32 s80, s80, 589824\n"
    "s_add_u32 s76, s76, 53248\n"
    "s_cmp_ge_u32 s76, 159744\n"
    "s_cselect_b32 s73, 159744, 0\n"
    "s_sub_u32 s76, s76, s73\n"
    "v_lshlrev_b32 v246, 4, v253\n"
    "v_lshrrev_b32 v106, 4, v253\n"
    "v_and_b32 v107, 3, v104\n"
    "v_lshl_add_u32 v107, v106, 3, v107\n"
    "v_lshlrev_b32 v107, 6, v107\n"
    "v_add_u32 v107, 106496, v107\n"
    "v_lshl_add_u32 v240, v105, 4, v107\n"
    "v_and_b32 v108, 1, v106\n"
    "v_lshlrev_b32 v108, 5, v108\n"
    "v_lshl_add_u32 v107, v105, 3, v107\n"
    "v_add_u32 v241, v107, v108\n"
    "v_xor_b32 v108, 32, v108\n"
    "v_add_u32 v242, v107, v108\n"
    "v_and_b32 v107, 15, v253\n"
    "v_xor_b32 v107, v107, v106\n"
    "v_lshlrev_b32 v107, 4, v107\n"
    "v_lshl_add_u32 v247, v106, 8, v107\n"
    "v_xor_b32 v248, 64, v247\n"
    "v_xor_b32 v249, 128, v247\n"
    "v_xor_b32 v250, 192, v247\n"
    "v_and_b32 v107, 7, v104\n"
    "v_lshrrev_b32 v108, 3, v253\n"
    "v_and_b32 v108, 2, v108\n"
    "v_xor_b32 v108, v108, v105\n"
    "v_lshlrev_b32 v108, 4, v108\n"
    "v_lshl_add_u32 v108, v107, 8, v108\n"
    "v_lshrrev_b32 v107, 5, v253\n"
    "v_lshl_add_u32 v251, v107, 6, v108\n"
    "v_add_u32 v252, 128, v251\n"
    "s_lshl_b32 s72, s70, 4\n"
    "s_lshl_b32 s73, s68, 2\n"
    "s_add_u32 s72, s72, s73\n"
    "s_mul_i32 s81, s72, 18432\n"
    "s_mul_i32 s33, s72, 147456\n"
    "s_lshl_b32 s84, s68, 12\n"
    "s_add_u32 s85, s84, 98304\n"
    "s_mov_b32 s82, 106496\n"
    "s_mov_b32 s83, 53248\n"
    "s_mov_b32 s71, 2\n"
    "v_accvgpr_write_b32 a240, 0\n"
    "v_accvgpr_write_b32 a241, 0\n"
    "v_accvgpr_write_b32 a242, 0\n"
    "v_accvgpr_write_b32 a243, 0\n"
    "s_cmp_lt_u32 s68, 2\n"
    "s_cbranch_scc1 L_ln\n"
    "s_mov_b32 m0, 161792\n"
    "s_lshl_b32 s72, s70, 1\n"
    "s_add_u32 s72, s72, s68\n"
    "s_sub_u32 s72, s72, 2\n"
    "s_mul_i32 s73, s72, 1179648\n"
    "s_lshl_b32 s72, s72, 20\n"
    "buffer_load_dword off, s[36:39], 0 sc1 lds\n"
    "buffer_load_dword off, s[40:43], 0 sc1 lds\n"
    "buffer_load_dword off, s[44:47], s73 sc1 lds\n"
    "buffer_load_dword off, s[48:51], s72 sc1 lds\n"
    "s_lshl_b32 s72, s68, 21\n"
    "s_sub_u32 s72, s72, 2097152\n"
    "buffer_load_dword off, s[20:23], s72 sc1 lds\n"
    "buffer_load_dword off, s[24:27], s72 sc1 lds\n"
    "buffer_load_dword off, s[28:31], s72 sc1 lds\n"
    "s_mul_i32 s72, s70, 294912\n"
    "buffer_load_dword off, s[60:63], s72 sc1 lds\n"
    "s_getpc_b64 s[86:87]\n"
    "L_pc0:\n"
    "s_lshr_b32 s72, %1, 3\n"
    "s_lshl_b32 s72, s72, 1\n"
    "s_add_u32 s72, s72, s68\n"
    "s_sub_u32 s72, s72, 2\n"
    "s_lshl_b32 s72, s72, 10\n"
    "s_min_u32 s72, s72, L_end-L_pc0-1024\n"
    "s_add_u32 s86, s86, s72\n"
    "s_addc_u32 s87, s87, 0\n"
    "global_load_dwordx4 v[0:3], v246, s[86:87]\n"
    "s_waitcnt vmcnt(9)\n"
    "v_readfirstlane_b32 s74, v112\n"
    "s_nop 1\n"
    "s_add_u32 s74, s74, 1\n"
    "s_branch L_join\n"
    "L_ln:\n"
    "v_lshrrev_b32 v103, 2, v101\n"
    "v_lshlrev_b32 v103, 10, v103\n"
    "v_and_b32 v113, 3, v101\n"
    "v_lshl_add_u32 v103, v113, 8, v103\n"
    "v_lshl_add_u32 v103, v100, 4, v103\n"
    "s_lshl_b32 s4, s70, 4\n"
    "s_lshr_b32 s5, s69, 1\n"
    "s_add_u32 s4, s4, s5\n"
    "s_mul_i32 s4, s4, 18432\n"
    "s_lshl_b32 s5, s75, 7\n"
    "s_add_u32 s4, s4, s5\n"
    "s_lshl_b32 s5, s68, 11\n"
    "s_add_u32 s4, s4, s5\n"
    "s_lshl_b32 s86, s69, 1\n"
    "s_add_u32 s86, s86, s68\n"
    "s_lshl_b32 s86, s86, 7\n"
    "v_mov_b32 v96, 0\n"
    "v_mov_b32 v97, 0\n"
    "v_mov_b32 v98, 0\n"
    "v_mov_b32 v99, 0\n"
    "v_mov_b32 v116, 0x3f803f80\n"
    "s_waitcnt vmcnt(0)\n"
    "v_readfirstlane_b32 s74, v112\n"
    "v_dot2_f32_bf16 v96, v0, v116, v96\n"
    "v_dot2_f32_bf16 v97, v0, v0, v97\n"
    "v_dot2_f32_bf16 v98, v1, v116, v98\n"
    "v_dot2_f32_bf16 v99, v1, v1, v99\n"
    "v_dot2_f32_bf16 v96, v2, v116, v96\n"
    "v_dot2_f32_bf16 v97, v2, v2, v97\n"
    "v_dot2_f32_bf16 v98, v3, v116, v98\n"
    "v_dot2_f32_bf16 v99, v3, v3, v99\n"
    "v_dot2_f32_bf16 v96, v4, v116, v96\n"
    "v_dot2_f32_bf16 v97, v4, v4, v97\n"
    "v_dot2_f32_bf16 v98, v5, v116, v98\n"
    "v_dot2_f32_bf16 v99, v5, v5, v99\n"
    "v_dot2_f32_bf16 v96, v6, v116, v96\n"
    "v_dot2_f32_bf16 v97, v6, v6, v97\n"
    "v_dot2_f32_bf16 v98, v7, v116, v98\n"
    "v_dot2_f32_bf16 v99, v7, v7, v99\n"
    "v_dot2_f32_bf16 v96, v8, v116, v96\n"
    "v_dot2_f32_bf16 v97, v8, v8, v97\n"
    "v_dot2_f32_bf16 v98, v9, v116, v98\n"
    "v_dot2_f32_bf16 v99, v9, v9, v99\n"
    "v_dot2_f32_bf16 v96, v10, v116, v96\n"
    "v_dot2_f32_bf16 v97, v10, v10, v97\n"
    "v_dot2_f32_bf16 v98, v11, v116, v98\n"
    "v_dot2_f32_bf16 v99, v11, v11, v99\n"
    "v_dot2_f32_bf16 v96, v12, v116, v96\n"
    "v_dot2_f32_bf16 v97, v12, v12, v97\n"
    "v_dot2_f32_bf16 v98, v13, v116, v98\n"
    "v_dot2_f32_bf16 v99, v13, v13, v99\n"
    "v_dot2_f32_bf16 v96, v14, v116, v96\n"
    "v_dot2_f32_bf16 v97, v14, v14, v97\n"
    "v_dot2_f32_bf16 v98, v15, v116, v98\n"
    "v_dot2_f32_bf16 v99, v15, v15, v99\n"
    "v_dot2_f32_bf16 v96, v16, v116, v96\n"
    "v_dot2_f32_bf16 v97, v16, v16, v97\n"
    "v_dot2_f32_bf16 v98, v17, v116, v98\n"
    "v_dot2_f32_bf16 v99, v17, v17, v99\n"
    "v_dot2_f32_bf16 v96, v18, v116, v96\n"
    "v_dot2_f32_bf16 v97, v18, v18, v97\n"
    "v_dot2_f32_bf16 v98, v19, v116, v98\n"
    "v_dot2_f32_bf16 v99, v19, v19, v99\n"
    "s_add_u32 s74, s74, 1\n"
    "s_nop 1\n"
    "v_add_f32 v96, v96, v98\n"
    "v_add_f32 v97, v97, v99\n"
    "s_nop 1\n"
    "v_add_f32_dpp v96, v96, v96 row_ror:8 row_mask:0xf bank_mask:0xf\n"
    "v_add_f32_dpp v97, v97, v97 row_ror:8 row_mask:0xf bank_mask:0xf\n"
    "v_mov_b32 v98, v96\n"
    "v_mov_b32 v99, v97\n"
    "s_nop 1\n"
    "v_permlane16_swap_b32 v96, v98\n"
    "v_permlane16_swap_b32 v97, v99\n"
    "s_nop 1\n"
    "v_add_f32 v96, v96, v98\n"
    "v_add_f32 v97, v97, v99\n"
    "v_mov_b32 v98, v96\n"
    "v_mov_b32 v99, v97\n"
    "s_nop 1\n"
    "v_permlane32_swap_b32 v96, v98\n"
    "v_permlane32_swap_b32 v97, v99\n"
    "s_nop 1\n"
    "v_add_f32 v96, v96, v98\n"
    "v_add_f32 v97, v97, v99\n"
    "v_mov_b32 v98, s74\n"
    "v_mov_b32 v99, s74\n"
    "ds_write_b128 v115, v[96:99]\n"
    "v_lshlrev_b32 v20, 16, v0\n"
    "v_and_b32 v21, 0xffff0000, v0\n"
    "v_lshlrev_b32 v22, 16, v1\n"
    "v_and_b32 v23, 0xffff0000, v1\n"
    "v_lshlrev_b32 v24, 16, v2\n"
    "v_and_b32 v25, 0xffff0000, v2\n"
    "v_lshlrev_b32 v26, 16, v3\n"
    "v_and_b32 v27, 0xffff0000, v3\n"
    "v_lshlrev_b32 v28, 16, v4\n"
    "v_and_b32 v29, 0xffff0000, v4\n"
    "v_lshlrev_b32 v30, 16, v5\n"
    "v_and_b32 v31, 0xffff0000, v5\n"
    "v_lshlrev_b32 v32, 16, v6\n"
    "v_and_b32 v33, 0xffff0000, v6\n"
    "v_lshlrev_b32 v34, 16, v7\n"
    "v_and_b32 v35, 0xffff0000, v7\n"
    "v_lshlrev_b32 v36, 16, v8\n"
    "v_and_b32 v37, 0xffff0000, v8\n"
    "v_lshlrev_b32 v38, 16, v9\n"
    "v_and_b32 v39, 0xffff0000, v9\n"
    "v_lshlrev_b32 v40, 16, v10\n"
    "v_and_b32 v41, 0xffff0000, v10\n"
    "v_lshlrev_b32 v42, 16, v11\n"
    "v_and_b32 v43, 0xffff0000, v11\n"
    "v_lshlrev_b32 v44, 16, v12\n"
    "v_and_b32 v45, 0xffff0000, v12\n"
    "v_lshlrev_b32 v46, 16, v13\n"
    "v_and_b32 v47, 0xffff0000, v13\n"
    "v_lshlrev_b32 v48, 16, v14\n"
    "v_and_b32 v49, 0xffff0000, v14\n"
    "v_lshlrev_b32 v50, 16, v15\n"
    "v_and_b32 v51, 0xffff0000, v15\n"
    "v_lshlrev_b32 v52, 16, v16\n"
    "v_and_b32 v53, 0xffff0000, v16\n"
    "v_lshlrev_b32 v54, 16, v17\n"
    "v_and_b32 v55, 0xffff0000, v17\n"
    "v_lshlrev_b32 v56, 16, v18\n"
    "v_and_b32 v57, 0xffff0000, v18\n"
    "v_lshlrev_b32 v58, 16, v19\n"
    "v_and_b32 v59, 0xffff0000, v19\n"
    "s_mov_b32 s87, 0x100000\n"
    "L_lnx:\n"
    "ds_read_b128 v[108:111], v114\n"
    "s_waitcnt lgkmcnt(0)\n"
    "v_xor_b32 v110, s74, v110\n"
    "v_xor_b32 v111, s74, v111\n"
    "v_or_b32 v110, v110, v111\n"
    "v_cmp_ne_u32 vcc, 0, v110\n"
    "s_cbranch_vccz L_lny\n"
    "s_sub_u32 s87, s87, 1\n"
    "s_cbranch_scc0 L_lnx\n"
    "L_lny:\n"
    "v_add_f32 v0, v96, v108\n"
    "v_add_f32 v1, v97, v109\n"
    "v_mul_f32 v0, 0x3ae38e39, v0\n"
    "v_mul_f32 v1, 0x3ae38e39, v1\n"
    "v_fma_f32 v1, -v0, v0, v1\n"
    "v_max_f32 v1, 0, v1\n"
    "v_add_f32 v1, 0x3727c5ac, v1\n"
    "v_rsq_f32 v1, v1\n"
    "s_nop 0\n"
    "v_mul_f32 v1, 0x3e0293ee, v1\n"
    "v_mul_f32 v0, v0, v1\n"
    "v_fma_f32 v20, v20, v1, -v0\n"
    "v_fma_f32 v21, v21, v1, -v0\n"
    "v_fma_f32 v22, v22, v1, -v0\n"
    "v_fma_f32 v23, v23, v1, -v0\n"
    "v_fma_f32 v24, v24, v1, -v0\n"
    "v_fma_f32 v25, v25, v1, -v0\n"
    "v_fma_f32 v26, v26, v1, -v0\n"
    "v_fma_f32 v27, v27, v1, -v0\n"
    "v_cvt_pk_bf16_f32 v20, v20, v21\n"
    "v_cvt_pk_bf16_f32 v21, v22, v23\n"
    "v_cvt_pk_bf16_f32 v22, v24, v25\n"
    "v_cvt_pk_bf16_f32 v23, v26, v27\n"
    "buffer_store_dwordx4 v[20:23], v103, s[16:19], s4 offen sc1\n"
    "s_add_u32 s4, s4, 4096\n"
    "v_fma_f32 v28, v28, v1, -v0\n"
    "v_fma_f32 v29, v29, v1, -v0\n"
    "v_fma_f32 v30, v30, v1, -v0\n"
    "v_fma_f32 v31, v31, v1, -v0\n"
    "v_fma_f32 v32, v32, v1, -v0\n"
    "v_fma_f32 v33, v33, v1, -v0\n"
    "v_fma_f32 v34, v34, v1, -v0\n"
    "v_fma_f32 v35, v35, v1, -v0\n"
    "v_cvt_pk_bf16_f32 v28, v28, v29\n"
    "v_cvt_pk_bf16_f32 v29, v30, v31\n"
    "v_cvt_pk_bf16_f32 v30, v32, v33\n"
    "v_cvt_pk_bf16_f32 v31, v34, v35\n"
    "buffer_store_dwordx4 v[28:31], v103, s[16:19], s4 offen sc1\n"
    "s_add_u32 s4, s4, 4096\n"
    "v_fma_f32 v36, v36, v1, -v0\n"
    "v_fma_f32 v37, v37, v1, -v0\n"
    "v_fma_f32 v38, v38, v1, -v0\n"
    "v_fma_f32 v39, v39, v1, -v0\n"
    "v_fma_f32 v40, v40, v1, -v0\n"
    "v_fma_f32 v41, v41, v1, -v0\n"
    "v_fma_f32 v42, v42, v1, -v0\n"
    "v_fma_f32 v43, v43, v1, -v0\n"
    "v_cvt_pk_bf16_f32 v36, v36, v37\n"
    "v_cvt_pk_bf16_f32 v37, v38, v39\n"
    "v_cvt_pk_bf16_f32 v38, v40, v41\n"
    "v_cvt_pk_bf16_f32 v39, v42, v43\n"
    "buffer_store_dwordx4 v[36:39], v103, s[16:19], s4 offen sc1\n"
    "s_add_u32 s4, s4, 4096\n"
    "v_fma_f32 v44, v44, v1, -v0\n"
    "v_fma_f32 v45, v45, v1, -v0\n"
    "v_fma_f32 v46, v46, v1, -v0\n"
    "v_fma_f32 v47, v47, v1, -v0\n"
    "v_fma_f32 v48, v48, v1, -v0\n"
    "v_fma_f32 v49, v49, v1, -v0\n"
    "v_fma_f32 v50, v50, v1, -v0\n"
    "v_fma_f32 v51, v51, v1, -v0\n"
    "v_cvt_pk_bf16_f32 v44, v44, v45\n"
    "v_cvt_pk_bf16_f32 v45, v46, v47\n"
    "v_cvt_pk_bf16_f32 v46, v48, v49\n"
    "v_cvt_pk_bf16_f32 v47, v50, v51\n"
    "buffer_store_dwordx4 v[44:47], v103, s[16:19], s4 offen sc1\n"
    "s_add_u32 s4, s4, 4096\n"
    "s_add_u32 s4, s4, s6\n"
    "v_fma_f32 v52, v52, v1, -v0\n"
    "v_fma_f32 v53, v53, v1, -v0\n"
    "v_fma_f32 v54, v54, v1, -v0\n"
    "v_fma_f32 v55, v55, v1, -v0\n"
    "v_fma_f32 v56, v56, v1, -v0\n"
    "v_fma_f32 v57, v57, v1, -v0\n"
    "v_fma_f32 v58, v58, v1, -v0\n"
    "v_fma_f32 v59, v59, v1, -v0\n"
    "v_cvt_pk_bf16_f32 v52, v52, v53\n"
    "v_cvt_pk_bf16_f32 v53, v54, v55\n"
    "v_cvt_pk_bf16_f32 v54, v56, v57\n"
    "v_cvt_pk_bf16_f32 v55, v58, v59\n"
    "buffer_store_dwordx4 v[52:55], v103, s[16:19], s4 offen sc1\n"
    "v_mfma_f32_32x32x16_bf16 a[0:15], a[240:243], a[240:243], 0\n"
    "v_mfma_f32_32x32x16_bf16 a[16:31], a[240:243], a[240:243], 0\n"
    "v_mfma_f32_32x32x16_bf16 a[32:47], a[240:243], a[240:243], 0\n"
    "v_mfma_f32_32x32x16_bf16 a[48:63], a[240:243], a[240:243], 0\n"
    "v_mfma_f32_32x32x16_bf16 a[64:79], a[240:243], a[240:243], 0\n"
    "v_mfma_f32_32x32x16_bf16 a[80:95], a[240:243], a[240:243], 0\n"
    "v_mfma_f32_32x32x16_bf16 a[96:111], a[240:243], a[240:243], 0\n"
    "v_mfma_f32_32x32x16_bf16 a[112:127], a[240:243], a[240:243], 0\n"
    "v_mfma_f32_32x32x16_bf16 a[128:143], a[240:243], a[240:243], 0\n"
    "v_mfma_f32_32x32x16_bf16 a[144:159], a[240:243], a[240:243], 0\n"
    "v_mfma_f32_32x32x16_bf16 a[160:175], a[240:243], a[240:243], 0\n"
    "s_waitcnt vmcnt(0)\n"
    "s_add_u32 s73, s86, 4096\n"
    "v_mov_b32 v101, s74\n"
    "s_mov_b64 exec, 1\n"
    "buffer_store_dword v101, off, s[52:55], s73 sc1\n"
    "s_mov_b64 exec, -1\n"
    "L_join:\n"
    "v_and_b32 v100, 15, v253\n"
    "v_lshlrev_b32 v100, 7, v100\n"
    "s_lshl_b32 s86, s68, 11\n"
    "s_add_u32 s86, s86, 4096\n"
    "v_mfma_f32_32x32x16_bf16 v[128:143], a[240:243], a[240:243], 0\n"
    "v_mfma_f32_32x32x16_bf16 v[144:159], a[240:243], a[240:243], 0\n"
    "v_mfma_f32_32x32x16_bf16 v[160:175], a[240:243], a[240:243], 0\n"
    "v_mfma_f32_32x32x16_bf16 v[176:191], a[240:243], a[240:243], 0\n"
    "v_mfma_f32_32x32x16_bf16 v[192:207], a[240:243], a[240:243], 0\n"
    "v_mfma_f32_32x32x16_bf16 v[208:223], a[240:243], a[240:243], 0\n"
    "v_mfma_f32_32x32x16_bf16 v[224:239], a[240:243], a[240:243], 0\n"
    "s_cmp_lt_u32 s68, 2\n"
    "s_cbranch_scc1 L_z1\n"
    "v_mfma_f32_32x32x16_bf16 a[0:15], a[240:243], a[240:243], 0\n"
    "v_mfma_f32_32x32x16_bf16 a[16:31], a[240:243], a[240:243], 0\n"
    "v_mfma_f32_32x32x16_bf16 a[32:47], a[240:243], a[240:243], 0\n"
    "v_mfma_f32_32x32x16_bf16 a[48:63], a[240:243], a[240:243], 0\n"
    "v_mfma_f32_32x32x16_bf16 a[64:79], a[240:243], a[240:243], 0\n"
    "v_mfma_f32_32x32x16_bf16 a[80:95], a[240:243], a[240:243], 0\n"
    "v_mfma_f32_32x32x16_bf16 a[96:111], a[240:243], a[240:243], 0\n"
    "v_mfma_f32_32x32x16_bf16 a[112:127], a[240:243], a[240:243], 0\n"
    "v_mfma_f32_32x32x16_bf16 a[128:143], a[240:243], a[240:243], 0\n"
    "v_mfma_f32_32x32x16_bf16 a[144:159], a[240:243], a[240:243], 0\n"
    "v_mfma_f32_32x32x16_bf16 a[160:175], a[240:243], a[240:243], 0\n"
    "L_z1:\n"
    "s_barrier\n"
    "s_sleep 12\n"
    "s_mov_b32 s87, 0x40000\n"
    "L_spin_x:\n"
    "buffer_load_dword v101, v100, s[52:55], s86 offen sc1\n"
    "s_waitcnt vmcnt(0)\n"
    "v_cmp_ne_u32 vcc, s74, v101\n"
    "s_cbranch_vccz L_spun_x\n"
    "s_sleep 1\n"
    "s_sub_u32 s87, s87, 1\n"
    "s_cbranch_scc0 L_spin_x\n"
    "L_spun_x:\n"
    "s_add_u32 s72, s81, 0\n"
    "buffer_load_dwordx4 a[176:179], v246, s[16:19], s72 offen sc1\n"
    "buffer_load_dwordx4 a[180:183], v246, s[16:19], s72 offen offset:1024 sc1\n"
    "s_add_u32 s72, s81, 18432\n"
    "buffer_load_dwordx4 a[184:187], v246, s[16:19], s72 offen sc1\n"
    "buffer_load_dwordx4 a[188:191], v246, s[16:19], s72 offen offset:1024 sc1\n"
    "s_add_u32 s72, s81, 36864\n"
    "buffer_load_dwordx4 a[192:195], v246, s[16:19], s72 offen sc1\n"
    "buffer_load_dwordx4 a[196:199], v246, s[16:19], s72 offen offset:1024 sc1\n"
    "s_add_u32 s72, s81, 55296\n"
    "buffer_load_dwordx4 a[200:203], v246, s[16:19], s72 offen sc1\n"
    "buffer_load_dwordx4 a[204:207], v246, s[16:19], s72 offen offset:1024 sc1\n"
    "s_add_u32 s81, s81, 2048\n"
    "v_mfma_f32_32x32x16_bf16 v[0:15], a[240:243], a[240:243], 0\n"
    "v_mfma_f32_32x32x16_bf16 v[16:31], a[240:243], a[240:243], 0\n"
    "v_mfma_f32_32x32x16_bf16 v[32:47], a[240:243], a[240:243], 0\n"
    "v_mfma_f32_32x32x16_bf16 v[48:63], a[240:243], a[240:243], 0\n"
    "v_mfma_f32_32x32x16_bf16 v[64:79], a[240:243], a[240:243], 0\n"
    "v_mfma_f32_32x32x16_bf16 v[80:95], a[240:243], a[240:243], 0\n"
    "v_mfma_f32_32x32x16_bf16 v[96:111], a[240:243], a[240:243], 0\n"
    "v_mfma_f32_32x32x16_bf16 v[112:127], a[240:243], a[240:243], 0\n"
    "s_waitcnt vmcnt(0)\n"
    "s_barrier\n"
    "ds_read_b64_tr_b16 a[240:241], v241 offset:16384\n"
    "ds_read_b64_tr_b16 a[242:243], v241 offset:16640\n"
    "ds_read_b64_tr_b16 a[244:245], v241 offset:18432\n"
    "ds_read_b64_tr_b16 a[246:247], v241 offset:18688\n"
    "s_or_b32 s72, s68, s69\n"
    "s_cmp_lg_u32 s72, 0\n"
    "s_cbranch_scc1 L_noep\n"
    "s_add_u32 s72, s74, 1\n"
    "v_mov_b32 v254, s72\n"
    "s_mov_b64 exec, 1\n"
    "buffer_store_dword v254, off, s[52:55], 0 sc1\n"
    "s_mov_b64 exec, -1\n"
    "L_noep:\n"
    "s_waitcnt lgkmcnt(0)\n"
    "v_mfma_f32_16x16x32_bf16 v[128:131], a[240:243], a[176:179], v[128:131]\n"
    "ds_read_b64_tr_b16 a[248:249], v241 offset:32768\n"
    "v_mfma_f32_16x16x32_bf16 v[128:131], a[244:247], a[180:183], v[128:131]\n"
    "ds_read_b64_tr_b16 a[250:251], v241 offset:33024\n"
    "v_mfma_f32_16x16x32_bf16 v[132:135], a[240:243], a[184:187], v[132:135]\n"
    "ds_read_b64_tr_b16 a[252:253], v241 offset:34816\n"
    "v_mfma_f32_16x16x32_bf16 v[132:135], a[244:247], a[188:191], v[132:135]\n"
    "ds_read_b64_tr_b16 a[254:255], v241 offset:35072\n"
    "v_mfma_f32_16x16x32_bf16 v[136:139], a[240:243], a[192:195], v[136:139]\n"
    "s_add_u32 m0, s76, 0\n"
    "v_mfma_f32_16x16x32_bf16 v[136:139], a[244:247], a[196:199], v[136:139]\n"
    "s_add_u32 s72, s78, 0\n"
    "buffer_load_dwordx4 v243, s[20:23], s72 offen lds\n"
    "v_mfma_f32_16x16x32_bf16 v[140:143], a[240:243], a[200:203], v[140:143]\n"
    "s_add_u32 m0, s76, 4096\n"
    "v_mfma_f32_16x16x32_bf16 v[140:143], a[244:247], a[204:207], v[140:143]\n"
    "s_add_u32 s72, s78, 64\n"
    "buffer_load_dwordx4 v243, s[20:23], s72 offen lds\n"
    "s_waitcnt lgkmcnt(0)\n"
    "v_mfma_f32_16x16x32_bf16 a[0:3], a[248:251], a[176:179], a[0:3]\n"
    "ds_read_b64_tr_b16 a[240:241], v242 offset:16384\n"
    "v_mfma_f32_16x16x32_bf16 a[0:3], a[252:255], a[180:183], a[0:3]\n"
    "ds_read_b64_tr_b16 a[242:243], v242 offset:16640\n"
    "v_mfma_f32_16x16x32_bf16 a[4:7], a[248:251], a[184:187], a[4:7]\n"
    "ds_read_b64_tr_b16 a[244:245], v242 offset:18432\n"
    "v_mfma_f32_16x16x32_bf16 a[4:7], a[252:255], a[188:191], a[4:7]\n"
    "ds_read_b64_tr_b16 a[246:247], v242 offset:18688\n"
    "v_mfma_f32_16x16x32_bf16 a[8:11], a[248:251], a[192:195], a[8:11]\n"
    "s_add_u32 m0, s76, 8192\n"
    "v_mfma_f32_16x16x32_bf16 a[8:11], a[252:255], a[196:199], a[8:11]\n"
    "s_add_u32 s72, s78, 128\n"
    "buffer_load_dwordx4 v243, s[20:23], s72 offen lds\n"
    "v_mfma_f32_16x16x32_bf16 a[12:15], a[248:251], a[200:203], a[12:15]\n"
    "s_add_u32 m0, s76, 12288\n"
    "v_mfma_f32_16x16x32_bf16 a[12:15], a[252:255], a[204:207], a[12:15]\n"
    "s_add_u32 s72, s78, 192\n"
    "buffer_load_dwordx4 v243, s[20:23], s72 offen lds\n"
    "s_waitcnt lgkmcnt(0)\n"
    "v_mfma_f32_16x16x32_bf16 v[144:147], a[240:243], a[176:179], v[144:147]\n"
    "ds_read_b64_tr_b16 a[248:249], v242 offset:32768\n"
    "v_mfma_f32_16x16x32_bf16 v[144:147], a[244:247], a[180:183], v[144:147]\n"
    "ds_read_b64_tr_b16 a[250:251], v242 offset:33024\n"
    "v_mfma_f32_16x16x32_bf16 v[148:151], a[240:243], a[184:187], v[148:151]\n"
    "ds_read_b64_tr_b16 a[252:253], v242 offset:34816\n"
    "v_mfma_f32_16x16x32_bf16 v[148:151], a[244:247], a[188:191], v[148:151]\n"
    "ds_read_b64_tr_b16 a[254:255], v242 offset:35072\n"
    "v_mfma_f32_16x16x32_bf16 v[152:155], a[240:243], a[192:195], v[152:155]\n"
    "s_add_u32 m0, s76, 16384\n"
    "v_mfma_f32_16x16x32_bf16 v[152:155], a[244:247], a[196:199], v[152:155]\n"
    "s_add_u32 s72, s79, 0\n"
    "buffer_load_dwordx4 v244, s[24:27], s72 offen lds\n"
    "v_mfma_f32_16x16x32_bf16 v[156:159], a[240:243], a[200:203], v[156:159]\n"
    "s_add_u32 m0, s76, 20480\n"
    "v_mfma_f32_16x16x32_bf16 v[156:159], a[244:247], a[204:207], v[156:159]\n"
    "s_add_u32 s72, s79, 64\n"
    "buffer_load_dwordx4 v244, s[24:27], s72 offen lds\n"
    "s_waitcnt lgkmcnt(0)\n"
    "v_mfma_f32_16x16x32_bf16 a[16:19], a[248:251], a[176:179], a[16:19]\n"
    "ds_read_b64_tr_b16 a[240:241], v241 offset:20480\n"
    "v_mfma_f32_16x16x32_bf16 a[16:19], a[252:255], a[180:183], a[16:19]\n"
    "ds_read_b64_tr_b16 a[242:243], v241 offset:20736\n"
    "v_mfma_f32_16x16x32_bf16 a[20:23], a[248:251], a[184:187], a[20:23]\n"
    "ds_read_b64_tr_b16 a[244:245], v241 offset:22528\n"
    "v_mfma_f32_16x16x32_bf16 a[20:23], a[252:255], a[188:191], a[20:23]\n"
    "ds_read_b64_tr_b16 a[246:247], v241 offset:22784\n"
    "v_mfma_f32_16x16x32_bf16 a[24:27], a[248:251], a[192:195], a[24:27]\n"
    "s_add_u32 m0, s76, 24576\n"
    "v_mfma_f32_16x16x32_bf16 a[24:27], a[252:255], a[196:199], a[24:27]\n"
    "s_add_u32 s72, s79, 128\n"
    "buffer_load_dwordx4 v244, s[24:27], s72 offen lds\n"
    "v_mfma_f32_16x16x32_bf16 a[28:31], a[248:251], a[200:203], a[28:31]\n"
    "s_add_u32 m0, s76, 28672\n"
    "v_mfma_f32_16x16x32_bf16 a[28:31], a[252:255], a[204:207], a[28:31]\n"
    "s_add_u32 s72, s79, 192\n"
    "buffer_load_dwordx4 v244, s[24:27], s72 offen lds\n"
    "s_waitcnt lgkmcnt(0)\n"
    "v_mfma_f32_16x16x32_bf16 v[160:163], a[240:243], a[176:179], v[160:163]\n"
    "ds_read_b64_tr_b16 a[248:249], v241 offset:36864\n"
    "v_mfma_f32_16x16x32_bf16 v[160:163], a[244:247], a[180:183], v[160:163]\n"
    "ds_read_b64_tr_b16 a[250:251], v241 offset:37120\n"
    "v_mfma_f32_16x16x32_bf16 v[164:167], a[240:243], a[184:187], v[164:167]\n"
    "ds_read_b64_tr_b16 a[252:253], v241 offset:38912\n"
    "v_mfma_f32_16x16x32_bf16 v[164:167], a[244:247], a[188:191], v[164:167]\n"
    "ds_read_b64_tr_b16 a[254:255], v241 offset:39168\n"
    "v_mfma_f32_16x16x32_bf16 v[168:171], a[240:243], a[192:195], v[168:171]\n"
    "s_add_u32 m0, s76, 49152\n"
    "v_mfma_f32_16x16x32_bf16 v[168:171], a[244:247], a[196:199], v[168:171]\n"
    "s_mov_b64 exec, s[12:13]\n"
    "buffer_load_dwordx4 v245, s[24:27], s80 offen lds\n"
    "s_mov_b64 exec, -1\n"
    "v_mfma_f32_16x16x32_bf16 v[172:175], a[240:243], a[200:203], v[172:175]\n"
    "s_add_u32 m0, s76, 32768\n"
    "v_mfma_f32_16x16x32_bf16 v[172:175], a[244:247], a[204:207], v[172:175]\n"
    "s_add_u32 s72, s79, 0\n"
    "buffer_load_dwordx4 v244, s[28:31], s72 offen lds\n"
    "s_waitcnt lgkmcnt(0)\n"
    "v_mfma_f32_16x16x32_bf16 a[32:35], a[248:251], a[176:179], a[32:35]\n"
    "ds_read_b64_tr_b16 a[240:241], v242 offset:20480\n"
    "v_mfma_f32_16x16x32_bf16 a[32:35], a[252:255], a[180:183], a[32:35]\n"
    "ds_read_b64_tr_b16 a[242:243], v242 offset:20736\n"
    "v_mfma_f32_16x16x32_bf16 a[36:39], a[248:251], a[184:187], a[36:39]\n"
    "ds_read_b64_tr_b16 a[244:245], v242 offset:22528\n"
    "v_mfma_f32_16x16x32_bf16 a[36:39], a[252:255], a[188:191], a[36:39]\n"
    "ds_read_b64_tr_b16 a[246:247], v242 offset:22784\n"
    "v_mfma_f32_16x16x32_bf16 a[40:43], a[248:251], a[192:195], a[40:43]\n"
    "s_add_u32 m0, s76, 36864\n"
    "v_mfma_f32_16x16x32_bf16 a[40:43], a[252:255], a[196:199], a[40:43]\n"
    "s_add_u32 s72, s79, 64\n"
    "buffer_load_dwordx4 v244, s[28:31], s72 offen lds\n"
    "v_mfma_f32_16x16x32_bf16 a[44:47], a[248:251], a[200:203], a[44:47]\n"
    "s_add_u32 m0, s76, 40960\n"
    "v_mfma_f32_16x16x32_bf16 a[44:47], a[252:255], a[204:207], a[44:47]\n"
    "s_add_u32 s72, s79, 128\n"
    "buffer_load_dwordx4 v244, s[28:31], s72 offen lds\n"
    "s_waitcnt lgkmcnt(0)\n"
    "v_mfma_f32_16x16x32_bf16 v[176:179], a[240:243], a[176:179], v[176:179]\n"
    "ds_read_b64_tr_b16 a[248:249], v242 offset:36864\n"
    "v_mfma_f32_16x16x32_bf16 v[176:179], a[244:247], a[180:183], v[176:179]\n"
    "ds_read_b64_tr_b16 a[250:251], v242 offset:37120\n"
    "v_mfma_f32_16x16x32_bf16 v[180:183], a[240:243], a[184:187], v[180:183]\n"
    "ds_read_b64_tr_b16 a[252:253], v242 offset:38912\n"
    "v_mfma_f32_16x16x32_bf16 v[180:183], a[244:247], a[188:191], v[180:183]\n"
    "ds_read_b64_tr_b16 a[254:255], v242 offset:39168\n"
    "v_mfma_f32_16x16x32_bf16 v[184:187], a[240:243], a[192:195], v[184:187]\n"
    "s_add_u32 m0, s76, 45056\n"
    "v_mfma_f32_16x16x32_bf16 v[184:187], a[244:247], a[196:199], v[184:187]\n"
    "s_add_u32 s72, s79, 192\n"
    "buffer_load_dwordx4 v244, s[28:31], s72 offen lds\n"
    "v_mfma_f32_16x16x32_bf16 v[188:191], a[240:243], a[200:203], v[188:191]\n"
    "s_add_u32 m0, s76, 49152\n"
    "v_mfma_f32_16x16x32_bf16 v[188:191], a[244:247], a[204:207], v[188:191]\n"
    "s_mov_b64 exec, s[14:15]\n"
    "buffer_load_dwordx4 v245, s[28:31], s80 offen lds\n"
    "s_mov_b64 exec, -1\n"
    "s_waitcnt lgkmcnt(0)\n"
    "v_mfma_f32_16x16x32_bf16 a[48:51], a[248:251], a[176:179], a[48:51]\n"
    "s_add_u32 s78, s78, 524288\n"
    "ds_read_b64_tr_b16 a[240:241], v241 offset:24576\n"
    "v_mfma_f32_16x16x32_bf16 a[48:51], a[252:255], a[180:183], a[48:51]\n"
    "s_add_u32 s79, s79, 589824\n"
    "ds_read_b64_tr_b16 a[242:243], v241 offset:24832\n"
    "v_mfma_f32_16x16x32_bf16 a[52:55], a[248:251], a[184:187], a[52:55]\n"
    "s_add_u32 s80, s80, 589824\n"
    "ds_read_b64_tr_b16 a[244:245], v241 offset:26624\n"
    "v_mfma_f32_16x16x32_bf16 a[52:55], a[252:255], a[188:191], a[52:55]\n"
    "s_add_u32 s76, s76, 53248\n"
    "ds_read_b64_tr_b16 a[246:247], v241 offset:26880\n"
    "v_mfma_f32_16x16x32_bf16 a[56:59], a[248:251], a[192:195], a[56:59]\n"
    "v_mfma_f32_16x16x32_bf16 a[56:59], a[252:255], a[196:199], a[56:59]\n"
    "s_add_u32 s72, s81, 0\n"
    "buffer_load_dwordx4 a[208:211], v246, s[16:19], s72 offen sc1\n"
    "v_mfma_f32_16x16x32_bf16 a[60:63], a[248:251], a[200:203], a[60:63]\n"
    "v_mfma_f32_16x16x32_bf16 a[60:63], a[252:255], a[204:207], a[60:63]\n"
    "buffer_load_dwordx4 a[212:215], v246, s[16:19], s72 offen offset:1024 sc1\n"
    "s_waitcnt lgkmcnt(0)\n"
    "v_mfma_f32_16x16x32_bf16 v[192:195], a[240:243], a[176:179], v[192:195]\n"
    "s_cmp_ge_u32 s76, 159744\n"
    "s_cselect_b32 s73, 159744, 0\n"
    "ds_read_b64_tr_b16 a[248:249], v241 offset:40960\n"
    "v_mfma_f32_16x16x32_bf16 v[192:195], a[244:247], a[180:183], v[192:195]\n"
    "s_sub_u32 s76, s76, s73\n"
    "ds_read_b64_tr_b16 a[250:251], v241 offset:41216\n"
    "v_mfma_f32_16x16x32_bf16 v[196:199], a[240:243], a[184:187], v[196:199]\n"
    "ds_read_b64_tr_b16 a[252:253], v241 offset:43008\n"
    "v_mfma_f32_16x16x32_bf16 v[196:199], a[244:247], a[188:191], v[196:199]\n"
    "ds_read_b64_tr_b16 a[254:255], v241 offset:43264\n"
    "v_mfma_f32_16x16x32_bf16 v[200:203], a[240:243], a[192:195], v[200:203]\n"
    "v_mfma_f32_16x16x32_bf16 v[200:203], a[244:247], a[196:199], v[200:203]\n"
    "s_add_u32 s72, s81, 18432\n"
    "buffer_load_dwordx4 a[216:219], v246, s[16:19], s72 offen sc1\n"
    "v_mfma_f32_16x16x32_bf16 v[204:207], a[240:243], a[200:203], v[204:207]\n"
    "v_mfma_f32_16x16x32_bf16 v[204:207], a[244:247], a[204:207], v[204:207]\n"
    "buffer_load_dwordx4 a[220:223], v246, s[16:19], s72 offen offset:1024 sc1\n"
    "s_waitcnt lgkmcnt(0)\n"
    "v_mfma_f32_16x16x32_bf16 a[64:67], a[248:251], a[176:179], a[64:67]\n"
    "ds_read_b64_tr_b16 a[240:241], v242 offset:24576\n"
    "v_mfma_f32_16x16x32_bf16 a[64:67], a[252:255], a[180:183], a[64:67]\n"
    "ds_read_b64_tr_b16 a[242:243], v242 offset:24832\n"
    "v_mfma_f32_16x16x32_bf16 a[68:71], a[248:251], a[184:187], a[68:71]\n"
    "ds_read_b64_tr_b16 a[244:245], v242 offset:26624\n"
    "v_mfma_f32_16x16x32_bf16 a[68:71], a[252:255], a[188:191], a[68:71]\n"
    "ds_read_b64_tr_b16 a[246:247], v242 offset:26880\n"
    "v_mfma_f32_16x16x32_bf16 a[72:75], a[248:251], a[192:195], a[72:75]\n"
    "v_mfma_f32_16x16x32_bf16 a[72:75], a[252:255], a[196:199], a[72:75]\n"
    "s_add_u32 s72, s81, 36864\n"
    "buffer_load_dwordx4 a[224:227], v246, s[16:19], s72 offen sc1\n"
    "v_mfma_f32_16x16x32_bf16 a[76:79], a[248:251], a[200:203], a[76:79]\n"
    "v_mfma_f32_16x16x32_bf16 a[76:79], a[252:255], a[204:207], a[76:79]\n"
    "buffer_load_dwordx4 a[228:231], v246, s[16:19], s72 offen offset:1024 sc1\n"
    "s_waitcnt lgkmcnt(0)\n"
    "v_mfma_f32_16x16x32_bf16 v[208:211], a[240:243], a[176:179], v[208:211]\n"
    "ds_read_b64_tr_b16 a[248:249], v242 offset:40960\n"
    "v_mfma_f32_16x16x32_bf16 v[208:211], a[244:247], a[180:183], v[208:211]\n"
    "ds_read_b64_tr_b16 a[250:251], v242 offset:41216\n"
    "v_mfma_f32_16x16x32_bf16 v[212:215], a[240:243], a[184:187], v[212:215]\n"
    "ds_read_b64_tr_b16 a[252:253], v242 offset:43008\n"
    "v_mfma_f32_16x16x32_bf16 v[212:215], a[244:247], a[188:191], v[212:215]\n"
    "ds_read_b64_tr_b16 a[254:255], v242 offset:43264\n"
    "v_mfma_f32_16x16x32_bf16 v[216:219], a[240:243], a[192:195], v[216:219]\n"
    "v_mfma_f32_16x16x32_bf16 v[216:219], a[244:247], a[196:199], v[216:219]\n"
    "s_add_u32 s72, s81, 55296\n"
    "buffer_load_dwordx4 a[232:235], v246, s[16:19], s72 offen sc1\n"
    "v_mfma_f32_16x16x32_bf16 v[220:223], a[240:243], a[200:203], v[220:223]\n"
    "v_mfma_f32_16x16x32_bf16 v[220:223], a[244:247], a[204:207], v[220:223]\n"
    "buffer_load_dwordx4 a[236:239], v246, s[16:19], s72 offen offset:1024 sc1\n"
    "s_waitcnt lgkmcnt(0)\n"
    "v_mfma_f32_16x16x32_bf16 a[80:83], a[248:251], a[176:179], a[80:83]\n"
    "ds_read_b64_tr_b16 a[240:241], v241 offset:28672\n"
    "v_mfma_f32_16x16x32_bf16 a[80:83], a[252:255], a[180:183], a[80:83]\n"
    "s_add_u32 s81, s81, 2048\n"
    "ds_read_b64_tr_b16 a[242:243], v241 offset:28928\n"
    "v_mfma_f32_16x16x32_bf16 a[84:87], a[248:251], a[184:187], a[84:87]\n"
    "ds_read_b64_tr_b16 a[244:245], v241 offset:30720\n"
    "v_mfma_f32_16x16x32_bf16 a[84:87], a[252:255], a[188:191], a[84:87]\n"
    "ds_read_b64_tr_b16 a[246:247], v241 offset:30976\n"
    "v_mfma_f32_16x16x32_bf16 a[88:91], a[248:251], a[192:195], a[88:91]\n"
    "v_mfma_f32_16x16x32_bf16 a[88:91], a[252:255], a[196:199], a[88:91]\n"
    "s_add_u32 m0, s76, 0\n"
    "v_mfma_f32_16x16x32_bf16 a[92:95], a[248:251], a[200:203], a[92:95]\n"
    "s_add_u32 s72, s78, 0\n"
    "buffer_load_dwordx4 v243, s[20:23], s72 offen lds\n"
    "v_mfma_f32_16x16x32_bf16 a[92:95], a[252:255], a[204:207], a[92:95]\n"
    "s_waitcnt lgkmcnt(0)\n"
    "v_mfma_f32_16x16x32_bf16 v[224:227], a[240:243], a[176:179], v[224:227]\n"
    "ds_read_b64_tr_b16 a[248:249], v241 offset:45056\n"
    "v_mfma_f32_16x16x32_bf16 v[224:227], a[244:247], a[180:183], v[224:227]\n"
    "ds_read_b64_tr_b16 a[250:251], v241 offset:45312\n"
    "v_mfma_f32_16x16x32_bf16 v[228:231], a[240:243], a[184:187], v[228:231]\n"
    "ds_read_b64_tr_b16 a[252:253], v241 offset:47104\n"
    "v_mfma_f32_16x16x32_bf16 v[228:231], a[244:247], a[188:191], v[228:231]\n"
    "ds_read_b64_tr_b16 a[254:255], v241 offset:47360\n"
    "v_mfma_f32_16x16x32_bf16 v[232:235], a[240:243], a[192:195], v[232:235]\n"
    "v_mfma_f32_16x16x32_bf16 v[232:235], a[244:247], a[196:199], v[232:235]\n"
    "s_add_u32 m0, s76, 4096\n"
    "v_mfma_f32_16x16x32_bf16 v[236:239], a[240:243], a[200:203], v[236:239]\n"
    "s_add_u32 s72, s78, 64\n"
    "buffer_load_dwordx4 v243, s[20:23], s72 offen lds\n"
    "v_mfma_f32_16x16x32_bf16 v[236:239], a[244:247], a[204:207], v[236:239]\n"
    "s_waitcnt lgkmcnt(0)\n"
    "v_mfma_f32_16x16x32_bf16 a[96:99], a[248:251], a[176:179], a[96:99]\n"
    "ds_read_b64_tr_b16 a[240:241], v242 offset:28672\n"
    "v_mfma_f32_16x16x32_bf16 a[96:99], a[252:255], a[180:183], a[96:99]\n"
    "ds_read_b64_tr_b16 a[242:243], v242 offset:28928\n"
    "v_mfma_f32_16x16x32_bf16 a[100:103], a[248:251], a[184:187], a[100:103]\n"
    "ds_read_b64_tr_b16 a[244:245], v242 offset:30720\n"
    "v_mfma_f32_16x16x32_bf16 a[100:103], a[252:255], a[188:191], a[100:103]\n"
    "ds_read_b64_tr_b16 a[246:247], v242 offset:30976\n"
    "v_mfma_f32_16x16x32_bf16 a[104:107], a[248:251], a[192:195], a[104:107]\n"
    "v_mfma_f32_16x16x32_bf16 a[104:107], a[252:255], a[196:199], a[104:107]\n"
    "s_add_u32 m0, s76, 8192\n"
    "v_mfma_f32_16x16x32_bf16 a[108:111], a[248:251], a[200:203], a[108:111]\n"
    "s_add_u32 s72, s78, 128\n"
    "buffer_load_dwordx4 v243, s[20:23], s72 offen lds\n"
    "v_mfma_f32_16x16x32_bf16 a[108:111], a[252:255], a[204:207], a[108:111]\n"
    "s_waitcnt lgkmcnt(0)\n"
    "v_mfma_f32_16x16x32_bf16 a[144:147], a[240:243], a[176:179], a[144:147]\n"
    "ds_read_b64_tr_b16 a[248:249], v242 offset:45056\n"
    "v_mfma_f32_16x16x32_bf16 a[144:147], a[244:247], a[180:183], a[144:147]\n"
    "ds_read_b64_tr_b16 a[250:251], v242 offset:45312\n"
    "v_mfma_f32_16x16x32_bf16 a[148:151], a[240:243], a[184:187], a[148:151]\n"
    "ds_read_b64_tr_b16 a[252:253], v242 offset:47104\n"
    "v_mfma_f32_16x16x32_bf16 a[148:151], a[244:247], a[188:191], a[148:151]\n"
    "ds_read_b64_tr_b16 a[254:255], v242 offset:47360\n"
    "v_mfma_f32_16x16x32_bf16 a[152:155], a[240:243], a[192:195], a[152:155]\n"
    "v_mfma_f32_16x16x32_bf16 a[152:155], a[244:247], a[196:199], a[152:155]\n"
    "s_add_u32 m0, s76, 12288\n"
    "v_mfma_f32_16x16x32_bf16 a[156:159], a[240:243], a[200:203], a[156:159]\n"
    "s_add_u32 s72, s78, 192\n"
    "buffer_load_dwordx4 v243, s[20:23], s72 offen lds\n"
    "v_mfma_f32_16x16x32_bf16 a[156:159], a[244:247], a[204:207], a[156:159]\n"
    "s_waitcnt lgkmcnt(0)\n"
    "v_mfma_f32_16x16x32_bf16 a[112:115], a[248:251], a[176:179], a[112:115]\n"
    "ds_read_b64_tr_b16 a[240:241], v241 offset:49152\n"
    "v_mfma_f32_16x16x32_bf16 a[112:115], a[252:255], a[180:183], a[112:115]\n"
    "ds_read_b64_tr_b16 a[242:243], v241 offset:49408\n"
    "v_mfma_f32_16x16x32_bf16 a[116:119], a[248:251], a[184:187], a[116:119]\n"
    "ds_read_b64_tr_b16 a[244:245], v241 offset:51200\n"
    "v_mfma_f32_16x16x32_bf16 a[116:119], a[252:255], a[188:191], a[116:119]\n"
    "ds_read_b64_tr_b16 a[246:247], v241 offset:51456\n"
    "v_mfma_f32_16x16x32_bf16 a[120:123], a[248:251], a[192:195], a[120:123]\n"
    "v_mfma_f32_16x16x32_bf16 a[120:123], a[252:255], a[196:199], a[120:123]\n"
    "s_add_u32 m0, s76, 16384\n"
    "v_mfma_f32_16x16x32_bf16 a[124:127], a[248:251], a[200:203], a[124:127]\n"
    "s_add_u32 s72, s79, 0\n"
    "buffer_load_dwordx4 v244, s[24:27], s72 offen lds\n"
    "v_mfma_f32_16x16x32_bf16 a[124:127], a[252:255], a[204:207], a[124:127]\n"
    "s_waitcnt lgkmcnt(0)\n"
    "v_mfma_f32_16x16x32_bf16 a[160:163], a[240:243], a[176:179], a[160:163]\n"
    "ds_read_b64_tr_b16 a[248:249], v242 offset:49152\n"
    "v_mfma_f32_16x16x32_bf16 a[160:163], a[244:247], a[180:183], a[160:163]\n"
    "ds_read_b64_tr_b16 a[250:251], v242 offset:49408\n"
    "v_mfma_f32_16x16x32_bf16 a[164:167], a[240:243], a[184:187], a[164:167]\n"
    "ds_read_b64_tr_b16 a[252:253], v242 offset:51200\n"
    "v_mfma_f32_16x16x32_bf16 a[164:167], a[244:247], a[188:191], a[164:167]\n"
    "ds_read_b64_tr_b16 a[254:255], v242 offset:51456\n"
    "v_mfma_f32_16x16x32_bf16 a[168:171], a[240:243], a[192:195], a[168:171]\n"
    "s_add_u32 m0, s76, 20480\n"
    "v_mfma_f32_16x16x32_bf16 a[168:171], a[244:247], a[196:199], a[168:171]\n"
    "s_add_u32 s72, s79, 64\n"
    "buffer_load_dwordx4 v244, s[24:27], s72 offen lds\n"
    "v_mfma_f32_16x16x32_bf16 a[172:175], a[240:243], a[200:203], a[172:175]\n"
    "s_add_u32 m0, s76, 24576\n"
    "v_mfma_f32_16x16x32_bf16 a[172:175], a[244:247], a[204:207], a[172:175]\n"
    "s_add_u32 s72, s79, 128\n"
    "buffer_load_dwordx4 v244, s[24:27], s72 offen lds\n"
    "s_waitcnt lgkmcnt(0)\n"
    "v_mfma_f32_16x16x32_bf16 a[128:131], a[248:251], a[176:179], a[128:131]\n"
    "ds_read_b64_tr_b16 a[240:241], v240 offset:0\n"
    "v_mfma_f32_16x16x32_bf16 a[128:131], a[252:255], a[180:183], a[128:131]\n"
    "ds_read_b64_tr_b16 a[242:243], v240 offset:256\n"
    "v_mfma_f32_16x16x32_bf16 a[132:135], a[248:251], a[184:187], a[132:135]\n"
    "ds_read_b64_tr_b16 a[244:245], v240 offset:2048\n"
    "v_mfma_f32_16x16x32_bf16 a[132:135], a[252:255], a[188:191], a[132:135]\n"
    "ds_read_b64_tr_b16 a[246:247], v240 offset:2304\n"
    "v_mfma_f32_16x16x32_bf16 a[136:139], a[248:251], a[192:195], a[136:139]\n"
    "s_add_u32 m0, s76, 28672\n"
    "v_mfma_f32_16x16x32_bf16 a[136:139], a[252:255], a[196:199], a[136:139]\n"
    "s_add_u32 s72, s79, 192\n"
    "buffer_load_dwordx4 v244, s[24:27], s72 offen lds\n"
    "v_mfma_f32_16x16x32_bf16 a[140:143], a[248:251], a[200:203], a[140:143]\n"
    "s_add_u32 m0, s76, 49152\n"
    "v_mfma_f32_16x16x32_bf16 a[140:143], a[252:255], a[204:207], a[140:143]\n"
    "s_mov_b64 exec, s[12:13]\n"
    "buffer_load_dwordx4 v245, s[24:27], s80 offen lds\n"
    "s_mov_b64 exec, -1\n"
    "s_waitcnt lgkmcnt(0)\n"
    "v_mfma_f32_16x16x32_bf16 v[0:3], a[240:243], a[176:179], v[0:3]\n"
    "ds_read_b64_tr_b16 a[248:249], v240 offset:8\n"
    "v_mfma_f32_16x16x32_bf16 v[0:3], a[244:247], a[180:183], v[0:3]\n"
    "ds_read_b64_tr_b16 a[250:251], v240 offset:264\n"
    "v_mfma_f32_16x16x32_bf16 v[32:35], a[240:243], a[184:187], v[32:35]\n"
    "ds_read_b64_tr_b16 a[252:253], v240 offset:2056\n"
    "v_mfma_f32_16x16x32_bf16 v[32:35], a[244:247], a[188:191], v[32:35]\n"
    "ds_read_b64_tr_b16 a[254:255], v240 offset:2312\n"
    "v_mfma_f32_16x16x32_bf16 v[64:67], a[240:243], a[192:195], v[64:67]\n"
    "s_add_u32 m0, s76, 32768\n"
    "v_mfma_f32_16x16x32_bf16 v[64:67], a[244:247], a[196:199], v[64:67]\n"
    "s_add_u32 s72, s79, 0\n"
    "buffer_load_dwordx4 v244, s[28:31], s72 offen lds\n"
    "v_mfma_f32_16x16x32_bf16 v[96:99], a[240:243], a[200:203], v[96:99]\n"
    "s_add_u32 m0, s76, 36864\n"
    "v_mfma_f32_16x16x32_bf16 v[96:99], a[244:247], a[204:207], v[96:99]\n"
    "s_add_u32 s72, s79, 64\n"
    "buffer_load_dwordx4 v244, s[28:31], s72 offen lds\n"
    "s_waitcnt lgkmcnt(0)\n"
    "v_mfma_f32_16x16x32_bf16 v[4:7], a[248:251], a[176:179], v[4:7]\n"
    "ds_read_b64_tr_b16 a[240:241], v240 offset:4096\n"
    "v_mfma_f32_16x16x32_bf16 v[4:7], a[252:255], a[180:183], v[4:7]\n"
    "ds_read_b64_tr_b16 a[242:243], v240 offset:4352\n"
    "v_mfma_f32_16x16x32_bf16 v[36:39], a[248:251], a[184:187], v[36:39]\n"
    "ds_read_b64_tr_b16 a[244:245], v240 offset:6144\n"
    "v_mfma_f32_16x16x32_bf16 v[36:39], a[252:255], a[188:191], v[36:39]\n"
    "ds_read_b64_tr_b16 a[246:247], v240 offset:6400\n"
    "v_mfma_f32_16x16x32_bf16 v[68:71], a[248:251], a[192:195], v[68:71]\n"
    "s_add_u32 m0, s76, 40960\n"
    "v_mfma_f32_16x16x32_bf16 v[68:71], a[252:255], a[196:199], v[68:71]\n"
    "s_add_u32 s72, s79, 128\n"
    "buffer_load_dwordx4 v244, s[28:31], s72 offen lds\n"
    "v_mfma_f32_16x16x32_bf16 v[100:103], a[248:251], a[200:203], v[100:103]\n"
    "s_add_u32 m0, s76, 45056\n"
    "v_mfma_f32_16x16x32_bf16 v[100:103], a[252:255], a[204:207], v[100:103]\n"
    "s_add_u32 s72, s79, 192\n"
    "buffer_load_dwordx4 v244, s[28:31], s72 offen lds\n"
    "s_waitcnt lgkmcnt(0)\n"
    "v_mfma_f32_16x16x32_bf16 v[8:11], a[240:243], a[176:179], v[8:11]\n"
    "ds_read_b64_tr_b16 a[248:249], v240 offset:4104\n"
    "v_mfma_f32_16x16x32_bf16 v[8:11], a[244:247], a[180:183], v[8:11]\n"
    "ds_read_b64_tr_b16 a[250:251], v240 offset:4360\n"
    "v_mfma_f32_16x16x32_bf16 v[40:43], a[240:243], a[184:187], v[40:43]\n"
    "ds_read_b64_tr_b16 a[252:253], v240 offset:6152\n"
    "v_mfma_f32_16x16x32_bf16 v[40:43], a[244:247], a[188:191], v[40:43]\n"
    "ds_read_b64_tr_b16 a[254:255], v240 offset:6408\n"
    "v_mfma_f32_16x16x32_bf16 v[72:75], a[240:243], a[192:195], v[72:75]\n"
    "s_add_u32 m0, s76, 49152\n"
    "v_mfma_f32_16x16x32_bf16 v[72:75], a[244:247], a[196:199], v[72:75]\n"
    "s_mov_b64 exec, s[14:15]\n"
    "buffer_load_dwordx4 v245, s[28:31], s80 offen lds\n"
    "s_mov_b64 exec, -1\n"
    "v_mfma_f32_16x16x32_bf16 v[104:107], a[240:243], a[200:203], v[104:107]\n"
    "s_mov_b32 m0, 160768\n"
    "v_mfma_f32_16x16x32_bf16 v[104:107], a[244:247], a[204:207], v[104:107]\n"
    "s_add_u32 s7, s7, s32\n"
    "s_mov_b64 exec, 0xffff\n"
    "buffer_load_dword v255, s[8:11], s7 offen lds\n"
    "s_mov_b64 exec, -1\n"
    "s_waitcnt lgkmcnt(0)\n"
    "v_mfma_f32_16x16x32_bf16 v[12:15], a[248:251], a[176:179], v[12:15]\n"
    "ds_read_b64_tr_b16 a[240:241], v240 offset:8192\n"
    "v_mfma_f32_16x16x32_bf16 v[12:15], a[252:255], a[180:183], v[12:15]\n"
    "ds_read_b64_tr_b16 a[242:243], v240 offset:8448\n"
    "v_mfma_f32_16x16x32_bf16 v[44:47], a[248:251], a[184:187], v[44:47]\n"
    "ds_read_b64_tr_b16 a[244:245], v240 offset:10240\n"
    "v_mfma_f32_16x16x32_bf16 v[44:47], a[252:255], a[188:191], v[44:47]\n"
    "ds_read_b64_tr_b16 a[246:247], v240 offset:10496\n"
    "v_mfma_f32_16x16x32_bf16 v[76:79], a[248:251], a[192:195], v[76:79]\n"
    "s_mov_b32 m0, 160768\n"
    "v_mfma_f32_16x16x32_bf16 v[76:79], a[252:255], a[196:199], v[76:79]\n"
    "s_add_u32 s7, s7, s32\n"
    "s_mov_b64 exec, 0xffff\n"
    "buffer_load_dword v255, s[8:11], s7 offen lds\n"
    "s_mov_b64 exec, -1\n"
    "v_mfma_f32_16x16x32_bf16 v[108:111], a[248:251], a[200:203], v[108:111]\n"
    "s_mov_b32 m0, 160768\n"
    "v_mfma_f32_16x16x32_bf16 v[108:111], a[252:255], a[204:207], v[108:111]\n"
    "s_add_u32 s7, s7, s32\n"
    "s_mov_b64 exec, 0xffff\n"
    "buffer_load_dword v255, s[8:11], s7 offen lds\n"
    "s_mov_b64 exec, -1\n"
    "s_waitcnt lgkmcnt(0)\n"
    "v_mfma_f32_16x16x32_bf16 v[16:19], a[240:243], a[176:179], v[16:19]\n"
    "ds_read_b64_tr_b16 a[248:249], v240 offset:8200\n"
    "v_mfma_f32_16x16x32_bf16 v[16:19], a[244:247], a[180:183], v[16:19]\n"
    "ds_read_b64_tr_b16 a[250:251], v240 offset:8456\n"
    "v_mfma_f32_16x16x32_bf16 v[48:51], a[240:243], a[184:187], v[48:51]\n"
    "ds_read_b64_tr_b16 a[252:253], v240 offset:10248\n"
    "v_mfma_f32_16x16x32_bf16 v[48:51], a[244:247], a[188:191], v[48:51]\n"
    "ds_read_b64_tr_b16 a[254:255], v240 offset:10504\n"
    "v_mfma_f32_16x16x32_bf16 v[80:83], a[240:243], a[192:195], v[80:83]\n"
    "v_mfma_f32_16x16x32_bf16 v[80:83], a[244:247], a[196:199], v[80:83]\n"
    "v_mfma_f32_16x16x32_bf16 v[112:115], a[240:243], a[200:203], v[112:115]\n"
    "v_mfma_f32_16x16x32_bf16 v[112:115], a[244:247], a[204:207], v[112:115]\n"
    "s_waitcnt lgkmcnt(0)\n"
    "v_mfma_f32_16x16x32_bf16 v[20:23], a[248:251], a[176:179], v[20:23]\n"
    "s_add_u32 s78, s78, 524288\n"
    "ds_read_b64_tr_b16 a[240:241], v240 offset:12288\n"
    "v_mfma_f32_16x16x32_bf16 v[20:23], a[252:255], a[180:183], v[20:23]\n"
    "s_add_u32 s79, s79, 589824\n"
    "ds_read_b64_tr_b16 a[242:243], v240 offset:12544\n"
    "v_mfma_f32_16x16x32_bf16 v[52:55], a[248:251], a[184:187], v[52:55]\n"
    "s_add_u32 s80, s80, 589824\n"
    "ds_read_b64_tr_b16 a[244:245], v240 offset:14336\n"
    "v_mfma_f32_16x16x32_bf16 v[52:55], a[252:255], a[188:191], v[52:55]\n"
    "s_add_u32 s76, s76, 53248\n"
    "ds_read_b64_tr_b16 a[246:247], v240 offset:14592\n"
    "v_mfma_f32_16x16x32_bf16 v[84:87], a[248:251], a[192:195], v[84:87]\n"
    "s_cmp_ge_u32 s76, 159744\n"
    "v_mfma_f32_16x16x32_bf16 v[84:87], a[252:255], a[196:199], v[84:87]\n"
    "s_cselect_b32 s73, 159744, 0\n"
    "v_mfma_f32_16x16x32_bf16 v[116:119], a[248:251], a[200:203], v[116:119]\n"
    "s_sub_u32 s76, s76, s73\n"
    "v_mfma_f32_16x16x32_bf16 v[116:119], a[252:255], a[204:207], v[116:119]\n"
    "s_waitcnt lgkmcnt(0)\n"
    "v_mfma_f32_16x16x32_bf16 v[24:27], a[240:243], a[176:179], v[24:27]\n"
    "ds_read_b64_tr_b16 a[248:249], v240 offset:12296\n"
    "v_mfma_f32_16x16x32_bf16 v[24:27], a[244:247], a[180:183], v[24:27]\n"
    "ds_read_b64_tr_b16 a[250:251], v240 offset:12552\n"
    "v_mfma_f32_16x16x32_bf16 v[56:59], a[240:243], a[184:187], v[56:59]\n"
    "ds_read_b64_tr_b16 a[252:253], v240 offset:14344\n"
    "v_mfma_f32_16x16x32_bf16 v[56:59], a[244:247], a[188:191], v[56:59]\n"
    "s_add_u32 s82, s82, 53248\n"
    "ds_read_b64_tr_b16 a[254:255], v240 offset:14600\n"
    "v_mfma_f32_16x16x32_bf16 v[88:91], a[240:243], a[192:195], v[88:91]\n"
    "s_cmp_ge_u32 s82, 159744\n"
    "v_mfma_f32_16x16x32_bf16 v[88:91], a[244:247], a[196:199], v[88:91]\n"
    "s_cselect_b32 s77, 4294860800, s83\n"
    "s_cselect_b32 s82, 0, s82\n"
    "v_mfma_f32_16x16x32_bf16 v[120:123], a[240:243], a[200:203], v[120:123]\n"
    "v_add_u32 v240, s77, v240\n"
    "v_add_u32 v241, s77, v241\n"
    "v_mfma_f32_16x16x32_bf16 v[120:123], a[244:247], a[204:207], v[120:123]\n"
    "v_add_u32 v242, s77, v242\n"
    "s_waitcnt lgkmcnt(0)\n"
    "v_mfma_f32_16x16x32_bf16 v[28:31], a[248:251], a[176:179], v[28:31]\n"
    "v_mfma_f32_16x16x32_bf16 v[28:31], a[252:255], a[180:183], v[28:31]\n"
    "s_waitcnt vmcnt(17)\n"
    "s_barrier\n"
    "v_mfma_f32_16x16x32_bf16 v[60:63], a[248:251], a[184:187], v[60:63]\n"
    "ds_read_b64_tr_b16 a[240:241], v241 offset:16384\n"
    "v_mfma_f32_16x16x32_bf16 v[60:63], a[252:255], a[188:191], v[60:63]\n"
    "ds_read_b64_tr_b16 a[242:243], v241 offset:16640\n"
    "v_mfma_f32_16x16x32_bf16 v[92:95], a[248:251], a[192:195], v[92:95]\n"
    "ds_read_b64_tr_b16 a[244:245], v241 offset:18432\n"
    "v_mfma_f32_16x16x32_bf16 v[92:95], a[252:255], a[196:199], v[92:95]\n"
    "ds_read_b64_tr_b16 a[246:247], v241 offset:18688\n"
    "v_mfma_f32_16x16x32_bf16 v[124:127], a[248:251], a[200:203], v[124:127]\n"
    "v_mfma_f32_16x16x32_bf16 v[124:127], a[252:255], a[204:207], v[124:127]\n"
    ".p2align 6\n"
    "L_loop_p1:\n"
    "s_waitcnt lgkmcnt(0)\n"
    "v_mfma_f32_16x16x32_bf16 v[128:131], a[240:243], a[208:211], v[128:131]\n"
    "ds_read_b64_tr_b16 a[248:249], v241 offset:32768\n"
    "v_mfma_f32_16x16x32_bf16 v[128:131], a[244:247], a[212:215], v[128:131]\n"
    "ds_read_b64_tr_b16 a[250:251], v241 offset:33024\n"
    "v_mfma_f32_16x16x32_bf16 v[132:135], a[240:243], a[216:219], v[132:135]\n"
    "ds_read_b64_tr_b16 a[252:253], v241 offset:34816\n"
    "v_mfma_f32_16x16x32_bf16 v[132:135], a[244:247], a[220:223], v[132:135]\n"
    "ds_read_b64_tr_b16 a[254:255], v241 offset:35072\n"
    "v_mfma_f32_16x16x32_bf16 v[136:139], a[240:243], a[224:227], v[136:139]\n"
    "v_mfma_f32_16x16x32_bf16 v[136:139], a[244:247], a[228:231], v[136:139]\n"
    "v_mfma_f32_16x16x32_bf16 v[140:143], a[240:243], a[232:235], v[140:143]\n"
    "s_add_u32 s72, s81, 0\n"
    "buffer_load_dwordx4 a[176:179], v246, s[16:19], s72 offen sc1\n"
    "v_mfma_f32_16x16x32_bf16 v[140:143], a[244:247], a[236:239], v[140:143]\n"
    "s_waitcnt lgkmcnt(0)\n"
    "v_mfma_f32_16x16x32_bf16 a[0:3], a[248:251], a[208:211], a[0:3]\n"
    "ds_read_b64_tr_b16 a[240:241], v242 offset:16384\n"
    "v_mfma_f32_16x16x32_bf16 a[0:3], a[252:255], a[212:215], a[0:3]\n"
    "ds_read_b64_tr_b16 a[242:243], v242 offset:16640\n"
    "v_mfma_f32_16x16x32_bf16 a[4:7], a[248:251], a[216:219], a[4:7]\n"
    "ds_read_b64_tr_b16 a[244:245], v242 offset:18432\n"
    "v_mfma_f32_16x16x32_bf16 a[4:7], a[252:255], a[220:223], a[4:7]\n"
    "ds_read_b64_tr_b16 a[246:247], v242 offset:18688\n"
    "v_mfma_f32_16x16x32_bf16 a[8:11], a[248:251], a[224:227], a[8:11]\n"
    "v_mfma_f32_16x16x32_bf16 a[8:11], a[252:255], a[228:231], a[8:11]\n"
    "v_mfma_f32_16x16x32_bf16 a[12:15], a[248:251], a[232:235], a[12:15]\n"
    "buffer_load_dwordx4 a[180:183], v246, s[16:19], s72 offen offset:1024 sc1\n"
    "v_mfma_f32_16x16x32_bf16 a[12:15], a[252:255], a[236:239], a[12:15]\n"
    "s_waitcnt lgkmcnt(0)\n"
    "v_mfma_f32_16x16x32_bf16 v[144:147], a[240:243], a[208:211], v[144:147]\n"
    "ds_read_b64_tr_b16 a[248:249], v242 offset:32768\n"
    "v_mfma_f32_16x16x32_bf16 v[144:147], a[244:247], a[212:215], v[144:147]\n"
    "ds_read_b64_tr_b16 a[250:251], v242 offset:33024\n"
    "v_mfma_f32_16x16x32_bf16 v[148:151], a[240:243], a[216:219], v[148:151]\n"
    "ds_read_b64_tr_b16 a[252:253], v242 offset:34816\n"
    "v_mfma_f32_16x16x32_bf16 v[148:151], a[244:247], a[220:223], v[148:151]\n"
    "ds_read_b64_tr_b16 a[254:255], v242 offset:35072\n"
    "v_mfma_f32_16x16x32_bf16 v[152:155], a[240:243], a[224:227], v[152:155]\n"
    "v_mfma_f32_16x16x32_bf16 v[152:155], a[244:247], a[228:231], v[152:155]\n"
    "v_mfma_f32_16x16x32_bf16 v[156:159], a[240:243], a[232:235], v[156:159]\n"
    "s_add_u32 s72, s81, 18432\n"
    "buffer_load_dwordx4 a[184:187], v246, s[16:19], s72 offen sc1\n"
    "v_mfma_f32_16x16x32_bf16 v[156:159], a[244:247], a[236:239], v[156:159]\n"
    "s_waitcnt lgkmcnt(0)\n"
    "v_mfma_f32_16x16x32_bf16 a[16:19], a[248:251], a[208:211], a[16:19]\n"
    "ds_read_b64_tr_b16 a[240:241], v241 offset:20480\n"
    "v_mfma_f32_16x16x32_bf16 a[16:19], a[252:255], a[212:215], a[16:19]\n"
    "ds_read_b64_tr_b16 a[242:243], v241 offset:20736\n"
    "v_mfma_f32_16x16x32_bf16 a[20:23], a[248:251], a[216:219], a[20:23]\n"
    "ds_read_b64_tr_b16 a[244:245], v241 offset:22528\n"
    "v_mfma_f32_16x16x32_bf16 a[20:23], a[252:255], a[220:223], a[20:23]\n"
    "ds_read_b64_tr_b16 a[246:247], v241 offset:22784\n"
    "v_mfma_f32_16x16x32_bf16 a[24:27], a[248:251], a[224:227], a[24:27]\n"
    "v_mfma_f32_16x16x32_bf16 a[24:27], a[252:255], a[228:231], a[24:27]\n"
    "v_mfma_f32_16x16x32_bf16 a[28:31], a[248:251], a[232:235], a[28:31]\n"
    "buffer_load_dwordx4 a[188:191], v246, s[16:19], s72 offen offset:1024 sc1\n"
    "v_mfma_f32_16x16x32_bf16 a[28:31], a[252:255], a[236:239], a[28:31]\n"
    "s_waitcnt lgkmcnt(0)\n"
    "v_mfma_f32_16x16x32_bf16 v[160:163], a[240:243], a[208:211], v[160:163]\n"
    "ds_read_b64_tr_b16 a[248:249], v241 offset:36864\n"
    "v_mfma_f32_16x16x32_bf16 v[160:163], a[244:247], a[212:215], v[160:163]\n"
    "ds_read_b64_tr_b16 a[250:251], v241 offset:37120\n"
    "v_mfma_f32_16x16x32_bf16 v[164:167], a[240:243], a[216:219], v[164:167]\n"
    "ds_read_b64_tr_b16 a[252:253], v241 offset:38912\n"
    "v_mfma_f32_16x16x32_bf16 v[164:167], a[244:247], a[220:223], v[164:167]\n"
    "ds_read_b64_tr_b16 a[254:255], v241 offset:39168\n"
    "v_mfma_f32_16x16x32_bf16 v[168:171], a[240:243], a[224:227], v[168:171]\n"
    "v_mfma_f32_16x16x32_bf16 v[168:171], a[244:247], a[228:231], v[168:171]\n"
    "v_mfma_f32_16x16x32_bf16 v[172:175], a[240:243], a[232:235], v[172:175]\n"
    "s_add_u32 s72, s81, 36864\n"
    "buffer_load_dwordx4 a[192:195], v246, s[16:19], s72 offen sc1\n"
    "v_mfma_f32_16x16x32_bf16 v[172:175], a[244:247], a[236:239], v[172:175]\n"
    "s_waitcnt lgkmcnt(0)\n"
    "v_mfma_f32_16x16x32_bf16 a[32:35], a[248:251], a[208:211], a[32:35]\n"
    "ds_read_b64_tr_b16 a[240:241], v242 offset:20480\n"
    "v_mfma_f32_16x16x32_bf16 a[32:35], a[252:255], a[212:215], a[32:35]\n"
    "ds_read_b64_tr_b16 a[242:243], v242 offset:20736\n"
    "v_mfma_f32_16x16x32_bf16 a[36:39], a[248:251], a[216:219], a[36:39]\n"
    "ds_read_b64_tr_b16 a[244:245], v242 offset:22528\n"
    "v_mfma_f32_16x16x32_bf16 a[36:39], a[252:255], a[220:223], a[36:39]\n"
    "ds_read_b64_tr_b16 a[246:247], v242 offset:22784\n"
    "v_mfma_f32_16x16x32_bf16 a[40:43], a[248:251], a[224:227], a[40:43]\n"
    "v_mfma_f32_16x16x32_bf16 a[40:43], a[252:255], a[228:231], a[40:43]\n"
    "v_mfma_f32_16x16x32_bf16 a[44:47], a[248:251], a[232:235], a[44:47]\n"
    "buffer_load_dwordx4 a[196:199], v246, s[16:19], s72 offen offset:1024 sc1\n"
    "v_mfma_f32_16x16x32_bf16 a[44:47], a[252:255], a[236:239], a[44:47]\n"
    "s_waitcnt lgkmcnt(0)\n"
    "v_mfma_f32_16x16x32_bf16 v[176:179], a[240:243], a[208:211], v[176:179]\n"
    "ds_read_b64_tr_b16 a[248:249], v242 offset:36864\n"
    "v_mfma_f32_16x16x32_bf16 v[176:179], a[244:247], a[212:215], v[176:179]\n"
    "ds_read_b64_tr_b16 a[250:251], v242 offset:37120\n"
    "v_mfma_f32_16x16x32_bf16 v[180:183], a[240:243], a[216:219], v[180:183]\n"
    "ds_read_b64_tr_b16 a[252:253], v242 offset:38912\n"
    "v_mfma_f32_16x16x32_bf16 v[180:183], a[244:247], a[220:223], v[180:183]\n"
    "ds_read_b64_tr_b16 a[254:255], v242 offset:39168\n"
    "v_mfma_f32_16x16x32_bf16 v[184:187], a[240:243], a[224:227], v[184:187]\n"
    "v_mfma_f32_16x16x32_bf16 v[184:187], a[244:247], a[228:231], v[184:187]\n"
    "v_mfma_f32_16x16x32_bf16 v[188:191], a[240:243], a[232:235], v[188:191]\n"
    "s_add_u32 s72, s81, 55296\n"
    "buffer_load_dwordx4 a[200:203], v246, s[16:19], s72 offen sc1\n"
    "v_mfma_f32_16x16x32_bf16 v[188:191], a[244:247], a[236:239], v[188:191]\n"
    "s_waitcnt lgkmcnt(0)\n"
    "v_mfma_f32_16x16x32_bf16 a[48:51], a[248:251], a[208:211], a[48:51]\n"
    "ds_read_b64_tr_b16 a[240:241], v241 offset:24576\n"
    "v_mfma_f32_16x16x32_bf16 a[48:51], a[252:255], a[212:215], a[48:51]\n"
    "ds_read_b64_tr_b16 a[242:243], v241 offset:24832\n"
    "v_mfma_f32_16x16x32_bf16 a[52:55], a[248:251], a[216:219], a[52:55]\n"
    "ds_read_b64_tr_b16 a[244:245], v241 offset:26624\n"
    "v_mfma_f32_16x16x32_bf16 a[52:55], a[252:255], a[220:223], a[52:55]\n"
    "ds_read_b64_tr_b16 a[246:247], v241 offset:26880\n"
    "v_mfma_f32_16x16x32_bf16 a[56:59], a[248:251], a[224:227], a[56:59]\n"
    "v_mfma_f32_16x16x32_bf16 a[56:59], a[252:255], a[228:231], a[56:59]\n"
    "v_mfma_f32_16x16x32_bf16 a[60:63], a[248:251], a[232:235], a[60:63]\n"
    "buffer_load_dwordx4 a[204:207], v246, s[16:19], s72 offen offset:1024 sc1\n"
    "v_mfma_f32_16x16x32_bf16 a[60:63], a[252:255], a[236:239], a[60:63]\n"
    "s_waitcnt lgkmcnt(0)\n"
    "v_mfma_f32_16x16x32_bf16 v[192:195], a[240:243], a[208:211], v[192:195]\n"
    "ds_read_b64_tr_b16 a[248:249], v241 offset:40960\n"
    "v_mfma_f32_16x16x32_bf16 v[192:195], a[244:247], a[212:215], v[192:195]\n"
    "s_add_u32 s81, s81, 2048\n"
    "ds_read_b64_tr_b16 a[250:251], v241 offset:41216\n"
    "v_mfma_f32_16x16x32_bf16 v[196:199], a[240:243], a[216:219], v[196:199]\n"
    "ds_read_b64_tr_b16 a[252:253], v241 offset:43008\n"
    "v_mfma_f32_16x16x32_bf16 v[196:199], a[244:247], a[220:223], v[196:199]\n"
    "ds_read_b64_tr_b16 a[254:255], v241 offset:43264\n"
    "v_mfma_f32_16x16x32_bf16 v[200:203], a[240:243], a[224:227], v[200:203]\n"
    "v_mfma_f32_16x16x32_bf16 v[200:203], a[244:247], a[228:231], v[200:203]\n"
    "s_add_u32 m0, s76, 0\n"
    "v_mfma_f32_16x16x32_bf16 v[204:207], a[240:243], a[232:235], v[204:207]\n"
    "s_add_u32 s72, s78, 0\n"
    "buffer_load_dwordx4 v243, s[20:23], s72 offen lds\n"
    "v_mfma_f32_16x16x32_bf16 v[204:207], a[244:247], a[236:239], v[204:207]\n"
    "s_waitcnt lgkmcnt(0)\n"
    "v_mfma_f32_16x16x32_bf16 a[64:67], a[248:251], a[208:211], a[64:67]\n"
    "ds_read_b64_tr_b16 a[240:241], v242 offset:24576\n"
    "v_mfma_f32_16x16x32_bf16 a[64:67], a[252:255], a[212:215], a[64:67]\n"
    "ds_read_b64_tr_b16 a[242:243], v242 offset:24832\n"
    "v_mfma_f32_16x16x32_bf16 a[68:71], a[248:251], a[216:219], a[68:71]\n"
    "ds_read_b64_tr_b16 a[244:245], v242 offset:26624\n"
    "v_mfma_f32_16x16x32_bf16 a[68:71], a[252:255], a[220:223], a[68:71]\n"
    "ds_read_b64_tr_b16 a[246:247], v242 offset:26880\n"
    "v_mfma_f32_16x16x32_bf16 a[72:75], a[248:251], a[224:227], a[72:75]\n"
    "v_mfma_f32_16x16x32_bf16 a[72:75], a[252:255], a[228:231], a[72:75]\n"
    "s_add_u32 m0, s76, 4096\n"
    "v_mfma_f32_16x16x32_bf16 a[76:79], a[248:251], a[232:235], a[76:79]\n"
    "s_add_u32 s72, s78, 64\n"
    "buffer_load_dwordx4 v243, s[20:23], s72 offen lds\n"
    "v_mfma_f32_16x16x32_bf16 a[76:79], a[252:255], a[236:239], a[76:79]\n"
    "s_waitcnt lgkmcnt(0)\n"
    "v_mfma_f32_16x16x32_bf16 v[208:211], a[240:243], a[208:211], v[208:211]\n"
    "ds_read_b64_tr_b16 a[248:249], v242 offset:40960\n"
    "v_mfma_f32_16x16x32_bf16 v[208:211], a[244:247], a[212:215], v[208:211]\n"
    "ds_read_b64_tr_b16 a[250:251], v242 offset:41216\n"
    "v_mfma_f32_16x16x32_bf16 v[212:215], a[240:243], a[216:219], v[212:215]\n"
    "ds_read_b64_tr_b16 a[252:253], v242 offset:43008\n"
    "v_mfma_f32_16x16x32_bf16 v[212:215], a[244:247], a[220:223], v[212:215]\n"
    "ds_read_b64_tr_b16 a[254:255], v242 offset:43264\n"
    "v_mfma_f32_16x16x32_bf16 v[216:219], a[240:243], a[224:227], v[216:219]\n"
    "v_mfma_f32_16x16x32_bf16 v[216:219], a[244:247], a[228:231], v[216:219]\n"
    "s_add_u32 m0, s76, 8192\n"
    "v_mfma_f32_16x16x32_bf16 v[220:223], a[240:243], a[232:235], v[220:223]\n"
    "s_add_u32 s72, s78, 128\n"
    "buffer_load_dwordx4 v243, s[20:23], s72 offen lds\n"
    "v_mfma_f32_16x16x32_bf16 v[220:223], a[244:247], a[236:239], v[220:223]\n"
    "s_waitcnt lgkmcnt(0)\n"
    "v_mfma_f32_16x16x32_bf16 a[80:83], a[248:251], a[208:211], a[80:83]\n"
    "ds_read_b64_tr_b16 a[240:241], v241 offset:28672\n"
    "v_mfma_f32_16x16x32_bf16 a[80:83], a[252:255], a[212:215], a[80:83]\n"
    "ds_read_b64_tr_b16 a[242:243], v241 offset:28928\n"
    "v_mfma_f32_16x16x32_bf16 a[84:87], a[248:251], a[216:219], a[84:87]\n"
    "ds_read_b64_tr_b16 a[244:245], v241 offset:30720\n"
    "v_mfma_f32_16x16x32_bf16 a[84:87], a[252:255], a[220:223], a[84:87]\n"
    "ds_read_b64_tr_b16 a[246:247], v241 offset:30976\n"
    "v_mfma_f32_16x16x32_bf16 a[88:91], a[248:251], a[224:227], a[88:91]\n"
    "v_mfma_f32_16x16x32_bf16 a[88:91], a[252:255], a[228:231], a[88:91]\n"
    "s_add_u32 m0, s76, 12288\n"
    "v_mfma_f32_16x16x32_bf16 a[92:95], a[248:251], a[232:235], a[92:95]\n"
    "s_add_u32 s72, s78, 192\n"
    "buffer_load_dwordx4 v243, s[20:23], s72 offen lds\n"
    "v_mfma_f32_16x16x32_bf16 a[92:95], a[252:255], a[236:239], a[92:95]\n"
    "s_waitcnt lgkmcnt(0)\n"
    "v_mfma_f32_16x16x32_bf16 v[224:227], a[240:243], a[208:211], v[224:227]\n"
    "ds_read_b64_tr_b16 a[248:249], v241 offset:45056\n"
    "v_mfma_f32_16x16x32_bf16 v[224:227], a[244:247], a[212:215], v[224:227]\n"
    "ds_read_b64_tr_b16 a[250:251], v241 offset:45312\n"
    "v_mfma_f32_16x16x32_bf16 v[228:231], a[240:243], a[216:219], v[228:231]\n"
    "ds_read_b64_tr_b16 a[252:253], v241 offset:47104\n"
    "v_mfma_f32_16x16x32_bf16 v[228:231], a[244:247], a[220:223], v[228:231]\n"
    "ds_read_b64_tr_b16 a[254:255], v241 offset:47360\n"
    "v_mfma_f32_16x16x32_bf16 v[232:235], a[240:243], a[224:227], v[232:235]\n"
    "v_mfma_f32_16x16x32_bf16 v[232:235], a[244:247], a[228:231], v[232:235]\n"
    "s_add_u32 m0, s76, 16384\n"
    "v_mfma_f32_16x16x32_bf16 v[236:239], a[240:243], a[232:235], v[236:239]\n"
    "s_add_u32 s72, s79, 0\n"
    "buffer_load_dwordx4 v244, s[24:27], s72 offen lds\n"
    "v_mfma_f32_16x16x32_bf16 v[236:239], a[244:247], a[236:239], v[236:239]\n"
    "s_waitcnt lgkmcnt(0)\n"
    "v_mfma_f32_16x16x32_bf16 a[96:99], a[248:251], a[208:211], a[96:99]\n"
    "ds_read_b64_tr_b16 a[240:241], v242 offset:28672\n"
    "v_mfma_f32_16x16x32_bf16 a[96:99], a[252:255], a[212:215], a[96:99]\n"
    "ds_read_b64_tr_b16 a[242:243], v242 offset:28928\n"
    "v_mfma_f32_16x16x32_bf16 a[100:103], a[248:251], a[216:219], a[100:103]\n"
    "ds_read_b64_tr_b16 a[244:245], v242 offset:30720\n"
    "v_mfma_f32_16x16x32_bf16 a[100:103], a[252:255], a[220:223], a[100:103]\n"
    "ds_read_b64_tr_b16 a[246:247], v242 offset:30976\n"
    "v_mfma_f32_16x16x32_bf16 a[104:107], a[248:251], a[224:227], a[104:107]\n"
    "v_mfma_f32_16x16x32_bf16 a[104:107], a[252:255], a[228:231], a[104:107]\n"
    "s_add_u32 m0, s76, 20480\n"
    "v_mfma_f32_16x16x32_bf16 a[108:111], a[248:251], a[232:235], a[108:111]\n"
    "s_add_u32 s72, s79, 64\n"
    "buffer_load_dwordx4 v244, s[24:27], s72 offen lds\n"
    "v_mfma_f32_16x16x32_bf16 a[108:111], a[252:255], a[236:239], a[108:111]\n"
    "s_waitcnt lgkmcnt(0)\n"
    "v_mfma_f32_16x16x32_bf16 a[144:147], a[240:243], a[208:211], a[144:147]\n"
    "ds_read_b64_tr_b16 a[248:249], v242 offset:45056\n"
    "v_mfma_f32_16x16x32_bf16 a[144:147], a[244:247], a[212:215], a[144:147]\n"
    "ds_read_b64_tr_b16 a[250:251], v242 offset:45312\n"
    "v_mfma_f32_16x16x32_bf16 a[148:151], a[240:243], a[216:219], a[148:151]\n"
    "ds_read_b64_tr_b16 a[252:253], v242 offset:47104\n"
    "v_mfma_f32_16x16x32_bf16 a[148:151], a[244:247], a[220:223], a[148:151]\n"
    "ds_read_b64_tr_b16 a[254:255], v242 offset:47360\n"
    "v_mfma_f32_16x16x32_bf16 a[152:155], a[240:243], a[224:227], a[152:155]\n"
    "v_mfma_f32_16x16x32_bf16 a[152:155], a[244:247], a[228:231], a[152:155]\n"
    "s_add_u32 m0, s76, 24576\n"
    "v_mfma_f32_16x16x32_bf16 a[156:159], a[240:243], a[232:235], a[156:159]\n"
    "s_add_u32 s72, s79, 128\n"
    "buffer_load_dwordx4 v244, s[24:27], s72 offen lds\n"
    "v_mfma_f32_16x16x32_bf16 a[156:159], a[244:247], a[236:239], a[156:159]\n"
    "s_waitcnt lgkmcnt(0)\n"
    "v_mfma_f32_16x16x32_bf16 a[112:115], a[248:251], a[208:211], a[112:115]\n"
    "ds_read_b64_tr_b16 a[240:241], v241 offset:49152\n"
    "v_mfma_f32_16x16x32_bf16 a[112:115], a[252:255], a[212:215], a[112:115]\n"
    "ds_read_b64_tr_b16 a[242:243], v241 offset:49408\n"
    "v_mfma_f32_16x16x32_bf16 a[116:119], a[248:251], a[216:219], a[116:119]\n"
    "ds_read_b64_tr_b16 a[244:245], v241 offset:51200\n"
    "v_mfma_f32_16x16x32_bf16 a[116:119], a[252:255], a[220:223], a[116:119]\n"
    "ds_read_b64_tr_b16 a[246:247], v241 offset:51456\n"
    "v_mfma_f32_16x16x32_bf16 a[120:123], a[248:251], a[224:227], a[120:123]\n"
    "v_mfma_f32_16x16x32_bf16 a[120:123], a[252:255], a[228:231], a[120:123]\n"
    "s_add_u32 m0, s76, 28672\n"
    "v_mfma_f32_16x16x32_bf16 a[124:127], a[248:251], a[232:235], a[124:127]\n"
    "s_add_u32 s72, s79, 192\n"
    "buffer_load_dwordx4 v244, s[24:27], s72 offen lds\n"
    "v_mfma_f32_16x16x32_bf16 a[124:127], a[252:255], a[236:239], a[124:127]\n"
    "s_waitcnt lgkmcnt(0)\n"
    "v_mfma_f32_16x16x32_bf16 a[160:163], a[240:243], a[208:211], a[160:163]\n"
    "ds_read_b64_tr_b16 a[248:249], v242 offset:49152\n"
    "v_mfma_f32_16x16x32_bf16 a[160:163], a[244:247], a[212:215], a[160:163]\n"
    "ds_read_b64_tr_b16 a[250:251], v242 offset:49408\n"
    "v_mfma_f32_16x16x32_bf16 a[164:167], a[240:243], a[216:219], a[164:167]\n"
    "ds_read_b64_tr_b16 a[252:253], v242 offset:51200\n"
    "v_mfma_f32_16x16x32_bf16 a[164:167], a[244:247], a[220:223], a[164:167]\n"
    "ds_read_b64_tr_b16 a[254:255], v242 offset:51456\n"
    "v_mfma_f32_16x16x32_bf16 a[168:171], a[240:243], a[224:227], a[168:171]\n"
    "v_mfma_f32_16x16x32_bf16 a[168:171], a[244:247], a[228:231], a[168:171]\n"
    "s_add_u32 m0, s76, 49152\n"
    "v_mfma_f32_16x16x32_bf16 a[172:175], a[240:243], a[232:235], a[172:175]\n"
    "s_mov_b64 exec, s[12:13]\n"
    "buffer_load_dwordx4 v245, s[24:27], s80 offen lds\n"
    "s_mov_b64 exec, -1\n"
    "v_mfma_f32_16x16x32_bf16 a[172:175], a[244:247], a[236:239], a[172:175]\n"
    "s_waitcnt lgkmcnt(0)\n"
    "v_mfma_f32_16x16x32_bf16 a[128:131], a[248:251], a[208:211], a[128:131]\n"
    "ds_read_b64_tr_b16 a[240:241], v240 offset:0\n"
    "v_mfma_f32_16x16x32_bf16 a[128:131], a[252:255], a[212:215], a[128:131]\n"
    "ds_read_b64_tr_b16 a[242:243], v240 offset:256\n"
    "v_mfma_f32_16x16x32_bf16 a[132:135], a[248:251], a[216:219], a[132:135]\n"
    "ds_read_b64_tr_b16 a[244:245], v240 offset:2048\n"
    "v_mfma_f32_16x16x32_bf16 a[132:135], a[252:255], a[220:223], a[132:135]\n"
    "ds_read_b64_tr_b16 a[246:247], v240 offset:2304\n"
    "v_mfma_f32_16x16x32_bf16 a[136:139], a[248:251], a[224:227], a[136:139]\n"
    "v_mfma_f32_16x16x32_bf16 a[136:139], a[252:255], a[228:231], a[136:139]\n"
    "s_add_u32 m0, s76, 32768\n"
    "v_mfma_f32_16x16x32_bf16 a[140:143], a[248:251], a[232:235], a[140:143]\n"
    "s_add_u32 s72, s79, 0\n"
    "buffer_load_dwordx4 v244, s[28:31], s72 offen lds\n"
    "v_mfma_f32_16x16x32_bf16 a[140:143], a[252:255], a[236:239], a[140:143]\n"
    "s_waitcnt lgkmcnt(0)\n"
    "v_mfma_f32_16x16x32_bf16 v[0:3], a[240:243], a[208:211], v[0:3]\n"
    "ds_read_b64_tr_b16 a[248:249], v240 offset:8\n"
    "v_mfma_f32_16x16x32_bf16 v[0:3], a[244:247], a[212:215], v[0:3]\n"
    "ds_read_b64_tr_b16 a[250:251], v240 offset:264\n"
    "v_mfma_f32_16x16x32_bf16 v[32:35], a[240:243], a[216:219], v[32:35]\n"
    "ds_read_b64_tr_b16 a[252:253], v240 offset:2056\n"
    "v_mfma_f32_16x16x32_bf16 v[32:35], a[244:247], a[220:223], v[32:35]\n"
    "ds_read_b64_tr_b16 a[254:255], v240 offset:2312\n"
    "v_mfma_f32_16x16x32_bf16 v[64:67], a[240:243], a[224:227], v[64:67]\n"
    "v_mfma_f32_16x16x32_bf16 v[64:67], a[244:247], a[228:231], v[64:67]\n"
    "s_add_u32 m0, s76, 36864\n"
    "v_mfma_f32_16x16x32_bf16 v[96:99], a[240:243], a[232:235], v[96:99]\n"
    "s_add_u32 s72, s79, 64\n"
    "buffer_load_dwordx4 v244, s[28:31], s72 offen lds\n"
    "v_mfma_f32_16x16x32_bf16 v[96:99], a[244:247], a[236:239], v[96:99]\n"
    "s_waitcnt lgkmcnt(0)\n"
    "v_mfma_f32_16x16x32_bf16 v[4:7], a[248:251], a[208:211], v[4:7]\n"
    "ds_read_b64_tr_b16 a[240:241], v240 offset:4096\n"
    "v_mfma_f32_16x16x32_bf16 v[4:7], a[252:255], a[212:215], v[4:7]\n"
    "ds_read_b64_tr_b16 a[242:243], v240 offset:4352\n"
    "v_mfma_f32_16x16x32_bf16 v[36:39], a[248:251], a[216:219], v[36:39]\n"
    "ds_read_b64_tr_b16 a[244:245], v240 offset:6144\n"
    "v_mfma_f32_16x16x32_bf16 v[36:39], a[252:255], a[220:223], v[36:39]\n"
    "ds_read_b64_tr_b16 a[246:247], v240 offset:6400\n"
    "v_mfma_f32_16x16x32_bf16 v[68:71], a[248:251], a[224:227], v[68:71]\n"
    "v_mfma_f32_16x16x32_bf16 v[68:71], a[252:255], a[228:231], v[68:71]\n"
    "s_add_u32 m0, s76, 40960\n"
    "v_mfma_f32_16x16x32_bf16 v[100:103], a[248:251], a[232:235], v[100:103]\n"
    "s_add_u32 s72, s79, 128\n"
    "buffer_load_dwordx4 v244, s[28:31], s72 offen lds\n"
    "v_mfma_f32_16x16x32_bf16 v[100:103], a[252:255], a[236:239], v[100:103]\n"
    "s_waitcnt lgkmcnt(0)\n"
    "v_mfma_f32_16x16x32_bf16 v[8:11], a[240:243], a[208:211], v[8:11]\n"
    "ds_read_b64_tr_b16 a[248:249], v240 offset:4104\n"
    "v_mfma_f32_16x16x32_bf16 v[8:11], a[244:247], a[212:215], v[8:11]\n"
    "ds_read_b64_tr_b16 a[250:251], v240 offset:4360\n"
    "v_mfma_f32_16x16x32_bf16 v[40:43], a[240:243], a[216:219], v[40:43]\n"
    "ds_read_b64_tr_b16 a[252:253], v240 offset:6152\n"
    "v_mfma_f32_16x16x32_bf16 v[40:43], a[244:247], a[220:223], v[40:43]\n"
    "ds_read_b64_tr_b16 a[254:255], v240 offset:6408\n"
    "v_mfma_f32_16x16x32_bf16 v[72:75], a[240:243], a[224:227], v[72:75]\n"
    "v_mfma_f32_16x16x32_bf16 v[72:75], a[244:247], a[228:231], v[72:75]\n"
    "s_add_u32 m0, s76, 45056\n"
    "v_mfma_f32_16x16x32_bf16 v[104:107], a[240:243], a[232:235], v[104:107]\n"
    "s_add_u32 s72, s79, 192\n"
    "buffer_load_dwordx4 v244, s[28:31], s72 offen lds\n"
    "v_mfma_f32_16x16x32_bf16 v[104:107], a[244:247], a[236:239], v[104:107]\n"
    "s_waitcnt lgkmcnt(0)\n"
    "v_mfma_f32_16x16x32_bf16 v[12:15], a[248:251], a[208:211], v[12:15]\n"
    "ds_read_b64_tr_b16 a[240:241], v240 offset:8192\n"
    "v_mfma_f32_16x16x32_bf16 v[12:15], a[252:255], a[212:215], v[12:15]\n"
    "ds_read_b64_tr_b16 a[242:243], v240 offset:8448\n"
    "v_mfma_f32_16x16x32_bf16 v[44:47], a[248:251], a[216:219], v[44:47]\n"
    "ds_read_b64_tr_b16 a[244:245], v240 offset:10240\n"
    "v_mfma_f32_16x16x32_bf16 v[44:47], a[252:255], a[220:223], v[44:47]\n"
    "ds_read_b64_tr_b16 a[246:247], v240 offset:10496\n"
    "v_mfma_f32_16x16x32_bf16 v[76:79], a[248:251], a[224:227], v[76:79]\n"
    "v_mfma_f32_16x16x32_bf16 v[76:79], a[252:255], a[228:231], v[76:79]\n"
    "s_add_u32 m0, s76, 49152\n"
    "v_mfma_f32_16x16x32_bf16 v[108:111], a[248:251], a[232:235], v[108:111]\n"
    "s_mov_b64 exec, s[14:15]\n"
    "buffer_load_dwordx4 v245, s[28:31], s80 offen lds\n"
    "s_mov_b64 exec, -1\n"
    "v_mfma_f32_16x16x32_bf16 v[108:111], a[252:255], a[236:239], v[108:111]\n"
    "s_waitcnt lgkmcnt(0)\n"
    "v_mfma_f32_16x16x32_bf16 v[16:19], a[240:243], a[208:211], v[16:19]\n"
    "ds_read_b64_tr_b16 a[248:249], v240 offset:8200\n"
    "v_mfma_f32_16x16x32_bf16 v[16:19], a[244:247], a[212:215], v[16:19]\n"
    "ds_read_b64_tr_b16 a[250:251], v240 offset:8456\n"
    "v_mfma_f32_16x16x32_bf16 v[48:51], a[240:243], a[216:219], v[48:51]\n"
    "ds_read_b64_tr_b16 a[252:253], v240 offset:10248\n"
    "v_mfma_f32_16x16x32_bf16 v[48:51], a[244:247], a[220:223], v[48:51]\n"
    "s_mov_b32 m0, 160768\n"
    "ds_read_b64_tr_b16 a[254:255], v240 offset:10504\n"
    "v_mfma_f32_16x16x32_bf16 v[80:83], a[240:243], a[224:227], v[80:83]\n"
    "s_add_u32 s7, s7, s32\n"
    "s_mov_b64 exec, 0xffff\n"
    "buffer_load_dword v255, s[8:11], s7 offen lds\n"
    "s_mov_b64 exec, -1\n"
    "v_mfma_f32_16x16x32_bf16 v[80:83], a[244:247], a[228:231], v[80:83]\n"
    "v_mfma_f32_16x16x32_bf16 v[112:115], a[240:243], a[232:235], v[112:115]\n"
    "v_mfma_f32_16x16x32_bf16 v[112:115], a[244:247], a[236:239], v[112:115]\n"
    "s_add_u32 s78, s78, 524288\n"
    "s_waitcnt lgkmcnt(0)\n"
    "v_mfma_f32_16x16x32_bf16 v[20:23], a[248:251], a[208:211], v[20:23]\n"
    "s_add_u32 s79, s79, 589824\n"
    "ds_read_b64_tr_b16 a[240:241], v240 offset:12288\n"
    "v_mfma_f32_16x16x32_bf16 v[20:23], a[252:255], a[212:215], v[20:23]\n"
    "s_add_u32 s80, s80, 589824\n"
    "ds_read_b64_tr_b16 a[242:243], v240 offset:12544\n"
    "v_mfma_f32_16x16x32_bf16 v[52:55], a[248:251], a[216:219], v[52:55]\n"
    "s_add_u32 s76, s76, 53248\n"
    "ds_read_b64_tr_b16 a[244:245], v240 offset:14336\n"
    "v_mfma_f32_16x16x32_bf16 v[52:55], a[252:255], a[220:223], v[52:55]\n"
    "s_cmp_ge_u32 s76, 159744\n"
    "ds_read_b64_tr_b16 a[246:247], v240 offset:14592\n"
    "v_mfma_f32_16x16x32_bf16 v[84:87], a[248:251], a[224:227], v[84:87]\n"
    "s_cselect_b32 s73, 159744, 0\n"
    "v_mfma_f32_16x16x32_bf16 v[84:87], a[252:255], a[228:231], v[84:87]\n"
    "s_sub_u32 s76, s76, s73\n"
    "v_mfma_f32_16x16x32_bf16 v[116:119], a[248:251], a[232:235], v[116:119]\n"
    "v_mfma_f32_16x16x32_bf16 v[116:119], a[252:255], a[236:239], v[116:119]\n"
    "s_waitcnt lgkmcnt(0)\n"
    "v_mfma_f32_16x16x32_bf16 v[24:27], a[240:243], a[208:211], v[24:27]\n"
    "ds_read_b64_tr_b16 a[248:249], v240 offset:12296\n"
    "v_mfma_f32_16x16x32_bf16 v[24:27], a[244:247], a[212:215], v[24:27]\n"
    "ds_read_b64_tr_b16 a[250:251], v240 offset:12552\n"
    "v_mfma_f32_16x16x32_bf16 v[56:59], a[240:243], a[216:219], v[56:59]\n"
    "ds_read_b64_tr_b16 a[252:253], v240 offset:14344\n"
    "v_mfma_f32_16x16x32_bf16 v[56:59], a[244:247], a[220:223], v[56:59]\n"
    "s_add_u32 s82, s82, 53248\n"
    "ds_read_b64_tr_b16 a[254:255], v240 offset:14600\n"
    "v_mfma_f32_16x16x32_bf16 v[88:91], a[240:243], a[224:227], v[88:91]\n"
    "s_cmp_ge_u32 s82, 159744\n"
    "v_mfma_f32_16x16x32_bf16 v[88:91], a[244:247], a[228:231], v[88:91]\n"
    "s_cselect_b32 s77, 4294860800, s83\n"
    "s_cselect_b32 s82, 0, s82\n"
    "v_mfma_f32_16x16x32_bf16 v[120:123], a[240:243], a[232:235], v[120:123]\n"
    "v_add_u32 v240, s77, v240\n"
    "v_add_u32 v241, s77, v241\n"
    "v_mfma_f32_16x16x32_bf16 v[120:123], a[244:247], a[236:239], v[120:123]\n"
    "v_add_u32 v242, s77, v242\n"
    "s_waitcnt lgkmcnt(0)\n"
    "v_mfma_f32_16x16x32_bf16 v[28:31], a[248:251], a[208:211], v[28:31]\n"
    "v_mfma_f32_16x16x32_bf16 v[28:31], a[252:255], a[212:215], v[28:31]\n"
    "s_waitcnt vmcnt(15)\n"
    "s_barrier\n"
    "v_mfma_f32_16x16x32_bf16 v[60:63], a[248:251], a[216:219], v[60:63]\n"
    "ds_read_b64_tr_b16 a[240:241], v241 offset:16384\n"
    "v_mfma_f32_16x16x32_bf16 v[60:63], a[252:255], a[220:223], v[60:63]\n"
    "ds_read_b64_tr_b16 a[242:243], v241 offset:16640\n"
    "v_mfma_f32_16x16x32_bf16 v[92:95], a[248:251], a[224:227], v[92:95]\n"
    "ds_read_b64_tr_b16 a[244:245], v241 offset:18432\n"
    "v_mfma_f32_16x16x32_bf16 v[92:95], a[252:255], a[228:231], v[92:95]\n"
    "ds_read_b64_tr_b16 a[246:247], v241 offset:18688\n"
    "v_mfma_f32_16x16x32_bf16 v[124:127], a[248:251], a[232:235], v[124:127]\n"
    "v_mfma_f32_16x16x32_bf16 v[124:127], a[252:255], a[236:239], v[124:127]\n"
    "s_waitcnt lgkmcnt(0)\n"
    "v_mfma_f32_16x16x32_bf16 v[128:131], a[240:243], a[176:179], v[128:131]\n"
    "ds_read_b64_tr_b16 a[248:249], v241 offset:32768\n"
    "v_mfma_f32_16x16x32_bf16 v[128:131], a[244:247], a[180:183], v[128:131]\n"
    "ds_read_b64_tr_b16 a[250:251], v241 offset:33024\n"
    "v_mfma_f32_16x16x32_bf16 v[132:135], a[240:243], a[184:187], v[132:135]\n"
    "ds_read_b64_tr_b16 a[252:253], v241 offset:34816\n"
    "v_mfma_f32_16x16x32_bf16 v[132:135], a[244:247], a[188:191], v[132:135]\n"
    "ds_read_b64_tr_b16 a[254:255], v241 offset:35072\n"
    "v_mfma_f32_16x16x32_bf16 v[136:139], a[240:243], a[192:195], v[136:139]\n"
    "v_mfma_f32_16x16x32_bf16 v[136:139], a[244:247], a[196:199], v[136:139]\n"
    "v_mfma_f32_16x16x32_bf16 v[140:143], a[240:243], a[200:203], v[140:143]\n"
    "s_add_u32 s72, s81, 0\n"
    "buffer_load_dwordx4 a[208:211], v246, s[16:19], s72 offen sc1\n"
    "v_mfma_f32_16x16x32_bf16 v[140:143], a[244:247], a[204:207], v[140:143]\n"
    "s_waitcnt lgkmcnt(0)\n"
    "v_mfma_f32_16x16x32_bf16 a[0:3], a[248:251], a[176:179], a[0:3]\n"
    "ds_read_b64_tr_b16 a[240:241], v242 offset:16384\n"
    "v_mfma_f32_16x16x32_bf16 a[0:3], a[252:255], a[180:183], a[0:3]\n"
    "ds_read_b64_tr_b16 a[242:243], v242 offset:16640\n"
    "v_mfma_f32_16x16x32_bf16 a[4:7], a[248:251], a[184:187], a[4:7]\n"
    "ds_read_b64_tr_b16 a[244:245], v242 offset:18432\n"
    "v_mfma_f32_16x16x32_bf16 a[4:7], a[252:255], a[188:191], a[4:7]\n"
    "ds_read_b64_tr_b16 a[246:247], v242 offset:18688\n"
    "v_mfma_f32_16x16x32_bf16 a[8:11], a[248:251], a[192:195], a[8:11]\n"
    "v_mfma_f32_16x16x32_bf16 a[8:11], a[252:255], a[196:199], a[8:11]\n"
    "v_mfma_f32_16x16x32_bf16 a[12:15], a[248:251], a[200:203], a[12:15]\n"
    "buffer_load_dwordx4 a[212:215], v246, s[16:19], s72 offen offset:1024 sc1\n"
    "v_mfma_f32_16x16x32_bf16 a[12:15], a[252:255], a[204:207], a[12:15]\n"
    "s_waitcnt lgkmcnt(0)\n"
    "v_mfma_f32_16x16x32_bf16 v[144:147], a[240:243], a[176:179], v[144:147]\n"
    "ds_read_b64_tr_b16 a[248:249], v242 offset:32768\n"
    "v_mfma_f32_16x16x32_bf16 v[144:147], a[244:247], a[180:183], v[144:147]\n"
    "ds_read_b64_tr_b16 a[250:251], v242 offset:33024\n"
    "v_mfma_f32_16x16x32_bf16 v[148:151], a[240:243], a[184:187], v[148:151]\n"
    "ds_read_b64_tr_b16 a[252:253], v242 offset:34816\n"
    "v_mfma_f32_16x16x32_bf16 v[148:151], a[244:247], a[188:191], v[148:151]\n"
    "ds_read_b64_tr_b16 a[254:255], v242 offset:35072\n"
    "v_mfma_f32_16x16x32_bf16 v[152:155], a[240:243], a[192:195], v[152:155]\n"
    "v_mfma_f32_16x16x32_bf16 v[152:155], a[244:247], a[196:199], v[152:155]\n"
    "v_mfma_f32_16x16x32_bf16 v[156:159], a[240:243], a[200:203], v[156:159]\n"
    "s_add_u32 s72, s81, 18432\n"
    "buffer_load_dwordx4 a[216:219], v246, s[16:19], s72 offen sc1\n"
    "v_mfma_f32_16x16x32_bf16 v[156:159], a[244:247], a[204:207], v[156:159]\n"
    "s_waitcnt lgkmcnt(0)\n"
    "v_mfma_f32_16x16x32_bf16 a[16:19], a[248:251], a[176:179], a[16:19]\n"
    "ds_read_b64_tr_b16 a[240:241], v241 offset:20480\n"
    "v_mfma_f32_16x16x32_bf16 a[16:19], a[252:255], a[180:183], a[16:19]\n"
    "ds_read_b64_tr_b16 a[242:243], v241 offset:20736\n"
    "v_mfma_f32_16x16x32_bf16 a[20:23], a[248:251], a[184:187], a[20:23]\n"
    "ds_read_b64_tr_b16 a[244:245], v241 offset:22528\n"
    "v_mfma_f32_16x16x32_bf16 a[20:23], a[252:255], a[188:191], a[20:23]\n"
    "ds_read_b64_tr_b16 a[246:247], v241 offset:22784\n"
    "v_mfma_f32_16x16x32_bf16 a[24:27], a[248:251], a[192:195], a[24:27]\n"
    "v_mfma_f32_16x16x32_bf16 a[24:27], a[252:255], a[196:199], a[24:27]\n"
    "v_mfma_f32_16x16x32_bf16 a[28:31], a[248:251], a[200:203], a[28:31]\n"
    "buffer_load_dwordx4 a[220:223], v246, s[16:19], s72 offen offset:1024 sc1\n"
    "v_mfma_f32_16x16x32_bf16 a[28:31], a[252:255], a[204:207], a[28:31]\n"
    "s_waitcnt lgkmcnt(0)\n"
    "v_mfma_f32_16x16x32_bf16 v[160:163], a[240:243], a[176:179], v[160:163]\n"
    "ds_read_b64_tr_b16 a[248:249], v241 offset:36864\n"
    "v_mfma_f32_16x16x32_bf16 v[160:163], a[244:247], a[180:183], v[160:163]\n"
    "ds_read_b64_tr_b16 a[250:251], v241 offset:37120\n"
    "v_mfma_f32_16x16x32_bf16 v[164:167], a[240:243], a[184:187], v[164:167]\n"
    "ds_read_b64_tr_b16 a[252:253], v241 offset:38912\n"
    "v_mfma_f32_16x16x32_bf16 v[164:167], a[244:247], a[188:191], v[164:167]\n"
    "ds_read_b64_tr_b16 a[254:255], v241 offset:39168\n"
    "v_mfma_f32_16x16x32_bf16 v[168:171], a[240:243], a[192:195], v[168:171]\n"
    "v_mfma_f32_16x16x32_bf16 v[168:171], a[244:247], a[196:199], v[168:171]\n"
    "v_mfma_f32_16x16x32_bf16 v[172:175], a[240:243], a[200:203], v[172:175]\n"
    "s_add_u32 s72, s81, 36864\n"
    "buffer_load_dwordx4 a[224:227], v246, s[16:19], s72 offen sc1\n"
    "v_mfma_f32_16x16x32_bf16 v[172:175], a[244:247], a[204:207], v[172:175]\n"
    "s_waitcnt lgkmcnt(0)\n"
    "v_mfma_f32_16x16x32_bf16 a[32:35], a[248:251], a[176:179], a[32:35]\n"
    "ds_read_b64_tr_b16 a[240:241], v242 offset:20480\n"
    "v_mfma_f32_16x16x32_bf16 a[32:35], a[252:255], a[180:183], a[32:35]\n"
    "ds_read_b64_tr_b16 a[242:243], v242 offset:20736\n"
    "v_mfma_f32_16x16x32_bf16 a[36:39], a[248:251], a[184:187], a[36:39]\n"
    "ds_read_b64_tr_b16 a[244:245], v242 offset:22528\n"
    "v_mfma_f32_16x16x32_bf16 a[36:39], a[252:255], a[188:191], a[36:39]\n"
    "ds_read_b64_tr_b16 a[246:247], v242 offset:22784\n"
    "v_mfma_f32_16x16x32_bf16 a[40:43], a[248:251], a[192:195], a[40:43]\n"
    "v_mfma_f32_16x16x32_bf16 a[40:43], a[252:255], a[196:199], a[40:43]\n"
    "v_mfma_f32_16x16x32_bf16 a[44:47], a[248:251], a[200:203], a[44:47]\n"
    "buffer_load_dwordx4 a[228:231], v246, s[16:19], s72 offen offset:1024 sc1\n"
    "v_mfma_f32_16x16x32_bf16 a[44:47], a[252:255], a[204:207], a[44:47]\n"
    "s_waitcnt lgkmcnt(0)\n"
    "v_mfma_f32_16x16x32_bf16 v[176:179], a[240:243], a[176:179], v[176:179]\n"
    "ds_read_b64_tr_b16 a[248:249], v242 offset:36864\n"
    "v_mfma_f32_16x16x32_bf16 v[176:179], a[244:247], a[180:183], v[176:179]\n"
    "ds_read_b64_tr_b16 a[250:251], v242 offset:37120\n"
    "v_mfma_f32_16x16x32_bf16 v[180:183], a[240:243], a[184:187], v[180:183]\n"
    "ds_read_b64_tr_b16 a[252:253], v242 offset:38912\n"
    "v_mfma_f32_16x16x32_bf16 v[180:183], a[244:247], a[188:191], v[180:183]\n"
    "ds_read_b64_tr_b16 a[254:255], v242 offset:39168\n"
    "v_mfma_f32_16x16x32_bf16 v[184:187], a[240:243], a[192:195], v[184:187]\n"
    "v_mfma_f32_16x16x32_bf16 v[184:187], a[244:247], a[196:199], v[184:187]\n"
    "v_mfma_f32_16x16x32_bf16 v[188:191], a[240:243], a[200:203], v[188:191]\n"
    "s_add_u32 s72, s81, 55296\n"
    "buffer_load_dwordx4 a[232:235], v246, s[16:19], s72 offen sc1\n"
    "v_mfma_f32_16x16x32_bf16 v[188:191], a[244:247], a[204:207], v[188:191]\n"
    "s_waitcnt lgkmcnt(0)\n"
    "v_mfma_f32_16x16x32_bf16 a[48:51], a[248:251], a[176:179], a[48:51]\n"
    "ds_read_b64_tr_b16 a[240:241], v241 offset:24576\n"
    "v_mfma_f32_16x16x32_bf16 a[48:51], a[252:255], a[180:183], a[48:51]\n"
    "ds_read_b64_tr_b16 a[242:243], v241 offset:24832\n"
    "v_mfma_f32_16x16x32_bf16 a[52:55], a[248:251], a[184:187], a[52:55]\n"
    "ds_read_b64_tr_b16 a[244:245], v241 offset:26624\n"
    "v_mfma_f32_16x16x32_bf16 a[52:55], a[252:255], a[188:191], a[52:55]\n"
    "ds_read_b64_tr_b16 a[246:247], v241 offset:26880\n"
    "v_mfma_f32_16x16x32_bf16 a[56:59], a[248:251], a[192:195], a[56:59]\n"
    "v_mfma_f32_16x16x32_bf16 a[56:59], a[252:255], a[196:199], a[56:59]\n"
    "v_mfma_f32_16x16x32_bf16 a[60:63], a[248:251], a[200:203], a[60:63]\n"
    "buffer_load_dwordx4 a[236:239], v246, s[16:19], s72 offen offset:1024 sc1\n"
    "v_mfma_f32_16x16x32_bf16 a[60:63], a[252:255], a[204:207], a[60:63]\n"
    "s_waitcnt lgkmcnt(0)\n"
    "v_mfma_f32_16x16x32_bf16 v[192:195], a[240:243], a[176:179], v[192:195]\n"
    "ds_read_b64_tr_b16 a[248:249], v241 offset:40960\n"
    "v_mfma_f32_16x16x32_bf16 v[192:195], a[244:247], a[180:183], v[192:195]\n"
    "s_add_u32 s81, s81, 2048\n"
    "ds_read_b64_tr_b16 a[250:251], v241 offset:41216\n"
    "v_mfma_f32_16x16x32_bf16 v[196:199], a[240:243], a[184:187], v[196:199]\n"
    "ds_read_b64_tr_b16 a[252:253], v241 offset:43008\n"
    "v_mfma_f32_16x16x32_bf16 v[196:199], a[244:247], a[188:191], v[196:199]\n"
    "ds_read_b64_tr_b16 a[254:255], v241 offset:43264\n"
    "v_mfma_f32_16x16x32_bf16 v[200:203], a[240:243], a[192:195], v[200:203]\n"
    "v_mfma_f32_16x16x32_bf16 v[200:203], a[244:247], a[196:199], v[200:203]\n"
    "s_add_u32 m0, s76, 0\n"
    "v_mfma_f32_16x16x32_bf16 v[204:207], a[240:243], a[200:203], v[204:207]\n"
    "s_add_u32 s72, s78, 0\n"
    "buffer_load_dwordx4 v243, s[20:23], s72 offen lds\n"
    "v_mfma_f32_16x16x32_bf16 v[204:207], a[244:247], a[204:207], v[204:207]\n"
    "s_waitcnt lgkmcnt(0)\n"
    "v_mfma_f32_16x16x32_bf16 a[64:67], a[248:251], a[176:179], a[64:67]\n"
    "ds_read_b64_tr_b16 a[240:241], v242 offset:24576\n"
    "v_mfma_f32_16x16x32_bf16 a[64:67], a[252:255], a[180:183], a[64:67]\n"
    "ds_read_b64_tr_b16 a[242:243], v242 offset:24832\n"
    "v_mfma_f32_16x16x32_bf16 a[68:71], a[248:251], a[184:187], a[68:71]\n"
    "ds_read_b64_tr_b16 a[244:245], v242 offset:26624\n"
    "v_mfma_f32_16x16x32_bf16 a[68:71], a[252:255], a[188:191], a[68:71]\n"
    "ds_read_b64_tr_b16 a[246:247], v242 offset:26880\n"
    "v_mfma_f32_16x16x32_bf16 a[72:75], a[248:251], a[192:195], a[72:75]\n"
    "v_mfma_f32_16x16x32_bf16 a[72:75], a[252:255], a[196:199], a[72:75]\n"
    "s_add_u32 m0, s76, 4096\n"
    "v_mfma_f32_16x16x32_bf16 a[76:79], a[248:251], a[200:203], a[76:79]\n"
    "s_add_u32 s72, s78, 64\n"
    "buffer_load_dwordx4 v243, s[20:23], s72 offen lds\n"
    "v_mfma_f32_16x16x32_bf16 a[76:79], a[252:255], a[204:207], a[76:79]\n"
    "s_waitcnt lgkmcnt(0)\n"
    "v_mfma_f32_16x16x32_bf16 v[208:211], a[240:243], a[176:179], v[208:211]\n"
    "ds_read_b64_tr_b16 a[248:249], v242 offset:40960\n"
    "v_mfma_f32_16x16x32_bf16 v[208:211], a[244:247], a[180:183], v[208:211]\n"
    "ds_read_b64_tr_b16 a[250:251], v242 offset:41216\n"
    "v_mfma_f32_16x16x32_bf16 v[212:215], a[240:243], a[184:187], v[212:215]\n"
    "ds_read_b64_tr_b16 a[252:253], v242 offset:43008\n"
    "v_mfma_f32_16x16x32_bf16 v[212:215], a[244:247], a[188:191], v[212:215]\n"
    "ds_read_b64_tr_b16 a[254:255], v242 offset:43264\n"
    "v_mfma_f32_16x16x32_bf16 v[216:219], a[240:243], a[192:195], v[216:219]\n"
    "v_mfma_f32_16x16x32_bf16 v[216:219], a[244:247], a[196:199], v[216:219]\n"
    "s_add_u32 m0, s76, 8192\n"
    "v_mfma_f32_16x16x32_bf16 v[220:223], a[240:243], a[200:203], v[220:223]\n"
    "s_add_u32 s72, s78, 128\n"
    "buffer_load_dwordx4 v243, s[20:23], s72 offen lds\n"
    "v_mfma_f32_16x16x32_bf16 v[220:223], a[244:247], a[204:207], v[220:223]\n"
    "s_waitcnt lgkmcnt(0)\n"
    "v_mfma_f32_16x16x32_bf16 a[80:83], a[248:251], a[176:179], a[80:83]\n"
    "ds_read_b64_tr_b16 a[240:241], v241 offset:28672\n"
    "v_mfma_f32_16x16x32_bf16 a[80:83], a[252:255], a[180:183], a[80:83]\n"
    "ds_read_b64_tr_b16 a[242:243], v241 offset:28928\n"
    "v_mfma_f32_16x16x32_bf16 a[84:87], a[248:251], a[184:187], a[84:87]\n"
    "ds_read_b64_tr_b16 a[244:245], v241 offset:30720\n"
    "v_mfma_f32_16x16x32_bf16 a[84:87], a[252:255], a[188:191], a[84:87]\n"
    "ds_read_b64_tr_b16 a[246:247], v241 offset:30976\n"
    "v_mfma_f32_16x16x32_bf16 a[88:91], a[248:251], a[192:195], a[88:91]\n"
    "v_mfma_f32_16x16x32_bf16 a[88:91], a[252:255], a[196:199], a[88:91]\n"
    "s_add_u32 m0, s76, 12288\n"
    "v_mfma_f32_16x16x32_bf16 a[92:95], a[248:251], a[200:203], a[92:95]\n"
    "s_add_u32 s72, s78, 192\n"
    "buffer_load_dwordx4 v243, s[20:23], s72 offen lds\n"
    "v_mfma_f32_16x16x32_bf16 a[92:95], a[252:255], a[204:207], a[92:95]\n"
    "s_waitcnt lgkmcnt(0)\n"
    "v_mfma_f32_16x16x32_bf16 v[224:227], a[240:243], a[176:179], v[224:227]\n"
    "ds_read_b64_tr_b16 a[248:249], v241 offset:45056\n"
    "v_mfma_f32_16x16x32_bf16 v[224:227], a[244:247], a[180:183], v[224:227]\n"
    "ds_read_b64_tr_b16 a[250:251], v241 offset:45312\n"
    "v_mfma_f32_16x16x32_bf16 v[228:231], a[240:243], a[184:187], v[228:231]\n"
    "ds_read_b64_tr_b16 a[252:253], v241 offset:47104\n"
    "v_mfma_f32_16x16x32_bf16 v[228:231], a[244:247], a[188:191], v[228:231]\n"
    "ds_read_b64_tr_b16 a[254:255], v241 offset:47360\n"
    "v_mfma_f32_16x16x32_bf16 v[232:235], a[240:243], a[192:195], v[232:235]\n"
    "v_mfma_f32_16x16x32_bf16 v[232:235], a[244:247], a[196:199], v[232:235]\n"
    "s_add_u32 m0, s76, 16384\n"
    "v_mfma_f32_16x16x32_bf16 v[236:239], a[240:243], a[200:203], v[236:239]\n"
    "s_add_u32 s72, s79, 0\n"
    "buffer_load_dwordx4 v244, s[24:27], s72 offen lds\n"
    "v_mfma_f32_16x16x32_bf16 v[236:239], a[244:247], a[204:207], v[236:239]\n"
    "s_waitcnt lgkmcnt(0)\n"
    "v_mfma_f32_16x16x32_bf16 a[96:99], a[248:251], a[176:179], a[96:99]\n"
    "ds_read_b64_tr_b16 a[240:241], v242 offset:28672\n"
    "v_mfma_f32_16x16x32_bf16 a[96:99], a[252:255], a[180:183], a[96:99]\n"
    "ds_read_b64_tr_b16 a[242:243], v242 offset:28928\n"
    "v_mfma_f32_16x16x32_bf16 a[100:103], a[248:251], a[184:187], a[100:103]\n"
    "ds_read_b64_tr_b16 a[244:245], v242 offset:30720\n"
    "v_mfma_f32_16x16x32_bf16 a[100:103], a[252:255], a[188:191], a[100:103]\n"
    "ds_read_b64_tr_b16 a[246:247], v242 offset:30976\n"
    "v_mfma_f32_16x16x32_bf16 a[104:107], a[248:251], a[192:195], a[104:107]\n"
    "v_mfma_f32_16x16x32_bf16 a[104:107], a[252:255], a[196:199], a[104:107]\n"
    "s_add_u32 m0, s76, 20480\n"
    "v_mfma_f32_16x16x32_bf16 a[108:111], a[248:251], a[200:203], a[108:111]\n"
    "s_add_u32 s72, s79, 64\n"
    "buffer_load_dwordx4 v244, s[24:27], s72 offen lds\n"
    "v_mfma_f32_16x16x32_bf16 a[108:111], a[252:255], a[204:207], a[108:111]\n"
    "s_waitcnt lgkmcnt(0)\n"
    "v_mfma_f32_16x16x32_bf16 a[144:147], a[240:243], a[176:179], a[144:147]\n"
    "ds_read_b64_tr_b16 a[248:249], v242 offset:45056\n"
    "v_mfma_f32_16x16x32_bf16 a[144:147], a[244:247], a[180:183], a[144:147]\n"
    "ds_read_b64_tr_b16 a[250:251], v242 offset:45312\n"
    "v_mfma_f32_16x16x32_bf16 a[148:151], a[240:243], a[184:187], a[148:151]\n"
    "ds_read_b64_tr_b16 a[252:253], v242 offset:47104\n"
    "v_mfma_f32_16x16x32_bf16 a[148:151], a[244:247], a[188:191], a[148:151]\n"
    "ds_read_b64_tr_b16 a[254:255], v242 offset:47360\n"
    "v_mfma_f32_16x16x32_bf16 a[152:155], a[240:243], a[192:195], a[152:155]\n"
    "v_mfma_f32_16x16x32_bf16 a[152:155], a[244:247], a[196:199], a[152:155]\n"
    "s_add_u32 m0, s76, 24576\n"
    "v_mfma_f32_16x16x32_bf16 a[156:159], a[240:243], a[200:203], a[156:159]\n"
    "s_add_u32 s72, s79, 128\n"
    "buffer_load_dwordx4 v244, s[24:27], s72 offen lds\n"
    "v_mfma_f32_16x16x32_bf16 a[156:159], a[244:247], a[204:207], a[156:159]\n"
    "s_waitcnt lgkmcnt(0)\n"
    "v_mfma_f32_16x16x32_bf16 a[112:115], a[248:251], a[176:179], a[112:115]\n"
    "ds_read_b64_tr_b16 a[240:241], v241 offset:49152\n"
    "v_mfma_f32_16x16x32_bf16 a[112:115], a[252:255], a[180:183], a[112:115]\n"
    "ds_read_b64_tr_b16 a[242:243], v241 offset:49408\n"
    "v_mfma_f32_16x16x32_bf16 a[116:119], a[248:251], a[184:187], a[116:119]\n"
    "ds_read_b64_tr_b16 a[244:245], v241 offset:51200\n"
    "v_mfma_f32_16x16x32_bf16 a[116:119], a[252:255], a[188:191], a[116:119]\n"
    "ds_read_b64_tr_b16 a[246:247], v241 offset:51456\n"
    "v_mfma_f32_16x16x32_bf16 a[120:123], a[248:251], a[192:195], a[120:123]\n"
    "v_mfma_f32_16x16x32_bf16 a[120:123], a[252:255], a[196:199], a[120:123]\n"
    "s_add_u32 m0, s76, 28672\n"
    "v_mfma_f32_16x16x32_bf16 a[124:127], a[248:251], a[200:203], a[124:127]\n"
    "s_add_u32 s72, s79, 192\n"
    "buffer_load_dwordx4 v244, s[24:27], s72 offen lds\n"
    "v_mfma_f32_16x16x32_bf16 a[124:127], a[252:255], a[204:207], a[124:127]\n"
    "s_waitcnt lgkmcnt(0)\n"
    "v_mfma_f32_16x16x32_bf16 a[160:163], a[240:243], a[176:179], a[160:163]\n"
    "ds_read_b64_tr_b16 a[248:249], v242 offset:49152\n"
    "v_mfma_f32_16x16x32_bf16 a[160:163], a[244:247], a[180:183], a[160:163]\n"
    "ds_read_b64_tr_b16 a[250:251], v242 offset:49408\n"
    "v_mfma_f32_16x16x32_bf16 a[164:167], a[240:243], a[184:187], a[164:167]\n"
    "ds_read_b64_tr_b16 a[252:253], v242 offset:51200\n"
    "v_mfma_f32_16x16x32_bf16 a[164:167], a[244:247], a[188:191], a[164:167]\n"
    "ds_read_b64_tr_b16 a[254:255], v242 offset:51456\n"
    "v_mfma_f32_16x16x32_bf16 a[168:171], a[240:243], a[192:195], a[168:171]\n"
    "v_mfma_f32_16x16x32_bf16 a[168:171], a[244:247], a[196:199], a[168:171]\n"
    "s_add_u32 m0, s76, 49152\n"
    "v_mfma_f32_16x16x32_bf16 a[172:175], a[240:243], a[200:203], a[172:175]\n"
    "s_mov_b64 exec, s[12:13]\n"
    "buffer_load_dwordx4 v245, s[24:27], s80 offen lds\n"
    "s_mov_b64 exec, -1\n"
    "v_mfma_f32_16x16x32_bf16 a[172:175], a[244:247], a[204:207], a[172:175]\n"
    "s_waitcnt lgkmcnt(0)\n"
    "v_mfma_f32_16x16x32_bf16 a[128:131], a[248:251], a[176:179], a[128:131]\n"
    "ds_read_b64_tr_b16 a[240:241], v240 offset:0\n"
    "v_mfma_f32_16x16x32_bf16 a[128:131], a[252:255], a[180:183], a[128:131]\n"
    "ds_read_b64_tr_b16 a[242:243], v240 offset:256\n"
    "v_mfma_f32_16x16x32_bf16 a[132:135], a[248:251], a[184:187], a[132:135]\n"
    "ds_read_b64_tr_b16 a[244:245], v240 offset:2048\n"
    "v_mfma_f32_16x16x32_bf16 a[132:135], a[252:255], a[188:191], a[132:135]\n"
    "ds_read_b64_tr_b16 a[246:247], v240 offset:2304\n"
    "v_mfma_f32_16x16x32_bf16 a[136:139], a[248:251], a[192:195], a[136:139]\n"
    "v_mfma_f32_16x16x32_bf16 a[136:139], a[252:255], a[196:199], a[136:139]\n"
    "s_add_u32 m0, s76, 32768\n"
    "v_mfma_f32_16x16x32_bf16 a[140:143], a[248:251], a[200:203], a[140:143]\n"
    "s_add_u32 s72, s79, 0\n"
    "buffer_load_dwordx4 v244, s[28:31], s72 offen lds\n"
    "v_mfma_f32_16x16x32_bf16 a[140:143], a[252:255], a[204:207], a[140:143]\n"
    "s_waitcnt lgkmcnt(0)\n"
    "v_mfma_f32_16x16x32_bf16 v[0:3], a[240:243], a[176:179], v[0:3]\n"
    "ds_read_b64_tr_b16 a[248:249], v240 offset:8\n"
    "v_mfma_f32_16x16x32_bf16 v[0:3], a[244:247], a[180:183], v[0:3]\n"
    "ds_read_b64_tr_b16 a[250:251], v240 offset:264\n"
    "v_mfma_f32_16x16x32_bf16 v[32:35], a[240:243], a[184:187], v[32:35]\n"
    "ds_read_b64_tr_b16 a[252:253], v240 offset:2056\n"
    "v_mfma_f32_16x16x32_bf16 v[32:35], a[244:247], a[188:191], v[32:35]\n"
    "ds_read_b64_tr_b16 a[254:255], v240 offset:2312\n"
    "v_mfma_f32_16x16x32_bf16 v[64:67], a[240:243], a[192:195], v[64:67]\n"
    "v_mfma_f32_16x16x32_bf16 v[64:67], a[244:247], a[196:199], v[64:67]\n"
    "s_add_u32 m0, s76, 36864\n"
    "v_mfma_f32_16x16x32_bf16 v[96:99], a[240:243], a[200:203], v[96:99]\n"
    "s_add_u32 s72, s79, 64\n"
    "buffer_load_dwordx4 v244, s[28:31], s72 offen lds\n"
    "v_mfma_f32_16x16x32_bf16 v[96:99], a[244:247], a[204:207], v[96:99]\n"
    "s_waitcnt lgkmcnt(0)\n"
    "v_mfma_f32_16x16x32_bf16 v[4:7], a[248:251], a[176:179], v[4:7]\n"
    "ds_read_b64_tr_b16 a[240:241], v240 offset:4096\n"
    "v_mfma_f32_16x16x32_bf16 v[4:7], a[252:255], a[180:183], v[4:7]\n"
    "ds_read_b64_tr_b16 a[242:243], v240 offset:4352\n"
    "v_mfma_f32_16x16x32_bf16 v[36:39], a[248:251], a[184:187], v[36:39]\n"
    "ds_read_b64_tr_b16 a[244:245], v240 offset:6144\n"
    "v_mfma_f32_16x16x32_bf16 v[36:39], a[252:255], a[188:191], v[36:39]\n"
    "ds_read_b64_tr_b16 a[246:247], v240 offset:6400\n"
    "v_mfma_f32_16x16x32_bf16 v[68:71], a[248:251], a[192:195], v[68:71]\n"
    "v_mfma_f32_16x16x32_bf16 v[68:71], a[252:255], a[196:199], v[68:71]\n"
    "s_add_u32 m0, s76, 40960\n"
    "v_mfma_f32_16x16x32_bf16 v[100:103], a[248:251], a[200:203], v[100:103]\n"
    "s_add_u32 s72, s79, 128\n"
    "buffer_load_dwordx4 v244, s[28:31], s72 offen lds\n"
    "v_mfma_f32_16x16x32_bf16 v[100:103], a[252:255], a[204:207], v[100:103]\n"
    "s_waitcnt lgkmcnt(0)\n"
    "v_mfma_f32_16x16x32_bf16 v[8:11], a[240:243], a[176:179], v[8:11]\n"
    "ds_read_b64_tr_b16 a[248:249], v240 offset:4104\n"
    "v_mfma_f32_16x16x32_bf16 v[8:11], a[244:247], a[180:183], v[8:11]\n"
    "ds_read_b64_tr_b16 a[250:251], v240 offset:4360\n"
    "v_mfma_f32_16x16x32_bf16 v[40:43], a[240:243], a[184:187], v[40:43]\n"
    "ds_read_b64_tr_b16 a[252:253], v240 offset:6152\n"
    "v_mfma_f32_16x16x32_bf16 v[40:43], a[244:247], a[188:191], v[40:43]\n"
    "ds_read_b64_tr_b16 a[254:255], v240 offset:6408\n"
    "v_mfma_f32_16x16x32_bf16 v[72:75], a[240:243], a[192:195], v[72:75]\n"
    "v_mfma_f32_16x16x32_bf16 v[72:75], a[244:247], a[196:199], v[72:75]\n"
    "s_add_u32 m0, s76, 45056\n"
    "v_mfma_f32_16x16x32_bf16 v[104:107], a[240:243], a[200:203], v[104:107]\n"
    "s_add_u32 s72, s79, 192\n"
    "buffer_load_dwordx4 v244, s[28:31], s72 offen lds\n"
    "v_mfma_f32_16x16x32_bf16 v[104:107], a[244:247], a[204:207], v[104:107]\n"
    "s_waitcnt lgkmcnt(0)\n"
    "v_mfma_f32_16x16x32_bf16 v[12:15], a[248:251], a[176:179], v[12:15]\n"
    "ds_read_b64_tr_b16 a[240:241], v240 offset:8192\n"
    "v_mfma_f32_16x16x32_bf16 v[12:15], a[252:255], a[180:183], v[12:15]\n"
    "ds_read_b64_tr_b16 a[242:243], v240 offset:8448\n"
    "v_mfma_f32_16x16x32_bf16 v[44:47], a[248:251], a[184:187], v[44:47]\n"
    "ds_read_b64_tr_b16 a[244:245], v240 offset:10240\n"
    "v_mfma_f32_16x16x32_bf16 v[44:47], a[252:255], a[188:191], v[44:47]\n"
    "ds_read_b64_tr_b16 a[246:247], v240 offset:10496\n"
    "v_mfma_f32_16x16x32_bf16 v[76:79], a[248:251], a[192:195], v[76:79]\n"
    "v_mfma_f32_16x16x32_bf16 v[76:79], a[252:255], a[196:199], v[76:79]\n"
    "s_add_u32 m0, s76, 49152\n"
    "v_mfma_f32_16x16x32_bf16 v[108:111], a[248:251], a[200:203], v[108:111]\n"
    "s_mov_b64 exec, s[14:15]\n"
    "buffer_load_dwordx4 v245, s[28:31], s80 offen lds\n"
    "s_mov_b64 exec, -1\n"
    "v_mfma_f32_16x16x32_bf16 v[108:111], a[252:255], a[204:207], v[108:111]\n"
    "s_waitcnt lgkmcnt(0)\n"
    "v_mfma_f32_16x16x32_bf16 v[16:19], a[240:243], a[176:179], v[16:19]\n"
    "ds_read_b64_tr_b16 a[248:249], v240 offset:8200\n"
    "v_mfma_f32_16x16x32_bf16 v[16:19], a[244:247], a[180:183], v[16:19]\n"
    "ds_read_b64_tr_b16 a[250:251], v240 offset:8456\n"
    "v_mfma_f32_16x16x32_bf16 v[48:51], a[240:243], a[184:187], v[48:51]\n"
    "ds_read_b64_tr_b16 a[252:253], v240 offset:10248\n"
    "v_mfma_f32_16x16x32_bf16 v[48:51], a[244:247], a[188:191], v[48:51]\n"
    "s_mov_b32 m0, 160768\n"
    "ds_read_b64_tr_b16 a[254:255], v240 offset:10504\n"
    "v_mfma_f32_16x16x32_bf16 v[80:83], a[240:243], a[192:195], v[80:83]\n"
    "s_add_u32 s7, s7, s32\n"
    "s_mov_b64 exec, 0xffff\n"
    "buffer_load_dword v255, s[8:11], s7 offen lds\n"
    "s_mov_b64 exec, -1\n"
    "v_mfma_f32_16x16x32_bf16 v[80:83], a[244:247], a[196:199], v[80:83]\n"
    "v_mfma_f32_16x16x32_bf16 v[112:115], a[240:243], a[200:203], v[112:115]\n"
    "v_mfma_f32_16x16x32_bf16 v[112:115], a[244:247], a[204:207], v[112:115]\n"
    "s_add_u32 s78, s78, 524288\n"
    "s_waitcnt lgkmcnt(0)\n"
    "v_mfma_f32_16x16x32_bf16 v[20:23], a[248:251], a[176:179], v[20:23]\n"
    "s_add_u32 s79, s79, 589824\n"
    "ds_read_b64_tr_b16 a[240:241], v240 offset:12288\n"
    "v_mfma_f32_16x16x32_bf16 v[20:23], a[252:255], a[180:183], v[20:23]\n"
    "s_add_u32 s80, s80, 589824\n"
    "ds_read_b64_tr_b16 a[242:243], v240 offset:12544\n"
    "v_mfma_f32_16x16x32_bf16 v[52:55], a[248:251], a[184:187], v[52:55]\n"
    "s_add_u32 s76, s76, 53248\n"
    "ds_read_b64_tr_b16 a[244:245], v240 offset:14336\n"
    "v_mfma_f32_16x16x32_bf16 v[52:55], a[252:255], a[188:191], v[52:55]\n"
    "s_cmp_ge_u32 s76, 159744\n"
    "ds_read_b64_tr_b16 a[246:247], v240 offset:14592\n"
    "v_mfma_f32_16x16x32_bf16 v[84:87], a[248:251], a[192:195], v[84:87]\n"
    "s_cselect_b32 s73, 159744, 0\n"
    "v_mfma_f32_16x16x32_bf16 v[84:87], a[252:255], a[196:199], v[84:87]\n"
    "s_sub_u32 s76, s76, s73\n"
    "v_mfma_f32_16x16x32_bf16 v[116:119], a[248:251], a[200:203], v[116:119]\n"
    "v_mfma_f32_16x16x32_bf16 v[116:119], a[252:255], a[204:207], v[116:119]\n"
    "s_waitcnt lgkmcnt(0)\n"
    "v_mfma_f32_16x16x32_bf16 v[24:27], a[240:243], a[176:179], v[24:27]\n"
    "ds_read_b64_tr_b16 a[248:249], v240 offset:12296\n"
    "v_mfma_f32_16x16x32_bf16 v[24:27], a[244:247], a[180:183], v[24:27]\n"
    "ds_read_b64_tr_b16 a[250:251], v240 offset:12552\n"
    "v_mfma_f32_16x16x32_bf16 v[56:59], a[240:243], a[184:187], v[56:59]\n"
    "ds_read_b64_tr_b16 a[252:253], v240 offset:14344\n"
    "v_mfma_f32_16x16x32_bf16 v[56:59], a[244:247], a[188:191], v[56:59]\n"
    "s_add_u32 s82, s82, 53248\n"
    "ds_read_b64_tr_b16 a[254:255], v240 offset:14600\n"
    "v_mfma_f32_16x16x32_bf16 v[88:91], a[240:243], a[192:195], v[88:91]\n"
    "s_cmp_ge_u32 s82, 159744\n"
    "v_mfma_f32_16x16x32_bf16 v[88:91], a[244:247], a[196:199], v[88:91]\n"
    "s_cselect_b32 s77, 4294860800, s83\n"
    "s_cselect_b32 s82, 0, s82\n"
    "v_mfma_f32_16x16x32_bf16 v[120:123], a[240:243], a[200:203], v[120:123]\n"
    "v_add_u32 v240, s77, v240\n"
    "v_add_u32 v241, s77, v241\n"
    "v_mfma_f32_16x16x32_bf16 v[120:123], a[244:247], a[204:207], v[120:123]\n"
    "v_add_u32 v242, s77, v242\n"
    "s_waitcnt lgkmcnt(0)\n"
    "v_mfma_f32_16x16x32_bf16 v[28:31], a[248:251], a[176:179], v[28:31]\n"
    "v_mfma_f32_16x16x32_bf16 v[28:31], a[252:255], a[180:183], v[28:31]\n"
    "s_waitcnt vmcnt(15)\n"
    "s_barrier\n"
    "v_mfma_f32_16x16x32_bf16 v[60:63], a[248:251], a[184:187], v[60:63]\n"
    "ds_read_b64_tr_b16 a[240:241], v241 offset:16384\n"
    "v_mfma_f32_16x16x32_bf16 v[60:63], a[252:255], a[188:191], v[60:63]\n"
    "ds_read_b64_tr_b16 a[242:243], v241 offset:16640\n"
    "v_mfma_f32_16x16x32_bf16 v[92:95], a[248:251], a[192:195], v[92:95]\n"
    "ds_read_b64_tr_b16 a[244:245], v241 offset:18432\n"
    "v_mfma_f32_16x16x32_bf16 v[92:95], a[252:255], a[196:199], v[92:95]\n"
    "ds_read_b64_tr_b16 a[246:247], v241 offset:18688\n"
    "v_mfma_f32_16x16x32_bf16 v[124:127], a[248:251], a[200:203], v[124:127]\n"
    "v_mfma_f32_16x16x32_bf16 v[124:127], a[252:255], a[204:207], v[124:127]\n"
    "s_sub_u32 s71, s71, 1\n"
    "s_cmp_lg_u32 s71, 0\n"
    "s_cbranch_scc1 L_loop_p1\n"
    "s_waitcnt lgkmcnt(0)\n"
    "v_mfma_f32_16x16x32_bf16 v[128:131], a[240:243], a[208:211], v[128:131]\n"
    "ds_read_b64_tr_b16 a[248:249], v241 offset:32768\n"
    "v_mfma_f32_16x16x32_bf16 v[128:131], a[244:247], a[212:215], v[128:131]\n"
    "ds_read_b64_tr_b16 a[250:251], v241 offset:33024\n"
    "v_mfma_f32_16x16x32_bf16 v[132:135], a[240:243], a[216:219], v[132:135]\n"
    "ds_read_b64_tr_b16 a[252:253], v241 offset:34816\n"
    "v_mfma_f32_16x16x32_bf16 v[132:135], a[244:247], a[220:223], v[132:135]\n"
    "ds_read_b64_tr_b16 a[254:255], v241 offset:35072\n"
    "v_mfma_f32_16x16x32_bf16 v[136:139], a[240:243], a[224:227], v[136:139]\n"
    "v_mfma_f32_16x16x32_bf16 v[136:139], a[244:247], a[228:231], v[136:139]\n"
    "v_mfma_f32_16x16x32_bf16 v[140:143], a[240:243], a[232:235], v[140:143]\n"
    "s_add_u32 s72, s81, 0\n"
    "buffer_load_dwordx4 a[176:179], v246, s[16:19], s72 offen sc1\n"
    "v_mfma_f32_16x16x32_bf16 v[140:143], a[244:247], a[236:239], v[140:143]\n"
    "s_waitcnt lgkmcnt(0)\n"
    "v_mfma_f32_16x16x32_bf16 a[0:3], a[248:251], a[208:211], a[0:3]\n"
    "ds_read_b64_tr_b16 a[240:241], v242 offset:16384\n"
    "v_mfma_f32_16x16x32_bf16 a[0:3], a[252:255], a[212:215], a[0:3]\n"
    "ds_read_b64_tr_b16 a[242:243], v242 offset:16640\n"
    "v_mfma_f32_16x16x32_bf16 a[4:7], a[248:251], a[216:219], a[4:7]\n"
    "ds_read_b64_tr_b16 a[244:245], v242 offset:18432\n"
    "v_mfma_f32_16x16x32_bf16 a[4:7], a[252:255], a[220:223], a[4:7]\n"
    "ds_read_b64_tr_b16 a[246:247], v242 offset:18688\n"
    "v_mfma_f32_16x16x32_bf16 a[8:11], a[248:251], a[224:227], a[8:11]\n"
    "v_mfma_f32_16x16x32_bf16 a[8:11], a[252:255], a[228:231], a[8:11]\n"
    "v_mfma_f32_16x16x32_bf16 a[12:15], a[248:251], a[232:235], a[12:15]\n"
    "buffer_load_dwordx4 a[180:183], v246, s[16:19], s72 offen offset:1024 sc1\n"
    "v_mfma_f32_16x16x32_bf16 a[12:15], a[252:255], a[236:239], a[12:15]\n"
    "s_waitcnt lgkmcnt(0)\n"
    "v_mfma_f32_16x16x32_bf16 v[144:147], a[240:243], a[208:211], v[144:147]\n"
    "ds_read_b64_tr_b16 a[248:249], v242 offset:32768\n"
    "v_mfma_f32_16x16x32_bf16 v[144:147], a[244:247], a[212:215], v[144:147]\n"
    "ds_read_b64_tr_b16 a[250:251], v242 offset:33024\n"
    "v_mfma_f32_16x16x32_bf16 v[148:151], a[240:243], a[216:219], v[148:151]\n"
    "ds_read_b64_tr_b16 a[252:253], v242 offset:34816\n"
    "v_mfma_f32_16x16x32_bf16 v[148:151], a[244:247], a[220:223], v[148:151]\n"
    "ds_read_b64_tr_b16 a[254:255], v242 offset:35072\n"
    "v_mfma_f32_16x16x32_bf16 v[152:155], a[240:243], a[224:227], v[152:155]\n"
    "v_mfma_f32_16x16x32_bf16 v[152:155], a[244:247], a[228:231], v[152:155]\n"
    "v_mfma_f32_16x16x32_bf16 v[156:159], a[240:243], a[232:235], v[156:159]\n"
    "s_add_u32 s72, s81, 18432\n"
    "buffer_load_dwordx4 a[184:187], v246, s[16:19], s72 offen sc1\n"
    "v_mfma_f32_16x16x32_bf16 v[156:159], a[244:247], a[236:239], v[156:159]\n"
    "s_waitcnt lgkmcnt(0)\n"
    "v_mfma_f32_16x16x32_bf16 a[16:19], a[248:251], a[208:211], a[16:19]\n"
    "ds_read_b64_tr_b16 a[240:241], v241 offset:20480\n"
    "v_mfma_f32_16x16x32_bf16 a[16:19], a[252:255], a[212:215], a[16:19]\n"
    "ds_read_b64_tr_b16 a[242:243], v241 offset:20736\n"
    "v_mfma_f32_16x16x32_bf16 a[20:23], a[248:251], a[216:219], a[20:23]\n"
    "ds_read_b64_tr_b16 a[244:245], v241 offset:22528\n"
    "v_mfma_f32_16x16x32_bf16 a[20:23], a[252:255], a[220:223], a[20:23]\n"
    "ds_read_b64_tr_b16 a[246:247], v241 offset:22784\n"
    "v_mfma_f32_16x16x32_bf16 a[24:27], a[248:251], a[224:227], a[24:27]\n"
    "v_mfma_f32_16x16x32_bf16 a[24:27], a[252:255], a[228:231], a[24:27]\n"
    "v_mfma_f32_16x16x32_bf16 a[28:31], a[248:251], a[232:235], a[28:31]\n"
    "buffer_load_dwordx4 a[188:191], v246, s[16:19], s72 offen offset:1024 sc1\n"
    "v_mfma_f32_16x16x32_bf16 a[28:31], a[252:255], a[236:239], a[28:31]\n"
    "s_waitcnt lgkmcnt(0)\n"
    "v_mfma_f32_16x16x32_bf16 v[160:163], a[240:243], a[208:211], v[160:163]\n"
    "ds_read_b64_tr_b16 a[248:249], v241 offset:36864\n"
    "v_mfma_f32_16x16x32_bf16 v[160:163], a[244:247], a[212:215], v[160:163]\n"
    "ds_read_b64_tr_b16 a[250:251], v241 offset:37120\n"
    "v_mfma_f32_16x16x32_bf16 v[164:167], a[240:243], a[216:219], v[164:167]\n"
    "ds_read_b64_tr_b16 a[252:253], v241 offset:38912\n"
    "v_mfma_f32_16x16x32_bf16 v[164:167], a[244:247], a[220:223], v[164:167]\n"
    "ds_read_b64_tr_b16 a[254:255], v241 offset:39168\n"
    "v_mfma_f32_16x16x32_bf16 v[168:171], a[240:243], a[224:227], v[168:171]\n"
    "v_mfma_f32_16x16x32_bf16 v[168:171], a[244:247], a[228:231], v[168:171]\n"
    "v_mfma_f32_16x16x32_bf16 v[172:175], a[240:243], a[232:235], v[172:175]\n"
    "s_add_u32 s72, s81, 36864\n"
    "buffer_load_dwordx4 a[192:195], v246, s[16:19], s72 offen sc1\n"
    "v_mfma_f32_16x16x32_bf16 v[172:175], a[244:247], a[236:239], v[172:175]\n"
    "s_waitcnt lgkmcnt(0)\n"
    "v_mfma_f32_16x16x32_bf16 a[32:35], a[248:251], a[208:211], a[32:35]\n"
    "ds_read_b64_tr_b16 a[240:241], v242 offset:20480\n"
    "v_mfma_f32_16x16x32_bf16 a[32:35], a[252:255], a[212:215], a[32:35]\n"
    "ds_read_b64_tr_b16 a[242:243], v242 offset:20736\n"
    "v_mfma_f32_16x16x32_bf16 a[36:39], a[248:251], a[216:219], a[36:39]\n"
    "ds_read_b64_tr_b16 a[244:245], v242 offset:22528\n"
    "v_mfma_f32_16x16x32_bf16 a[36:39], a[252:255], a[220:223], a[36:39]\n"
    "ds_read_b64_tr_b16 a[246:247], v242 offset:22784\n"
    "v_mfma_f32_16x16x32_bf16 a[40:43], a[248:251], a[224:227], a[40:43]\n"
    "v_mfma_f32_16x16x32_bf16 a[40:43], a[252:255], a[228:231], a[40:43]\n"
    "v_mfma_f32_16x16x32_bf16 a[44:47], a[248:251], a[232:235], a[44:47]\n"
    "buffer_load_dwordx4 a[196:199], v246, s[16:19], s72 offen offset:1024 sc1\n"
    "v_mfma_f32_16x16x32_bf16 a[44:47], a[252:255], a[236:239], a[44:47]\n"
    "s_waitcnt lgkmcnt(0)\n"
    "v_mfma_f32_16x16x32_bf16 v[176:179], a[240:243], a[208:211], v[176:179]\n"
    "ds_read_b64_tr_b16 a[248:249], v242 offset:36864\n"
    "v_mfma_f32_16x16x32_bf16 v[176:179], a[244:247], a[212:215], v[176:179]\n"
    "ds_read_b64_tr_b16 a[250:251], v242 offset:37120\n"
    "v_mfma_f32_16x16x32_bf16 v[180:183], a[240:243], a[216:219], v[180:183]\n"
    "ds_read_b64_tr_b16 a[252:253], v242 offset:38912\n"
    "v_mfma_f32_16x16x32_bf16 v[180:183], a[244:247], a[220:223], v[180:183]\n"
    "ds_read_b64_tr_b16 a[254:255], v242 offset:39168\n"
    "v_mfma_f32_16x16x32_bf16 v[184:187], a[240:243], a[224:227], v[184:187]\n"
    "v_mfma_f32_16x16x32_bf16 v[184:187], a[244:247], a[228:231], v[184:187]\n"
    "v_mfma_f32_16x16x32_bf16 v[188:191], a[240:243], a[232:235], v[188:191]\n"
    "s_add_u32 s72, s81, 55296\n"
    "buffer_load_dwordx4 a[200:203], v246, s[16:19], s72 offen sc1\n"
    "v_mfma_f32_16x16x32_bf16 v[188:191], a[244:247], a[236:239], v[188:191]\n"
    "s_waitcnt lgkmcnt(0)\n"
    "v_mfma_f32_16x16x32_bf16 a[48:51], a[248:251], a[208:211], a[48:51]\n"
    "ds_read_b64_tr_b16 a[240:241], v241 offset:24576\n"
    "v_mfma_f32_16x16x32_bf16 a[48:51], a[252:255], a[212:215], a[48:51]\n"
    "ds_read_b64_tr_b16 a[242:243], v241 offset:24832\n"
    "v_mfma_f32_16x16x32_bf16 a[52:55], a[248:251], a[216:219], a[52:55]\n"
    "ds_read_b64_tr_b16 a[244:245], v241 offset:26624\n"
    "v_mfma_f32_16x16x32_bf16 a[52:55], a[252:255], a[220:223], a[52:55]\n"
    "ds_read_b64_tr_b16 a[246:247], v241 offset:26880\n"
    "v_mfma_f32_16x16x32_bf16 a[56:59], a[248:251], a[224:227], a[56:59]\n"
    "v_mfma_f32_16x16x32_bf16 a[56:59], a[252:255], a[228:231], a[56:59]\n"
    "v_mfma_f32_16x16x32_bf16 a[60:63], a[248:251], a[232:235], a[60:63]\n"
    "buffer_load_dwordx4 a[204:207], v246, s[16:19], s72 offen offset:1024 sc1\n"
    "v_mfma_f32_16x16x32_bf16 a[60:63], a[252:255], a[236:239], a[60:63]\n"
    "s_waitcnt lgkmcnt(0)\n"
    "v_mfma_f32_16x16x32_bf16 v[192:195], a[240:243], a[208:211], v[192:195]\n"
    "ds_read_b64_tr_b16 a[248:249], v241 offset:40960\n"
    "v_mfma_f32_16x16x32_bf16 v[192:195], a[244:247], a[212:215], v[192:195]\n"
    "s_add_u32 s81, s81, 2048\n"
    "ds_read_b64_tr_b16 a[250:251], v241 offset:41216\n"
    "v_mfma_f32_16x16x32_bf16 v[196:199], a[240:243], a[216:219], v[196:199]\n"
    "ds_read_b64_tr_b16 a[252:253], v241 offset:43008\n"
    "v_mfma_f32_16x16x32_bf16 v[196:199], a[244:247], a[220:223], v[196:199]\n"
    "ds_read_b64_tr_b16 a[254:255], v241 offset:43264\n"
    "v_mfma_f32_16x16x32_bf16 v[200:203], a[240:243], a[224:227], v[200:203]\n"
    "v_mfma_f32_16x16x32_bf16 v[200:203], a[244:247], a[228:231], v[200:203]\n"
    "s_add_u32 m0, s76, 0\n"
    "v_mfma_f32_16x16x32_bf16 v[204:207], a[240:243], a[232:235], v[204:207]\n"
    "s_add_u32 s72, s78, 0\n"
    "buffer_load_dwordx4 v243, s[20:23], s72 offen lds\n"
    "v_mfma_f32_16x16x32_bf16 v[204:207], a[244:247], a[236:239], v[204:207]\n"
    "s_waitcnt lgkmcnt(0)\n"
    "v_mfma_f32_16x16x32_bf16 a[64:67], a[248:251], a[208:211], a[64:67]\n"
    "ds_read_b64_tr_b16 a[240:241], v242 offset:24576\n"
    "v_mfma_f32_16x16x32_bf16 a[64:67], a[252:255], a[212:215], a[64:67]\n"
    "ds_read_b64_tr_b16 a[242:243], v242 offset:24832\n"
    "v_mfma_f32_16x16x32_bf16 a[68:71], a[248:251], a[216:219], a[68:71]\n"
    "ds_read_b64_tr_b16 a[244:245], v242 offset:26624\n"
    "v_mfma_f32_16x16x32_bf16 a[68:71], a[252:255], a[220:223], a[68:71]\n"
    "ds_read_b64_tr_b16 a[246:247], v242 offset:26880\n"
    "v_mfma_f32_16x16x32_bf16 a[72:75], a[248:251], a[224:227], a[72:75]\n"
    "v_mfma_f32_16x16x32_bf16 a[72:75], a[252:255], a[228:231], a[72:75]\n"
    "s_add_u32 m0, s76, 4096\n"
    "v_mfma_f32_16x16x32_bf16 a[76:79], a[248:251], a[232:235], a[76:79]\n"
    "s_add_u32 s72, s78, 64\n"
    "buffer_load_dwordx4 v243, s[20:23], s72 offen lds\n"
    "v_mfma_f32_16x16x32_bf16 a[76:79], a[252:255], a[236:239], a[76:79]\n"
    "s_waitcnt lgkmcnt(0)\n"
    "v_mfma_f32_16x16x32_bf16 v[208:211], a[240:243], a[208:211], v[208:211]\n"
    "ds_read_b64_tr_b16 a[248:249], v242 offset:40960\n"
    "v_mfma_f32_16x16x32_bf16 v[208:211], a[244:247], a[212:215], v[208:211]\n"
    "ds_read_b64_tr_b16 a[250:251], v242 offset:41216\n"
    "v_mfma_f32_16x16x32_bf16 v[212:215], a[240:243], a[216:219], v[212:215]\n"
    "ds_read_b64_tr_b16 a[252:253], v242 offset:43008\n"
    "v_mfma_f32_16x16x32_bf16 v[212:215], a[244:247], a[220:223], v[212:215]\n"
    "ds_read_b64_tr_b16 a[254:255], v242 offset:43264\n"
    "v_mfma_f32_16x16x32_bf16 v[216:219], a[240:243], a[224:227], v[216:219]\n"
    "v_mfma_f32_16x16x32_bf16 v[216:219], a[244:247], a[228:231], v[216:219]\n"
    "s_add_u32 m0, s76, 8192\n"
    "v_mfma_f32_16x16x32_bf16 v[220:223], a[240:243], a[232:235], v[220:223]\n"
    "s_add_u32 s72, s78, 128\n"
    "buffer_load_dwordx4 v243, s[20:23], s72 offen lds\n"
    "v_mfma_f32_16x16x32_bf16 v[220:223], a[244:247], a[236:239], v[220:223]\n"
    "s_waitcnt lgkmcnt(0)\n"
    "v_mfma_f32_16x16x32_bf16 a[80:83], a[248:251], a[208:211], a[80:83]\n"
    "ds_read_b64_tr_b16 a[240:241], v241 offset:28672\n"
    "v_mfma_f32_16x16x32_bf16 a[80:83], a[252:255], a[212:215], a[80:83]\n"
    "ds_read_b64_tr_b16 a[242:243], v241 offset:28928\n"
    "v_mfma_f32_16x16x32_bf16 a[84:87], a[248:251], a[216:219], a[84:87]\n"
    "ds_read_b64_tr_b16 a[244:245], v241 offset:30720\n"
    "v_mfma_f32_16x16x32_bf16 a[84:87], a[252:255], a[220:223], a[84:87]\n"
    "ds_read_b64_tr_b16 a[246:247], v241 offset:30976\n"
    "v_mfma_f32_16x16x32_bf16 a[88:91], a[248:251], a[224:227], a[88:91]\n"
    "v_mfma_f32_16x16x32_bf16 a[88:91], a[252:255], a[228:231], a[88:91]\n"
    "s_add_u32 m0, s76, 12288\n"
    "v_mfma_f32_16x16x32_bf16 a[92:95], a[248:251], a[232:235], a[92:95]\n"
    "s_add_u32 s72, s78, 192\n"
    "buffer_load_dwordx4 v243, s[20:23], s72 offen lds\n"
    "v_mfma_f32_16x16x32_bf16 a[92:95], a[252:255], a[236:239], a[92:95]\n"
    "s_waitcnt lgkmcnt(0)\n"
    "v_mfma_f32_16x16x32_bf16 v[224:227], a[240:243], a[208:211], v[224:227]\n"
    "ds_read_b64_tr_b16 a[248:249], v241 offset:45056\n"
    "v_mfma_f32_16x16x32_bf16 v[224:227], a[244:247], a[212:215], v[224:227]\n"
    "ds_read_b64_tr_b16 a[250:251], v241 offset:45312\n"
    "v_mfma_f32_16x16x32_bf16 v[228:231], a[240:243], a[216:219], v[228:231]\n"
    "ds_read_b64_tr_b16 a[252:253], v241 offset:47104\n"
    "v_mfma_f32_16x16x32_bf16 v[228:231], a[244:247], a[220:223], v[228:231]\n"
    "ds_read_b64_tr_b16 a[254:255], v241 offset:47360\n"
    "v_mfma_f32_16x16x32_bf16 v[232:235], a[240:243], a[224:227], v[232:235]\n"
    "v_mfma_f32_16x16x32_bf16 v[232:235], a[244:247], a[228:231], v[232:235]\n"
    "s_add_u32 m0, s76, 16384\n"
    "v_mfma_f32_16x16x32_bf16 v[236:239], a[240:243], a[232:235], v[236:239]\n"
    "s_add_u32 s72, s79, 0\n"
    "buffer_load_dwordx4 v244, s[24:27], s72 offen lds\n"
    "v_mfma_f32_16x16x32_bf16 v[236:239], a[244:247], a[236:239], v[236:239]\n"
    "s_waitcnt lgkmcnt(0)\n"
    "v_mfma_f32_16x16x32_bf16 a[96:99], a[248:251], a[208:211], a[96:99]\n"
    "ds_read_b64_tr_b16 a[240:241], v242 offset:28672\n"
    "v_mfma_f32_16x16x32_bf16 a[96:99], a[252:255], a[212:215], a[96:99]\n"
    "ds_read_b64_tr_b16 a[242:243], v242 offset:28928\n"
    "v_mfma_f32_16x16x32_bf16 a[100:103], a[248:251], a[216:219], a[100:103]\n"
    "ds_read_b64_tr_b16 a[244:245], v242 offset:30720\n"
    "v_mfma_f32_16x16x32_bf16 a[100:103], a[252:255], a[220:223], a[100:103]\n"
    "ds_read_b64_tr_b16 a[246:247], v242 offset:30976\n"
    "v_mfma_f32_16x16x32_bf16 a[104:107], a[248:251], a[224:227], a[104:107]\n"
    "v_mfma_f32_16x16x32_bf16 a[104:107], a[252:255], a[228:231], a[104:107]\n"
    "s_add_u32 m0, s76, 20480\n"
    "v_mfma_f32_16x16x32_bf16 a[108:111], a[248:251], a[232:235], a[108:111]\n"
    "s_add_u32 s72, s79, 64\n"
    "buffer_load_dwordx4 v244, s[24:27], s72 offen lds\n"
    "v_mfma_f32_16x16x32_bf16 a[108:111], a[252:255], a[236:239], a[108:111]\n"
    "s_waitcnt lgkmcnt(0)\n"
    "v_mfma_f32_16x16x32_bf16 a[144:147], a[240:243], a[208:211], a[144:147]\n"
    "ds_read_b64_tr_b16 a[248:249], v242 offset:45056\n"
    "v_mfma_f32_16x16x32_bf16 a[144:147], a[244:247], a[212:215], a[144:147]\n"
    "ds_read_b64_tr_b16 a[250:251], v242 offset:45312\n"
    "v_mfma_f32_16x16x32_bf16 a[148:151], a[240:243], a[216:219], a[148:151]\n"
    "ds_read_b64_tr_b16 a[252:253], v242 offset:47104\n"
    "v_mfma_f32_16x16x32_bf16 a[148:151], a[244:247], a[220:223], a[148:151]\n"
    "ds_read_b64_tr_b16 a[254:255], v242 offset:47360\n"
    "v_mfma_f32_16x16x32_bf16 a[152:155], a[240:243], a[224:227], a[152:155]\n"
    "v_mfma_f32_16x16x32_bf16 a[152:155], a[244:247], a[228:231], a[152:155]\n"
    "s_add_u32 m0, s76, 24576\n"
    "v_mfma_f32_16x16x32_bf16 a[156:159], a[240:243], a[232:235], a[156:159]\n"
    "s_add_u32 s72, s79, 128\n"
    "buffer_load_dwordx4 v244, s[24:27], s72 offen lds\n"
    "v_mfma_f32_16x16x32_bf16 a[156:159], a[244:247], a[236:239], a[156:159]\n"
    "s_waitcnt lgkmcnt(0)\n"
    "v_mfma_f32_16x16x32_bf16 a[112:115], a[248:251], a[208:211], a[112:115]\n"
    "ds_read_b64_tr_b16 a[240:241], v241 offset:49152\n"
    "v_mfma_f32_16x16x32_bf16 a[112:115], a[252:255], a[212:215], a[112:115]\n"
    "ds_read_b64_tr_b16 a[242:243], v241 offset:49408\n"
    "v_mfma_f32_16x16x32_bf16 a[116:119], a[248:251], a[216:219], a[116:119]\n"
    "ds_read_b64_tr_b16 a[244:245], v241 offset:51200\n"
    "v_mfma_f32_16x16x32_bf16 a[116:119], a[252:255], a[220:223], a[116:119]\n"
    "ds_read_b64_tr_b16 a[246:247], v241 offset:51456\n"
    "v_mfma_f32_16x16x32_bf16 a[120:123], a[248:251], a[224:227], a[120:123]\n"
    "v_mfma_f32_16x16x32_bf16 a[120:123], a[252:255], a[228:231], a[120:123]\n"
    "s_add_u32 m0, s76, 28672\n"
    "v_mfma_f32_16x16x32_bf16 a[124:127], a[248:251], a[232:235], a[124:127]\n"
    "s_add_u32 s72, s79, 192\n"
    "buffer_load_dwordx4 v244, s[24:27], s72 offen lds\n"
    "v_mfma_f32_16x16x32_bf16 a[124:127], a[252:255], a[236:239], a[124:127]\n"
    "s_waitcnt lgkmcnt(0)\n"
    "v_mfma_f32_16x16x32_bf16 a[160:163], a[240:243], a[208:211], a[160:163]\n"
    "ds_read_b64_tr_b16 a[248:249], v242 offset:49152\n"
    "v_mfma_f32_16x16x32_bf16 a[160:163], a[244:247], a[212:215], a[160:163]\n"
    "ds_read_b64_tr_b16 a[250:251], v242 offset:49408\n"
    "v_mfma_f32_16x16x32_bf16 a[164:167], a[240:243], a[216:219], a[164:167]\n"
    "ds_read_b64_tr_b16 a[252:253], v242 offset:51200\n"
    "v_mfma_f32_16x16x32_bf16 a[164:167], a[244:247], a[220:223], a[164:167]\n"
    "ds_read_b64_tr_b16 a[254:255], v242 offset:51456\n"
    "v_mfma_f32_16x16x32_bf16 a[168:171], a[240:243], a[224:227], a[168:171]\n"
    "v_mfma_f32_16x16x32_bf16 a[168:171], a[244:247], a[228:231], a[168:171]\n"
    "s_add_u32 m0, s76, 49152\n"
    "v_mfma_f32_16x16x32_bf16 a[172:175], a[240:243], a[232:235], a[172:175]\n"
    "s_mov_b64 exec, s[12:13]\n"
    "buffer_load_dwordx4 v245, s[24:27], s80 offen lds\n"
    "s_mov_b64 exec, -1\n"
    "v_mfma_f32_16x16x32_bf16 a[172:175], a[244:247], a[236:239], a[172:175]\n"
    "s_waitcnt lgkmcnt(0)\n"
    "v_mfma_f32_16x16x32_bf16 a[128:131], a[248:251], a[208:211], a[128:131]\n"
    "ds_read_b64_tr_b16 a[240:241], v240 offset:0\n"
    "v_mfma_f32_16x16x32_bf16 a[128:131], a[252:255], a[212:215], a[128:131]\n"
    "ds_read_b64_tr_b16 a[242:243], v240 offset:256\n"
    "v_mfma_f32_16x16x32_bf16 a[132:135], a[248:251], a[216:219], a[132:135]\n"
    "ds_read_b64_tr_b16 a[244:245], v240 offset:2048\n"
    "v_mfma_f32_16x16x32_bf16 a[132:135], a[252:255], a[220:223], a[132:135]\n"
    "ds_read_b64_tr_b16 a[246:247], v240 offset:2304\n"
    "v_mfma_f32_16x16x32_bf16 a[136:139], a[248:251], a[224:227], a[136:139]\n"
    "v_mfma_f32_16x16x32_bf16 a[136:139], a[252:255], a[228:231], a[136:139]\n"
    "s_add_u32 m0, s76, 32768\n"
    "v_mfma_f32_16x16x32_bf16 a[140:143], a[248:251], a[232:235], a[140:143]\n"
    "s_add_u32 s72, s79, 0\n"
    "buffer_load_dwordx4 v244, s[28:31], s72 offen lds\n"
    "v_mfma_f32_16x16x32_bf16 a[140:143], a[252:255], a[236:239], a[140:143]\n"
    "s_waitcnt lgkmcnt(0)\n"
    "v_mfma_f32_16x16x32_bf16 v[0:3], a[240:243], a[208:211], v[0:3]\n"
    "ds_read_b64_tr_b16 a[248:249], v240 offset:8\n"
    "v_mfma_f32_16x16x32_bf16 v[0:3], a[244:247], a[212:215], v[0:3]\n"
    "ds_read_b64_tr_b16 a[250:251], v240 offset:264\n"
    "v_mfma_f32_16x16x32_bf16 v[32:35], a[240:243], a[216:219], v[32:35]\n"
    "ds_read_b64_tr_b16 a[252:253], v240 offset:2056\n"
    "v_mfma_f32_16x16x32_bf16 v[32:35], a[244:247], a[220:223], v[32:35]\n"
    "ds_read_b64_tr_b16 a[254:255], v240 offset:2312\n"
    "v_mfma_f32_16x16x32_bf16 v[64:67], a[240:243], a[224:227], v[64:67]\n"
    "v_mfma_f32_16x16x32_bf16 v[64:67], a[244:247], a[228:231], v[64:67]\n"
    "s_add_u32 m0, s76, 36864\n"
    "v_mfma_f32_16x16x32_bf16 v[96:99], a[240:243], a[232:235], v[96:99]\n"
    "s_add_u32 s72, s79, 64\n"
    "buffer_load_dwordx4 v244, s[28:31], s72 offen lds\n"
    "v_mfma_f32_16x16x32_bf16 v[96:99], a[244:247], a[236:239], v[96:99]\n"
    "s_waitcnt lgkmcnt(0)\n"
    "v_mfma_f32_16x16x32_bf16 v[4:7], a[248:251], a[208:211], v[4:7]\n"
    "ds_read_b64_tr_b16 a[240:241], v240 offset:4096\n"
    "v_mfma_f32_16x16x32_bf16 v[4:7], a[252:255], a[212:215], v[4:7]\n"
    "ds_read_b64_tr_b16 a[242:243], v240 offset:4352\n"
    "v_mfma_f32_16x16x32_bf16 v[36:39], a[248:251], a[216:219], v[36:39]\n"
    "ds_read_b64_tr_b16 a[244:245], v240 offset:6144\n"
    "v_mfma_f32_16x16x32_bf16 v[36:39], a[252:255], a[220:223], v[36:39]\n"
    "ds_read_b64_tr_b16 a[246:247], v240 offset:6400\n"
    "v_mfma_f32_16x16x32_bf16 v[68:71], a[248:251], a[224:227], v[68:71]\n"
    "v_mfma_f32_16x16x32_bf16 v[68:71], a[252:255], a[228:231], v[68:71]\n"
    "s_add_u32 m0, s76, 40960\n"
    "v_mfma_f32_16x16x32_bf16 v[100:103], a[248:251], a[232:235], v[100:103]\n"
    "s_add_u32 s72, s79, 128\n"
    "buffer_load_dwordx4 v244, s[28:31], s72 offen lds\n"
    "v_mfma_f32_16x16x32_bf16 v[100:103], a[252:255], a[236:239], v[100:103]\n"
    "s_waitcnt lgkmcnt(0)\n"
    "v_mfma_f32_16x16x32_bf16 v[8:11], a[240:243], a[208:211], v[8:11]\n"
    "ds_read_b64_tr_b16 a[248:249], v240 offset:4104\n"
    "v_mfma_f32_16x16x32_bf16 v[8:11], a[244:247], a[212:215], v[8:11]\n"
    "ds_read_b64_tr_b16 a[250:251], v240 offset:4360\n"
    "v_mfma_f32_16x16x32_bf16 v[40:43], a[240:243], a[216:219], v[40:43]\n"
    "ds_read_b64_tr_b16 a[252:253], v240 offset:6152\n"
    "v_mfma_f32_16x16x32_bf16 v[40:43], a[244:247], a[220:223], v[40:43]\n"
    "ds_read_b64_tr_b16 a[254:255], v240 offset:6408\n"
    "v_mfma_f32_16x16x32_bf16 v[72:75], a[240:243], a[224:227], v[72:75]\n"
    "v_mfma_f32_16x16x32_bf16 v[72:75], a[244:247], a[228:231], v[72:75]\n"
    "s_add_u32 m0, s76, 45056\n"
    "v_mfma_f32_16x16x32_bf16 v[104:107], a[240:243], a[232:235], v[104:107]\n"
    "s_add_u32 s72, s79, 192\n"
    "buffer_load_dwordx4 v244, s[28:31], s72 offen lds\n"
    "v_mfma_f32_16x16x32_bf16 v[104:107], a[244:247], a[236:239], v[104:107]\n"
    "s_waitcnt lgkmcnt(0)\n"
    "v_mfma_f32_16x16x32_bf16 v[12:15], a[248:251], a[208:211], v[12:15]\n"
    "ds_read_b64_tr_b16 a[240:241], v240 offset:8192\n"
    "v_mfma_f32_16x16x32_bf16 v[12:15], a[252:255], a[212:215], v[12:15]\n"
    "ds_read_b64_tr_b16 a[242:243], v240 offset:8448\n"
    "v_mfma_f32_16x16x32_bf16 v[44:47], a[248:251], a[216:219], v[44:47]\n"
    "ds_read_b64_tr_b16 a[244:245], v240 offset:10240\n"
    "v_mfma_f32_16x16x32_bf16 v[44:47], a[252:255], a[220:223], v[44:47]\n"
    "ds_read_b64_tr_b16 a[246:247], v240 offset:10496\n"
    "v_mfma_f32_16x16x32_bf16 v[76:79], a[248:251], a[224:227], v[76:79]\n"
    "v_mfma_f32_16x16x32_bf16 v[76:79], a[252:255], a[228:231], v[76:79]\n"
    "s_add_u32 m0, s76, 49152\n"
    "v_mfma_f32_16x16x32_bf16 v[108:111], a[248:251], a[232:235], v[108:111]\n"
    "s_mov_b64 exec, s[14:15]\n"
    "buffer_load_dwordx4 v245, s[28:31], s80 offen lds\n"
    "s_mov_b64 exec, -1\n"
    "v_mfma_f32_16x16x32_bf16 v[108:111], a[252:255], a[236:239], v[108:111]\n"
    "s_waitcnt lgkmcnt(0)\n"
    "v_mfma_f32_16x16x32_bf16 v[16:19], a[240:243], a[208:211], v[16:19]\n"
    "ds_read_b64_tr_b16 a[248:249], v240 offset:8200\n"
    "v_mfma_f32_16x16x32_bf16 v[16:19], a[244:247], a[212:215], v[16:19]\n"
    "ds_read_b64_tr_b16 a[250:251], v240 offset:8456\n"
    "v_mfma_f32_16x16x32_bf16 v[48:51], a[240:243], a[216:219], v[48:51]\n"
    "ds_read_b64_tr_b16 a[252:253], v240 offset:10248\n"
    "v_mfma_f32_16x16x32_bf16 v[48:51], a[244:247], a[220:223], v[48:51]\n"
    "s_mov_b32 m0, 160768\n"
    "ds_read_b64_tr_b16 a[254:255], v240 offset:10504\n"
    "v_mfma_f32_16x16x32_bf16 v[80:83], a[240:243], a[224:227], v[80:83]\n"
    "s_add_u32 s7, s7, s32\n"
    "s_mov_b64 exec, 0xffff\n"
    "buffer_load_dword v255, s[8:11], s7 offen lds\n"
    "s_mov_b64 exec, -1\n"
    "v_mfma_f32_16x16x32_bf16 v[80:83], a[244:247], a[228:231], v[80:83]\n"
    "v_mfma_f32_16x16x32_bf16 v[112:115], a[240:243], a[232:235], v[112:115]\n"
    "v_mfma_f32_16x16x32_bf16 v[112:115], a[244:247], a[236:239], v[112:115]\n"
    "s_add_u32 s78, s78, 524288\n"
    "s_waitcnt lgkmcnt(0)\n"
    "v_mfma_f32_16x16x32_bf16 v[20:23], a[248:251], a[208:211], v[20:23]\n"
    "s_add_u32 s79, s79, 589824\n"
    "ds_read_b64_tr_b16 a[240:241], v240 offset:12288\n"
    "v_mfma_f32_16x16x32_bf16 v[20:23], a[252:255], a[212:215], v[20:23]\n"
    "s_add_u32 s80, s80, 589824\n"
    "ds_read_b64_tr_b16 a[242:243], v240 offset:12544\n"
    "v_mfma_f32_16x16x32_bf16 v[52:55], a[248:251], a[216:219], v[52:55]\n"
    "s_add_u32 s76, s76, 53248\n"
    "ds_read_b64_tr_b16 a[244:245], v240 offset:14336\n"
    "v_mfma_f32_16x16x32_bf16 v[52:55], a[252:255], a[220:223], v[52:55]\n"
    "s_cmp_ge_u32 s76, 159744\n"
    "ds_read_b64_tr_b16 a[246:247], v240 offset:14592\n"
    "v_mfma_f32_16x16x32_bf16 v[84:87], a[248:251], a[224:227], v[84:87]\n"
    "s_cselect_b32 s73, 159744, 0\n"
    "v_mfma_f32_16x16x32_bf16 v[84:87], a[252:255], a[228:231], v[84:87]\n"
    "s_sub_u32 s76, s76, s73\n"
    "v_mfma_f32_16x16x32_bf16 v[116:119], a[248:251], a[232:235], v[116:119]\n"
    "v_mfma_f32_16x16x32_bf16 v[116:119], a[252:255], a[236:239], v[116:119]\n"
    "s_waitcnt lgkmcnt(0)\n"
    "v_mfma_f32_16x16x32_bf16 v[24:27], a[240:243], a[208:211], v[24:27]\n"
    "ds_read_b64_tr_b16 a[248:249], v240 offset:12296\n"
    "v_mfma_f32_16x16x32_bf16 v[24:27], a[244:247], a[212:215], v[24:27]\n"
    "ds_read_b64_tr_b16 a[250:251], v240 offset:12552\n"
    "v_mfma_f32_16x16x32_bf16 v[56:59], a[240:243], a[216:219], v[56:59]\n"
    "ds_read_b64_tr_b16 a[252:253], v240 offset:14344\n"
    "v_mfma_f32_16x16x32_bf16 v[56:59], a[244:247], a[220:223], v[56:59]\n"
    "s_add_u32 s82, s82, 53248\n"
    "ds_read_b64_tr_b16 a[254:255], v240 offset:14600\n"
    "v_mfma_f32_16x16x32_bf16 v[88:91], a[240:243], a[224:227], v[88:91]\n"
    "s_cmp_ge_u32 s82, 159744\n"
    "v_mfma_f32_16x16x32_bf16 v[88:91], a[244:247], a[228:231], v[88:91]\n"
    "s_cselect_b32 s77, 4294860800, s83\n"
    "s_cselect_b32 s82, 0, s82\n"
    "v_mfma_f32_16x16x32_bf16 v[120:123], a[240:243], a[232:235], v[120:123]\n"
    "v_add_u32 v240, s77, v240\n"
    "v_add_u32 v241, s77, v241\n"
    "v_mfma_f32_16x16x32_bf16 v[120:123], a[244:247], a[236:239], v[120:123]\n"
    "v_add_u32 v242, s77, v242\n"
    "s_waitcnt lgkmcnt(0)\n"
    "v_mfma_f32_16x16x32_bf16 v[28:31], a[248:251], a[208:211], v[28:31]\n"
    "v_mfma_f32_16x16x32_bf16 v[28:31], a[252:255], a[212:215], v[28:31]\n"
    "s_waitcnt vmcnt(15)\n"
    "s_barrier\n"
    "v_mfma_f32_16x16x32_bf16 v[60:63], a[248:251], a[216:219], v[60:63]\n"
    "ds_read_b64_tr_b16 a[240:241], v241 offset:16384\n"
    "v_mfma_f32_16x16x32_bf16 v[60:63], a[252:255], a[220:223], v[60:63]\n"
    "ds_read_b64_tr_b16 a[242:243], v241 offset:16640\n"
    "v_mfma_f32_16x16x32_bf16 v[92:95], a[248:251], a[224:227], v[92:95]\n"
    "ds_read_b64_tr_b16 a[244:245], v241 offset:18432\n"
    "v_mfma_f32_16x16x32_bf16 v[92:95], a[252:255], a[228:231], v[92:95]\n"
    "ds_read_b64_tr_b16 a[246:247], v241 offset:18688\n"
    "v_mfma_f32_16x16x32_bf16 v[124:127], a[248:251], a[232:235], v[124:127]\n"
    "v_mfma_f32_16x16x32_bf16 v[124:127], a[252:255], a[236:239], v[124:127]\n"
    "s_waitcnt lgkmcnt(0)\n"
    "v_mfma_f32_16x16x32_bf16 v[128:131], a[240:243], a[176:179], v[128:131]\n"
    "ds_read_b64_tr_b16 a[248:249], v241 offset:32768\n"
    "v_mfma_f32_16x16x32_bf16 v[128:131], a[244:247], a[180:183], v[128:131]\n"
    "s_lshl_b32 s86, s70, 2\n"
    "ds_read_b64_tr_b16 a[250:251], v241 offset:33024\n"
    "v_mfma_f32_16x16x32_bf16 v[132:135], a[240:243], a[184:187], v[132:135]\n"
    "s_add_u32 s86, s86, s68\n"
    "ds_read_b64_tr_b16 a[252:253], v241 offset:34816\n"
    "v_mfma_f32_16x16x32_bf16 v[132:135], a[244:247], a[188:191], v[132:135]\n"
    "s_mul_i32 s86, s86, 1536\n"
    "ds_read_b64_tr_b16 a[254:255], v241 offset:35072\n"
    "v_mfma_f32_16x16x32_bf16 v[136:139], a[240:243], a[192:195], v[136:139]\n"
    "v_lshlrev_b32 v255, 7, v253\n"
    "v_mfma_f32_16x16x32_bf16 v[136:139], a[244:247], a[196:199], v[136:139]\n"
    "v_mfma_f32_16x16x32_bf16 v[140:143], a[240:243], a[200:203], v[140:143]\n"
    "s_add_u32 s72, s81, 0\n"
    "buffer_load_dwordx4 a[208:211], v246, s[16:19], s72 offen sc1\n"
    "v_mfma_f32_16x16x32_bf16 v[140:143], a[244:247], a[204:207], v[140:143]\n"
    "s_waitcnt lgkmcnt(0)\n"
    "v_mfma_f32_16x16x32_bf16 a[0:3], a[248:251], a[176:179], a[0:3]\n"
    "ds_read_b64_tr_b16 a[240:241], v242 offset:16384\n"
    "v_mfma_f32_16x16x32_bf16 a[0:3], a[252:255], a[180:183], a[0:3]\n"
    "ds_read_b64_tr_b16 a[242:243], v242 offset:16640\n"
    "v_mfma_f32_16x16x32_bf16 a[4:7], a[248:251], a[184:187], a[4:7]\n"
    "ds_read_b64_tr_b16 a[244:245], v242 offset:18432\n"
    "v_mfma_f32_16x16x32_bf16 a[4:7], a[252:255], a[188:191], a[4:7]\n"
    "s_mov_b32 m0, 160768\n"
    "ds_read_b64_tr_b16 a[246:247], v242 offset:18688\n"
    "v_mfma_f32_16x16x32_bf16 a[8:11], a[248:251], a[192:195], a[8:11]\n"
    "s_mov_b64 exec, 0xfff\n"
    "buffer_load_dword v255, s[36:39], s86 offen lds\n"
    "s_mov_b64 exec, -1\n"
    "v_mfma_f32_16x16x32_bf16 a[8:11], a[252:255], a[196:199], a[8:11]\n"
    "v_mfma_f32_16x16x32_bf16 a[12:15], a[248:251], a[200:203], a[12:15]\n"
    "buffer_load_dwordx4 a[212:215], v246, s[16:19], s72 offen offset:1024 sc1\n"
    "v_mfma_f32_16x16x32_bf16 a[12:15], a[252:255], a[204:207], a[12:15]\n"
    "s_waitcnt lgkmcnt(0)\n"
    "v_mfma_f32_16x16x32_bf16 v[144:147], a[240:243], a[176:179], v[144:147]\n"
    "ds_read_b64_tr_b16 a[248:249], v242 offset:32768\n"
    "v_mfma_f32_16x16x32_bf16 v[144:147], a[244:247], a[180:183], v[144:147]\n"
    "ds_read_b64_tr_b16 a[250:251], v242 offset:33024\n"
    "v_mfma_f32_16x16x32_bf16 v[148:151], a[240:243], a[184:187], v[148:151]\n"
    "ds_read_b64_tr_b16 a[252:253], v242 offset:34816\n"
    "v_mfma_f32_16x16x32_bf16 v[148:151], a[244:247], a[188:191], v[148:151]\n"
    "ds_read_b64_tr_b16 a[254:255], v242 offset:35072\n"
    "v_mfma_f32_16x16x32_bf16 v[152:155], a[240:243], a[192:195], v[152:155]\n"
    "v_mfma_f32_16x16x32_bf16 v[152:155], a[244:247], a[196:199], v[152:155]\n"
    "v_mfma_f32_16x16x32_bf16 v[156:159], a[240:243], a[200:203], v[156:159]\n"
    "s_add_u32 s72, s81, 18432\n"
    "buffer_load_dwordx4 a[216:219], v246, s[16:19], s72 offen sc1\n"
    "v_mfma_f32_16x16x32_bf16 v[156:159], a[244:247], a[204:207], v[156:159]\n"
    "s_waitcnt lgkmcnt(0)\n"
    "v_mfma_f32_16x16x32_bf16 a[16:19], a[248:251], a[176:179], a[16:19]\n"
    "ds_read_b64_tr_b16 a[240:241], v241 offset:20480\n"
    "v_mfma_f32_16x16x32_bf16 a[16:19], a[252:255], a[180:183], a[16:19]\n"
    "ds_read_b64_tr_b16 a[242:243], v241 offset:20736\n"
    "v_mfma_f32_16x16x32_bf16 a[20:23], a[248:251], a[184:187], a[20:23]\n"
    "ds_read_b64_tr_b16 a[244:245], v241 offset:22528\n"
    "v_mfma_f32_16x16x32_bf16 a[20:23], a[252:255], a[188:191], a[20:23]\n"
    "s_mov_b32 m0, 160768\n"
    "ds_read_b64_tr_b16 a[246:247], v241 offset:22784\n"
    "v_mfma_f32_16x16x32_bf16 a[24:27], a[248:251], a[192:195], a[24:27]\n"
    "s_mov_b64 exec, 0xfff\n"
    "buffer_load_dword v255, s[40:43], s86 offen lds\n"
    "s_mov_b64 exec, -1\n"
    "v_mfma_f32_16x16x32_bf16 a[24:27], a[252:255], a[196:199], a[24:27]\n"
    "v_mfma_f32_16x16x32_bf16 a[28:31], a[248:251], a[200:203], a[28:31]\n"
    "buffer_load_dwordx4 a[220:223], v246, s[16:19], s72 offen offset:1024 sc1\n"
    "v_mfma_f32_16x16x32_bf16 a[28:31], a[252:255], a[204:207], a[28:31]\n"
    "s_waitcnt lgkmcnt(0)\n"
    "v_mfma_f32_16x16x32_bf16 v[160:163], a[240:243], a[176:179], v[160:163]\n"
    "ds_read_b64_tr_b16 a[248:249], v241 offset:36864\n"
    "v_mfma_f32_16x16x32_bf16 v[160:163], a[244:247], a[180:183], v[160:163]\n"
    "ds_read_b64_tr_b16 a[250:251], v241 offset:37120\n"
    "v_mfma_f32_16x16x32_bf16 v[164:167], a[240:243], a[184:187], v[164:167]\n"
    "ds_read_b64_tr_b16 a[252:253], v241 offset:38912\n"
    "v_mfma_f32_16x16x32_bf16 v[164:167], a[244:247], a[188:191], v[164:167]\n"
    "ds_read_b64_tr_b16 a[254:255], v241 offset:39168\n"
    "v_mfma_f32_16x16x32_bf16 v[168:171], a[240:243], a[192:195], v[168:171]\n"
    "v_mfma_f32_16x16x32_bf16 v[168:171], a[244:247], a[196:199], v[168:171]\n"
    "v_mfma_f32_16x16x32_bf16 v[172:175], a[240:243], a[200:203], v[172:175]\n"
    "s_add_u32 s72, s81, 36864\n"
    "buffer_load_dwordx4 a[224:227], v246, s[16:19], s72 offen sc1\n"
    "v_mfma_f32_16x16x32_bf16 v[172:175], a[244:247], a[204:207], v[172:175]\n"
    "s_waitcnt lgkmcnt(0)\n"
    "v_mfma_f32_16x16x32_bf16 a[32:35], a[248:251], a[176:179], a[32:35]\n"
    "ds_read_b64_tr_b16 a[240:241], v242 offset:20480\n"
    "v_mfma_f32_16x16x32_bf16 a[32:35], a[252:255], a[180:183], a[32:35]\n"
    "ds_read_b64_tr_b16 a[242:243], v242 offset:20736\n"
    "v_mfma_f32_16x16x32_bf16 a[36:39], a[248:251], a[184:187], a[36:39]\n"
    "ds_read_b64_tr_b16 a[244:245], v242 offset:22528\n"
    "v_mfma_f32_16x16x32_bf16 a[36:39], a[252:255], a[188:191], a[36:39]\n"
    "ds_read_b64_tr_b16 a[246:247], v242 offset:22784\n"
    "v_mfma_f32_16x16x32_bf16 a[40:43], a[248:251], a[192:195], a[40:43]\n"
    "v_mfma_f32_16x16x32_bf16 a[40:43], a[252:255], a[196:199], a[40:43]\n"
    "v_mfma_f32_16x16x32_bf16 a[44:47], a[248:251], a[200:203], a[44:47]\n"
    "buffer_load_dwordx4 a[228:231], v246, s[16:19], s72 offen offset:1024 sc1\n"
    "v_mfma_f32_16x16x32_bf16 a[44:47], a[252:255], a[204:207], a[44:47]\n"
    "s_waitcnt lgkmcnt(0)\n"
    "v_mfma_f32_16x16x32_bf16 v[176:179], a[240:243], a[176:179], v[176:179]\n"
    "ds_read_b64_tr_b16 a[248:249], v242 offset:36864\n"
    "v_mfma_f32_16x16x32_bf16 v[176:179], a[244:247], a[180:183], v[176:179]\n"
    "ds_read_b64_tr_b16 a[250:251], v242 offset:37120\n"
    "v_mfma_f32_16x16x32_bf16 v[180:183], a[240:243], a[184:187], v[180:183]\n"
    "ds_read_b64_tr_b16 a[252:253], v242 offset:38912\n"
    "v_mfma_f32_16x16x32_bf16 v[180:183], a[244:247], a[188:191], v[180:183]\n"
    "ds_read_b64_tr_b16 a[254:255], v242 offset:39168\n"
    "v_mfma_f32_16x16x32_bf16 v[184:187], a[240:243], a[192:195], v[184:187]\n"
    "v_mfma_f32_16x16x32_bf16 v[184:187], a[244:247], a[196:199], v[184:187]\n"
    "v_mfma_f32_16x16x32_bf16 v[188:191], a[240:243], a[200:203], v[188:191]\n"
    "s_add_u32 s72, s81, 55296\n"
    "buffer_load_dwordx4 a[232:235], v246, s[16:19], s72 offen sc1\n"
    "v_mfma_f32_16x16x32_bf16 v[188:191], a[244:247], a[204:207], v[188:191]\n"
    "s_waitcnt lgkmcnt(0)\n"
    "v_mfma_f32_16x16x32_bf16 a[48:51], a[248:251], a[176:179], a[48:51]\n"
    "ds_read_b64_tr_b16 a[240:241], v241 offset:24576\n"
    "v_mfma_f32_16x16x32_bf16 a[48:51], a[252:255], a[180:183], a[48:51]\n"
    "ds_read_b64_tr_b16 a[242:243], v241 offset:24832\n"
    "v_mfma_f32_16x16x32_bf16 a[52:55], a[248:251], a[184:187], a[52:55]\n"
    "ds_read_b64_tr_b16 a[244:245], v241 offset:26624\n"
    "v_mfma_f32_16x16x32_bf16 a[52:55], a[252:255], a[188:191], a[52:55]\n"
    "ds_read_b64_tr_b16 a[246:247], v241 offset:26880\n"
    "v_mfma_f32_16x16x32_bf16 a[56:59], a[248:251], a[192:195], a[56:59]\n"
    "v_mfma_f32_16x16x32_bf16 a[56:59], a[252:255], a[196:199], a[56:59]\n"
    "v_mfma_f32_16x16x32_bf16 a[60:63], a[248:251], a[200:203], a[60:63]\n"
    "buffer_load_dwordx4 a[236:239], v246, s[16:19], s72 offen offset:1024 sc1\n"
    "v_mfma_f32_16x16x32_bf16 a[60:63], a[252:255], a[204:207], a[60:63]\n"
    "s_waitcnt lgkmcnt(0)\n"
    "v_mfma_f32_16x16x32_bf16 v[192:195], a[240:243], a[176:179], v[192:195]\n"
    "ds_read_b64_tr_b16 a[248:249], v241 offset:40960\n"
    "v_mfma_f32_16x16x32_bf16 v[192:195], a[244:247], a[180:183], v[192:195]\n"
    "s_add_u32 s81, s81, 2048\n"
    "ds_read_b64_tr_b16 a[250:251], v241 offset:41216\n"
    "v_mfma_f32_16x16x32_bf16 v[196:199], a[240:243], a[184:187], v[196:199]\n"
    "ds_read_b64_tr_b16 a[252:253], v241 offset:43008\n"
    "v_mfma_f32_16x16x32_bf16 v[196:199], a[244:247], a[188:191], v[196:199]\n"
    "ds_read_b64_tr_b16 a[254:255], v241 offset:43264\n"
    "v_mfma_f32_16x16x32_bf16 v[200:203], a[240:243], a[192:195], v[200:203]\n"
    "v_mfma_f32_16x16x32_bf16 v[200:203], a[244:247], a[196:199], v[200:203]\n"
    "s_add_u32 m0, s76, 0\n"
    "v_mfma_f32_16x16x32_bf16 v[204:207], a[240:243], a[200:203], v[204:207]\n"
    "s_add_u32 s72, s78, 0\n"
    "buffer_load_dwordx4 v243, s[20:23], s72 offen lds\n"
    "v_mfma_f32_16x16x32_bf16 v[204:207], a[244:247], a[204:207], v[204:207]\n"
    "s_waitcnt lgkmcnt(0)\n"
    "v_mfma_f32_16x16x32_bf16 a[64:67], a[248:251], a[176:179], a[64:67]\n"
    "ds_read_b64_tr_b16 a[240:241], v242 offset:24576\n"
    "v_mfma_f32_16x16x32_bf16 a[64:67], a[252:255], a[180:183], a[64:67]\n"
    "ds_read_b64_tr_b16 a[242:243], v242 offset:24832\n"
    "v_mfma_f32_16x16x32_bf16 a[68:71], a[248:251], a[184:187], a[68:71]\n"
    "ds_read_b64_tr_b16 a[244:245], v242 offset:26624\n"
    "v_mfma_f32_16x16x32_bf16 a[68:71], a[252:255], a[188:191], a[68:71]\n"
    "ds_read_b64_tr_b16 a[246:247], v242 offset:26880\n"
    "v_mfma_f32_16x16x32_bf16 a[72:75], a[248:251], a[192:195], a[72:75]\n"
    "v_lshrrev_b32 v254, 4, v253\n"
    "v_mul_u32_u24 v254, 18432, v254\n"
    "v_mfma_f32_16x16x32_bf16 a[72:75], a[252:255], a[196:199], a[72:75]\n"
    "s_add_u32 m0, s76, 4096\n"
    "v_mfma_f32_16x16x32_bf16 a[76:79], a[248:251], a[200:203], a[76:79]\n"
    "s_add_u32 s72, s78, 64\n"
    "buffer_load_dwordx4 v243, s[20:23], s72 offen lds\n"
    "v_mfma_f32_16x16x32_bf16 a[76:79], a[252:255], a[204:207], a[76:79]\n"
    "s_waitcnt lgkmcnt(0)\n"
    "v_mfma_f32_16x16x32_bf16 v[208:211], a[240:243], a[176:179], v[208:211]\n"
    "ds_read_b64_tr_b16 a[248:249], v242 offset:40960\n"
    "v_mfma_f32_16x16x32_bf16 v[208:211], a[244:247], a[180:183], v[208:211]\n"
    "ds_read_b64_tr_b16 a[250:251], v242 offset:41216\n"
    "v_mfma_f32_16x16x32_bf16 v[212:215], a[240:243], a[184:187], v[212:215]\n"
    "ds_read_b64_tr_b16 a[252:253], v242 offset:43008\n"
    "v_mfma_f32_16x16x32_bf16 v[212:215], a[244:247], a[188:191], v[212:215]\n"
    "ds_read_b64_tr_b16 a[254:255], v242 offset:43264\n"
    "v_mfma_f32_16x16x32_bf16 v[216:219], a[240:243], a[192:195], v[216:219]\n"
    "v_and_b32 v255, 15, v253\n"
    "v_lshl_add_u32 v254, v255, 7, v254\n"
    "v_mfma_f32_16x16x32_bf16 v[216:219], a[244:247], a[196:199], v[216:219]\n"
    "s_add_u32 m0, s76, 8192\n"
    "v_mfma_f32_16x16x32_bf16 v[220:223], a[240:243], a[200:203], v[220:223]\n"
    "s_add_u32 s72, s78, 128\n"
    "buffer_load_dwordx4 v243, s[20:23], s72 offen lds\n"
    "v_mfma_f32_16x16x32_bf16 v[220:223], a[244:247], a[204:207], v[220:223]\n"
    "s_waitcnt lgkmcnt(0)\n"
    "v_mfma_f32_16x16x32_bf16 a[80:83], a[248:251], a[176:179], a[80:83]\n"
    "ds_read_b64_tr_b16 a[240:241], v241 offset:28672\n"
    "v_mfma_f32_16x16x32_bf16 a[80:83], a[252:255], a[180:183], a[80:83]\n"
    "ds_read_b64_tr_b16 a[242:243], v241 offset:28928\n"
    "v_mfma_f32_16x16x32_bf16 a[84:87], a[248:251], a[184:187], a[84:87]\n"
    "ds_read_b64_tr_b16 a[244:245], v241 offset:30720\n"
    "v_mfma_f32_16x16x32_bf16 a[84:87], a[252:255], a[188:191], a[84:87]\n"
    "ds_read_b64_tr_b16 a[246:247], v241 offset:30976\n"
    "v_mfma_f32_16x16x32_bf16 a[88:91], a[248:251], a[192:195], a[88:91]\n"
    "v_mfma_f32_16x16x32_bf16 a[88:91], a[252:255], a[196:199], a[88:91]\n"
    "s_add_u32 m0, s76, 12288\n"
    "v_mfma_f32_16x16x32_bf16 a[92:95], a[248:251], a[200:203], a[92:95]\n"
    "s_add_u32 s72, s78, 192\n"
    "buffer_load_dwordx4 v243, s[20:23], s72 offen lds\n"
    "v_mfma_f32_16x16x32_bf16 a[92:95], a[252:255], a[204:207], a[92:95]\n"
    "s_waitcnt lgkmcnt(0)\n"
    "v_mfma_f32_16x16x32_bf16 v[224:227], a[240:243], a[176:179], v[224:227]\n"
    "ds_read_b64_tr_b16 a[248:249], v241 offset:45056\n"
    "v_mfma_f32_16x16x32_bf16 v[224:227], a[244:247], a[180:183], v[224:227]\n"
    "ds_read_b64_tr_b16 a[250:251], v241 offset:45312\n"
    "v_mfma_f32_16x16x32_bf16 v[228:231], a[240:243], a[184:187], v[228:231]\n"
    "ds_read_b64_tr_b16 a[252:253], v241 offset:47104\n"
    "v_mfma_f32_16x16x32_bf16 v[228:231], a[244:247], a[188:191], v[228:231]\n"
    "ds_read_b64_tr_b16 a[254:255], v241 offset:47360\n"
    "v_mfma_f32_16x16x32_bf16 v[232:235], a[240:243], a[192:195], v[232:235]\n"
    "v_mfma_f32_16x16x32_bf16 v[232:235], a[244:247], a[196:199], v[232:235]\n"
    "s_add_u32 m0, s76, 16384\n"
    "v_mfma_f32_16x16x32_bf16 v[236:239], a[240:243], a[200:203], v[236:239]\n"
    "s_add_u32 s72, s79, 0\n"
    "buffer_load_dwordx4 v244, s[24:27], s72 offen lds\n"
    "v_mfma_f32_16x16x32_bf16 v[236:239], a[244:247], a[204:207], v[236:239]\n"
    "s_waitcnt lgkmcnt(0)\n"
    "v_mfma_f32_16x16x32_bf16 a[96:99], a[248:251], a[176:179], a[96:99]\n"
    "ds_read_b64_tr_b16 a[240:241], v242 offset:28672\n"
    "v_mfma_f32_16x16x32_bf16 a[96:99], a[252:255], a[180:183], a[96:99]\n"
    "ds_read_b64_tr_b16 a[242:243], v242 offset:28928\n"
    "v_mfma_f32_16x16x32_bf16 a[100:103], a[248:251], a[184:187], a[100:103]\n"
    "ds_read_b64_tr_b16 a[244:245], v242 offset:30720\n"
    "v_mfma_f32_16x16x32_bf16 a[100:103], a[252:255], a[188:191], a[100:103]\n"
    "ds_read_b64_tr_b16 a[246:247], v242 offset:30976\n"
    "v_mfma_f32_16x16x32_bf16 a[104:107], a[248:251], a[192:195], a[104:107]\n"
    "v_mfma_f32_16x16x32_bf16 a[104:107], a[252:255], a[196:199], a[104:107]\n"
    "s_add_u32 m0, s76, 20480\n"
    "v_mfma_f32_16x16x32_bf16 a[108:111], a[248:251], a[200:203], a[108:111]\n"
    "s_add_u32 s72, s79, 64\n"
    "buffer_load_dwordx4 v244, s[24:27], s72 offen lds\n"
    "v_mfma_f32_16x16x32_bf16 a[108:111], a[252:255], a[204:207], a[108:111]\n"
    "s_waitcnt lgkmcnt(0)\n"
    "v_mfma_f32_16x16x32_bf16 a[144:147], a[240:243], a[176:179], a[144:147]\n"
    "ds_read_b64_tr_b16 a[248:249], v242 offset:45056\n"
    "v_mfma_f32_16x16x32_bf16 a[144:147], a[244:247], a[180:183], a[144:147]\n"
    "ds_read_b64_tr_b16 a[250:251], v242 offset:45312\n"
    "v_mfma_f32_16x16x32_bf16 a[148:151], a[240:243], a[184:187], a[148:151]\n"
    "ds_read_b64_tr_b16 a[252:253], v242 offset:47104\n"
    "v_mfma_f32_16x16x32_bf16 a[148:151], a[244:247], a[188:191], a[148:151]\n"
    "ds_read_b64_tr_b16 a[254:255], v242 offset:47360\n"
    "v_mfma_f32_16x16x32_bf16 a[152:155], a[240:243], a[192:195], a[152:155]\n"
    "v_mfma_f32_16x16x32_bf16 a[152:155], a[244:247], a[196:199], a[152:155]\n"
    "s_add_u32 m0, s76, 24576\n"
    "v_mfma_f32_16x16x32_bf16 a[156:159], a[240:243], a[200:203], a[156:159]\n"
    "s_add_u32 s72, s79, 128\n"
    "buffer_load_dwordx4 v244, s[24:27], s72 offen lds\n"
    "v_mfma_f32_16x16x32_bf16 a[156:159], a[244:247], a[204:207], a[156:159]\n"
    "s_waitcnt lgkmcnt(0)\n"
    "v_mfma_f32_16x16x32_bf16 a[112:115], a[248:251], a[176:179], a[112:115]\n"
    "ds_read_b64_tr_b16 a[240:241], v241 offset:49152\n"
    "v_mfma_f32_16x16x32_bf16 a[112:115], a[252:255], a[180:183], a[112:115]\n"
    "ds_read_b64_tr_b16 a[242:243], v241 offset:49408\n"
    "v_mfma_f32_16x16x32_bf16 a[116:119], a[248:251], a[184:187], a[116:119]\n"
    "ds_read_b64_tr_b16 a[244:245], v241 offset:51200\n"
    "v_mfma_f32_16x16x32_bf16 a[116:119], a[252:255], a[188:191], a[116:119]\n"
    "ds_read_b64_tr_b16 a[246:247], v241 offset:51456\n"
    "v_mfma_f32_16x16x32_bf16 a[120:123], a[248:251], a[192:195], a[120:123]\n"
    "v_mfma_f32_16x16x32_bf16 a[120:123], a[252:255], a[196:199], a[120:123]\n"
    "s_add_u32 m0, s76, 28672\n"
    "v_mfma_f32_16x16x32_bf16 a[124:127], a[248:251], a[200:203], a[124:127]\n"
    "s_add_u32 s72, s79, 192\n"
    "buffer_load_dwordx4 v244, s[24:27], s72 offen lds\n"
    "v_mfma_f32_16x16x32_bf16 a[124:127], a[252:255], a[204:207], a[124:127]\n"
    "s_waitcnt lgkmcnt(0)\n"
    "v_mfma_f32_16x16x32_bf16 a[160:163], a[240:243], a[176:179], a[160:163]\n"
    "ds_read_b64_tr_b16 a[248:249], v242 offset:49152\n"
    "v_mfma_f32_16x16x32_bf16 a[160:163], a[244:247], a[180:183], a[160:163]\n"
    "ds_read_b64_tr_b16 a[250:251], v242 offset:49408\n"
    "v_mfma_f32_16x16x32_bf16 a[164:167], a[240:243], a[184:187], a[164:167]\n"
    "ds_read_b64_tr_b16 a[252:253], v242 offset:51200\n"
    "v_mfma_f32_16x16x32_bf16 a[164:167], a[244:247], a[188:191], a[164:167]\n"
    "ds_read_b64_tr_b16 a[254:255], v242 offset:51456\n"
    "v_mfma_f32_16x16x32_bf16 a[168:171], a[240:243], a[192:195], a[168:171]\n"
    "v_mfma_f32_16x16x32_bf16 a[168:171], a[244:247], a[196:199], a[168:171]\n"
    "s_add_u32 m0, s76, 49152\n"
    "v_mfma_f32_16x16x32_bf16 a[172:175], a[240:243], a[200:203], a[172:175]\n"
    "s_mov_b64 exec, s[12:13]\n"
    "buffer_load_dwordx4 v245, s[24:27], s80 offen lds\n"
    "s_mov_b64 exec, -1\n"
    "v_mfma_f32_16x16x32_bf16 a[172:175], a[244:247], a[204:207], a[172:175]\n"
    "s_waitcnt lgkmcnt(0)\n"
    "v_mfma_f32_16x16x32_bf16 a[128:131], a[248:251], a[176:179], a[128:131]\n"
    "ds_read_b64_tr_b16 a[240:241], v240 offset:0\n"
    "v_mfma_f32_16x16x32_bf16 a[128:131], a[252:255], a[180:183], a[128:131]\n"
    "ds_read_b64_tr_b16 a[242:243], v240 offset:256\n"
    "v_mfma_f32_16x16x32_bf16 a[132:135], a[248:251], a[184:187], a[132:135]\n"
    "ds_read_b64_tr_b16 a[244:245], v240 offset:2048\n"
    "v_mfma_f32_16x16x32_bf16 a[132:135], a[252:255], a[188:191], a[132:135]\n"
    "ds_read_b64_tr_b16 a[246:247], v240 offset:2304\n"
    "v_mfma_f32_16x16x32_bf16 a[136:139], a[248:251], a[192:195], a[136:139]\n"
    "v_mfma_f32_16x16x32_bf16 a[136:139], a[252:255], a[196:199], a[136:139]\n"
    "s_add_u32 m0, s76, 32768\n"
    "v_mfma_f32_16x16x32_bf16 a[140:143], a[248:251], a[200:203], a[140:143]\n"
    "s_add_u32 s72, s79, 0\n"
    "buffer_load_dwordx4 v244, s[28:31], s72 offen lds\n"
    "v_mfma_f32_16x16x32_bf16 a[140:143], a[252:255], a[204:207], a[140:143]\n"
    "s_waitcnt lgkmcnt(0)\n"
    "v_mfma_f32_16x16x32_bf16 v[0:3], a[240:243], a[176:179], v[0:3]\n"
    "ds_read_b64_tr_b16 a[248:249], v240 offset:8\n"
    "v_mfma_f32_16x16x32_bf16 v[0:3], a[244:247], a[180:183], v[0:3]\n"
    "ds_read_b64_tr_b16 a[250:251], v240 offset:264\n"
    "v_mfma_f32_16x16x32_bf16 v[32:35], a[240:243], a[184:187], v[32:35]\n"
    "ds_read_b64_tr_b16 a[252:253], v240 offset:2056\n"
    "v_mfma_f32_16x16x32_bf16 v[32:35], a[244:247], a[188:191], v[32:35]\n"
    "ds_read_b64_tr_b16 a[254:255], v240 offset:2312\n"
    "v_mfma_f32_16x16x32_bf16 v[64:67], a[240:243], a[192:195], v[64:67]\n"
    "v_mfma_f32_16x16x32_bf16 v[64:67], a[244:247], a[196:199], v[64:67]\n"
    "s_add_u32 m0, s76, 36864\n"
    "v_mfma_f32_16x16x32_bf16 v[96:99], a[240:243], a[200:203], v[96:99]\n"
    "s_add_u32 s72, s79, 64\n"
    "buffer_load_dwordx4 v244, s[28:31], s72 offen lds\n"
    "v_mfma_f32_16x16x32_bf16 v[96:99], a[244:247], a[204:207], v[96:99]\n"
    "s_waitcnt lgkmcnt(0)\n"
    "v_mfma_f32_16x16x32_bf16 v[4:7], a[248:251], a[176:179], v[4:7]\n"
    "ds_read_b64_tr_b16 a[240:241], v240 offset:4096\n"
    "v_mfma_f32_16x16x32_bf16 v[4:7], a[252:255], a[180:183], v[4:7]\n"
    "ds_read_b64_tr_b16 a[242:243], v240 offset:4352\n"
    "v_mfma_f32_16x16x32_bf16 v[36:39], a[248:251], a[184:187], v[36:39]\n"
    "ds_read_b64_tr_b16 a[244:245], v240 offset:6144\n"
    "v_mfma_f32_16x16x32_bf16 v[36:39], a[252:255], a[188:191], v[36:39]\n"
    "ds_read_b64_tr_b16 a[246:247], v240 offset:6400\n"
    "v_mfma_f32_16x16x32_bf16 v[68:71], a[248:251], a[192:195], v[68:71]\n"
    "v_mfma_f32_16x16x32_bf16 v[68:71], a[252:255], a[196:199], v[68:71]\n"
    "s_add_u32 m0, s76, 40960\n"
    "v_mfma_f32_16x16x32_bf16 v[100:103], a[248:251], a[200:203], v[100:103]\n"
    "s_add_u32 s72, s79, 128\n"
    "buffer_load_dwordx4 v244, s[28:31], s72 offen lds\n"
    "v_mfma_f32_16x16x32_bf16 v[100:103], a[252:255], a[204:207], v[100:103]\n"
    "s_waitcnt lgkmcnt(0)\n"
    "v_mfma_f32_16x16x32_bf16 v[8:11], a[240:243], a[176:179], v[8:11]\n"
    "ds_read_b64_tr_b16 a[248:249], v240 offset:4104\n"
    "v_mfma_f32_16x16x32_bf16 v[8:11], a[244:247], a[180:183], v[8:11]\n"
    "ds_read_b64_tr_b16 a[250:251], v240 offset:4360\n"
    "v_mfma_f32_16x16x32_bf16 v[40:43], a[240:243], a[184:187], v[40:43]\n"
    "ds_read_b64_tr_b16 a[252:253], v240 offset:6152\n"
    "v_mfma_f32_16x16x32_bf16 v[40:43], a[244:247], a[188:191], v[40:43]\n"
    "ds_read_b64_tr_b16 a[254:255], v240 offset:6408\n"
    "v_mfma_f32_16x16x32_bf16 v[72:75], a[240:243], a[192:195], v[72:75]\n"
    "v_mfma_f32_16x16x32_bf16 v[72:75], a[244:247], a[196:199], v[72:75]\n"
    "s_add_u32 m0, s76, 45056\n"
    "v_mfma_f32_16x16x32_bf16 v[104:107], a[240:243], a[200:203], v[104:107]\n"
    "s_add_u32 s72, s79, 192\n"
    "buffer_load_dwordx4 v244, s[28:31], s72 offen lds\n"
    "v_mfma_f32_16x16x32_bf16 v[104:107], a[244:247], a[204:207], v[104:107]\n"
    "s_waitcnt lgkmcnt(0)\n"
    "v_mfma_f32_16x16x32_bf16 v[12:15], a[248:251], a[176:179], v[12:15]\n"
    "ds_read_b64_tr_b16 a[240:241], v240 offset:8192\n"
    "v_mfma_f32_16x16x32_bf16 v[12:15], a[252:255], a[180:183], v[12:15]\n"
    "ds_read_b64_tr_b16 a[242:243], v240 offset:8448\n"
    "v_mfma_f32_16x16x32_bf16 v[44:47], a[248:251], a[184:187], v[44:47]\n"
    "ds_read_b64_tr_b16 a[244:245], v240 offset:10240\n"
    "v_mfma_f32_16x16x32_bf16 v[44:47], a[252:255], a[188:191], v[44:47]\n"
    "ds_read_b64_tr_b16 a[246:247], v240 offset:10496\n"
    "v_mfma_f32_16x16x32_bf16 v[76:79], a[248:251], a[192:195], v[76:79]\n"
    "v_mfma_f32_16x16x32_bf16 v[76:79], a[252:255], a[196:199], v[76:79]\n"
    "s_add_u32 m0, s76, 49152\n"
    "v_mfma_f32_16x16x32_bf16 v[108:111], a[248:251], a[200:203], v[108:111]\n"
    "s_mov_b64 exec, s[14:15]\n"
    "buffer_load_dwordx4 v245, s[28:31], s80 offen lds\n"
    "s_mov_b64 exec, -1\n"
    "v_mfma_f32_16x16x32_bf16 v[108:111], a[252:255], a[204:207], v[108:111]\n"
    "s_waitcnt lgkmcnt(0)\n"
    "v_mfma_f32_16x16x32_bf16 v[16:19], a[240:243], a[176:179], v[16:19]\n"
    "ds_read_b64_tr_b16 a[248:249], v240 offset:8200\n"
    "v_mfma_f32_16x16x32_bf16 v[16:19], a[244:247], a[180:183], v[16:19]\n"
    "ds_read_b64_tr_b16 a[250:251], v240 offset:8456\n"
    "v_mfma_f32_16x16x32_bf16 v[48:51], a[240:243], a[184:187], v[48:51]\n"
    "ds_read_b64_tr_b16 a[252:253], v240 offset:10248\n"
    "v_mfma_f32_16x16x32_bf16 v[48:51], a[244:247], a[188:191], v[48:51]\n"
    "s_mov_b32 m0, 160768\n"
    "ds_read_b64_tr_b16 a[254:255], v240 offset:10504\n"
    "v_mfma_f32_16x16x32_bf16 v[80:83], a[240:243], a[192:195], v[80:83]\n"
    "buffer_load_dword v254, s[16:19], s81 offen sc1 lds\n"
    "v_mfma_f32_16x16x32_bf16 v[80:83], a[244:247], a[196:199], v[80:83]\n"
    "v_mfma_f32_16x16x32_bf16 v[112:115], a[240:243], a[200:203], v[112:115]\n"
    "v_mfma_f32_16x16x32_bf16 v[112:115], a[244:247], a[204:207], v[112:115]\n"
    "s_add_u32 s78, s78, 524288\n"
    "s_waitcnt lgkmcnt(0)\n"
    "v_mfma_f32_16x16x32_bf16 v[20:23], a[248:251], a[176:179], v[20:23]\n"
    "s_add_u32 s79, s79, 589824\n"
    "ds_read_b64_tr_b16 a[240:241], v240 offset:12288\n"
    "v_mfma_f32_16x16x32_bf16 v[20:23], a[252:255], a[180:183], v[20:23]\n"
    "s_add_u32 s80, s80, 589824\n"
    "ds_read_b64_tr_b16 a[242:243], v240 offset:12544\n"
    "v_mfma_f32_16x16x32_bf16 v[52:55], a[248:251], a[184:187], v[52:55]\n"
    "s_add_u32 s76, s76, 53248\n"
    "ds_read_b64_tr_b16 a[244:245], v240 offset:14336\n"
    "v_mfma_f32_16x16x32_bf16 v[52:55], a[252:255], a[188:191], v[52:55]\n"
    "s_cmp_ge_u32 s76, 159744\n"
    "ds_read_b64_tr_b16 a[246:247], v240 offset:14592\n"
    "v_mfma_f32_16x16x32_bf16 v[84:87], a[248:251], a[192:195], v[84:87]\n"
    "s_cselect_b32 s73, 159744, 0\n"
    "v_mfma_f32_16x16x32_bf16 v[84:87], a[252:255], a[196:199], v[84:87]\n"
    "s_sub_u32 s76, s76, s73\n"
    "v_mfma_f32_16x16x32_bf16 v[116:119], a[248:251], a[200:203], v[116:119]\n"
    "v_mfma_f32_16x16x32_bf16 v[116:119], a[252:255], a[204:207], v[116:119]\n"
    "s_waitcnt lgkmcnt(0)\n"
    "v_mfma_f32_16x16x32_bf16 v[24:27], a[240:243], a[176:179], v[24:27]\n"
    "ds_read_b64_tr_b16 a[248:249], v240 offset:12296\n"
    "v_mfma_f32_16x16x32_bf16 v[24:27], a[244:247], a[180:183], v[24:27]\n"
    "ds_read_b64_tr_b16 a[250:251], v240 offset:12552\n"
    "v_mfma_f32_16x16x32_bf16 v[56:59], a[240:243], a[184:187], v[56:59]\n"
    "ds_read_b64_tr_b16 a[252:253], v240 offset:14344\n"
    "v_mfma_f32_16x16x32_bf16 v[56:59], a[244:247], a[188:191], v[56:59]\n"
    "s_add_u32 s82, s82, 53248\n"
    "ds_read_b64_tr_b16 a[254:255], v240 offset:14600\n"
    "v_mfma_f32_16x16x32_bf16 v[88:91], a[240:243], a[192:195], v[88:91]\n"
    "s_cmp_ge_u32 s82, 159744\n"
    "v_mfma_f32_16x16x32_bf16 v[88:91], a[244:247], a[196:199], v[88:91]\n"
    "s_cselect_b32 s77, 4294860800, s83\n"
    "s_cselect_b32 s82, 0, s82\n"
    "v_mfma_f32_16x16x32_bf16 v[120:123], a[240:243], a[200:203], v[120:123]\n"
    "v_add_u32 v240, s77, v240\n"
    "v_add_u32 v241, s77, v241\n"
    "v_mfma_f32_16x16x32_bf16 v[120:123], a[244:247], a[204:207], v[120:123]\n"
    "v_add_u32 v242, s77, v242\n"
    "s_waitcnt lgkmcnt(0)\n"
    "v_mfma_f32_16x16x32_bf16 v[28:31], a[248:251], a[176:179], v[28:31]\n"
    "v_mfma_f32_16x16x32_bf16 v[28:31], a[252:255], a[180:183], v[28:31]\n"
    "s_waitcnt vmcnt(14)\n"
    "s_barrier\n"
    "v_mfma_f32_16x16x32_bf16 v[60:63], a[248:251], a[184:187], v[60:63]\n"
    "ds_read_b64_tr_b16 a[240:241], v240 offset:0\n"
    "v_mfma_f32_16x16x32_bf16 v[60:63], a[252:255], a[188:191], v[60:63]\n"
    "ds_read_b64_tr_b16 a[242:243], v240 offset:256\n"
    "v_mfma_f32_16x16x32_bf16 v[92:95], a[248:251], a[192:195], v[92:95]\n"
    "ds_read_b64_tr_b16 a[244:245], v240 offset:2048\n"
    "v_mfma_f32_16x16x32_bf16 v[92:95], a[252:255], a[196:199], v[92:95]\n"
    "ds_read_b64_tr_b16 a[246:247], v240 offset:2304\n"
    "v_mfma_f32_16x16x32_bf16 v[124:127], a[248:251], a[200:203], v[124:127]\n"
    "v_mfma_f32_16x16x32_bf16 v[124:127], a[252:255], a[204:207], v[124:127]\n"
    "s_waitcnt lgkmcnt(0)\n"
    "v_mfma_f32_16x16x32_bf16 v[0:3], a[240:243], a[208:211], v[0:3]\n"
    "ds_read_b64_tr_b16 a[248:249], v240 offset:8\n"
    "v_mfma_f32_16x16x32_bf16 v[0:3], a[244:247], a[212:215], v[0:3]\n"
    "ds_read_b64_tr_b16 a[250:251], v240 offset:264\n"
    "v_mfma_f32_16x16x32_bf16 v[32:35], a[240:243], a[216:219], v[32:35]\n"
    "ds_read_b64_tr_b16 a[252:253], v240 offset:2056\n"
    "v_mfma_f32_16x16x32_bf16 v[32:35], a[244:247], a[220:223], v[32:35]\n"
    "ds_read_b64_tr_b16 a[254:255], v240 offset:2312\n"
    "v_mfma_f32_16x16x32_bf16 v[64:67], a[240:243], a[224:227], v[64:67]\n"
    "v_add_u32 v254, 53248, v241\n"
    "v_add_u32 v255, 53248, v242\n"
    "v_mfma_f32_16x16x32_bf16 v[64:67], a[244:247], a[228:231], v[64:67]\n"
    "s_add_u32 s72, s81, 0\n"
    "buffer_load_dwordx4 a[176:179], v246, s[16:19], s72 offen sc1\n"
    "v_mfma_f32_16x16x32_bf16 v[96:99], a[240:243], a[232:235], v[96:99]\n"
    "v_mfma_f32_16x16x32_bf16 v[96:99], a[244:247], a[236:239], v[96:99]\n"
    "s_waitcnt lgkmcnt(0)\n"
    "buffer_load_dwordx4 a[180:183], v246, s[16:19], s72 offen offset:1024 sc1\n"
    "v_mfma_f32_16x16x32_bf16 v[4:7], a[248:251], a[208:211], v[4:7]\n"
    "ds_read_b64_tr_b16 a[240:241], v240 offset:4096\n"
    "v_mfma_f32_16x16x32_bf16 v[4:7], a[252:255], a[212:215], v[4:7]\n"
    "ds_read_b64_tr_b16 a[242:243], v240 offset:4352\n"
    "v_mfma_f32_16x16x32_bf16 v[36:39], a[248:251], a[216:219], v[36:39]\n"
    "ds_read_b64_tr_b16 a[244:245], v240 offset:6144\n"
    "v_mfma_f32_16x16x32_bf16 v[36:39], a[252:255], a[220:223], v[36:39]\n"
    "ds_read_b64_tr_b16 a[246:247], v240 offset:6400\n"
    "v_mfma_f32_16x16x32_bf16 v[68:71], a[248:251], a[224:227], v[68:71]\n"
    "v_mfma_f32_16x16x32_bf16 v[68:71], a[252:255], a[228:231], v[68:71]\n"
    "s_add_u32 s72, s81, 18432\n"
    "buffer_load_dwordx4 a[184:187], v246, s[16:19], s72 offen sc1\n"
    "v_mfma_f32_16x16x32_bf16 v[100:103], a[248:251], a[232:235], v[100:103]\n"
    "v_mfma_f32_16x16x32_bf16 v[100:103], a[252:255], a[236:239], v[100:103]\n"
    "s_waitcnt lgkmcnt(0)\n"
    "buffer_load_dwordx4 a[188:191], v246, s[16:19], s72 offen offset:1024 sc1\n"
    "v_mfma_f32_16x16x32_bf16 v[8:11], a[240:243], a[208:211], v[8:11]\n"
    "ds_read_b64_tr_b16 a[248:249], v240 offset:4104\n"
    "v_mfma_f32_16x16x32_bf16 v[8:11], a[244:247], a[212:215], v[8:11]\n"
    "ds_read_b64_tr_b16 a[250:251], v240 offset:4360\n"
    "v_mfma_f32_16x16x32_bf16 v[40:43], a[240:243], a[216:219], v[40:43]\n"
    "ds_read_b64_tr_b16 a[252:253], v240 offset:6152\n"
    "v_mfma_f32_16x16x32_bf16 v[40:43], a[244:247], a[220:223], v[40:43]\n"
    "ds_read_b64_tr_b16 a[254:255], v240 offset:6408\n"
    "v_mfma_f32_16x16x32_bf16 v[72:75], a[240:243], a[224:227], v[72:75]\n"
    "v_mfma_f32_16x16x32_bf16 v[72:75], a[244:247], a[228:231], v[72:75]\n"
    "s_add_u32 s72, s81, 36864\n"
    "buffer_load_dwordx4 a[192:195], v246, s[16:19], s72 offen sc1\n"
    "v_mfma_f32_16x16x32_bf16 v[104:107], a[240:243], a[232:235], v[104:107]\n"
    "v_mfma_f32_16x16x32_bf16 v[104:107], a[244:247], a[236:239], v[104:107]\n"
    "s_waitcnt lgkmcnt(0)\n"
    "buffer_load_dwordx4 a[196:199], v246, s[16:19], s72 offen offset:1024 sc1\n"
    "v_mfma_f32_16x16x32_bf16 v[12:15], a[248:251], a[208:211], v[12:15]\n"
    "ds_read_b64_tr_b16 a[240:241], v240 offset:8192\n"
    "v_mfma_f32_16x16x32_bf16 v[12:15], a[252:255], a[212:215], v[12:15]\n"
    "ds_read_b64_tr_b16 a[242:243], v240 offset:8448\n"
    "v_mfma_f32_16x16x32_bf16 v[44:47], a[248:251], a[216:219], v[44:47]\n"
    "ds_read_b64_tr_b16 a[244:245], v240 offset:10240\n"
    "v_mfma_f32_16x16x32_bf16 v[44:47], a[252:255], a[220:223], v[44:47]\n"
    "ds_read_b64_tr_b16 a[246:247], v240 offset:10496\n"
    "v_mfma_f32_16x16x32_bf16 v[76:79], a[248:251], a[224:227], v[76:79]\n"
    "v_mfma_f32_16x16x32_bf16 v[76:79], a[252:255], a[228:231], v[76:79]\n"
    "s_add_u32 s72, s81, 55296\n"
    "buffer_load_dwordx4 a[200:203], v246, s[16:19], s72 offen sc1\n"
    "v_mfma_f32_16x16x32_bf16 v[108:111], a[248:251], a[232:235], v[108:111]\n"
    "v_mfma_f32_16x16x32_bf16 v[108:111], a[252:255], a[236:239], v[108:111]\n"
    "s_waitcnt lgkmcnt(0)\n"
    "buffer_load_dwordx4 a[204:207], v246, s[16:19], s72 offen offset:1024 sc1\n"
    "v_mfma_f32_16x16x32_bf16 v[16:19], a[240:243], a[208:211], v[16:19]\n"
    "ds_read_b64_tr_b16 a[248:249], v240 offset:8200\n"
    "v_mfma_f32_16x16x32_bf16 v[16:19], a[244:247], a[212:215], v[16:19]\n"
    "ds_read_b64_tr_b16 a[250:251], v240 offset:8456\n"
    "v_mfma_f32_16x16x32_bf16 v[48:51], a[240:243], a[216:219], v[48:51]\n"
    "ds_read_b64_tr_b16 a[252:253], v240 offset:10248\n"
    "v_mfma_f32_16x16x32_bf16 v[48:51], a[244:247], a[220:223], v[48:51]\n"
    "ds_read_b64_tr_b16 a[254:255], v240 offset:10504\n"
    "v_mfma_f32_16x16x32_bf16 v[80:83], a[240:243], a[224:227], v[80:83]\n"
    "v_mfma_f32_16x16x32_bf16 v[80:83], a[244:247], a[228:231], v[80:83]\n"
    "v_mfma_f32_16x16x32_bf16 v[112:115], a[240:243], a[232:235], v[112:115]\n"
    "v_mfma_f32_16x16x32_bf16 v[112:115], a[244:247], a[236:239], v[112:115]\n"
    "s_waitcnt lgkmcnt(0)\n"
    "v_mfma_f32_16x16x32_bf16 v[20:23], a[248:251], a[208:211], v[20:23]\n"
    "s_lshr_b32 s10, s69, 1\n"
    "s_and_b32 s10, s10, 1\n"
    "ds_read_b64_tr_b16 a[240:241], v240 offset:12288\n"
    "v_mfma_f32_16x16x32_bf16 v[20:23], a[252:255], a[212:215], v[20:23]\n"
    "ds_read_b64_tr_b16 a[242:243], v240 offset:12544\n"
    "v_mfma_f32_16x16x32_bf16 v[52:55], a[248:251], a[216:219], v[52:55]\n"
    "ds_read_b64_tr_b16 a[244:245], v240 offset:14336\n"
    "v_mfma_f32_16x16x32_bf16 v[52:55], a[252:255], a[220:223], v[52:55]\n"
    "ds_read_b64_tr_b16 a[246:247], v240 offset:14592\n"
    "v_mfma_f32_16x16x32_bf16 v[84:87], a[248:251], a[224:227], v[84:87]\n"
    "s_cmp_eq_u32 s10, 0\n"
    "s_cselect_b32 s20, s44, s48\n"
    "s_cselect_b32 s21, s45, s49\n"
    "s_cselect_b32 s22, s46, s50\n"
    "v_mfma_f32_16x16x32_bf16 v[84:87], a[252:255], a[228:231], v[84:87]\n"
    "s_cmp_eq_u32 s10, 0\n"
    "s_cselect_b32 s24, s66, s64\n"
    "s_cselect_b32 s25, s67, s65\n"
    "s_and_b32 s25, s25, 0xffff\n"
    "v_mfma_f32_16x16x32_bf16 v[116:119], a[248:251], a[232:235], v[116:119]\n"
    "s_mul_i32 s4, s10, 589824\n"
    "s_sub_u32 s26, 5308416, s4\n"
    "s_lshl_b32 s4, s10, 14\n"
    "s_sub_u32 s13, 147456, s4\n"
    "v_mfma_f32_16x16x32_bf16 v[116:119], a[252:255], a[236:239], v[116:119]\n"
    "s_waitcnt lgkmcnt(0)\n"
    "s_add_u32 s28, s44, 41943040\n"
    "s_addc_u32 s29, s45, 0\n"
    "s_mov_b32 s30, 18874368\n"
    "v_mfma_f32_16x16x32_bf16 v[24:27], a[240:243], a[208:211], v[24:27]\n"
    "s_lshl_b32 s82, s68, 12\n"
    "s_mul_i32 s83, s68, 9216\n"
    "s_lshr_b32 s5, s69, 2\n"
    "ds_read_b64_tr_b16 a[248:249], v240 offset:12296\n"
    "v_mfma_f32_16x16x32_bf16 v[24:27], a[244:247], a[212:215], v[24:27]\n"
    "s_and_b32 s5, s5, 1\n"
    "s_lshl_b32 s6, s70, 1\n"
    "s_add_u32 s5, s5, s6\n"
    "ds_read_b64_tr_b16 a[250:251], v240 offset:12552\n"
    "v_mfma_f32_16x16x32_bf16 v[56:59], a[240:243], a[216:219], v[56:59]\n"
    "s_lshl_b32 s80, s5, 2\n"
    "s_add_u32 s80, s80, s68\n"
    "s_lshl_b32 s80, s80, 1\n"
    "ds_read_b64_tr_b16 a[252:253], v240 offset:14344\n"
    "v_mfma_f32_16x16x32_bf16 v[56:59], a[244:247], a[220:223], v[56:59]\n"
    "s_mul_i32 s80, s80, s13\n"
    "s_mul_i32 s81, s68, 18432\n"
    "s_mul_i32 s4, s75, 576\n"
    "ds_read_b64_tr_b16 a[254:255], v240 offset:14600\n"
    "v_mfma_f32_16x16x32_bf16 v[88:91], a[240:243], a[224:227], v[88:91]\n"
    "s_add_u32 s81, s81, s4\n"
    "s_lshr_b32 s6, s69, 3\n"
    "s_sub_u32 s4, 9, s10\n"
    "s_mul_i32 s12, s6, s4\n"
    "v_mfma_f32_16x16x32_bf16 v[88:91], a[244:247], a[228:231], v[88:91]\n"
    "s_and_b32 s11, s69, 7\n"
    "s_mul_i32 s11, s11, s10\n"
    "s_lshl_b32 s79, s70, 2\n"
    "s_add_u32 s79, s79, s68\n"
    "v_mfma_f32_16x16x32_bf16 v[120:123], a[240:243], a[232:235], v[120:123]\n"
    "s_lshl_b32 s79, s79, 19\n"
    "s_lshl_b32 s4, s69, 12\n"
    "s_add_u32 s79, s79, s4\n"
    "s_lshl_b32 s7, s69, 7\n"
    "v_mfma_f32_16x16x32_bf16 v[120:123], a[244:247], a[236:239], v[120:123]\n"
    "s_waitcnt lgkmcnt(0)\n"
    "s_lshl_b32 s65, s5, 1\n"
    "s_add_u32 s65, s65, s75\n"
    "s_lshl_b32 s65, s65, 1\n"
    "v_mfma_f32_16x16x32_bf16 v[28:31], a[248:251], a[208:211], v[28:31]\n"
    "s_add_u32 s65, s65, s10\n"
    "s_lshl_b32 s65, s65, 2\n"
    "s_add_u32 s65, s65, s6\n"
    "ds_read_b64_tr_b16 a[240:241], v241 offset:16384\n"
    "v_mfma_f32_16x16x32_bf16 v[28:31], a[252:255], a[212:215], v[28:31]\n"
    "s_lshl_b32 s4, s75, 12\n"
    "s_sub_u32 s66, s83, s4\n"
    "s_add_u32 s66, s66, 4096\n"
    "ds_read_b64_tr_b16 a[242:243], v241 offset:16640\n"
    "v_mfma_f32_16x16x32_bf16 v[60:63], a[248:251], a[216:219], v[60:63]\n"
    "s_lshl_b32 s4, s75, 9\n"
    "s_add_u32 s83, s83, s4\n"
    "ds_read_b64_tr_b16 a[244:245], v241 offset:18432\n"
    "v_mfma_f32_16x16x32_bf16 v[60:63], a[252:255], a[220:223], v[60:63]\n"
    "ds_read_b64_tr_b16 a[246:247], v241 offset:18688\n"
    "v_mfma_f32_16x16x32_bf16 v[92:95], a[248:251], a[224:227], v[92:95]\n"
    "v_add_u32 v240, 53248, v240\n"
    "v_mfma_f32_16x16x32_bf16 v[92:95], a[252:255], a[228:231], v[92:95]\n"
    "v_mfma_f32_16x16x32_bf16 v[124:127], a[248:251], a[232:235], v[124:127]\n"
    "v_mfma_f32_16x16x32_bf16 v[124:127], a[252:255], a[236:239], v[124:127]\n"
    "s_waitcnt lgkmcnt(0)\n"
    "v_mfma_f32_16x16x32_bf16 v[128:131], a[240:243], a[208:211], v[128:131]\n"
    "ds_read_b64_tr_b16 a[248:249], v241 offset:32768\n"
    "v_mfma_f32_16x16x32_bf16 v[128:131], a[244:247], a[212:215], v[128:131]\n"
    "ds_read_b64_tr_b16 a[250:251], v241 offset:33024\n"
    "v_mfma_f32_16x16x32_bf16 v[132:135], a[240:243], a[216:219], v[132:135]\n"
    "ds_read_b64_tr_b16 a[252:253], v241 offset:34816\n"
    "v_mfma_f32_16x16x32_bf16 v[132:135], a[244:247], a[220:223], v[132:135]\n"
    "ds_read_b64_tr_b16 a[254:255], v241 offset:35072\n"
    "v_mfma_f32_16x16x32_bf16 v[136:139], a[240:243], a[224:227], v[136:139]\n"
    "v_mfma_f32_16x16x32_bf16 v[136:139], a[244:247], a[228:231], v[136:139]\n"
    "s_add_u32 m0, s85, 32768\n"
    "v_mfma_f32_16x16x32_bf16 v[140:143], a[240:243], a[232:235], v[140:143]\n"
    "buffer_load_dwordx4 v247, s[36:39], s84 offen offset:0 nt lds\n"
    "v_mfma_f32_16x16x32_bf16 v[140:143], a[244:247], a[236:239], v[140:143]\n"
    "s_waitcnt lgkmcnt(0)\n"
    "v_mfma_f32_16x16x32_bf16 a[0:3], a[248:251], a[208:211], a[0:3]\n"
    "ds_read_b64_tr_b16 a[240:241], v242 offset:16384\n"
    "v_mfma_f32_16x16x32_bf16 a[0:3], a[252:255], a[212:215], a[0:3]\n"
    "ds_read_b64_tr_b16 a[242:243], v242 offset:16640\n"
    "v_mfma_f32_16x16x32_bf16 a[4:7], a[248:251], a[216:219], a[4:7]\n"
    "ds_read_b64_tr_b16 a[244:245], v242 offset:18432\n"
    "v_mfma_f32_16x16x32_bf16 a[4:7], a[252:255], a[220:223], a[4:7]\n"
    "ds_read_b64_tr_b16 a[246:247], v242 offset:18688\n"
    "v_mfma_f32_16x16x32_bf16 a[8:11], a[248:251], a[224:227], a[8:11]\n"
    "v_mfma_f32_16x16x32_bf16 a[8:11], a[252:255], a[228:231], a[8:11]\n"
    "v_mfma_f32_16x16x32_bf16 a[12:15], a[248:251], a[232:235], a[12:15]\n"
    "buffer_load_dwordx4 v248, s[36:39], s84 offen offset:1024 nt lds\n"
    "v_mfma_f32_16x16x32_bf16 a[12:15], a[252:255], a[236:239], a[12:15]\n"
    "s_waitcnt lgkmcnt(0)\n"
    "v_mfma_f32_16x16x32_bf16 v[144:147], a[240:243], a[208:211], v[144:147]\n"
    "ds_read_b64_tr_b16 a[248:249], v242 offset:32768\n"
    "v_mfma_f32_16x16x32_bf16 v[144:147], a[244:247], a[212:215], v[144:147]\n"
    "ds_read_b64_tr_b16 a[250:251], v242 offset:33024\n"
    "v_mfma_f32_16x16x32_bf16 v[148:151], a[240:243], a[216:219], v[148:151]\n"
    "ds_read_b64_tr_b16 a[252:253], v242 offset:34816\n"
    "v_mfma_f32_16x16x32_bf16 v[148:151], a[244:247], a[220:223], v[148:151]\n"
    "ds_read_b64_tr_b16 a[254:255], v242 offset:35072\n"
    "v_mfma_f32_16x16x32_bf16 v[152:155], a[240:243], a[224:227], v[152:155]\n"
    "v_mfma_f32_16x16x32_bf16 v[152:155], a[244:247], a[228:231], v[152:155]\n"
    "v_mfma_f32_16x16x32_bf16 v[156:159], a[240:243], a[232:235], v[156:159]\n"
    "buffer_load_dwordx4 v249, s[36:39], s84 offen offset:2048 nt lds\n"
    "v_mfma_f32_16x16x32_bf16 v[156:159], a[244:247], a[236:239], v[156:159]\n"
    "s_waitcnt lgkmcnt(0)\n"
    "s_waitcnt vmcnt(3)\n"
    "s_barrier\n"
    "v_mfma_f32_16x16x32_bf16 a[16:19], a[248:251], a[208:211], a[16:19]\n"
    "ds_read_b64_tr_b16 a[240:241], v254 offset:16384\n"
    "v_mfma_f32_16x16x32_bf16 a[16:19], a[252:255], a[212:215], a[16:19]\n"
    "ds_read_b64_tr_b16 a[242:243], v254 offset:16640\n"
    "v_mfma_f32_16x16x32_bf16 a[20:23], a[248:251], a[216:219], a[20:23]\n"
    "ds_read_b64_tr_b16 a[244:245], v254 offset:18432\n"
    "v_mfma_f32_16x16x32_bf16 a[20:23], a[252:255], a[220:223], a[20:23]\n"
    "ds_read_b64_tr_b16 a[246:247], v254 offset:18688\n"
    "v_mfma_f32_16x16x32_bf16 a[24:27], a[248:251], a[224:227], a[24:27]\n"
    "v_mfma_f32_16x16x32_bf16 a[24:27], a[252:255], a[228:231], a[24:27]\n"
    "v_mfma_f32_16x16x32_bf16 a[28:31], a[248:251], a[232:235], a[28:31]\n"
    "buffer_load_dwordx4 v250, s[36:39], s84 offen offset:3072 nt lds\n"
    "v_mfma_f32_16x16x32_bf16 a[28:31], a[252:255], a[236:239], a[28:31]\n"
    "s_waitcnt lgkmcnt(0)\n"
    "v_mfma_f32_16x16x32_bf16 v[128:131], a[240:243], a[176:179], v[128:131]\n"
    "ds_read_b64_tr_b16 a[248:249], v254 offset:32768\n"
    "v_mfma_f32_16x16x32_bf16 v[128:131], a[244:247], a[180:183], v[128:131]\n"
    "ds_read_b64_tr_b16 a[250:251], v254 offset:33024\n"
    "v_mfma_f32_16x16x32_bf16 v[132:135], a[240:243], a[184:187], v[132:135]\n"
    "ds_read_b64_tr_b16 a[252:253], v254 offset:34816\n"
    "v_mfma_f32_16x16x32_bf16 v[132:135], a[244:247], a[188:191], v[132:135]\n"
    "ds_read_b64_tr_b16 a[254:255], v254 offset:35072\n"
    "v_mfma_f32_16x16x32_bf16 v[136:139], a[240:243], a[192:195], v[136:139]\n"
    "v_mfma_f32_16x16x32_bf16 v[136:139], a[244:247], a[196:199], v[136:139]\n"
    "s_add_u32 m0, s85, 49152\n"
    "v_mfma_f32_16x16x32_bf16 v[140:143], a[240:243], a[200:203], v[140:143]\n"
    "buffer_load_dwordx4 v251, s[40:43], s84 offen offset:0 nt lds\n"
    "v_mfma_f32_16x16x32_bf16 v[140:143], a[244:247], a[204:207], v[140:143]\n"
    "s_waitcnt lgkmcnt(0)\n"
    "v_mfma_f32_16x16x32_bf16 a[0:3], a[248:251], a[176:179], a[0:3]\n"
    "s_mul_i32 s86, s69, 9\n"
    "s_add_u32 s86, s86, s75\n"
    "ds_read_b64_tr_b16 a[240:241], v255 offset:16384\n"
    "v_mfma_f32_16x16x32_bf16 a[0:3], a[252:255], a[180:183], a[0:3]\n"
    "s_lshl_b32 s86, s86, 9\n"
    "s_add_u32 s86, s86, s33\n"
    "ds_read_b64_tr_b16 a[242:243], v255 offset:16640\n"
    "v_mfma_f32_16x16x32_bf16 a[4:7], a[248:251], a[184:187], a[4:7]\n"
    "ds_read_b64_tr_b16 a[244:245], v255 offset:18432\n"
    "v_mfma_f32_16x16x32_bf16 a[4:7], a[252:255], a[188:191], a[4:7]\n"
    "ds_read_b64_tr_b16 a[246:247], v255 offset:18688\n"
    "v_mfma_f32_16x16x32_bf16 a[8:11], a[248:251], a[192:195], a[8:11]\n"
    "v_mfma_f32_16x16x32_bf16 a[8:11], a[252:255], a[196:199], a[8:11]\n"
    "v_mfma_f32_16x16x32_bf16 a[12:15], a[248:251], a[200:203], a[12:15]\n"
    "buffer_load_dwordx4 v251, s[40:43], s84 offen offset:2048 nt lds\n"
    "v_mfma_f32_16x16x32_bf16 a[12:15], a[252:255], a[204:207], a[12:15]\n"
    "s_waitcnt lgkmcnt(0)\n"
    "v_accvgpr_read_b32 v243, a0\n"
    "v_accvgpr_read_b32 v244, a1\n"
    "v_mfma_f32_16x16x32_bf16 v[144:147], a[240:243], a[176:179], v[144:147]\n"
    "ds_read_b64_tr_b16 a[248:249], v255 offset:32768\n"
    "v_accvgpr_read_b32 v245, a2\n"
    "v_mfma_f32_16x16x32_bf16 v[144:147], a[244:247], a[180:183], v[144:147]\n"
    "ds_read_b64_tr_b16 a[250:251], v255 offset:33024\n"
    "v_accvgpr_read_b32 v253, a3\n"
    "v_mfma_f32_16x16x32_bf16 v[148:151], a[240:243], a[184:187], v[148:151]\n"
    "ds_read_b64_tr_b16 a[252:253], v255 offset:34816\n"
    "v_mul_f32 v128, v128, v243\n"
    "v_mfma_f32_16x16x32_bf16 v[148:151], a[244:247], a[188:191], v[148:151]\n"
    "ds_read_b64_tr_b16 a[254:255], v255 offset:35072\n"
    "v_mul_f32 v129, v129, v244\n"
    "v_mfma_f32_16x16x32_bf16 v[152:155], a[240:243], a[192:195], v[152:155]\n"
    "v_mul_f32 v130, v130, v245\n"
    "v_mul_f32 v131, v131, v253\n"
    "v_mfma_f32_16x16x32_bf16 v[152:155], a[244:247], a[196:199], v[152:155]\n"
    "s_add_u32 m0, s85, 50176\n"
    "v_cvt_pk_bf16_f32 v128, v128, v129\n"
    "v_cvt_pk_bf16_f32 v129, v130, v131\n"
    "v_mfma_f32_16x16x32_bf16 v[156:159], a[240:243], a[200:203], v[156:159]\n"
    "buffer_load_dwordx4 v252, s[40:43], s84 offen offset:0 nt lds\n"
    "v_mfma_f32_16x16x32_bf16 v[156:159], a[244:247], a[204:207], v[156:159]\n"
    "s_waitcnt lgkmcnt(0)\n"
    "v_mfma_f32_16x16x32_bf16 a[16:19], a[248:251], a[176:179], a[16:19]\n"
    "ds_read_b64_tr_b16 a[240:241], v240 offset:0\n"
    "v_mfma_f32_16x16x32_bf16 a[16:19], a[252:255], a[180:183], a[16:19]\n"
    "ds_read_b64_tr_b16 a[242:243], v240 offset:256\n"
    "v_mfma_f32_16x16x32_bf16 a[20:23], a[248:251], a[184:187], a[20:23]\n"
    "ds_read_b64_tr_b16 a[244:245], v240 offset:2048\n"
    "v_mfma_f32_16x16x32_bf16 a[20:23], a[252:255], a[188:191], a[20:23]\n"
    "ds_read_b64_tr_b16 a[246:247], v240 offset:2304\n"
    "v_mfma_f32_16x16x32_bf16 a[24:27], a[248:251], a[192:195], a[24:27]\n"
    "v_mfma_f32_16x16x32_bf16 a[24:27], a[252:255], a[196:199], a[24:27]\n"
    "v_mfma_f32_16x16x32_bf16 a[28:31], a[248:251], a[200:203], a[28:31]\n"
    "buffer_load_dwordx4 v252, s[40:43], s84 offen offset:2048 nt lds\n"
    "v_mfma_f32_16x16x32_bf16 a[28:31], a[252:255], a[204:207], a[28:31]\n"
    "s_waitcnt lgkmcnt(0)\n"
    "v_accvgpr_read_b32 v243, a16\n"
    "v_accvgpr_read_b32 v244, a17\n"
    "v_mfma_f32_16x16x32_bf16 v[0:3], a[240:243], a[176:179], v[0:3]\n"
    "s_add_u32 s84, s84, 16384\n"
    "ds_read_b64_tr_b16 a[248:249], v240 offset:8\n"
    "v_accvgpr_read_b32 v245, a18\n"
    "v_mfma_f32_16x16x32_bf16 v[0:3], a[244:247], a[180:183], v[0:3]\n"
    "s_add_u32 s85, s85, 32768\n"
    "ds_read_b64_tr_b16 a[250:251], v240 offset:264\n"
    "v_accvgpr_read_b32 v253, a19\n"
    "v_mfma_f32_16x16x32_bf16 v[32:35], a[240:243], a[184:187], v[32:35]\n"
    "s_and_b32 s85, s85, 0x1ffff\n"
    "ds_read_b64_tr_b16 a[252:253], v240 offset:2056\n"
    "v_mul_f32 v144, v144, v243\n"
    "v_mfma_f32_16x16x32_bf16 v[32:35], a[244:247], a[188:191], v[32:35]\n"
    "ds_read_b64_tr_b16 a[254:255], v240 offset:2312\n"
    "v_mul_f32 v145, v145, v244\n"
    "v_mfma_f32_16x16x32_bf16 v[64:67], a[240:243], a[192:195], v[64:67]\n"
    "v_mul_f32 v146, v146, v245\n"
    "v_mul_f32 v147, v147, v253\n"
    "v_mfma_f32_16x16x32_bf16 v[64:67], a[244:247], a[196:199], v[64:67]\n"
    "v_cvt_pk_bf16_f32 v130, v144, v145\n"
    "v_cvt_pk_bf16_f32 v131, v146, v147\n"
    "v_mfma_f32_16x16x32_bf16 v[96:99], a[240:243], a[200:203], v[96:99]\n"
    "v_accvgpr_read_b32 v243, a4\n"
    "v_accvgpr_read_b32 v244, a5\n"
    "v_mfma_f32_16x16x32_bf16 v[96:99], a[244:247], a[204:207], v[96:99]\n"
    "s_waitcnt lgkmcnt(0)\n"
    "v_accvgpr_read_b32 v245, a6\n"
    "v_accvgpr_read_b32 v253, a7\n"
    "v_mfma_f32_16x16x32_bf16 v[4:7], a[248:251], a[176:179], v[4:7]\n"
    "ds_read_b64_tr_b16 a[240:241], v240 offset:4096\n"
    "v_mul_f32 v132, v132, v243\n"
    "v_mfma_f32_16x16x32_bf16 v[4:7], a[252:255], a[180:183], v[4:7]\n"
    "ds_read_b64_tr_b16 a[242:243], v240 offset:4352\n"
    "v_mul_f32 v133, v133, v244\n"
    "v_mfma_f32_16x16x32_bf16 v[36:39], a[248:251], a[184:187], v[36:39]\n"
    "ds_read_b64_tr_b16 a[244:245], v240 offset:6144\n"
    "v_mul_f32 v134, v134, v245\n"
    "v_mfma_f32_16x16x32_bf16 v[36:39], a[252:255], a[188:191], v[36:39]\n"
    "ds_read_b64_tr_b16 a[246:247], v240 offset:6400\n"
    "v_mul_f32 v135, v135, v253\n"
    "v_mfma_f32_16x16x32_bf16 v[68:71], a[248:251], a[192:195], v[68:71]\n"
    "v_cvt_pk_bf16_f32 v132, v132, v133\n"
    "v_cvt_pk_bf16_f32 v133, v134, v135\n"
    "v_mfma_f32_16x16x32_bf16 v[68:71], a[252:255], a[196:199], v[68:71]\n"
    "s_add_u32 s87, s86, 0\n"
    "buffer_store_dwordx4 v[128:131], v246, s[44:47], s87 offen offset:0 sc1\n"
    "v_mfma_f32_16x16x32_bf16 v[100:103], a[248:251], a[200:203], v[100:103]\n"
    "v_accvgpr_read_b32 v243, a20\n"
    "v_accvgpr_read_b32 v244, a21\n"
    "v_mfma_f32_16x16x32_bf16 v[100:103], a[252:255], a[204:207], v[100:103]\n"
    "s_waitcnt lgkmcnt(0)\n"
    "v_accvgpr_read_b32 v245, a22\n"
    "v_accvgpr_read_b32 v253, a23\n"
    "v_mfma_f32_16x16x32_bf16 v[8:11], a[240:243], a[176:179], v[8:11]\n"
    "ds_read_b64_tr_b16 a[248:249], v240 offset:4104\n"
    "v_mul_f32 v148, v148, v243\n"
    "v_mfma_f32_16x16x32_bf16 v[8:11], a[244:247], a[180:183], v[8:11]\n"
    "ds_read_b64_tr_b16 a[250:251], v240 offset:4360\n"
    "v_mul_f32 v149, v149, v244\n"
    "v_mfma_f32_16x16x32_bf16 v[40:43], a[240:243], a[184:187], v[40:43]\n"
    "ds_read_b64_tr_b16 a[252:253], v240 offset:6152\n"
    "v_mul_f32 v150, v150, v245\n"
    "v_mfma_f32_16x16x32_bf16 v[40:43], a[244:247], a[188:191], v[40:43]\n"
    "ds_read_b64_tr_b16 a[254:255], v240 offset:6408\n"
    "v_mul_f32 v151, v151, v253\n"
    "v_mfma_f32_16x16x32_bf16 v[72:75], a[240:243], a[192:195], v[72:75]\n"
    "v_cvt_pk_bf16_f32 v134, v148, v149\n"
    "v_cvt_pk_bf16_f32 v135, v150, v151\n"
    "v_mfma_f32_16x16x32_bf16 v[72:75], a[244:247], a[196:199], v[72:75]\n"
    "v_accvgpr_read_b32 v243, a8\n"
    "v_accvgpr_read_b32 v244, a9\n"
    "v_mfma_f32_16x16x32_bf16 v[104:107], a[240:243], a[200:203], v[104:107]\n"
    "v_accvgpr_read_b32 v245, a10\n"
    "v_accvgpr_read_b32 v253, a11\n"
    "v_mfma_f32_16x16x32_bf16 v[104:107], a[244:247], a[204:207], v[104:107]\n"
    "s_waitcnt lgkmcnt(0)\n"
    "v_mul_f32 v136, v136, v243\n"
    "v_mul_f32 v137, v137, v244\n"
    "v_mfma_f32_16x16x32_bf16 v[12:15], a[248:251], a[176:179], v[12:15]\n"
    "ds_read_b64_tr_b16 a[240:241], v240 offset:8192\n"
    "v_mul_f32 v138, v138, v245\n"
    "v_mfma_f32_16x16x32_bf16 v[12:15], a[252:255], a[180:183], v[12:15]\n"
    "ds_read_b64_tr_b16 a[242:243], v240 offset:8448\n"
    "v_mul_f32 v139, v139, v253\n"
    "v_mfma_f32_16x16x32_bf16 v[44:47], a[248:251], a[184:187], v[44:47]\n"
    "ds_read_b64_tr_b16 a[244:245], v240 offset:10240\n"
    "v_cvt_pk_bf16_f32 v136, v136, v137\n"
    "v_mfma_f32_16x16x32_bf16 v[44:47], a[252:255], a[188:191], v[44:47]\n"
    "ds_read_b64_tr_b16 a[246:247], v240 offset:10496\n"
    "v_cvt_pk_bf16_f32 v137, v138, v139\n"
    "v_mfma_f32_16x16x32_bf16 v[76:79], a[248:251], a[192:195], v[76:79]\n"
    "v_accvgpr_read_b32 v243, a24\n"
    "v_accvgpr_read_b32 v244, a25\n"
    "v_mfma_f32_16x16x32_bf16 v[76:79], a[252:255], a[196:199], v[76:79]\n"
    "s_add_u32 s87, s86, 147456\n"
    "buffer_store_dwordx4 v[132:135], v246, s[44:47], s87 offen offset:0 sc1\n"
    "v_mfma_f32_16x16x32_bf16 v[108:111], a[248:251], a[200:203], v[108:111]\n"
    "v_accvgpr_read_b32 v245, a26\n"
    "v_accvgpr_read_b32 v253, a27\n"
    "v_mfma_f32_16x16x32_bf16 v[108:111], a[252:255], a[204:207], v[108:111]\n"
    "s_waitcnt lgkmcnt(0)\n"
    "v_mul_f32 v152, v152, v243\n"
    "v_mul_f32 v153, v153, v244\n"
    "v_mfma_f32_16x16x32_bf16 v[16:19], a[240:243], a[176:179], v[16:19]\n"
    "ds_read_b64_tr_b16 a[248:249], v240 offset:8200\n"
    "v_mul_f32 v154, v154, v245\n"
    "v_mfma_f32_16x16x32_bf16 v[16:19], a[244:247], a[180:183], v[16:19]\n"
    "ds_read_b64_tr_b16 a[250:251], v240 offset:8456\n"
    "v_mul_f32 v155, v155, v253\n"
    "v_mfma_f32_16x16x32_bf16 v[48:51], a[240:243], a[184:187], v[48:51]\n"
    "ds_read_b64_tr_b16 a[252:253], v240 offset:10248\n"
    "v_cvt_pk_bf16_f32 v138, v152, v153\n"
    "v_mfma_f32_16x16x32_bf16 v[48:51], a[244:247], a[188:191], v[48:51]\n"
    "ds_read_b64_tr_b16 a[254:255], v240 offset:10504\n"
    "v_cvt_pk_bf16_f32 v139, v154, v155\n"
    "v_mfma_f32_16x16x32_bf16 v[80:83], a[240:243], a[192:195], v[80:83]\n"
    "v_accvgpr_read_b32 v243, a12\n"
    "v_accvgpr_read_b32 v244, a13\n"
    "v_mfma_f32_16x16x32_bf16 v[80:83], a[244:247], a[196:199], v[80:83]\n"
    "v_accvgpr_read_b32 v245, a14\n"
    "v_accvgpr_read_b32 v253, a15\n"
    "v_mfma_f32_16x16x32_bf16 v[112:115], a[240:243], a[200:203], v[112:115]\n"
    "v_mul_f32 v140, v140, v243\n"
    "v_mul_f32 v141, v141, v244\n"
    "v_mfma_f32_16x16x32_bf16 v[112:115], a[244:247], a[204:207], v[112:115]\n"
    "s_waitcnt lgkmcnt(0)\n"
    "v_mul_f32 v142, v142, v245\n"
    "v_mul_f32 v143, v143, v253\n"
    "v_mfma_f32_16x16x32_bf16 v[20:23], a[248:251], a[176:179], v[20:23]\n"
    "ds_read_b64_tr_b16 a[240:241], v240 offset:12288\n"
    "v_cvt_pk_bf16_f32 v140, v140, v141\n"
    "v_mfma_f32_16x16x32_bf16 v[20:23], a[252:255], a[180:183], v[20:23]\n"
    "ds_read_b64_tr_b16 a[242:243], v240 offset:12544\n"
    "v_cvt_pk_bf16_f32 v141, v142, v143\n"
    "v_mfma_f32_16x16x32_bf16 v[52:55], a[248:251], a[184:187], v[52:55]\n"
    "ds_read_b64_tr_b16 a[244:245], v240 offset:14336\n"
    "v_accvgpr_read_b32 v243, a28\n"
    "v_mfma_f32_16x16x32_bf16 v[52:55], a[252:255], a[188:191], v[52:55]\n"
    "ds_read_b64_tr_b16 a[246:247], v240 offset:14592\n"
    "v_accvgpr_read_b32 v244, a29\n"
    "v_mfma_f32_16x16x32_bf16 v[84:87], a[248:251], a[192:195], v[84:87]\n"
    "v_accvgpr_read_b32 v245, a30\n"
    "v_accvgpr_read_b32 v253, a31\n"
    "v_mfma_f32_16x16x32_bf16 v[84:87], a[252:255], a[196:199], v[84:87]\n"
    "s_add_u32 s87, s86, 294912\n"
    "buffer_store_dwordx4 v[136:139], v246, s[44:47], s87 offen offset:0 sc1\n"
    "v_mfma_f32_16x16x32_bf16 v[116:119], a[248:251], a[200:203], v[116:119]\n"
    "v_mul_f32 v156, v156, v243\n"
    "v_mul_f32 v157, v157, v244\n"
    "v_mfma_f32_16x16x32_bf16 v[116:119], a[252:255], a[204:207], v[116:119]\n"
    "s_waitcnt lgkmcnt(0)\n"
    "v_mul_f32 v158, v158, v245\n"
    "v_mul_f32 v159, v159, v253\n"
    "v_mfma_f32_16x16x32_bf16 v[24:27], a[240:243], a[176:179], v[24:27]\n"
    "ds_read_b64_tr_b16 a[248:249], v240 offset:12296\n"
    "v_cvt_pk_bf16_f32 v142, v156, v157\n"
    "v_mfma_f32_16x16x32_bf16 v[24:27], a[244:247], a[180:183], v[24:27]\n"
    "ds_read_b64_tr_b16 a[250:251], v240 offset:12552\n"
    "v_cvt_pk_bf16_f32 v143, v158, v159\n"
    "v_mfma_f32_16x16x32_bf16 v[56:59], a[240:243], a[184:187], v[56:59]\n"
    "ds_read_b64_tr_b16 a[252:253], v240 offset:14344\n"
    "v_cvt_pk_bf16_f32 v0, v0, v1\n"
    "v_mfma_f32_16x16x32_bf16 v[56:59], a[244:247], a[188:191], v[56:59]\n"
    "ds_read_b64_tr_b16 a[254:255], v240 offset:14600\n"
    "v_cvt_pk_bf16_f32 v1, v2, v3\n"
    "v_mfma_f32_16x16x32_bf16 v[88:91], a[240:243], a[192:195], v[88:91]\n"
    "v_cvt_pk_bf16_f32 v2, v4, v5\n"
    "v_cvt_pk_bf16_f32 v3, v6, v7\n"
    "v_mfma_f32_16x16x32_bf16 v[88:91], a[244:247], a[196:199], v[88:91]\n"
    "v_cvt_pk_bf16_f32 v32, v32, v33\n"
    "v_cvt_pk_bf16_f32 v33, v34, v35\n"
    "v_mfma_f32_16x16x32_bf16 v[120:123], a[240:243], a[200:203], v[120:123]\n"
    "v_cvt_pk_bf16_f32 v34, v36, v37\n"
    "v_cvt_pk_bf16_f32 v35, v38, v39\n"
    "v_mfma_f32_16x16x32_bf16 v[120:123], a[244:247], a[204:207], v[120:123]\n"
    "s_waitcnt lgkmcnt(0)\n"
    "v_cvt_pk_bf16_f32 v64, v64, v65\n"
    "v_cvt_pk_bf16_f32 v65, v66, v67\n"
    "v_mfma_f32_16x16x32_bf16 v[28:31], a[248:251], a[176:179], v[28:31]\n"
    "ds_read_b64_tr_b16 a[240:241], v241 offset:20480\n"
    "v_cvt_pk_bf16_f32 v66, v68, v69\n"
    "v_mfma_f32_16x16x32_bf16 v[28:31], a[252:255], a[180:183], v[28:31]\n"
    "ds_read_b64_tr_b16 a[242:243], v241 offset:20736\n"
    "v_cvt_pk_bf16_f32 v67, v70, v71\n"
    "v_mfma_f32_16x16x32_bf16 v[60:63], a[248:251], a[184:187], v[60:63]\n"
    "ds_read_b64_tr_b16 a[244:245], v241 offset:22528\n"
    "v_cvt_pk_bf16_f32 v96, v96, v97\n"
    "v_mfma_f32_16x16x32_bf16 v[60:63], a[252:255], a[188:191], v[60:63]\n"
    "ds_read_b64_tr_b16 a[246:247], v241 offset:22784\n"
    "v_cvt_pk_bf16_f32 v97, v98, v99\n"
    "v_mfma_f32_16x16x32_bf16 v[92:95], a[248:251], a[192:195], v[92:95]\n"
    "v_cvt_pk_bf16_f32 v98, v100, v101\n"
    "v_cvt_pk_bf16_f32 v99, v102, v103\n"
    "v_mfma_f32_16x16x32_bf16 v[92:95], a[252:255], a[196:199], v[92:95]\n"
    "s_add_u32 s87, s86, 442368\n"
    "buffer_store_dwordx4 v[140:143], v246, s[44:47], s87 offen offset:0 sc1\n"
    "v_mfma_f32_16x16x32_bf16 v[124:127], a[248:251], a[200:203], v[124:127]\n"
    "v_cvt_pk_bf16_f32 v8, v8, v9\n"
    "v_cvt_pk_bf16_f32 v9, v10, v11\n"
    "v_mfma_f32_16x16x32_bf16 v[124:127], a[252:255], a[204:207], v[124:127]\n"
    "s_waitcnt lgkmcnt(0)\n"
    "v_cvt_pk_bf16_f32 v10, v12, v13\n"
    "v_cvt_pk_bf16_f32 v11, v14, v15\n"
    "v_mfma_f32_16x16x32_bf16 v[160:163], a[240:243], a[208:211], v[160:163]\n"
    "ds_read_b64_tr_b16 a[248:249], v254 offset:20480\n"
    "v_cvt_pk_bf16_f32 v40, v40, v41\n"
    "v_mfma_f32_16x16x32_bf16 v[160:163], a[244:247], a[212:215], v[160:163]\n"
    "ds_read_b64_tr_b16 a[250:251], v254 offset:20736\n"
    "v_cvt_pk_bf16_f32 v41, v42, v43\n"
    "v_mfma_f32_16x16x32_bf16 v[164:167], a[240:243], a[216:219], v[164:167]\n"
    "ds_read_b64_tr_b16 a[252:253], v254 offset:22528\n"
    "v_cvt_pk_bf16_f32 v42, v44, v45\n"
    "v_mfma_f32_16x16x32_bf16 v[164:167], a[244:247], a[220:223], v[164:167]\n"
    "ds_read_b64_tr_b16 a[254:255], v254 offset:22784\n"
    "v_cvt_pk_bf16_f32 v43, v46, v47\n"
    "v_mfma_f32_16x16x32_bf16 v[168:171], a[240:243], a[224:227], v[168:171]\n"
    "v_cvt_pk_bf16_f32 v72, v72, v73\n"
    "v_cvt_pk_bf16_f32 v73, v74, v75\n"
    "v_mfma_f32_16x16x32_bf16 v[168:171], a[244:247], a[228:231], v[168:171]\n"
    "v_cvt_pk_bf16_f32 v74, v76, v77\n"
    "v_cvt_pk_bf16_f32 v75, v78, v79\n"
    "v_mfma_f32_16x16x32_bf16 v[172:175], a[240:243], a[232:235], v[172:175]\n"
    "v_cvt_pk_bf16_f32 v104, v104, v105\n"
    "v_cvt_pk_bf16_f32 v105, v106, v107\n"
    "v_mfma_f32_16x16x32_bf16 v[172:175], a[244:247], a[236:239], v[172:175]\n"
    "s_waitcnt lgkmcnt(0)\n"
    "v_cvt_pk_bf16_f32 v106, v108, v109\n"
    "v_cvt_pk_bf16_f32 v107, v110, v111\n"
    "v_mfma_f32_16x16x32_bf16 v[160:163], a[248:251], a[176:179], v[160:163]\n"
    "ds_read_b64_tr_b16 a[240:241], v241 offset:36864\n"
    "v_cvt_pk_bf16_f32 v16, v16, v17\n"
    "v_mfma_f32_16x16x32_bf16 v[160:163], a[252:255], a[180:183], v[160:163]\n"
    "ds_read_b64_tr_b16 a[242:243], v241 offset:37120\n"
    "v_cvt_pk_bf16_f32 v17, v18, v19\n"
    "v_mfma_f32_16x16x32_bf16 v[164:167], a[248:251], a[184:187], v[164:167]\n"
    "ds_read_b64_tr_b16 a[244:245], v241 offset:38912\n"
    "v_cvt_pk_bf16_f32 v18, v20, v21\n"
    "v_mfma_f32_16x16x32_bf16 v[164:167], a[252:255], a[188:191], v[164:167]\n"
    "ds_read_b64_tr_b16 a[246:247], v241 offset:39168\n"
    "v_cvt_pk_bf16_f32 v19, v22, v23\n"
    "v_mfma_f32_16x16x32_bf16 v[168:171], a[248:251], a[192:195], v[168:171]\n"
    "v_cvt_pk_bf16_f32 v48, v48, v49\n"
    "v_cvt_pk_bf16_f32 v49, v50, v51\n"
    "v_mfma_f32_16x16x32_bf16 v[168:171], a[252:255], a[196:199], v[168:171]\n"
    "v_cvt_pk_bf16_f32 v50, v52, v53\n"
    "v_cvt_pk_bf16_f32 v51, v54, v55\n"
    "v_mfma_f32_16x16x32_bf16 v[172:175], a[248:251], a[200:203], v[172:175]\n"
    "v_cvt_pk_bf16_f32 v80, v80, v81\n"
    "v_cvt_pk_bf16_f32 v81, v82, v83\n"
    "v_mfma_f32_16x16x32_bf16 v[172:175], a[252:255], a[204:207], v[172:175]\n"
    "s_waitcnt lgkmcnt(0)\n"
    "v_cvt_pk_bf16_f32 v82, v84, v85\n"
    "v_cvt_pk_bf16_f32 v83, v86, v87\n"
    "v_mfma_f32_16x16x32_bf16 a[32:35], a[240:243], a[208:211], a[32:35]\n"
    "ds_read_b64_tr_b16 a[248:249], v254 offset:36864\n"
    "v_cvt_pk_bf16_f32 v112, v112, v113\n"
    "v_mfma_f32_16x16x32_bf16 a[32:35], a[244:247], a[212:215], a[32:35]\n"
    "ds_read_b64_tr_b16 a[250:251], v254 offset:37120\n"
    "v_cvt_pk_bf16_f32 v113, v114, v115\n"
    "v_mfma_f32_16x16x32_bf16 a[36:39], a[240:243], a[216:219], a[36:39]\n"
    "ds_read_b64_tr_b16 a[252:253], v254 offset:38912\n"
    "v_cvt_pk_bf16_f32 v114, v116, v117\n"
    "v_mfma_f32_16x16x32_bf16 a[36:39], a[244:247], a[220:223], a[36:39]\n"
    "ds_read_b64_tr_b16 a[254:255], v254 offset:39168\n"
    "v_cvt_pk_bf16_f32 v115, v118, v119\n"
    "v_mfma_f32_16x16x32_bf16 a[40:43], a[240:243], a[224:227], a[40:43]\n"
    "v_cvt_pk_bf16_f32 v24, v24, v25\n"
    "v_cvt_pk_bf16_f32 v25, v26, v27\n"
    "v_mfma_f32_16x16x32_bf16 a[40:43], a[244:247], a[228:231], a[40:43]\n"
    "v_cvt_pk_bf16_f32 v26, v28, v29\n"
    "v_cvt_pk_bf16_f32 v27, v30, v31\n"
    "v_mfma_f32_16x16x32_bf16 a[44:47], a[240:243], a[232:235], a[44:47]\n"
    "v_cvt_pk_bf16_f32 v56, v56, v57\n"
    "v_cvt_pk_bf16_f32 v57, v58, v59\n"
    "v_mfma_f32_16x16x32_bf16 a[44:47], a[244:247], a[236:239], a[44:47]\n"
    "s_waitcnt lgkmcnt(0)\n"
    "v_cvt_pk_bf16_f32 v58, v60, v61\n"
    "v_cvt_pk_bf16_f32 v59, v62, v63\n"
    "v_mfma_f32_16x16x32_bf16 a[32:35], a[248:251], a[176:179], a[32:35]\n"
    "ds_read_b64_tr_b16 a[240:241], v242 offset:20480\n"
    "v_cvt_pk_bf16_f32 v88, v88, v89\n"
    "v_mfma_f32_16x16x32_bf16 a[32:35], a[252:255], a[180:183], a[32:35]\n"
    "ds_read_b64_tr_b16 a[242:243], v242 offset:20736\n"
    "v_cvt_pk_bf16_f32 v89, v90, v91\n"
    "v_mfma_f32_16x16x32_bf16 a[36:39], a[248:251], a[184:187], a[36:39]\n"
    "ds_read_b64_tr_b16 a[244:245], v242 offset:22528\n"
    "v_cvt_pk_bf16_f32 v90, v92, v93\n"
    "v_mfma_f32_16x16x32_bf16 a[36:39], a[252:255], a[188:191], a[36:39]\n"
    "ds_read_b64_tr_b16 a[246:247], v242 offset:22784\n"
    "v_cvt_pk_bf16_f32 v91, v94, v95\n"
    "v_mfma_f32_16x16x32_bf16 a[40:43], a[248:251], a[192:195], a[40:43]\n"
    "v_cvt_pk_bf16_f32 v120, v120, v121\n"
    "v_cvt_pk_bf16_f32 v121, v122, v123\n"
    "v_mfma_f32_16x16x32_bf16 a[40:43], a[252:255], a[196:199], a[40:43]\n"
    "v_cvt_pk_bf16_f32 v122, v124, v125\n"
    "v_cvt_pk_bf16_f32 v123, v126, v127\n"
    "v_mfma_f32_16x16x32_bf16 a[44:47], a[248:251], a[200:203], a[44:47]\n"
    "v_accvgpr_write_b32 a0, v0\n"
    "v_accvgpr_write_b32 a1, v1\n"
    "v_mfma_f32_16x16x32_bf16 a[44:47], a[252:255], a[204:207], a[44:47]\n"
    "s_waitcnt lgkmcnt(0)\n"
    "v_accvgpr_write_b32 a2, v2\n"
    "v_accvgpr_write_b32 a3, v3\n"
    "v_mfma_f32_16x16x32_bf16 v[176:179], a[240:243], a[208:211], v[176:179]\n"
    "ds_read_b64_tr_b16 a[248:249], v255 offset:20480\n"
    "v_accvgpr_write_b32 a4, v8\n"
    "v_mfma_f32_16x16x32_bf16 v[176:179], a[244:247], a[212:215], v[176:179]\n"
    "ds_read_b64_tr_b16 a[250:251], v255 offset:20736\n"
    "v_accvgpr_write_b32 a5, v9\n"
    "v_mfma_f32_16x16x32_bf16 v[180:183], a[240:243], a[216:219], v[180:183]\n"
    "ds_read_b64_tr_b16 a[252:253], v255 offset:22528\n"
    "v_accvgpr_write_b32 a6, v10\n"
    "v_mfma_f32_16x16x32_bf16 v[180:183], a[244:247], a[220:223], v[180:183]\n"
    "ds_read_b64_tr_b16 a[254:255], v255 offset:22784\n"
    "v_accvgpr_write_b32 a7, v11\n"
    "v_mfma_f32_16x16x32_bf16 v[184:187], a[240:243], a[224:227], v[184:187]\n"
    "v_accvgpr_write_b32 a8, v16\n"
    "v_accvgpr_write_b32 a9, v17\n"
    "v_mfma_f32_16x16x32_bf16 v[184:187], a[244:247], a[228:231], v[184:187]\n"
    "v_accvgpr_write_b32 a10, v18\n"
    "v_accvgpr_write_b32 a11, v19\n"
    "v_mfma_f32_16x16x32_bf16 v[188:191], a[240:243], a[232:235], v[188:191]\n"
    "v_accvgpr_write_b32 a12, v24\n"
    "v_accvgpr_write_b32 a13, v25\n"
    "v_mfma_f32_16x16x32_bf16 v[188:191], a[244:247], a[236:239], v[188:191]\n"
    "s_waitcnt lgkmcnt(0)\n"
    "v_accvgpr_write_b32 a14, v26\n"
    "v_accvgpr_write_b32 a15, v27\n"
    "v_mfma_f32_16x16x32_bf16 v[176:179], a[248:251], a[176:179], v[176:179]\n"
    "ds_read_b64_tr_b16 a[240:241], v242 offset:36864\n"
    "v_accvgpr_write_b32 a16, v32\n"
    "v_mfma_f32_16x16x32_bf16 v[176:179], a[252:255], a[180:183], v[176:179]\n"
    "ds_read_b64_tr_b16 a[242:243], v242 offset:37120\n"
    "v_accvgpr_write_b32 a17, v33\n"
    "v_mfma_f32_16x16x32_bf16 v[180:183], a[248:251], a[184:187], v[180:183]\n"
    "ds_read_b64_tr_b16 a[244:245], v242 offset:38912\n"
    "v_accvgpr_write_b32 a18, v34\n"
    "v_mfma_f32_16x16x32_bf16 v[180:183], a[252:255], a[188:191], v[180:183]\n"
    "ds_read_b64_tr_b16 a[246:247], v242 offset:39168\n"
    "v_accvgpr_write_b32 a19, v35\n"
    "v_mfma_f32_16x16x32_bf16 v[184:187], a[248:251], a[192:195], v[184:187]\n"
    "v_accvgpr_write_b32 a20, v40\n"
    "v_accvgpr_write_b32 a21, v41\n"
    "v_mfma_f32_16x16x32_bf16 v[184:187], a[252:255], a[196:199], v[184:187]\n"
    "v_accvgpr_write_b32 a22, v42\n"
    "v_accvgpr_write_b32 a23, v43\n"
    "v_mfma_f32_16x16x32_bf16 v[188:191], a[248:251], a[200:203], v[188:191]\n"
    "v_accvgpr_write_b32 a24, v48\n"
    "v_accvgpr_write_b32 a25, v49\n"
    "v_mfma_f32_16x16x32_bf16 v[188:191], a[252:255], a[204:207], v[188:191]\n"
    "s_waitcnt lgkmcnt(0)\n"
    "v_accvgpr_write_b32 a26, v50\n"
    "v_accvgpr_write_b32 a27, v51\n"
    "v_mfma_f32_16x16x32_bf16 a[48:51], a[240:243], a[208:211], a[48:51]\n"
    "ds_read_b64_tr_b16 a[248:249], v255 offset:36864\n"
    "v_accvgpr_write_b32 a28, v56\n"
    "v_mfma_f32_16x16x32_bf16 a[48:51], a[244:247], a[212:215], a[48:51]\n"
    "ds_read_b64_tr_b16 a[250:251], v255 offset:37120\n"
    "v_accvgpr_write_b32 a29, v57\n"
    "v_mfma_f32_16x16x32_bf16 a[52:55], a[240:243], a[216:219], a[52:55]\n"
    "ds_read_b64_tr_b16 a[252:253], v255 offset:38912\n"
    "v_accvgpr_write_b32 a30, v58\n"
    "v_mfma_f32_16x16x32_bf16 a[52:55], a[244:247], a[220:223], a[52:55]\n"
    "ds_read_b64_tr_b16 a[254:255], v255 offset:39168\n"
    "v_accvgpr_write_b32 a31, v59\n"
    "v_mfma_f32_16x16x32_bf16 a[56:59], a[240:243], a[224:227], a[56:59]\n"
    "v_accvgpr_read_b32 v243, a32\n"
    "v_accvgpr_read_b32 v244, a33\n"
    "v_mfma_f32_16x16x32_bf16 a[56:59], a[244:247], a[228:231], a[56:59]\n"
    "v_accvgpr_read_b32 v245, a34\n"
    "v_accvgpr_read_b32 v253, a35\n"
    "v_mfma_f32_16x16x32_bf16 a[60:63], a[240:243], a[232:235], a[60:63]\n"
    "v_mul_f32 v160, v160, v243\n"
    "v_mul_f32 v161, v161, v244\n"
    "v_mfma_f32_16x16x32_bf16 a[60:63], a[244:247], a[236:239], a[60:63]\n"
    "s_waitcnt lgkmcnt(0)\n"
    "v_mul_f32 v162, v162, v245\n"
    "v_mul_f32 v163, v163, v253\n"
    "v_mfma_f32_16x16x32_bf16 a[48:51], a[248:251], a[176:179], a[48:51]\n"
    "ds_read_b64_tr_b16 a[240:241], v241 offset:24576\n"
    "v_cvt_pk_bf16_f32 v160, v160, v161\n"
    "v_mfma_f32_16x16x32_bf16 a[48:51], a[252:255], a[180:183], a[48:51]\n"
    "ds_read_b64_tr_b16 a[242:243], v241 offset:24832\n"
    "v_cvt_pk_bf16_f32 v161, v162, v163\n"
    "v_mfma_f32_16x16x32_bf16 a[52:55], a[248:251], a[184:187], a[52:55]\n"
    "ds_read_b64_tr_b16 a[244:245], v241 offset:26624\n"
    "v_mfma_f32_16x16x32_bf16 a[52:55], a[252:255], a[188:191], a[52:55]\n"
    "ds_read_b64_tr_b16 a[246:247], v241 offset:26880\n"
    "v_mfma_f32_16x16x32_bf16 a[56:59], a[248:251], a[192:195], a[56:59]\n"
    "v_mfma_f32_16x16x32_bf16 a[56:59], a[252:255], a[196:199], a[56:59]\n"
    "v_mfma_f32_16x16x32_bf16 a[60:63], a[248:251], a[200:203], a[60:63]\n"
    "v_accvgpr_read_b32 v243, a48\n"
    "v_accvgpr_read_b32 v244, a49\n"
    "v_mfma_f32_16x16x32_bf16 a[60:63], a[252:255], a[204:207], a[60:63]\n"
    "s_waitcnt lgkmcnt(0)\n"
    "v_accvgpr_read_b32 v245, a50\n"
    "v_accvgpr_read_b32 v253, a51\n"
    "v_mfma_f32_16x16x32_bf16 v[192:195], a[240:243], a[208:211], v[192:195]\n"
    "ds_read_b64_tr_b16 a[248:249], v254 offset:24576\n"
    "v_mul_f32 v176, v176, v243\n"
    "v_mfma_f32_16x16x32_bf16 v[192:195], a[244:247], a[212:215], v[192:195]\n"
    "ds_read_b64_tr_b16 a[250:251], v254 offset:24832\n"
    "v_mul_f32 v177, v177, v244\n"
    "v_mfma_f32_16x16x32_bf16 v[196:199], a[240:243], a[216:219], v[196:199]\n"
    "ds_read_b64_tr_b16 a[252:253], v254 offset:26624\n"
    "v_mul_f32 v178, v178, v245\n"
    "v_mfma_f32_16x16x32_bf16 v[196:199], a[244:247], a[220:223], v[196:199]\n"
    "ds_read_b64_tr_b16 a[254:255], v254 offset:26880\n"
    "v_mul_f32 v179, v179, v253\n"
    "v_mfma_f32_16x16x32_bf16 v[200:203], a[240:243], a[224:227], v[200:203]\n"
    "v_cvt_pk_bf16_f32 v162, v176, v177\n"
    "v_cvt_pk_bf16_f32 v163, v178, v179\n"
    "v_mfma_f32_16x16x32_bf16 v[200:203], a[244:247], a[228:231], v[200:203]\n"
    "v_accvgpr_read_b32 v243, a36\n"
    "v_accvgpr_read_b32 v244, a37\n"
    "v_mfma_f32_16x16x32_bf16 v[204:207], a[240:243], a[232:235], v[204:207]\n"
    "v_accvgpr_read_b32 v245, a38\n"
    "v_accvgpr_read_b32 v253, a39\n"
    "v_mfma_f32_16x16x32_bf16 v[204:207], a[244:247], a[236:239], v[204:207]\n"
    "s_waitcnt lgkmcnt(0)\n"
    "v_mul_f32 v164, v164, v243\n"
    "v_mul_f32 v165, v165, v244\n"
    "v_mfma_f32_16x16x32_bf16 v[192:195], a[248:251], a[176:179], v[192:195]\n"
    "ds_read_b64_tr_b16 a[240:241], v241 offset:40960\n"
    "v_mul_f32 v166, v166, v245\n"
    "v_mfma_f32_16x16x32_bf16 v[192:195], a[252:255], a[180:183], v[192:195]\n"
    "ds_read_b64_tr_b16 a[242:243], v241 offset:41216\n"
    "v_mul_f32 v167, v167, v253\n"
    "v_mfma_f32_16x16x32_bf16 v[196:199], a[248:251], a[184:187], v[196:199]\n"
    "ds_read_b64_tr_b16 a[244:245], v241 offset:43008\n"
    "v_cvt_pk_bf16_f32 v164, v164, v165\n"
    "v_mfma_f32_16x16x32_bf16 v[196:199], a[252:255], a[188:191], v[196:199]\n"
    "ds_read_b64_tr_b16 a[246:247], v241 offset:43264\n"
    "v_cvt_pk_bf16_f32 v165, v166, v167\n"
    "v_mfma_f32_16x16x32_bf16 v[200:203], a[248:251], a[192:195], v[200:203]\n"
    "s_add_u32 s87, s86, 0\n"
    "buffer_store_dwordx4 v[160:163], v246, s[44:47], s87 offen offset:1024 sc1\n"
    "v_mfma_f32_16x16x32_bf16 v[200:203], a[252:255], a[196:199], v[200:203]\n"
    "v_accvgpr_read_b32 v243, a52\n"
    "v_accvgpr_read_b32 v244, a53\n"
    "v_mfma_f32_16x16x32_bf16 v[204:207], a[248:251], a[200:203], v[204:207]\n"
    "v_accvgpr_read_b32 v245, a54\n"
    "v_accvgpr_read_b32 v253, a55\n"
    "v_mfma_f32_16x16x32_bf16 v[204:207], a[252:255], a[204:207], v[204:207]\n"
    "s_waitcnt lgkmcnt(0)\n"
    "v_mul_f32 v180, v180, v243\n"
    "v_mul_f32 v181, v181, v244\n"
    "v_mfma_f32_16x16x32_bf16 a[64:67], a[240:243], a[208:211], a[64:67]\n"
    "ds_read_b64_tr_b16 a[248:249], v254 offset:40960\n"
    "v_mul_f32 v182, v182, v245\n"
    "v_mfma_f32_16x16x32_bf16 a[64:67], a[244:247], a[212:215], a[64:67]\n"
    "ds_read_b64_tr_b16 a[250:251], v254 offset:41216\n"
    "v_mul_f32 v183, v183, v253\n"
    "v_mfma_f32_16x16x32_bf16 a[68:71], a[240:243], a[216:219], a[68:71]\n"
    "ds_read_b64_tr_b16 a[252:253], v254 offset:43008\n"
    "v_cvt_pk_bf16_f32 v166, v180, v181\n"
    "v_mfma_f32_16x16x32_bf16 a[68:71], a[244:247], a[220:223], a[68:71]\n"
    "ds_read_b64_tr_b16 a[254:255], v254 offset:43264\n"
    "v_cvt_pk_bf16_f32 v167, v182, v183\n"
    "v_mfma_f32_16x16x32_bf16 a[72:75], a[240:243], a[224:227], a[72:75]\n"
    "v_accvgpr_read_b32 v243, a40\n"
    "v_accvgpr_read_b32 v244, a41\n"
    "v_mfma_f32_16x16x32_bf16 a[72:75], a[244:247], a[228:231], a[72:75]\n"
    "v_accvgpr_read_b32 v245, a42\n"
    "v_accvgpr_read_b32 v253, a43\n"
    "v_mfma_f32_16x16x32_bf16 a[76:79], a[240:243], a[232:235], a[76:79]\n"
    "v_mul_f32 v168, v168, v243\n"
    "v_mul_f32 v169, v169, v244\n"
    "v_mfma_f32_16x16x32_bf16 a[76:79], a[244:247], a[236:239], a[76:79]\n"
    "s_waitcnt lgkmcnt(0)\n"
    "v_mul_f32 v170, v170, v245\n"
    "v_mul_f32 v171, v171, v253\n"
    "v_mfma_f32_16x16x32_bf16 a[64:67], a[248:251], a[176:179], a[64:67]\n"
    "ds_read_b64_tr_b16 a[240:241], v242 offset:24576\n"
    "v_cvt_pk_bf16_f32 v168, v168, v169\n"
    "v_mfma_f32_16x16x32_bf16 a[64:67], a[252:255], a[180:183], a[64:67]\n"
    "ds_read_b64_tr_b16 a[242:243], v242 offset:24832\n"
    "v_cvt_pk_bf16_f32 v169, v170, v171\n"
    "v_mfma_f32_16x16x32_bf16 a[68:71], a[248:251], a[184:187], a[68:71]\n"
    "ds_read_b64_tr_b16 a[244:245], v242 offset:26624\n"
    "v_accvgpr_read_b32 v243, a56\n"
    "v_mfma_f32_16x16x32_bf16 a[68:71], a[252:255], a[188:191], a[68:71]\n"
    "ds_read_b64_tr_b16 a[246:247], v242 offset:26880\n"
    "v_accvgpr_read_b32 v244, a57\n"
    "v_mfma_f32_16x16x32_bf16 a[72:75], a[248:251], a[192:195], a[72:75]\n"
    "s_add_u32 s87, s86, 147456\n"
    "buffer_store_dwordx4 v[164:167], v246, s[44:47], s87 offen offset:1024 sc1\n"
    "v_mfma_f32_16x16x32_bf16 a[72:75], a[252:255], a[196:199], a[72:75]\n"
    "v_accvgpr_read_b32 v245, a58\n"
    "v_accvgpr_read_b32 v253, a59\n"
    "v_mfma_f32_16x16x32_bf16 a[76:79], a[248:251], a[200:203], a[76:79]\n"
    "v_mul_f32 v184, v184, v243\n"
    "v_mul_f32 v185, v185, v244\n"
    "v_mfma_f32_16x16x32_bf16 a[76:79], a[252:255], a[204:207], a[76:79]\n"
    "s_waitcnt lgkmcnt(0)\n"
    "v_mul_f32 v186, v186, v245\n"
    "v_mul_f32 v187, v187, v253\n"
    "v_mfma_f32_16x16x32_bf16 v[208:211], a[240:243], a[208:211], v[208:211]\n"
    "ds_read_b64_tr_b16 a[248:249], v255 offset:24576\n"
    "v_cvt_pk_bf16_f32 v170, v184, v185\n"
    "v_mfma_f32_16x16x32_bf16 v[208:211], a[244:247], a[212:215], v[208:211]\n"
    "ds_read_b64_tr_b16 a[250:251], v255 offset:24832\n"
    "v_cvt_pk_bf16_f32 v171, v186, v187\n"
    "v_mfma_f32_16x16x32_bf16 v[212:215], a[240:243], a[216:219], v[212:215]\n"
    "ds_read_b64_tr_b16 a[252:253], v255 offset:26624\n"
    "v_accvgpr_read_b32 v243, a44\n"
    "v_mfma_f32_16x16x32_bf16 v[212:215], a[244:247], a[220:223], v[212:215]\n"
    "ds_read_b64_tr_b16 a[254:255], v255 offset:26880\n"
    "v_accvgpr_read_b32 v244, a45\n"
    "v_mfma_f32_16x16x32_bf16 v[216:219], a[240:243], a[224:227], v[216:219]\n"
    "v_accvgpr_read_b32 v245, a46\n"
    "v_accvgpr_read_b32 v253, a47\n"
    "v_mfma_f32_16x16x32_bf16 v[216:219], a[244:247], a[228:231], v[216:219]\n"
    "v_mul_f32 v172, v172, v243\n"
    "v_mul_f32 v173, v173, v244\n"
    "v_mfma_f32_16x16x32_bf16 v[220:223], a[240:243], a[232:235], v[220:223]\n"
    "v_mul_f32 v174, v174, v245\n"
    "v_mul_f32 v175, v175, v253\n"
    "v_mfma_f32_16x16x32_bf16 v[220:223], a[244:247], a[236:239], v[220:223]\n"
    "s_waitcnt lgkmcnt(0)\n"
    "v_cvt_pk_bf16_f32 v172, v172, v173\n"
    "v_cvt_pk_bf16_f32 v173, v174, v175\n"
    "v_mfma_f32_16x16x32_bf16 v[208:211], a[248:251], a[176:179], v[208:211]\n"
    "ds_read_b64_tr_b16 a[240:241], v242 offset:40960\n"
    "v_accvgpr_read_b32 v243, a60\n"
    "v_mfma_f32_16x16x32_bf16 v[208:211], a[252:255], a[180:183], v[208:211]\n"
    "ds_read_b64_tr_b16 a[242:243], v242 offset:41216\n"
    "v_accvgpr_read_b32 v244, a61\n"
    "v_mfma_f32_16x16x32_bf16 v[212:215], a[248:251], a[184:187], v[212:215]\n"
    "ds_read_b64_tr_b16 a[244:245], v242 offset:43008\n"
    "v_accvgpr_read_b32 v245, a62\n"
    "v_mfma_f32_16x16x32_bf16 v[212:215], a[252:255], a[188:191], v[212:215]\n"
    "ds_read_b64_tr_b16 a[246:247], v242 offset:43264\n"
    "v_accvgpr_read_b32 v253, a63\n"
    "v_mfma_f32_16x16x32_bf16 v[216:219], a[248:251], a[192:195], v[216:219]\n"
    "s_add_u32 s87, s86, 294912\n"
    "buffer_store_dwordx4 v[168:171], v246, s[44:47], s87 offen offset:1024 sc1\n"
    "v_mfma_f32_16x16x32_bf16 v[216:219], a[252:255], a[196:199], v[216:219]\n"
    "v_mul_f32 v188, v188, v243\n"
    "v_mul_f32 v189, v189, v244\n"
    "v_mfma_f32_16x16x32_bf16 v[220:223], a[248:251], a[200:203], v[220:223]\n"
    "v_mul_f32 v190, v190, v245\n"
    "v_mul_f32 v191, v191, v253\n"
    "v_mfma_f32_16x16x32_bf16 v[220:223], a[252:255], a[204:207], v[220:223]\n"
    "s_waitcnt lgkmcnt(0)\n"
    "v_cvt_pk_bf16_f32 v174, v188, v189\n"
    "v_cvt_pk_bf16_f32 v175, v190, v191\n"
    "v_mfma_f32_16x16x32_bf16 a[80:83], a[240:243], a[208:211], a[80:83]\n"
    "ds_read_b64_tr_b16 a[248:249], v255 offset:40960\n"
    "v_accvgpr_write_b32 a32, v64\n"
    "v_mfma_f32_16x16x32_bf16 a[80:83], a[244:247], a[212:215], a[80:83]\n"
    "ds_read_b64_tr_b16 a[250:251], v255 offset:41216\n"
    "v_accvgpr_write_b32 a33, v65\n"
    "v_mfma_f32_16x16x32_bf16 a[84:87], a[240:243], a[216:219], a[84:87]\n"
    "ds_read_b64_tr_b16 a[252:253], v255 offset:43008\n"
    "v_accvgpr_write_b32 a34, v66\n"
    "v_mfma_f32_16x16x32_bf16 a[84:87], a[244:247], a[220:223], a[84:87]\n"
    "ds_read_b64_tr_b16 a[254:255], v255 offset:43264\n"
    "v_accvgpr_write_b32 a35, v67\n"
    "v_mfma_f32_16x16x32_bf16 a[88:91], a[240:243], a[224:227], a[88:91]\n"
    "v_accvgpr_write_b32 a36, v72\n"
    "v_accvgpr_write_b32 a37, v73\n"
    "v_mfma_f32_16x16x32_bf16 a[88:91], a[244:247], a[228:231], a[88:91]\n"
    "v_accvgpr_write_b32 a38, v74\n"
    "v_accvgpr_write_b32 a39, v75\n"
    "v_mfma_f32_16x16x32_bf16 a[92:95], a[240:243], a[232:235], a[92:95]\n"
    "v_accvgpr_write_b32 a40, v80\n"
    "v_accvgpr_write_b32 a41, v81\n"
    "v_mfma_f32_16x16x32_bf16 a[92:95], a[244:247], a[236:239], a[92:95]\n"
    "s_waitcnt lgkmcnt(0)\n"
    "v_accvgpr_write_b32 a42, v82\n"
    "v_accvgpr_write_b32 a43, v83\n"
    "v_mfma_f32_16x16x32_bf16 a[80:83], a[248:251], a[176:179], a[80:83]\n"
    "ds_read_b64_tr_b16 a[240:241], v241 offset:28672\n"
    "v_accvgpr_write_b32 a44, v88\n"
    "v_mfma_f32_16x16x32_bf16 a[80:83], a[252:255], a[180:183], a[80:83]\n"
    "ds_read_b64_tr_b16 a[242:243], v241 offset:28928\n"
    "v_accvgpr_write_b32 a45, v89\n"
    "v_mfma_f32_16x16x32_bf16 a[84:87], a[248:251], a[184:187], a[84:87]\n"
    "ds_read_b64_tr_b16 a[244:245], v241 offset:30720\n"
    "v_accvgpr_write_b32 a46, v90\n"
    "v_mfma_f32_16x16x32_bf16 a[84:87], a[252:255], a[188:191], a[84:87]\n"
    "ds_read_b64_tr_b16 a[246:247], v241 offset:30976\n"
    "v_accvgpr_write_b32 a47, v91\n"
    "v_mfma_f32_16x16x32_bf16 a[88:91], a[248:251], a[192:195], a[88:91]\n"
    "s_add_u32 s87, s86, 442368\n"
    "buffer_store_dwordx4 v[172:175], v246, s[44:47], s87 offen offset:1024 sc1\n"
    "v_mfma_f32_16x16x32_bf16 a[88:91], a[252:255], a[196:199], a[88:91]\n"
    "v_accvgpr_write_b32 a48, v96\n"
    "v_accvgpr_write_b32 a49, v97\n"
    "v_mfma_f32_16x16x32_bf16 a[92:95], a[248:251], a[200:203], a[92:95]\n"
    "v_accvgpr_write_b32 a50, v98\n"
    "v_accvgpr_write_b32 a51, v99\n"
    "v_mfma_f32_16x16x32_bf16 a[92:95], a[252:255], a[204:207], a[92:95]\n"
    "s_waitcnt lgkmcnt(0)\n"
    "v_accvgpr_write_b32 a52, v104\n"
    "v_accvgpr_write_b32 a53, v105\n"
    "v_mfma_f32_16x16x32_bf16 v[224:227], a[240:243], a[208:211], v[224:227]\n"
    "ds_read_b64_tr_b16 a[248:249], v254 offset:28672\n"
    "v_accvgpr_write_b32 a54, v106\n"
    "v_mfma_f32_16x16x32_bf16 v[224:227], a[244:247], a[212:215], v[224:227]\n"
    "ds_read_b64_tr_b16 a[250:251], v254 offset:28928\n"
    "v_accvgpr_write_b32 a55, v107\n"
    "v_mfma_f32_16x16x32_bf16 v[228:231], a[240:243], a[216:219], v[228:231]\n"
    "ds_read_b64_tr_b16 a[252:253], v254 offset:30720\n"
    "v_accvgpr_write_b32 a56, v112\n"
    "v_mfma_f32_16x16x32_bf16 v[228:231], a[244:247], a[220:223], v[228:231]\n"
    "ds_read_b64_tr_b16 a[254:255], v254 offset:30976\n"
    "v_accvgpr_write_b32 a57, v113\n"
    "v_mfma_f32_16x16x32_bf16 v[232:235], a[240:243], a[224:227], v[232:235]\n"
    "v_accvgpr_write_b32 a58, v114\n"
    "v_accvgpr_write_b32 a59, v115\n"
    "v_mfma_f32_16x16x32_bf16 v[232:235], a[244:247], a[228:231], v[232:235]\n"
    "v_accvgpr_write_b32 a60, v120\n"
    "v_accvgpr_write_b32 a61, v121\n"
    "v_mfma_f32_16x16x32_bf16 v[236:239], a[240:243], a[232:235], v[236:239]\n"
    "v_accvgpr_write_b32 a62, v122\n"
    "v_accvgpr_write_b32 a63, v123\n"
    "v_mfma_f32_16x16x32_bf16 v[236:239], a[244:247], a[236:239], v[236:239]\n"
    "s_waitcnt lgkmcnt(0)\n"
    "v_accvgpr_read_b32 v243, a64\n"
    "v_accvgpr_read_b32 v244, a65\n"
    "v_mfma_f32_16x16x32_bf16 v[224:227], a[248:251], a[176:179], v[224:227]\n"
    "ds_read_b64_tr_b16 a[240:241], v241 offset:45056\n"
    "v_accvgpr_read_b32 v245, a66\n"
    "v_mfma_f32_16x16x32_bf16 v[224:227], a[252:255], a[180:183], v[224:227]\n"
    "ds_read_b64_tr_b16 a[242:243], v241 offset:45312\n"
    "v_accvgpr_read_b32 v253, a67\n"
    "v_mfma_f32_16x16x32_bf16 v[228:231], a[248:251], a[184:187], v[228:231]\n"
    "ds_read_b64_tr_b16 a[244:245], v241 offset:47104\n"
    "v_mul_f32 v192, v192, v243\n"
    "v_mfma_f32_16x16x32_bf16 v[228:231], a[252:255], a[188:191], v[228:231]\n"
    "ds_read_b64_tr_b16 a[246:247], v241 offset:47360\n"
    "v_mul_f32 v193, v193, v244\n"
    "v_mfma_f32_16x16x32_bf16 v[232:235], a[248:251], a[192:195], v[232:235]\n"
    "v_mul_f32 v194, v194, v245\n"
    "v_mul_f32 v195, v195, v253\n"
    "v_mfma_f32_16x16x32_bf16 v[232:235], a[252:255], a[196:199], v[232:235]\n"
    "v_cvt_pk_bf16_f32 v192, v192, v193\n"
    "v_cvt_pk_bf16_f32 v193, v194, v195\n"
    "v_mfma_f32_16x16x32_bf16 v[236:239], a[248:251], a[200:203], v[236:239]\n"
    "v_accvgpr_read_b32 v243, a80\n"
    "v_accvgpr_read_b32 v244, a81\n"
    "v_mfma_f32_16x16x32_bf16 v[236:239], a[252:255], a[204:207], v[236:239]\n"
    "s_waitcnt lgkmcnt(0)\n"
    "v_accvgpr_read_b32 v245, a82\n"
    "v_accvgpr_read_b32 v253, a83\n"
    "v_mfma_f32_16x16x32_bf16 a[96:99], a[240:243], a[208:211], a[96:99]\n"
    "ds_read_b64_tr_b16 a[248:249], v254 offset:45056\n"
    "v_mul_f32 v208, v208, v243\n"
    "v_mfma_f32_16x16x32_bf16 a[96:99], a[244:247], a[212:215], a[96:99]\n"
    "ds_read_b64_tr_b16 a[250:251], v254 offset:45312\n"
    "v_mul_f32 v209, v209, v244\n"
    "v_mfma_f32_16x16x32_bf16 a[100:103], a[240:243], a[216:219], a[100:103]\n"
    "ds_read_b64_tr_b16 a[252:253], v254 offset:47104\n"
    "v_mul_f32 v210, v210, v245\n"
    "v_mfma_f32_16x16x32_bf16 a[100:103], a[244:247], a[220:223], a[100:103]\n"
    "ds_read_b64_tr_b16 a[254:255], v254 offset:47360\n"
    "v_mul_f32 v211, v211, v253\n"
    "v_mfma_f32_16x16x32_bf16 a[104:107], a[240:243], a[224:227], a[104:107]\n"
    "v_cvt_pk_bf16_f32 v194, v208, v209\n"
    "v_cvt_pk_bf16_f32 v195, v210, v211\n"
    "v_mfma_f32_16x16x32_bf16 a[104:107], a[244:247], a[228:231], a[104:107]\n"
    "v_accvgpr_read_b32 v243, a68\n"
    "v_accvgpr_read_b32 v244, a69\n"
    "v_mfma_f32_16x16x32_bf16 a[108:111], a[240:243], a[232:235], a[108:111]\n"
    "s_add_u32 s87, s86, 0\n"
    "buffer_store_dwordx4 v[192:195], v246, s[44:47], s87 offen offset:2048 sc1\n"
    "v_mfma_f32_16x16x32_bf16 a[108:111], a[244:247], a[236:239], a[108:111]\n"
    "s_waitcnt lgkmcnt(0)\n"
    "v_accvgpr_read_b32 v245, a70\n"
    "v_accvgpr_read_b32 v253, a71\n"
    "v_mfma_f32_16x16x32_bf16 a[96:99], a[248:251], a[176:179], a[96:99]\n"
    "ds_read_b64_tr_b16 a[240:241], v242 offset:28672\n"
    "v_mul_f32 v196, v196, v243\n"
    "v_mfma_f32_16x16x32_bf16 a[96:99], a[252:255], a[180:183], a[96:99]\n"
    "ds_read_b64_tr_b16 a[242:243], v242 offset:28928\n"
    "v_mul_f32 v197, v197, v244\n"
    "v_mfma_f32_16x16x32_bf16 a[100:103], a[248:251], a[184:187], a[100:103]\n"
    "ds_read_b64_tr_b16 a[244:245], v242 offset:30720\n"
    "v_mul_f32 v198, v198, v245\n"
    "v_mfma_f32_16x16x32_bf16 a[100:103], a[252:255], a[188:191], a[100:103]\n"
    "ds_read_b64_tr_b16 a[246:247], v242 offset:30976\n"
    "v_mul_f32 v199, v199, v253\n"
    "v_mfma_f32_16x16x32_bf16 a[104:107], a[248:251], a[192:195], a[104:107]\n"
    "v_cvt_pk_bf16_f32 v196, v196, v197\n"
    "v_cvt_pk_bf16_f32 v197, v198, v199\n"
    "v_mfma_f32_16x16x32_bf16 a[104:107], a[252:255], a[196:199], a[104:107]\n"
    "v_accvgpr_read_b32 v243, a84\n"
    "v_accvgpr_read_b32 v244, a85\n"
    "v_mfma_f32_16x16x32_bf16 a[108:111], a[248:251], a[200:203], a[108:111]\n"
    "v_accvgpr_read_b32 v245, a86\n"
    "v_accvgpr_read_b32 v253, a87\n"
    "v_mfma_f32_16x16x32_bf16 a[108:111], a[252:255], a[204:207], a[108:111]\n"
    "s_waitcnt lgkmcnt(0)\n"
    "v_mul_f32 v212, v212, v243\n"
    "v_mul_f32 v213, v213, v244\n"
    "v_mfma_f32_16x16x32_bf16 a[144:147], a[240:243], a[208:211], a[144:147]\n"
    "ds_read_b64_tr_b16 a[248:249], v255 offset:28672\n"
    "v_mul_f32 v214, v214, v245\n"
    "v_mfma_f32_16x16x32_bf16 a[144:147], a[244:247], a[212:215], a[144:147]\n"
    "ds_read_b64_tr_b16 a[250:251], v255 offset:28928\n"
    "v_mul_f32 v215, v215, v253\n"
    "v_mfma_f32_16x16x32_bf16 a[148:151], a[240:243], a[216:219], a[148:151]\n"
    "ds_read_b64_tr_b16 a[252:253], v255 offset:30720\n"
    "v_cvt_pk_bf16_f32 v198, v212, v213\n"
    "v_mfma_f32_16x16x32_bf16 a[148:151], a[244:247], a[220:223], a[148:151]\n"
    "ds_read_b64_tr_b16 a[254:255], v255 offset:30976\n"
    "v_cvt_pk_bf16_f32 v199, v214, v215\n"
    "v_mfma_f32_16x16x32_bf16 a[152:155], a[240:243], a[224:227], a[152:155]\n"
    "v_accvgpr_read_b32 v243, a72\n"
    "v_accvgpr_read_b32 v244, a73\n"
    "v_mfma_f32_16x16x32_bf16 a[152:155], a[244:247], a[228:231], a[152:155]\n"
    "v_accvgpr_read_b32 v245, a74\n"
    "v_accvgpr_read_b32 v253, a75\n"
    "v_mfma_f32_16x16x32_bf16 a[156:159], a[240:243], a[232:235], a[156:159]\n"
    "s_add_u32 s87, s86, 147456\n"
    "buffer_store_dwordx4 v[196:199], v246, s[44:47], s87 offen offset:2048 sc1\n"
    "v_mfma_f32_16x16x32_bf16 a[156:159], a[244:247], a[236:239], a[156:159]\n"
    "s_waitcnt lgkmcnt(0)\n"
    "v_mul_f32 v200, v200, v243\n"
    "v_mul_f32 v201, v201, v244\n"
    "v_mfma_f32_16x16x32_bf16 a[144:147], a[248:251], a[176:179], a[144:147]\n"
    "ds_read_b64_tr_b16 a[240:241], v242 offset:45056\n"
    "v_mul_f32 v202, v202, v245\n"
    "v_mfma_f32_16x16x32_bf16 a[144:147], a[252:255], a[180:183], a[144:147]\n"
    "ds_read_b64_tr_b16 a[242:243], v242 offset:45312\n"
    "v_mul_f32 v203, v203, v253\n"
    "v_mfma_f32_16x16x32_bf16 a[148:151], a[248:251], a[184:187], a[148:151]\n"
    "ds_read_b64_tr_b16 a[244:245], v242 offset:47104\n"
    "v_cvt_pk_bf16_f32 v200, v200, v201\n"
    "v_mfma_f32_16x16x32_bf16 a[148:151], a[252:255], a[188:191], a[148:151]\n"
    "ds_read_b64_tr_b16 a[246:247], v242 offset:47360\n"
    "v_cvt_pk_bf16_f32 v201, v202, v203\n"
    "v_mfma_f32_16x16x32_bf16 a[152:155], a[248:251], a[192:195], a[152:155]\n"
    "v_accvgpr_read_b32 v243, a88\n"
    "v_accvgpr_read_b32 v244, a89\n"
    "v_mfma_f32_16x16x32_bf16 a[152:155], a[252:255], a[196:199], a[152:155]\n"
    "v_accvgpr_read_b32 v245, a90\n"
    "v_accvgpr_read_b32 v253, a91\n"
    "v_mfma_f32_16x16x32_bf16 a[156:159], a[248:251], a[200:203], a[156:159]\n"
    "v_mul_f32 v216, v216, v243\n"
    "v_mul_f32 v217, v217, v244\n"
    "v_mfma_f32_16x16x32_bf16 a[156:159], a[252:255], a[204:207], a[156:159]\n"
    "s_waitcnt lgkmcnt(0)\n"
    "v_mul_f32 v218, v218, v245\n"
    "v_mul_f32 v219, v219, v253\n"
    "v_mfma_f32_16x16x32_bf16 a[112:115], a[240:243], a[208:211], a[112:115]\n"
    "ds_read_b64_tr_b16 a[248:249], v255 offset:45056\n"
    "v_cvt_pk_bf16_f32 v202, v216, v217\n"
    "v_mfma_f32_16x16x32_bf16 a[112:115], a[244:247], a[212:215], a[112:115]\n"
    "ds_read_b64_tr_b16 a[250:251], v255 offset:45312\n"
    "v_cvt_pk_bf16_f32 v203, v218, v219\n"
    "v_mfma_f32_16x16x32_bf16 a[116:119], a[240:243], a[216:219], a[116:119]\n"
    "ds_read_b64_tr_b16 a[252:253], v255 offset:47104\n"
    "v_accvgpr_read_b32 v243, a76\n"
    "v_mfma_f32_16x16x32_bf16 a[116:119], a[244:247], a[220:223], a[116:119]\n"
    "ds_read_b64_tr_b16 a[254:255], v255 offset:47360\n"
    "v_accvgpr_read_b32 v244, a77\n"
    "v_mfma_f32_16x16x32_bf16 a[120:123], a[240:243], a[224:227], a[120:123]\n"
    "v_accvgpr_read_b32 v245, a78\n"
    "v_accvgpr_read_b32 v253, a79\n"
    "v_mfma_f32_16x16x32_bf16 a[120:123], a[244:247], a[228:231], a[120:123]\n"
    "v_mul_f32 v204, v204, v243\n"
    "v_mul_f32 v205, v205, v244\n"
    "v_mfma_f32_16x16x32_bf16 a[124:127], a[240:243], a[232:235], a[124:127]\n"
    "s_add_u32 s87, s86, 294912\n"
    "buffer_store_dwordx4 v[200:203], v246, s[44:47], s87 offen offset:2048 sc1\n"
    "v_mfma_f32_16x16x32_bf16 a[124:127], a[244:247], a[236:239], a[124:127]\n"
    "s_waitcnt lgkmcnt(0)\n"
    "v_mul_f32 v206, v206, v245\n"
    "v_mul_f32 v207, v207, v253\n"
    "v_mfma_f32_16x16x32_bf16 a[112:115], a[248:251], a[176:179], a[112:115]\n"
    "ds_read_b64_tr_b16 a[240:241], v241 offset:49152\n"
    "v_cvt_pk_bf16_f32 v204, v204, v205\n"
    "v_mfma_f32_16x16x32_bf16 a[112:115], a[252:255], a[180:183], a[112:115]\n"
    "ds_read_b64_tr_b16 a[242:243], v241 offset:49408\n"
    "v_cvt_pk_bf16_f32 v205, v206, v207\n"
    "v_mfma_f32_16x16x32_bf16 a[116:119], a[248:251], a[184:187], a[116:119]\n"
    "ds_read_b64_tr_b16 a[244:245], v241 offset:51200\n"
    "v_accvgpr_read_b32 v243, a92\n"
    "v_mfma_f32_16x16x32_bf16 a[116:119], a[252:255], a[188:191], a[116:119]\n"
    "ds_read_b64_tr_b16 a[246:247], v241 offset:51456\n"
    "v_accvgpr_read_b32 v244, a93\n"
    "v_mfma_f32_16x16x32_bf16 a[120:123], a[248:251], a[192:195], a[120:123]\n"
    "v_accvgpr_read_b32 v245, a94\n"
    "v_accvgpr_read_b32 v253, a95\n"
    "v_mfma_f32_16x16x32_bf16 a[120:123], a[252:255], a[196:199], a[120:123]\n"
    "v_mul_f32 v220, v220, v243\n"
    "v_mul_f32 v221, v221, v244\n"
    "v_mfma_f32_16x16x32_bf16 a[124:127], a[248:251], a[200:203], a[124:127]\n"
    "v_mul_f32 v222, v222, v245\n"
    "v_mul_f32 v223, v223, v253\n"
    "v_mfma_f32_16x16x32_bf16 a[124:127], a[252:255], a[204:207], a[124:127]\n"
    "s_waitcnt lgkmcnt(0)\n"
    "v_cvt_pk_bf16_f32 v206, v220, v221\n"
    "v_cvt_pk_bf16_f32 v207, v222, v223\n"
    "v_mfma_f32_16x16x32_bf16 a[160:163], a[240:243], a[208:211], a[160:163]\n"
    "ds_read_b64_tr_b16 a[248:249], v254 offset:49152\n"
    "v_mfma_f32_16x16x32_bf16 a[160:163], a[244:247], a[212:215], a[160:163]\n"
    "ds_read_b64_tr_b16 a[250:251], v254 offset:49408\n"
    "v_mfma_f32_16x16x32_bf16 a[164:167], a[240:243], a[216:219], a[164:167]\n"
    "ds_read_b64_tr_b16 a[252:253], v254 offset:51200\n"
    "v_mfma_f32_16x16x32_bf16 a[164:167], a[244:247], a[220:223], a[164:167]\n"
    "ds_read_b64_tr_b16 a[254:255], v254 offset:51456\n"
    "v_mfma_f32_16x16x32_bf16 a[168:171], a[240:243], a[224:227], a[168:171]\n"
    "v_mfma_f32_16x16x32_bf16 a[168:171], a[244:247], a[228:231], a[168:171]\n"
    "v_mfma_f32_16x16x32_bf16 a[172:175], a[240:243], a[232:235], a[172:175]\n"
    "s_add_u32 s87, s86, 442368\n"
    "buffer_store_dwordx4 v[204:207], v246, s[44:47], s87 offen offset:2048 sc1\n"
    "v_mfma_f32_16x16x32_bf16 a[172:175], a[244:247], a[236:239], a[172:175]\n"
    "s_waitcnt lgkmcnt(0)\n"
    "v_mbcnt_lo_u32_b32 v143, -1, 0\n"
    "v_mbcnt_hi_u32_b32 v143, -1, v143\n"
    "v_mfma_f32_16x16x32_bf16 a[160:163], a[248:251], a[176:179], a[160:163]\n"
    "s_mov_b32 s76, 98304\n"
    "ds_read_b64_tr_b16 a[240:241], v242 offset:49152\n"
    "v_and_b32 v132, 15, v143\n"
    "v_mfma_f32_16x16x32_bf16 a[160:163], a[252:255], a[180:183], a[160:163]\n"
    "ds_read_b64_tr_b16 a[242:243], v242 offset:49408\n"
    "v_lshrrev_b32 v133, 4, v143\n"
    "v_mfma_f32_16x16x32_bf16 a[164:167], a[248:251], a[184:187], a[164:167]\n"
    "ds_read_b64_tr_b16 a[244:245], v242 offset:51200\n"
    "v_xor_b32 v134, v132, v133\n"
    "v_mfma_f32_16x16x32_bf16 a[164:167], a[252:255], a[188:191], a[164:167]\n"
    "ds_read_b64_tr_b16 a[246:247], v242 offset:51456\n"
    "v_lshlrev_b32 v134, 4, v134\n"
    "v_mfma_f32_16x16x32_bf16 a[168:171], a[248:251], a[192:195], a[168:171]\n"
    "v_lshl_add_u32 v134, v132, 8, v134\n"
    "v_add_u32 v216, 32768, v134\n"
    "v_mfma_f32_16x16x32_bf16 a[168:171], a[252:255], a[196:199], a[168:171]\n"
    "v_xor_b32 v217, 64, v216\n"
    "v_xor_b32 v218, 128, v216\n"
    "v_mfma_f32_16x16x32_bf16 a[172:175], a[248:251], a[200:203], a[172:175]\n"
    "v_xor_b32 v219, 192, v216\n"
    "v_add_u32 v212, s76, v216\n"
    "v_mfma_f32_16x16x32_bf16 a[172:175], a[252:255], a[204:207], a[172:175]\n"
    "s_waitcnt lgkmcnt(0)\n"
    "v_add_u32 v213, s76, v217\n"
    "v_add_u32 v214, s76, v218\n"
    "v_mfma_f32_16x16x32_bf16 a[128:131], a[240:243], a[208:211], a[128:131]\n"
    "ds_read_b64_tr_b16 a[248:249], v255 offset:49152\n"
    "v_add_u32 v215, s76, v219\n"
    "v_mfma_f32_16x16x32_bf16 a[128:131], a[244:247], a[212:215], a[128:131]\n"
    "ds_read_b64_tr_b16 a[250:251], v255 offset:49408\n"
    "v_mov_b32 v128, 0\n"
    "v_mfma_f32_16x16x32_bf16 a[132:135], a[240:243], a[216:219], a[132:135]\n"
    "ds_read_b64_tr_b16 a[252:253], v255 offset:51200\n"
    "v_mov_b32 v129, 0\n"
    "v_mfma_f32_16x16x32_bf16 a[132:135], a[244:247], a[220:223], a[132:135]\n"
    "ds_read_b64_tr_b16 a[254:255], v255 offset:51456\n"
    "v_mov_b32 v130, 0\n"
    "v_mfma_f32_16x16x32_bf16 a[136:139], a[240:243], a[224:227], a[136:139]\n"
    "v_mov_b32 v131, 0\n"
    "v_lshrrev_b32 v134, 5, v143\n"
    "v_mfma_f32_16x16x32_bf16 a[136:139], a[244:247], a[228:231], a[136:139]\n"
    "v_lshlrev_b32 v134, 11, v134\n"
    "v_lshrrev_b32 v135, 2, v143\n"
    "v_mfma_f32_16x16x32_bf16 a[140:143], a[240:243], a[232:235], a[140:143]\n"
    "v_and_b32 v135, 7, v135\n"
    "v_lshl_add_u32 v134, v135, 6, v134\n"
    "v_mfma_f32_16x16x32_bf16 a[140:143], a[244:247], a[236:239], a[140:143]\n"
    "s_waitcnt lgkmcnt(0)\n"
    "v_and_b32 v135, 3, v143\n"
    "v_lshl_add_u32 v134, v135, 3, v134\n"
    "v_mfma_f32_16x16x32_bf16 a[128:131], a[248:251], a[176:179], a[128:131]\n"
    "v_add_u32 v134, 49152, v134\n"
    "v_cmp_eq_u32 vcc, 0, v135\n"
    "v_mfma_f32_16x16x32_bf16 a[128:131], a[252:255], a[180:183], a[128:131]\n"
    "s_waitcnt vmcnt(12)\n"
    "s_barrier\n"
    "v_mfma_f32_16x16x32_bf16 a[132:135], a[248:251], a[184:187], a[132:135]\n"
    "v_and_b32 v135, 1, v133\n"
    "v_lshlrev_b32 v135, 5, v135\n"
    "v_mfma_f32_16x16x32_bf16 a[132:135], a[252:255], a[188:191], a[132:135]\n"
    "v_add_u32 v222, v134, v135\n"
    "v_xor_b32 v135, 32, v135\n"
    "v_mfma_f32_16x16x32_bf16 a[136:139], a[248:251], a[192:195], a[136:139]\n"
    "v_add_u32 v223, v134, v135\n"
    "v_add_u32 v220, s76, v222\n"
    "v_mfma_f32_16x16x32_bf16 a[136:139], a[252:255], a[196:199], a[136:139]\n"
    "v_add_u32 v221, s76, v223\n"
    "v_mov_b32 v136, 0x3f803f80\n"
    "v_mfma_f32_16x16x32_bf16 a[140:143], a[248:251], a[200:203], a[140:143]\n"
    "v_cndmask_b32 v208, 0, v136, vcc\n"
    "v_mov_b32 v209, v208\n"
    "v_mfma_f32_16x16x32_bf16 a[140:143], a[252:255], a[204:207], a[140:143]\n"
    "v_mov_b32 v210, v208\n"
    "v_mov_b32 v211, v208\n"
    "v_mfma_f32_32x32x16_bf16 v[0:15], v[128:131], v[128:131], 0\n"
    "s_cmp_eq_u32 s68, 0\n"
    "s_cselect_b32 s6, 0x81ba, 0x3a\n"
    "s_mov_b32 s71, 15\n"
    "ds_read_b128 a[64:67], v212 offset:0\n"
    "ds_read_b128 a[68:71], v213 offset:0\n"
    "ds_read_b128 a[72:75], v214 offset:0\n"
    "ds_read_b128 a[76:79], v215 offset:0\n"
    "v_mfma_f32_32x32x16_bf16 v[16:31], v[128:131], v[128:131], 0\n"
    "ds_read_b128 a[80:83], v212 offset:4096\n"
    "ds_read_b128 a[84:87], v213 offset:4096\n"
    "ds_read_b128 a[88:91], v214 offset:4096\n"
    "ds_read_b128 a[92:95], v215 offset:4096\n"
    "v_accvgpr_read_b32 v243, a96\n"
    "v_accvgpr_read_b32 v244, a97\n"
    "v_mfma_f32_32x32x16_bf16 v[32:47], v[128:131], v[128:131], 0\n"
    "s_add_u32 m0, s85, 32768\n"
    "s_nop 0\n"
    "buffer_load_dwordx4 v247, s[36:39], s84 offen offset:0 nt lds\n"
    "buffer_load_dwordx4 v248, s[36:39], s84 offen offset:1024 nt lds\n"
    "v_accvgpr_read_b32 v245, a98\n"
    "v_mfma_f32_32x32x16_bf16 v[48:63], v[128:131], v[128:131], 0\n"
    "buffer_load_dwordx4 v249, s[36:39], s84 offen offset:2048 nt lds\n"
    "buffer_load_dwordx4 v250, s[36:39], s84 offen offset:3072 nt lds\n"
    "v_accvgpr_read_b32 v253, a99\n"
    "v_mul_f32 v224, v224, v243\n"
    "v_mfma_f32_32x32x16_bf16 v[64:79], v[128:131], v[128:131], 0\n"
    "s_add_u32 m0, s85, 49152\n"
    "s_nop 0\n"
    "buffer_load_dwordx4 v251, s[40:43], s84 offen offset:0 nt lds\n"
    "buffer_load_dwordx4 v251, s[40:43], s84 offen offset:2048 nt lds\n"
    "v_mul_f32 v225, v225, v244\n"
    "v_mfma_f32_32x32x16_bf16 v[80:95], v[128:131], v[128:131], 0\n"
    "s_add_u32 m0, s85, 50176\n"
    "s_nop 0\n"
    "buffer_load_dwordx4 v252, s[40:43], s84 offen offset:0 nt lds\n"
    "buffer_load_dwordx4 v252, s[40:43], s84 offen offset:2048 nt lds\n"
    "v_mul_f32 v226, v226, v245\n"
    "v_mfma_f32_32x32x16_bf16 v[96:111], v[128:131], v[128:131], 0\n"
    "s_add_u32 s84, s84, 16384\n"
    "s_add_u32 s85, s85, 32768\n"
    "s_and_b32 s85, s85, 0x1ffff\n"
    "s_add_u32 m0, s85, 32768\n"
    "s_nop 0\n"
    "buffer_load_dwordx4 v247, s[36:39], s84 offen offset:0 nt lds\n"
    "v_mfma_f32_32x32x16_bf16 v[112:127], v[128:131], v[128:131], 0\n"
    "buffer_load_dwordx4 v248, s[36:39], s84 offen offset:1024 nt lds\n"
    "buffer_load_dwordx4 v249, s[36:39], s84 offen offset:2048 nt lds\n"
    "buffer_load_dwordx4 v250, s[36:39], s84 offen offset:3072 nt lds\n"
    "v_mfma_f32_32x32x16_bf16 v[176:191], v[128:131], v[128:131], 0\n"
    "s_add_u32 m0, s85, 49152\n"
    "s_nop 0\n"
    "buffer_load_dwordx4 v251, s[40:43], s84 offen offset:0 nt lds\n"
    "buffer_load_dwordx4 v251, s[40:43], s84 offen offset:2048 nt lds\n"
    "s_waitcnt lgkmcnt(4)\n"
    "v_mfma_f32_16x16x32_bf16 v[160:163], a[64:67], a[0:3], 0\n"
    "s_add_u32 m0, s85, 50176\n"
    "s_nop 0\n"
    "v_mul_f32 v227, v227, v253\n"
    "v_mfma_f32_16x16x32_bf16 v[160:163], a[68:71], a[4:7], v[160:163]\n"
    "buffer_load_dwordx4 v252, s[40:43], s84 offen offset:0 nt lds\n"
    "v_mfma_f32_16x16x32_bf16 v[160:163], a[72:75], a[8:11], v[160:163]\n"
    "buffer_load_dwordx4 v252, s[40:43], s84 offen offset:2048 nt lds\n"
    "v_mfma_f32_16x16x32_bf16 v[160:163], a[76:79], a[12:15], v[160:163]\n"
    "s_add_u32 s84, s84, 16384\n"
    "s_add_u32 s85, s85, 32768\n"
    "s_and_b32 s85, s85, 0x1ffff\n"
    "v_mfma_f32_16x16x32_bf16 v[164:167], a[64:67], a[16:19], 0\n"
    "v_cvt_pk_bf16_f32 v224, v224, v225\n"
    "v_cvt_pk_bf16_f32 v225, v226, v227\n"
    "v_mfma_f32_16x16x32_bf16 v[164:167], a[68:71], a[20:23], v[164:167]\n"
    "v_accvgpr_read_b32 v144, a144\n"
    "v_accvgpr_read_b32 v145, a145\n"
    "v_mfma_f32_16x16x32_bf16 v[164:167], a[72:75], a[24:27], v[164:167]\n"
    "v_accvgpr_read_b32 v146, a146\n"
    "v_accvgpr_read_b32 v147, a147\n"
    "v_mfma_f32_16x16x32_bf16 v[164:167], a[76:79], a[28:31], v[164:167]\n"
    "v_accvgpr_read_b32 v243, a112\n"
    "v_accvgpr_read_b32 v244, a113\n"
    "v_mfma_f32_16x16x32_bf16 v[168:171], a[64:67], a[32:35], 0\n"
    "v_accvgpr_read_b32 v245, a114\n"
    "v_accvgpr_read_b32 v253, a115\n"
    "v_mfma_f32_16x16x32_bf16 v[168:171], a[68:71], a[36:39], v[168:171]\n"
    "v_mul_f32 v144, v144, v243\n"
    "v_mul_f32 v145, v145, v244\n"
    "v_mfma_f32_16x16x32_bf16 v[168:171], a[72:75], a[40:43], v[168:171]\n"
    "v_mul_f32 v146, v146, v245\n"
    "v_mul_f32 v147, v147, v253\n"
    "v_mfma_f32_16x16x32_bf16 v[168:171], a[76:79], a[44:47], v[168:171]\n"
    "v_cvt_pk_bf16_f32 v226, v144, v145\n"
    "v_cvt_pk_bf16_f32 v227, v146, v147\n"
    "v_mfma_f32_16x16x32_bf16 v[172:175], a[64:67], a[48:51], 0\n"
    "v_accvgpr_read_b32 v243, a100\n"
    "v_accvgpr_read_b32 v244, a101\n"
    "v_mfma_f32_16x16x32_bf16 v[172:175], a[68:71], a[52:55], v[172:175]\n"
    "v_accvgpr_read_b32 v245, a102\n"
    "v_accvgpr_read_b32 v253, a103\n"
    "v_mfma_f32_16x16x32_bf16 v[172:175], a[72:75], a[56:59], v[172:175]\n"
    "v_mul_f32 v228, v228, v243\n"
    "v_mul_f32 v229, v229, v244\n"
    "v_mfma_f32_16x16x32_bf16 v[172:175], a[76:79], a[60:63], v[172:175]\n"
    "v_mul_f32 v230, v230, v245\n"
    "v_mul_f32 v231, v231, v253\n"
    "v_cvt_pk_bf16_f32 v228, v228, v229\n"
    "v_cvt_pk_bf16_f32 v229, v230, v231\n"
    "v_accvgpr_read_b32 v148, a148\n"
    "v_accvgpr_read_b32 v149, a149\n"
    "v_accvgpr_read_b32 v150, a150\n"
    "v_accvgpr_read_b32 v151, a151\n"
    "v_accvgpr_read_b32 v243, a116\n"
    "v_accvgpr_read_b32 v244, a117\n"
    "v_accvgpr_read_b32 v245, a118\n"
    "v_accvgpr_read_b32 v253, a119\n"
    "v_mul_f32 v148, v148, v243\n"
    "v_mul_f32 v149, v149, v244\n"
    "v_mul_f32 v150, v150, v245\n"
    "v_mul_f32 v151, v151, v253\n"
    "v_cvt_pk_bf16_f32 v230, v148, v149\n"
    "v_cvt_pk_bf16_f32 v231, v150, v151\n"
    "v_accvgpr_read_b32 v243, a104\n"
    "v_accvgpr_read_b32 v244, a105\n"
    "v_accvgpr_read_b32 v245, a106\n"
    "v_accvgpr_read_b32 v253, a107\n"
    "v_mul_f32 v232, v232, v243\n"
    "v_mul_f32 v233, v233, v244\n"
    "v_mul_f32 v234, v234, v245\n"
    "v_mul_f32 v235, v235, v253\n"
    "v_cvt_pk_bf16_f32 v232, v232, v233\n"
    "v_cvt_pk_bf16_f32 v233, v234, v235\n"
    "v_accvgpr_read_b32 v152, a152\n"
    "v_accvgpr_read_b32 v153, a153\n"
    "v_accvgpr_read_b32 v154, a154\n"
    "v_accvgpr_read_b32 v155, a155\n"
    "v_accvgpr_read_b32 v243, a120\n"
    "v_accvgpr_read_b32 v244, a121\n"
    "v_accvgpr_read_b32 v245, a122\n"
    "v_accvgpr_read_b32 v253, a123\n"
    "v_mul_f32 v152, v152, v243\n"
    "v_mul_f32 v153, v153, v244\n"
    "v_mul_f32 v154, v154, v245\n"
    "v_mul_f32 v155, v155, v253\n"
    "v_cvt_pk_bf16_f32 v234, v152, v153\n"
    "v_cvt_pk_bf16_f32 v235, v154, v155\n"
    "v_accvgpr_read_b32 v243, a108\n"
    "v_accvgpr_read_b32 v244, a109\n"
    "v_accvgpr_read_b32 v245, a110\n"
    "v_accvgpr_read_b32 v253, a111\n"
    "v_mul_f32 v236, v236, v243\n"
    "v_mul_f32 v237, v237, v244\n"
    "v_mul_f32 v238, v238, v245\n"
    "v_mul_f32 v239, v239, v253\n"
    "v_cvt_pk_bf16_f32 v236, v236, v237\n"
    "v_cvt_pk_bf16_f32 v237, v238, v239\n"
    "v_accvgpr_read_b32 v156, a156\n"
    "v_accvgpr_read_b32 v157, a157\n"
    "v_accvgpr_read_b32 v158, a158\n"
    "v_accvgpr_read_b32 v159, a159\n"
    "v_accvgpr_read_b32 v243, a124\n"
    "v_accvgpr_read_b32 v244, a125\n"
    "v_accvgpr_read_b32 v245, a126\n"
    "v_accvgpr_read_b32 v253, a127\n"
    "v_mul_f32 v156, v156, v243\n"
    "v_mul_f32 v157, v157, v244\n"
    "v_mul_f32 v158, v158, v245\n"
    "v_mul_f32 v159, v159, v253\n"
    "v_cvt_pk_bf16_f32 v238, v156, v157\n"
    "v_cvt_pk_bf16_f32 v239, v158, v159\n"
    "ds_read_b128 a[96:99], v212 offset:8192\n"
    "ds_read_b128 a[100:103], v213 offset:8192\n"
    "ds_read_b128 a[104:107], v214 offset:8192\n"
    "ds_read_b128 a[108:111], v215 offset:8192\n"
    "ds_read_b128 a[112:115], v212 offset:12288\n"
    "ds_read_b128 a[116:119], v213 offset:12288\n"
    "ds_read_b128 a[120:123], v214 offset:12288\n"
    "ds_read_b128 a[124:127], v215 offset:12288\n"
    "v_accvgpr_read_b32 v192, a160\n"
    "v_accvgpr_read_b32 v193, a161\n"
    "v_accvgpr_read_b32 v194, a162\n"
    "v_accvgpr_read_b32 v195, a163\n"
    "v_accvgpr_read_b32 v243, a128\n"
    "v_accvgpr_read_b32 v244, a129\n"
    "v_accvgpr_read_b32 v245, a130\n"
    "v_accvgpr_read_b32 v253, a131\n"
    "v_mul_f32 v192, v192, v243\n"
    "v_mul_f32 v193, v193, v244\n"
    "v_mul_f32 v194, v194, v245\n"
    "v_mul_f32 v195, v195, v253\n"
    "v_cvt_pk_bf16_f32 v192, v192, v193\n"
    "v_cvt_pk_bf16_f32 v193, v194, v195\n"
    "v_accvgpr_read_b32 v196, a164\n"
    "v_accvgpr_read_b32 v197, a165\n"
    "v_accvgpr_read_b32 v198, a166\n"
    "v_accvgpr_read_b32 v199, a167\n"
    "v_accvgpr_read_b32 v243, a132\n"
    "v_accvgpr_read_b32 v244, a133\n"
    "v_accvgpr_read_b32 v245, a134\n"
    "v_accvgpr_read_b32 v253, a135\n"
    "v_mul_f32 v196, v196, v243\n"
    "v_mul_f32 v197, v197, v244\n"
    "v_mul_f32 v198, v198, v245\n"
    "v_mul_f32 v199, v199, v253\n"
    "v_cvt_pk_bf16_f32 v196, v196, v197\n"
    "v_cvt_pk_bf16_f32 v197, v198, v199\n"
    "v_accvgpr_read_b32 v200, a168\n"
    "v_accvgpr_read_b32 v201, a169\n"
    "v_accvgpr_read_b32 v202, a170\n"
    "v_accvgpr_read_b32 v203, a171\n"
    "v_accvgpr_read_b32 v243, a136\n"
    "v_accvgpr_read_b32 v244, a137\n"
    "v_accvgpr_read_b32 v245, a138\n"
    "v_accvgpr_read_b32 v253, a139\n"
    "v_mul_f32 v200, v200, v243\n"
    "v_mul_f32 v201, v201, v244\n"
    "v_mul_f32 v202, v202, v245\n"
    "v_mul_f32 v203, v203, v253\n"
    "v_cvt_pk_bf16_f32 v200, v200, v201\n"
    "v_cvt_pk_bf16_f32 v201, v202, v203\n"
    "v_accvgpr_read_b32 v204, a172\n"
    "v_accvgpr_read_b32 v205, a173\n"
    "v_accvgpr_read_b32 v206, a174\n"
    "v_accvgpr_read_b32 v207, a175\n"
    "v_accvgpr_read_b32 v243, a140\n"
    "v_accvgpr_read_b32 v244, a141\n"
    "v_accvgpr_read_b32 v245, a142\n"
    "v_accvgpr_read_b32 v253, a143\n"
    "v_mul_f32 v204, v204, v243\n"
    "v_mul_f32 v205, v205, v244\n"
    "v_mul_f32 v206, v206, v245\n"
    "v_mul_f32 v207, v207, v253\n"
    "v_cvt_pk_bf16_f32 v204, v204, v205\n"
    "v_cvt_pk_bf16_f32 v205, v206, v207\n"
    "v_mbcnt_lo_u32_b32 v253, -1, 0\n"
    "v_mbcnt_hi_u32_b32 v253, -1, v253\n"
    "s_nop 2\n"
    "v_max3_f32 v160, v160, v161, v162\n"
    "v_max3_f32 v164, v164, v165, v166\n"
    "v_max3_f32 v168, v168, v169, v170\n"
    "v_max3_f32 v172, v172, v173, v174\n"
    "v_max_f32 v160, v160, v163\n"
    "v_max_f32 v164, v164, v167\n"
    "v_max_f32 v168, v168, v171\n"
    "v_max_f32 v172, v172, v175\n"
    "v_mov_b32 v161, v160\n"
    "v_mov_b32 v165, v164\n"
    "v_mov_b32 v169, v168\n"
    "v_mov_b32 v173, v172\n"
    "v_permlane16_swap_b32 v160, v161\n"
    "v_permlane16_swap_b32 v164, v165\n"
    "v_permlane16_swap_b32 v168, v169\n"
    "v_permlane16_swap_b32 v172, v173\n"
    "v_max_f32 v160, v160, v161\n"
    "v_max_f32 v164, v164, v165\n"
    "v_max_f32 v168, v168, v169\n"
    "v_max_f32 v172, v172, v173\n"
    "v_mov_b32 v161, v160\n"
    "v_mov_b32 v165, v164\n"
    "v_mov_b32 v169, v168\n"
    "v_mov_b32 v173, v172\n"
    "v_permlane32_swap_b32 v160, v161\n"
    "v_permlane32_swap_b32 v164, v165\n"
    "v_permlane32_swap_b32 v168, v169\n"
    "v_permlane32_swap_b32 v172, v173\n"
    "v_max_f32 v160, v160, v161\n"
    "v_max_f32 v164, v164, v165\n"
    "v_max_f32 v168, v168, v169\n"
    "v_max_f32 v172, v172, v173\n"
    "v_sub_f32 v160, 0, v160\n"
    "v_mov_b32 v161, v160\n"
    "v_mov_b32 v162, v160\n"
    "v_mov_b32 v163, v160\n"
    "v_sub_f32 v164, 0, v164\n"
    "v_mov_b32 v165, v164\n"
    "v_mov_b32 v166, v164\n"
    "v_mov_b32 v167, v164\n"
    "v_sub_f32 v168, 0, v168\n"
    "v_mov_b32 v169, v168\n"
    "v_mov_b32 v170, v168\n"
    "v_mov_b32 v171, v168\n"
    "v_sub_f32 v172, 0, v172\n"
    "v_mov_b32 v173, v172\n"
    "v_mov_b32 v174, v172\n"
    "v_mov_b32 v175, v172\n"
    "s_waitcnt lgkmcnt(0)\n"
    "v_mfma_f32_16x16x32_bf16 v[128:131], a[64:67], a[0:3], v[160:163]\n"
    "s_add_u32 s87, s86, 0\n"
    "ds_read_b64_tr_b16 a[128:129], v220 offset:0\n"
    "ds_read_b64_tr_b16 a[130:131], v220 offset:4096\n"
    "v_mfma_f32_16x16x32_bf16 v[128:131], a[68:71], a[4:7], v[128:131]\n"
    "buffer_store_dwordx4 v[224:227], v246, s[44:47], s87 offen offset:3072 sc1\n"
    "v_mfma_f32_16x16x32_bf16 v[128:131], a[72:75], a[8:11], v[128:131]\n"
    "s_add_u32 s87, s86, 147456\n"
    "ds_read_b64_tr_b16 a[132:133], v220 offset:8192\n"
    "ds_read_b64_tr_b16 a[134:135], v220 offset:12288\n"
    "v_mfma_f32_16x16x32_bf16 v[128:131], a[76:79], a[12:15], v[128:131]\n"
    "buffer_store_dwordx4 v[228:231], v246, s[44:47], s87 offen offset:3072 sc1\n"
    "v_mfma_f32_16x16x32_bf16 v[132:135], a[80:83], a[0:3], v[160:163]\n"
    "s_add_u32 s87, s86, 294912\n"
    "ds_read_b64_tr_b16 a[136:137], v221 offset:0\n"
    "ds_read_b64_tr_b16 a[138:139], v221 offset:4096\n"
    "v_mfma_f32_16x16x32_bf16 v[132:135], a[84:87], a[4:7], v[132:135]\n"
    "buffer_store_dwordx4 v[232:235], v246, s[44:47], s87 offen offset:3072 sc1\n"
    "v_mfma_f32_16x16x32_bf16 v[132:135], a[88:91], a[8:11], v[132:135]\n"
    "s_add_u32 s87, s86, 442368\n"
    "ds_read_b64_tr_b16 a[140:141], v221 offset:8192\n"
    "ds_read_b64_tr_b16 a[142:143], v221 offset:12288\n"
    "v_mfma_f32_16x16x32_bf16 v[132:135], a[92:95], a[12:15], v[132:135]\n"
    "buffer_store_dwordx4 v[236:239], v246, s[44:47], s87 offen offset:3072 sc1\n"
    "v_exp_f32 v128, v128\n"
    "v_mfma_f32_16x16x32_bf16 v[136:139], a[96:99], a[0:3], v[160:163]\n"
    "s_lshr_b32 s86, s69, 1\n"
    "ds_read_b64_tr_b16 a[144:145], v220 offset:512\n"
    "ds_read_b64_tr_b16 a[146:147], v220 offset:4608\n"
    "v_mfma_f32_16x16x32_bf16 v[136:139], a[100:103], a[4:7], v[136:139]\n"
    "s_mul_i32 s86, s86, 9\n"
    "v_exp_f32 v129, v129\n"
    "v_mfma_f32_16x16x32_bf16 v[136:139], a[104:107], a[8:11], v[136:139]\n"
    "s_add_u32 s86, s86, 4\n"
    "ds_read_b64_tr_b16 a[148:149], v220 offset:8704\n"
    "ds_read_b64_tr_b16 a[150:151], v220 offset:12800\n"
    "v_mfma_f32_16x16x32_bf16 v[136:139], a[108:111], a[12:15], v[136:139]\n"
    "s_lshl_b32 s86, s86, 10\n"
    "v_exp_f32 v130, v130\n"
    "v_mfma_f32_16x16x32_bf16 v[140:143], a[112:115], a[0:3], v[160:163]\n"
    "s_add_u32 s86, s86, s33\n"
    "ds_read_b64_tr_b16 a[152:153], v221 offset:512\n"
    "ds_read_b64_tr_b16 a[154:155], v221 offset:4608\n"
    "v_cvt_pk_bf16_f32 v144, v128, v129\n"
    "v_mfma_f32_16x16x32_bf16 v[140:143], a[116:119], a[4:7], v[140:143]\n"
    "s_lshl_b32 s87, s75, 3\n"
    "v_exp_f32 v131, v131\n"
    "v_mfma_f32_16x16x32_bf16 v[140:143], a[120:123], a[8:11], v[140:143]\n"
    "s_add_u32 s86, s86, s87\n"
    "ds_read_b64_tr_b16 a[156:157], v221 offset:8704\n"
    "ds_read_b64_tr_b16 a[158:159], v221 offset:12800\n"
    "v_mfma_f32_16x16x32_bf16 v[140:143], a[124:127], a[12:15], v[140:143]\n"
    "s_add_u32 s87, s86, 0\n"
    "v_exp_f32 v132, v132\n"
    "ds_read_b64_tr_b16 a[160:161], v220 offset:1024\n"
    "ds_read_b64_tr_b16 a[162:163], v220 offset:5120\n"
    "v_cvt_pk_bf16_f32 v145, v130, v131\n"
    "buffer_store_dwordx2 v[192:193], v246, s[44:47], s87 offen sc1\n"
    "v_exp_f32 v133, v133\n"
    "s_add_u32 s87, s86, 147456\n"
    "ds_read_b64_tr_b16 a[164:165], v220 offset:9216\n"
    "ds_read_b64_tr_b16 a[166:167], v220 offset:13312\n"
    "buffer_store_dwordx2 v[196:197], v246, s[44:47], s87 offen sc1\n"
    "s_add_u32 s87, s86, 294912\n"
    "v_exp_f32 v134, v134\n"
    "ds_read_b64_tr_b16 a[168:169], v221 offset:1024\n"
    "ds_read_b64_tr_b16 a[170:171], v221 offset:5120\n"
    "v_cvt_pk_bf16_f32 v146, v132, v133\n"
    "buffer_store_dwordx2 v[200:201], v246, s[44:47], s87 offen sc1\n"
    "v_exp_f32 v135, v135\n"
    "s_add_u32 s87, s86, 442368\n"
    "ds_read_b64_tr_b16 a[172:173], v221 offset:9216\n"
    "ds_read_b64_tr_b16 a[174:175], v221 offset:13312\n"
    "buffer_store_dwordx2 v[204:205], v246, s[44:47], s87 offen sc1\n"
    "v_exp_f32 v136, v136\n"
    "ds_read_b64_tr_b16 a[176:177], v220 offset:1536\n"
    "ds_read_b64_tr_b16 a[178:179], v220 offset:5632\n"
    "v_cvt_pk_bf16_f32 v147, v134, v135\n"
    "v_exp_f32 v137, v137\n"
    "ds_read_b64_tr_b16 a[180:181], v220 offset:9728\n"
    "ds_read_b64_tr_b16 a[182:183], v220 offset:13824\n"
    "v_exp_f32 v138, v138\n"
    "ds_read_b64_tr_b16 a[184:185], v221 offset:1536\n"
    "ds_read_b64_tr_b16 a[186:187], v221 offset:5632\n"
    "v_cvt_pk_bf16_f32 v148, v136, v137\n"
    "v_exp_f32 v139, v139\n"
    "ds_read_b64_tr_b16 a[188:189], v221 offset:9728\n"
    "ds_read_b64_tr_b16 a[190:191], v221 offset:13824\n"
    "v_exp_f32 v140, v140\n"
    "v_cvt_pk_bf16_f32 v149, v138, v139\n"
    "v_exp_f32 v141, v141\n"
    ".p2align 6\n"
    "L_loop_att:\n"
    "v_mfma_f32_16x16x32_bf16 v[128:131], a[64:67], a[16:19], v[164:167]\n"
    "s_add_u32 s76, s76, 32768\n"
    "s_and_b32 s76, s76, 0x1ffff\n"
    "v_add_u32 v220, s76, v222\n"
    "v_mfma_f32_16x16x32_bf16 v[128:131], a[68:71], a[20:23], v[128:131]\n"
    "s_waitcnt vmcnt(8)\n"
    "v_exp_f32 v142, v142\n"
    "v_mfma_f32_16x16x32_bf16 v[128:131], a[72:75], a[24:27], v[128:131]\n"
    "s_barrier\n"
    "v_cvt_pk_bf16_f32 v150, v140, v141\n"
    "v_add_u32 v221, s76, v223\n"
    "v_mfma_f32_16x16x32_bf16 v[128:131], a[76:79], a[28:31], v[128:131]\n"
    "v_exp_f32 v143, v143\n"
    "v_mfma_f32_16x16x32_bf16 v[132:135], a[80:83], a[16:19], v[164:167]\n"
    "ds_read_b64_tr_b16 a[192:193], v220 offset:0\n"
    "v_mfma_f32_16x16x32_bf16 v[132:135], a[84:87], a[20:23], v[132:135]\n"
    "ds_read_b64_tr_b16 a[194:195], v220 offset:4096\n"
    "v_mfma_f32_16x16x32_bf16 v[132:135], a[88:91], a[24:27], v[132:135]\n"
    "ds_read_b64_tr_b16 a[196:197], v220 offset:8192\n"
    "v_cvt_pk_bf16_f32 v151, v142, v143\n"
    "v_mfma_f32_16x16x32_bf16 v[132:135], a[92:95], a[28:31], v[132:135]\n"
    "v_exp_f32 v128, v128\n"
    "v_mfma_f32_16x16x32_bf16 v[136:139], a[96:99], a[16:19], v[164:167]\n"
    "ds_read_b64_tr_b16 a[198:199], v220 offset:12288\n"
    "v_mfma_f32_16x16x32_bf16 v[136:139], a[100:103], a[20:23], v[136:139]\n"
    "v_exp_f32 v129, v129\n"
    "v_mfma_f32_16x16x32_bf16 v[136:139], a[104:107], a[24:27], v[136:139]\n"
    "ds_read_b64_tr_b16 a[200:201], v221 offset:0\n"
    "v_mfma_f32_16x16x32_bf16 v[136:139], a[108:111], a[28:31], v[136:139]\n"
    "v_exp_f32 v130, v130\n"
    "v_mfma_f32_16x16x32_bf16 v[140:143], a[112:115], a[16:19], v[164:167]\n"
    "ds_read_b64_tr_b16 a[202:203], v221 offset:4096\n"
    "v_cvt_pk_bf16_f32 v152, v128, v129\n"
    "v_mfma_f32_16x16x32_bf16 v[140:143], a[116:119], a[20:23], v[140:143]\n"
    "s_add_u32 m0, s85, 32768\n"
    "v_exp_f32 v131, v131\n"
    "v_mfma_f32_16x16x32_bf16 v[140:143], a[120:123], a[24:27], v[140:143]\n"
    "buffer_load_dwordx4 v247, s[36:39], s84 offen offset:0 nt lds\n"
    "v_mfma_f32_16x16x32_bf16 v[140:143], a[124:127], a[28:31], v[140:143]\n"
    "s_waitcnt lgkmcnt(6)\n"
    "v_exp_f32 v132, v132\n"
    "v_mfma_f32_16x16x32_bf16 v[0:3], a[128:131], v[144:147], v[0:3]\n"
    "ds_read_b64_tr_b16 a[204:205], v221 offset:8192\n"
    "v_cvt_pk_bf16_f32 v153, v130, v131\n"
    "v_mfma_f32_16x16x32_bf16 v[0:3], a[132:135], v[148:151], v[0:3]\n"
    "v_exp_f32 v133, v133\n"
    "v_mfma_f32_16x16x32_bf16 v[4:7], a[136:139], v[144:147], v[4:7]\n"
    "buffer_load_dwordx4 v248, s[36:39], s84 offen offset:1024 nt lds\n"
    "v_mfma_f32_16x16x32_bf16 v[4:7], a[140:143], v[148:151], v[4:7]\n"
    "v_exp_f32 v134, v134\n"
    "v_mfma_f32_16x16x32_bf16 v[8:11], a[144:147], v[144:147], v[8:11]\n"
    "ds_read_b64_tr_b16 a[206:207], v221 offset:12288\n"
    "v_cvt_pk_bf16_f32 v154, v132, v133\n"
    "v_mfma_f32_16x16x32_bf16 v[8:11], a[148:151], v[148:151], v[8:11]\n"
    "v_exp_f32 v135, v135\n"
    "v_mfma_f32_16x16x32_bf16 v[12:15], a[152:155], v[144:147], v[12:15]\n"
    "buffer_load_dwordx4 v249, s[36:39], s84 offen offset:2048 nt lds\n"
    "v_mfma_f32_16x16x32_bf16 v[12:15], a[156:159], v[148:151], v[12:15]\n"
    "v_exp_f32 v136, v136\n"
    "v_mfma_f32_16x16x32_bf16 v[16:19], a[160:163], v[144:147], v[16:19]\n"
    "ds_read_b64_tr_b16 a[208:209], v220 offset:512\n"
    "v_cvt_pk_bf16_f32 v155, v134, v135\n"
    "v_mfma_f32_16x16x32_bf16 v[16:19], a[164:167], v[148:151], v[16:19]\n"
    "v_exp_f32 v137, v137\n"
    "v_mfma_f32_16x16x32_bf16 v[20:23], a[168:171], v[144:147], v[20:23]\n"
    "v_mfma_f32_16x16x32_bf16 v[20:23], a[172:175], v[148:151], v[20:23]\n"
    "v_exp_f32 v138, v138\n"
    "v_mfma_f32_16x16x32_bf16 v[24:27], a[176:179], v[144:147], v[24:27]\n"
    "ds_read_b64_tr_b16 a[210:211], v220 offset:4608\n"
    "v_cvt_pk_bf16_f32 v156, v136, v137\n"
    "v_mfma_f32_16x16x32_bf16 v[24:27], a[180:183], v[148:151], v[24:27]\n"
    "v_exp_f32 v139, v139\n"
    "v_mfma_f32_16x16x32_bf16 v[28:31], a[184:187], v[144:147], v[28:31]\n"
    "v_mfma_f32_16x16x32_bf16 v[28:31], a[188:191], v[148:151], v[28:31]\n"
    "v_exp_f32 v140, v140\n"
    "v_mfma_f32_16x16x32_bf16 v[176:179], v[208:211], v[144:147], v[176:179]\n"
    "ds_read_b64_tr_b16 a[212:213], v220 offset:8704\n"
    "v_cvt_pk_bf16_f32 v157, v138, v139\n"
    "v_mfma_f32_16x16x32_bf16 v[176:179], v[208:211], v[148:151], v[176:179]\n"
    "v_exp_f32 v141, v141\n"
    "v_mfma_f32_16x16x32_bf16 v[128:131], a[64:67], a[32:35], v[168:171]\n"
    "ds_read_b64_tr_b16 a[214:215], v220 offset:12800\n"
    "v_mfma_f32_16x16x32_bf16 v[128:131], a[68:71], a[36:39], v[128:131]\n"
    "v_exp_f32 v142, v142\n"
    "v_mfma_f32_16x16x32_bf16 v[128:131], a[72:75], a[40:43], v[128:131]\n"
    "ds_read_b64_tr_b16 a[216:217], v221 offset:512\n"
    "v_cvt_pk_bf16_f32 v158, v140, v141\n"
    "v_mfma_f32_16x16x32_bf16 v[128:131], a[76:79], a[44:47], v[128:131]\n"
    "v_exp_f32 v143, v143\n"
    "v_mfma_f32_16x16x32_bf16 v[132:135], a[80:83], a[32:35], v[168:171]\n"
    "ds_read_b64_tr_b16 a[218:219], v221 offset:4608\n"
    "v_mfma_f32_16x16x32_bf16 v[132:135], a[84:87], a[36:39], v[132:135]\n"
    "v_add_u32 v214, s76, v218\n"
    "v_add_u32 v215, s76, v219\n"
    "v_mfma_f32_16x16x32_bf16 v[132:135], a[88:91], a[40:43], v[132:135]\n"
    "ds_read_b64_tr_b16 a[220:221], v221 offset:8704\n"
    "v_cvt_pk_bf16_f32 v159, v142, v143\n"
    "v_mfma_f32_16x16x32_bf16 v[132:135], a[92:95], a[44:47], v[132:135]\n"
    "v_exp_f32 v128, v128\n"
    "v_mfma_f32_16x16x32_bf16 v[136:139], a[96:99], a[32:35], v[168:171]\n"
    "ds_read_b64_tr_b16 a[222:223], v221 offset:12800\n"
    "v_mfma_f32_16x16x32_bf16 v[136:139], a[100:103], a[36:39], v[136:139]\n"
    "v_exp_f32 v129, v129\n"
    "v_mfma_f32_16x16x32_bf16 v[136:139], a[104:107], a[40:43], v[136:139]\n"
    "ds_read_b64_tr_b16 a[224:225], v220 offset:1024\n"
    "v_mfma_f32_16x16x32_bf16 v[136:139], a[108:111], a[44:47], v[136:139]\n"
    "v_exp_f32 v130, v130\n"
    "v_mfma_f32_16x16x32_bf16 v[140:143], a[112:115], a[32:35], v[168:171]\n"
    "ds_read_b64_tr_b16 a[226:227], v220 offset:5120\n"
    "v_cvt_pk_bf16_f32 v144, v128, v129\n"
    "v_mfma_f32_16x16x32_bf16 v[140:143], a[116:119], a[36:39], v[140:143]\n"
    "v_exp_f32 v131, v131\n"
    "v_mfma_f32_16x16x32_bf16 v[140:143], a[120:123], a[40:43], v[140:143]\n"
    "buffer_load_dwordx4 v250, s[36:39], s84 offen offset:3072 nt lds\n"
    "v_mfma_f32_16x16x32_bf16 v[140:143], a[124:127], a[44:47], v[140:143]\n"
    "v_exp_f32 v132, v132\n"
    "v_mfma_f32_16x16x32_bf16 v[32:35], a[128:131], v[152:155], v[32:35]\n"
    "ds_read_b64_tr_b16 a[228:229], v220 offset:9216\n"
    "v_cvt_pk_bf16_f32 v145, v130, v131\n"
    "v_mfma_f32_16x16x32_bf16 v[32:35], a[132:135], v[156:159], v[32:35]\n"
    "s_add_u32 m0, s85, 49152\n"
    "v_exp_f32 v133, v133\n"
    "v_mfma_f32_16x16x32_bf16 v[36:39], a[136:139], v[152:155], v[36:39]\n"
    "buffer_load_dwordx4 v251, s[40:43], s84 offen offset:0 nt lds\n"
    "v_mfma_f32_16x16x32_bf16 v[36:39], a[140:143], v[156:159], v[36:39]\n"
    "v_exp_f32 v134, v134\n"
    "v_mfma_f32_16x16x32_bf16 v[40:43], a[144:147], v[152:155], v[40:43]\n"
    "ds_read_b64_tr_b16 a[230:231], v220 offset:13312\n"
    "v_cvt_pk_bf16_f32 v146, v132, v133\n"
    "v_mfma_f32_16x16x32_bf16 v[40:43], a[148:151], v[156:159], v[40:43]\n"
    "v_exp_f32 v135, v135\n"
    "v_mfma_f32_16x16x32_bf16 v[44:47], a[152:155], v[152:155], v[44:47]\n"
    "buffer_load_dwordx4 v251, s[40:43], s84 offen offset:2048 nt lds\n"
    "v_mfma_f32_16x16x32_bf16 v[44:47], a[156:159], v[156:159], v[44:47]\n"
    "v_exp_f32 v136, v136\n"
    "v_mfma_f32_16x16x32_bf16 v[48:51], a[160:163], v[152:155], v[48:51]\n"
    "ds_read_b64_tr_b16 a[232:233], v221 offset:1024\n"
    "v_cvt_pk_bf16_f32 v147, v134, v135\n"
    "v_mfma_f32_16x16x32_bf16 v[48:51], a[164:167], v[156:159], v[48:51]\n"
    "v_exp_f32 v137, v137\n"
    "v_mfma_f32_16x16x32_bf16 v[52:55], a[168:171], v[152:155], v[52:55]\n"
    "v_mfma_f32_16x16x32_bf16 v[52:55], a[172:175], v[156:159], v[52:55]\n"
    "v_exp_f32 v138, v138\n"
    "v_mfma_f32_16x16x32_bf16 v[56:59], a[176:179], v[152:155], v[56:59]\n"
    "ds_read_b64_tr_b16 a[234:235], v221 offset:5120\n"
    "v_cvt_pk_bf16_f32 v148, v136, v137\n"
    "v_mfma_f32_16x16x32_bf16 v[56:59], a[180:183], v[156:159], v[56:59]\n"
    "v_exp_f32 v139, v139\n"
    "v_mfma_f32_16x16x32_bf16 v[60:63], a[184:187], v[152:155], v[60:63]\n"
    "v_add_u32 v212, s76, v216\n"
    "v_add_u32 v213, s76, v217\n"
    "v_mfma_f32_16x16x32_bf16 v[60:63], a[188:191], v[156:159], v[60:63]\n"
    "v_exp_f32 v140, v140\n"
    "v_mfma_f32_16x16x32_bf16 v[180:183], v[208:211], v[152:155], v[180:183]\n"
    "v_cvt_pk_bf16_f32 v149, v138, v139\n"
    "v_mfma_f32_16x16x32_bf16 v[180:183], v[208:211], v[156:159], v[180:183]\n"
    "v_exp_f32 v141, v141\n"
    "v_mfma_f32_16x16x32_bf16 v[128:131], a[64:67], a[48:51], v[172:175]\n"
    "v_mfma_f32_16x16x32_bf16 v[128:131], a[68:71], a[52:55], v[128:131]\n"
    "v_exp_f32 v142, v142\n"
    "v_mfma_f32_16x16x32_bf16 v[128:131], a[72:75], a[56:59], v[128:131]\n"
    "ds_read_b128 a[64:67], v212 offset:0\n"
    "v_cvt_pk_bf16_f32 v150, v140, v141\n"
    "v_mfma_f32_16x16x32_bf16 v[128:131], a[76:79], a[60:63], v[128:131]\n"
    "v_exp_f32 v143, v143\n"
    "v_mfma_f32_16x16x32_bf16 v[132:135], a[80:83], a[48:51], v[172:175]\n"
    "ds_read_b128 a[68:71], v213 offset:0\n"
    "v_mfma_f32_16x16x32_bf16 v[132:135], a[84:87], a[52:55], v[132:135]\n"
    "v_mfma_f32_16x16x32_bf16 v[132:135], a[88:91], a[56:59], v[132:135]\n"
    "ds_read_b128 a[72:75], v214 offset:0\n"
    "v_cvt_pk_bf16_f32 v151, v142, v143\n"
    "v_mfma_f32_16x16x32_bf16 v[132:135], a[92:95], a[60:63], v[132:135]\n"
    "v_exp_f32 v128, v128\n"
    "v_mfma_f32_16x16x32_bf16 v[136:139], a[96:99], a[48:51], v[172:175]\n"
    "ds_read_b128 a[76:79], v215 offset:0\n"
    "v_mfma_f32_16x16x32_bf16 v[136:139], a[100:103], a[52:55], v[136:139]\n"
    "v_exp_f32 v129, v129\n"
    "v_mfma_f32_16x16x32_bf16 v[136:139], a[104:107], a[56:59], v[136:139]\n"
    "ds_read_b128 a[80:83], v212 offset:4096\n"
    "v_mfma_f32_16x16x32_bf16 v[136:139], a[108:111], a[60:63], v[136:139]\n"
    "v_exp_f32 v130, v130\n"
    "v_mfma_f32_16x16x32_bf16 v[140:143], a[112:115], a[48:51], v[172:175]\n"
    "ds_read_b128 a[84:87], v213 offset:4096\n"
    "v_cvt_pk_bf16_f32 v152, v128, v129\n"
    "v_mfma_f32_16x16x32_bf16 v[140:143], a[116:119], a[52:55], v[140:143]\n"
    "v_exp_f32 v131, v131\n"
    "v_mfma_f32_16x16x32_bf16 v[140:143], a[120:123], a[56:59], v[140:143]\n"
    "ds_read_b128 a[88:91], v214 offset:4096\n"
    "v_mfma_f32_16x16x32_bf16 v[140:143], a[124:127], a[60:63], v[140:143]\n"
    "v_exp_f32 v132, v132\n"
    "v_mfma_f32_16x16x32_bf16 v[64:67], a[128:131], v[144:147], v[64:67]\n"
    "ds_read_b128 a[92:95], v215 offset:4096\n"
    "v_cvt_pk_bf16_f32 v153, v130, v131\n"
    "v_mfma_f32_16x16x32_bf16 v[64:67], a[132:135], v[148:151], v[64:67]\n"
    "v_exp_f32 v133, v133\n"
    "v_mfma_f32_16x16x32_bf16 v[68:71], a[136:139], v[144:147], v[68:71]\n"
    "ds_read_b128 a[96:99], v212 offset:8192\n"
    "v_mfma_f32_16x16x32_bf16 v[68:71], a[140:143], v[148:151], v[68:71]\n"
    "v_exp_f32 v134, v134\n"
    "v_mfma_f32_16x16x32_bf16 v[72:75], a[144:147], v[144:147], v[72:75]\n"
    "ds_read_b128 a[100:103], v213 offset:8192\n"
    "v_cvt_pk_bf16_f32 v154, v132, v133\n"
    "v_mfma_f32_16x16x32_bf16 v[72:75], a[148:151], v[148:151], v[72:75]\n"
    "v_exp_f32 v135, v135\n"
    "v_mfma_f32_16x16x32_bf16 v[76:79], a[152:155], v[144:147], v[76:79]\n"
    "ds_read_b128 a[104:107], v214 offset:8192\n"
    "v_mfma_f32_16x16x32_bf16 v[76:79], a[156:159], v[148:151], v[76:79]\n"
    "v_exp_f32 v136, v136\n"
    "v_mfma_f32_16x16x32_bf16 v[80:83], a[160:163], v[144:147], v[80:83]\n"
    "ds_read_b128 a[108:111], v215 offset:8192\n"
    "v_cvt_pk_bf16_f32 v155, v134, v135\n"
    "v_mfma_f32_16x16x32_bf16 v[80:83], a[164:167], v[148:151], v[80:83]\n"
    "v_exp_f32 v137, v137\n"
    "v_mfma_f32_16x16x32_bf16 v[84:87], a[168:171], v[144:147], v[84:87]\n"
    "ds_read_b128 a[112:115], v212 offset:12288\n"
    "v_mfma_f32_16x16x32_bf16 v[84:87], a[172:175], v[148:151], v[84:87]\n"
    "v_exp_f32 v138, v138\n"
    "v_mfma_f32_16x16x32_bf16 v[88:91], a[176:179], v[144:147], v[88:91]\n"
    "ds_read_b128 a[116:119], v213 offset:12288\n"
    "v_cvt_pk_bf16_f32 v156, v136, v137\n"
    "v_mfma_f32_16x16x32_bf16 v[88:91], a[180:183], v[148:151], v[88:91]\n"
    "v_exp_f32 v139, v139\n"
    "v_mfma_f32_16x16x32_bf16 v[92:95], a[184:187], v[144:147], v[92:95]\n"
    "ds_read_b128 a[120:123], v214 offset:12288\n"
    "v_mfma_f32_16x16x32_bf16 v[92:95], a[188:191], v[148:151], v[92:95]\n"
    "v_exp_f32 v140, v140\n"
    "v_mfma_f32_16x16x32_bf16 v[184:187], v[208:211], v[144:147], v[184:187]\n"
    "ds_read_b128 a[124:127], v215 offset:12288\n"
    "v_cvt_pk_bf16_f32 v157, v138, v139\n"
    "v_mfma_f32_16x16x32_bf16 v[184:187], v[208:211], v[148:151], v[184:187]\n"
    "s_waitcnt lgkmcnt(4)\n"
    "v_exp_f32 v141, v141\n"
    "v_mfma_f32_16x16x32_bf16 v[128:131], a[64:67], a[0:3], v[160:163]\n"
    "ds_read_b64_tr_b16 a[236:237], v221 offset:9216\n"
    "ds_read_b64_tr_b16 a[238:239], v221 offset:13312\n"
    "v_mfma_f32_16x16x32_bf16 v[128:131], a[68:71], a[4:7], v[128:131]\n"
    "v_exp_f32 v142, v142\n"
    "v_mfma_f32_16x16x32_bf16 v[128:131], a[72:75], a[8:11], v[128:131]\n"
    "ds_read_b64_tr_b16 a[240:241], v220 offset:1536\n"
    "v_cvt_pk_bf16_f32 v158, v140, v141\n"
    "v_mfma_f32_16x16x32_bf16 v[128:131], a[76:79], a[12:15], v[128:131]\n"
    "v_exp_f32 v143, v143\n"
    "v_mfma_f32_16x16x32_bf16 v[132:135], a[80:83], a[0:3], v[160:163]\n"
    "ds_read_b64_tr_b16 a[242:243], v220 offset:5632\n"
    "ds_read_b64_tr_b16 a[244:245], v220 offset:9728\n"
    "v_mfma_f32_16x16x32_bf16 v[132:135], a[84:87], a[4:7], v[132:135]\n"
    "v_mfma_f32_16x16x32_bf16 v[132:135], a[88:91], a[8:11], v[132:135]\n"
    "ds_read_b64_tr_b16 a[246:247], v220 offset:13824\n"
    "v_cvt_pk_bf16_f32 v159, v142, v143\n"
    "v_mfma_f32_16x16x32_bf16 v[132:135], a[92:95], a[12:15], v[132:135]\n"
    "s_waitcnt lgkmcnt(6)\n"
    "v_exp_f32 v128, v128\n"
    "v_mfma_f32_16x16x32_bf16 v[136:139], a[96:99], a[0:3], v[160:163]\n"
    "ds_read_b64_tr_b16 a[248:249], v221 offset:1536\n"
    "ds_read_b64_tr_b16 a[250:251], v221 offset:5632\n"
    "v_mfma_f32_16x16x32_bf16 v[136:139], a[100:103], a[4:7], v[136:139]\n"
    "v_exp_f32 v129, v129\n"
    "v_mfma_f32_16x16x32_bf16 v[136:139], a[104:107], a[8:11], v[136:139]\n"
    "ds_read_b64_tr_b16 a[252:253], v221 offset:9728\n"
    "v_mfma_f32_16x16x32_bf16 v[136:139], a[108:111], a[12:15], v[136:139]\n"
    "v_exp_f32 v130, v130\n"
    "v_mfma_f32_16x16x32_bf16 v[140:143], a[112:115], a[0:3], v[160:163]\n"
    "ds_read_b64_tr_b16 a[254:255], v221 offset:13824\n"
    "v_cvt_pk_bf16_f32 v144, v128, v129\n"
    "v_mfma_f32_16x16x32_bf16 v[140:143], a[116:119], a[4:7], v[140:143]\n"
    "s_add_u32 m0, s85, 50176\n"
    "v_exp_f32 v131, v131\n"
    "v_mfma_f32_16x16x32_bf16 v[140:143], a[120:123], a[8:11], v[140:143]\n"
    "buffer_load_dwordx4 v252, s[40:43], s84 offen offset:0 nt lds\n"
    "v_mfma_f32_16x16x32_bf16 v[140:143], a[124:127], a[12:15], v[140:143]\n"
    "v_exp_f32 v132, v132\n"
    "v_mfma_f32_16x16x32_bf16 v[96:99], a[128:131], v[152:155], v[96:99]\n"
    "v_cvt_pk_bf16_f32 v145, v130, v131\n"
    "v_mfma_f32_16x16x32_bf16 v[96:99], a[132:135], v[156:159], v[96:99]\n"
    "v_exp_f32 v133, v133\n"
    "v_mfma_f32_16x16x32_bf16 v[100:103], a[136:139], v[152:155], v[100:103]\n"
    "buffer_load_dwordx4 v252, s[40:43], s84 offen offset:2048 nt lds\n"
    "v_mfma_f32_16x16x32_bf16 v[100:103], a[140:143], v[156:159], v[100:103]\n"
    "v_exp_f32 v134, v134\n"
    "v_mfma_f32_16x16x32_bf16 v[104:107], a[144:147], v[152:155], v[104:107]\n"
    "v_cvt_pk_bf16_f32 v146, v132, v133\n"
    "v_mfma_f32_16x16x32_bf16 v[104:107], a[148:151], v[156:159], v[104:107]\n"
    "v_exp_f32 v135, v135\n"
    "v_mfma_f32_16x16x32_bf16 v[108:111], a[152:155], v[152:155], v[108:111]\n"
    "v_mfma_f32_16x16x32_bf16 v[108:111], a[156:159], v[156:159], v[108:111]\n"
    "v_exp_f32 v136, v136\n"
    "v_mfma_f32_16x16x32_bf16 v[112:115], a[160:163], v[152:155], v[112:115]\n"
    "v_cvt_pk_bf16_f32 v147, v134, v135\n"
    "v_mfma_f32_16x16x32_bf16 v[112:115], a[164:167], v[156:159], v[112:115]\n"
    "v_exp_f32 v137, v137\n"
    "v_mfma_f32_16x16x32_bf16 v[116:119], a[168:171], v[152:155], v[116:119]\n"
    "s_add_u32 s84, s84, 16384\n"
    "v_mfma_f32_16x16x32_bf16 v[116:119], a[172:175], v[156:159], v[116:119]\n"
    "s_add_u32 s85, s85, 32768\n"
    "v_exp_f32 v138, v138\n"
    "v_mfma_f32_16x16x32_bf16 v[120:123], a[176:179], v[152:155], v[120:123]\n"
    "s_and_b32 s85, s85, 0x1ffff\n"
    "v_cvt_pk_bf16_f32 v148, v136, v137\n"
    "v_mfma_f32_16x16x32_bf16 v[120:123], a[180:183], v[156:159], v[120:123]\n"
    "v_exp_f32 v139, v139\n"
    "v_mfma_f32_16x16x32_bf16 v[124:127], a[184:187], v[152:155], v[124:127]\n"
    "v_mfma_f32_16x16x32_bf16 v[124:127], a[188:191], v[156:159], v[124:127]\n"
    "v_exp_f32 v140, v140\n"
    "v_mfma_f32_16x16x32_bf16 v[188:191], v[208:211], v[152:155], v[188:191]\n"
    "v_cvt_pk_bf16_f32 v149, v138, v139\n"
    "v_mfma_f32_16x16x32_bf16 v[188:191], v[208:211], v[156:159], v[188:191]\n"
    "v_exp_f32 v141, v141\n"
    "v_mfma_f32_16x16x32_bf16 v[128:131], a[64:67], a[16:19], v[164:167]\n"
    "s_add_u32 s76, s76, 32768\n"
    "s_and_b32 s76, s76, 0x1ffff\n"
    "v_add_u32 v220, s76, v222\n"
    "v_mfma_f32_16x16x32_bf16 v[128:131], a[68:71], a[20:23], v[128:131]\n"
    "s_waitcnt vmcnt(8)\n"
    "v_exp_f32 v142, v142\n"
    "v_mfma_f32_16x16x32_bf16 v[128:131], a[72:75], a[24:27], v[128:131]\n"
    "s_barrier\n"
    "v_cvt_pk_bf16_f32 v150, v140, v141\n"
    "v_add_u32 v221, s76, v223\n"
    "v_mfma_f32_16x16x32_bf16 v[128:131], a[76:79], a[28:31], v[128:131]\n"
    "v_exp_f32 v143, v143\n"
    "v_mfma_f32_16x16x32_bf16 v[132:135], a[80:83], a[16:19], v[164:167]\n"
    "ds_read_b64_tr_b16 a[128:129], v220 offset:0\n"
    "v_mfma_f32_16x16x32_bf16 v[132:135], a[84:87], a[20:23], v[132:135]\n"
    "ds_read_b64_tr_b16 a[130:131], v220 offset:4096\n"
    "v_mfma_f32_16x16x32_bf16 v[132:135], a[88:91], a[24:27], v[132:135]\n"
    "ds_read_b64_tr_b16 a[132:133], v220 offset:8192\n"
    "v_cvt_pk_bf16_f32 v151, v142, v143\n"
    "v_mfma_f32_16x16x32_bf16 v[132:135], a[92:95], a[28:31], v[132:135]\n"
    "v_exp_f32 v128, v128\n"
    "v_mfma_f32_16x16x32_bf16 v[136:139], a[96:99], a[16:19], v[164:167]\n"
    "ds_read_b64_tr_b16 a[134:135], v220 offset:12288\n"
    "v_mfma_f32_16x16x32_bf16 v[136:139], a[100:103], a[20:23], v[136:139]\n"
    "v_exp_f32 v129, v129\n"
    "v_mfma_f32_16x16x32_bf16 v[136:139], a[104:107], a[24:27], v[136:139]\n"
    "ds_read_b64_tr_b16 a[136:137], v221 offset:0\n"
    "v_mfma_f32_16x16x32_bf16 v[136:139], a[108:111], a[28:31], v[136:139]\n"
    "v_exp_f32 v130, v130\n"
    "v_mfma_f32_16x16x32_bf16 v[140:143], a[112:115], a[16:19], v[164:167]\n"
    "ds_read_b64_tr_b16 a[138:139], v221 offset:4096\n"
    "v_cvt_pk_bf16_f32 v152, v128, v129\n"
    "v_mfma_f32_16x16x32_bf16 v[140:143], a[116:119], a[20:23], v[140:143]\n"
    "s_add_u32 m0, s85, 32768\n"
    "v_exp_f32 v131, v131\n"
    "v_mfma_f32_16x16x32_bf16 v[140:143], a[120:123], a[24:27], v[140:143]\n"
    "buffer_load_dwordx4 v247, s[36:39], s84 offen offset:0 nt lds\n"
    "v_mfma_f32_16x16x32_bf16 v[140:143], a[124:127], a[28:31], v[140:143]\n"
    "s_waitcnt lgkmcnt(6)\n"
    "v_exp_f32 v132, v132\n"
    "v_mfma_f32_16x16x32_bf16 v[0:3], a[192:195], v[144:147], v[0:3]\n"
    "ds_read_b64_tr_b16 a[140:141], v221 offset:8192\n"
    "v_cvt_pk_bf16_f32 v153, v130, v131\n"
    "v_mfma_f32_16x16x32_bf16 v[0:3], a[196:199], v[148:151], v[0:3]\n"
    "v_exp_f32 v133, v133\n"
    "v_mfma_f32_16x16x32_bf16 v[4:7], a[200:203], v[144:147], v[4:7]\n"
    "buffer_load_dwordx4 v248, s[36:39], s84 offen offset:1024 nt lds\n"
    "v_mfma_f32_16x16x32_bf16 v[4:7], a[204:207], v[148:151], v[4:7]\n"
    "v_exp_f32 v134, v134\n"
    "v_mfma_f32_16x16x32_bf16 v[8:11], a[208:211], v[144:147], v[8:11]\n"
    "ds_read_b64_tr_b16 a[142:143], v221 offset:12288\n"
    "v_cvt_pk_bf16_f32 v154, v132, v133\n"
    "v_mfma_f32_16x16x32_bf16 v[8:11], a[212:215], v[148:151], v[8:11]\n"
    "v_exp_f32 v135, v135\n"
    "v_mfma_f32_16x16x32_bf16 v[12:15], a[216:219], v[144:147], v[12:15]\n"
    "buffer_load_dwordx4 v249, s[36:39], s84 offen offset:2048 nt lds\n"
    "v_mfma_f32_16x16x32_bf16 v[12:15], a[220:223], v[148:151], v[12:15]\n"
    "v_exp_f32 v136, v136\n"
    "v_mfma_f32_16x16x32_bf16 v[16:19], a[224:227], v[144:147], v[16:19]\n"
    "ds_read_b64_tr_b16 a[144:145], v220 offset:512\n"
    "v_cvt_pk_bf16_f32 v155, v134, v135\n"
    "v_mfma_f32_16x16x32_bf16 v[16:19], a[228:231], v[148:151], v[16:19]\n"
    "v_exp_f32 v137, v137\n"
    "v_mfma_f32_16x16x32_bf16 v[20:23], a[232:235], v[144:147], v[20:23]\n"
    "v_mfma_f32_16x16x32_bf16 v[20:23], a[236:239], v[148:151], v[20:23]\n"
    "v_exp_f32 v138, v138\n"
    "v_mfma_f32_16x16x32_bf16 v[24:27], a[240:243], v[144:147], v[24:27]\n"
    "ds_read_b64_tr_b16 a[146:147], v220 offset:4608\n"
    "v_cvt_pk_bf16_f32 v156, v136, v137\n"
    "v_mfma_f32_16x16x32_bf16 v[24:27], a[244:247], v[148:151], v[24:27]\n"
    "v_exp_f32 v139, v139\n"
    "v_mfma_f32_16x16x32_bf16 v[28:31], a[248:251], v[144:147], v[28:31]\n"
    "v_mfma_f32_16x16x32_bf16 v[28:31], a[252:255], v[148:151], v[28:31]\n"
    "v_exp_f32 v140, v140\n"
    "v_mfma_f32_16x16x32_bf16 v[176:179], v[208:211], v[144:147], v[176:179]\n"
    "ds_read_b64_tr_b16 a[148:149], v220 offset:8704\n"
    "v_cvt_pk_bf16_f32 v157, v138, v139\n"
    "v_mfma_f32_16x16x32_bf16 v[176:179], v[208:211], v[148:151], v[176:179]\n"
    "v_exp_f32 v141, v141\n"
    "v_mfma_f32_16x16x32_bf16 v[128:131], a[64:67], a[32:35], v[168:171]\n"
    "ds_read_b64_tr_b16 a[150:151], v220 offset:12800\n"
    "v_mfma_f32_16x16x32_bf16 v[128:131], a[68:71], a[36:39], v[128:131]\n"
    "v_exp_f32 v142, v142\n"
    "v_mfma_f32_16x16x32_bf16 v[128:131], a[72:75], a[40:43], v[128:131]\n"
    "ds_read_b64_tr_b16 a[152:153], v221 offset:512\n"
    "v_cvt_pk_bf16_f32 v158, v140, v141\n"
    "v_mfma_f32_16x16x32_bf16 v[128:131], a[76:79], a[44:47], v[128:131]\n"
    "v_exp_f32 v143, v143\n"
    "v_mfma_f32_16x16x32_bf16 v[132:135], a[80:83], a[32:35], v[168:171]\n"
    "ds_read_b64_tr_b16 a[154:155], v221 offset:4608\n"
    "v_mfma_f32_16x16x32_bf16 v[132:135], a[84:87], a[36:39], v[132:135]\n"
    "v_add_u32 v214, s76, v218\n"
    "v_add_u32 v215, s76, v219\n"
    "v_mfma_f32_16x16x32_bf16 v[132:135], a[88:91], a[40:43], v[132:135]\n"
    "ds_read_b64_tr_b16 a[156:157], v221 offset:8704\n"
    "v_cvt_pk_bf16_f32 v159, v142, v143\n"
    "v_mfma_f32_16x16x32_bf16 v[132:135], a[92:95], a[44:47], v[132:135]\n"
    "v_exp_f32 v128, v128\n"
    "v_mfma_f32_16x16x32_bf16 v[136:139], a[96:99], a[32:35], v[168:171]\n"
    "ds_read_b64_tr_b16 a[158:159], v221 offset:12800\n"
    "v_mfma_f32_16x16x32_bf16 v[136:139], a[100:103], a[36:39], v[136:139]\n"
    "v_exp_f32 v129, v129\n"
    "v_mfma_f32_16x16x32_bf16 v[136:139], a[104:107], a[40:43], v[136:139]\n"
    "ds_read_b64_tr_b16 a[160:161], v220 offset:1024\n"
    "v_mfma_f32_16x16x32_bf16 v[136:139], a[108:111], a[44:47], v[136:139]\n"
    "v_exp_f32 v130, v130\n"
    "v_mfma_f32_16x16x32_bf16 v[140:143], a[112:115], a[32:35], v[168:171]\n"
    "ds_read_b64_tr_b16 a[162:163], v220 offset:5120\n"
    "v_cvt_pk_bf16_f32 v144, v128, v129\n"
    "v_mfma_f32_16x16x32_bf16 v[140:143], a[116:119], a[36:39], v[140:143]\n"
    "v_exp_f32 v131, v131\n"
    "v_mfma_f32_16x16x32_bf16 v[140:143], a[120:123], a[40:43], v[140:143]\n"
    "buffer_load_dwordx4 v250, s[36:39], s84 offen offset:3072 nt lds\n"
    "v_mfma_f32_16x16x32_bf16 v[140:143], a[124:127], a[44:47], v[140:143]\n"
    "v_exp_f32 v132, v132\n"
    "v_mfma_f32_16x16x32_bf16 v[32:35], a[192:195], v[152:155], v[32:35]\n"
    "ds_read_b64_tr_b16 a[164:165], v220 offset:9216\n"
    "v_cvt_pk_bf16_f32 v145, v130, v131\n"
    "v_mfma_f32_16x16x32_bf16 v[32:35], a[196:199], v[156:159], v[32:35]\n"
    "s_add_u32 m0, s85, 49152\n"
    "v_exp_f32 v133, v133\n"
    "v_mfma_f32_16x16x32_bf16 v[36:39], a[200:203], v[152:155], v[36:39]\n"
    "buffer_load_dwordx4 v251, s[40:43], s84 offen offset:0 nt lds\n"
    "v_mfma_f32_16x16x32_bf16 v[36:39], a[204:207], v[156:159], v[36:39]\n"
    "v_exp_f32 v134, v134\n"
    "v_mfma_f32_16x16x32_bf16 v[40:43], a[208:211], v[152:155], v[40:43]\n"
    "ds_read_b64_tr_b16 a[166:167], v220 offset:13312\n"
    "v_cvt_pk_bf16_f32 v146, v132, v133\n"
    "v_mfma_f32_16x16x32_bf16 v[40:43], a[212:215], v[156:159], v[40:43]\n"
    "v_exp_f32 v135, v135\n"
    "v_mfma_f32_16x16x32_bf16 v[44:47], a[216:219], v[152:155], v[44:47]\n"
    "buffer_load_dwordx4 v251, s[40:43], s84 offen offset:2048 nt lds\n"
    "v_mfma_f32_16x16x32_bf16 v[44:47], a[220:223], v[156:159], v[44:47]\n"
    "v_exp_f32 v136, v136\n"
    "v_mfma_f32_16x16x32_bf16 v[48:51], a[224:227], v[152:155], v[48:51]\n"
    "ds_read_b64_tr_b16 a[168:169], v221 offset:1024\n"
    "v_cvt_pk_bf16_f32 v147, v134, v135\n"
    "v_mfma_f32_16x16x32_bf16 v[48:51], a[228:231], v[156:159], v[48:51]\n"
    "v_exp_f32 v137, v137\n"
    "v_mfma_f32_16x16x32_bf16 v[52:55], a[232:235], v[152:155], v[52:55]\n"
    "v_mfma_f32_16x16x32_bf16 v[52:55], a[236:239], v[156:159], v[52:55]\n"
    "v_exp_f32 v138, v138\n"
    "v_mfma_f32_16x16x32_bf16 v[56:59], a[240:243], v[152:155], v[56:59]\n"
    "ds_read_b64_tr_b16 a[170:171], v221 offset:5120\n"
    "v_cvt_pk_bf16_f32 v148, v136, v137\n"
    "v_mfma_f32_16x16x32_bf16 v[56:59], a[244:247], v[156:159], v[56:59]\n"
    "v_exp_f32 v139, v139\n"
    "v_mfma_f32_16x16x32_bf16 v[60:63], a[248:251], v[152:155], v[60:63]\n"
    "v_add_u32 v212, s76, v216\n"
    "v_add_u32 v213, s76, v217\n"
    "v_mfma_f32_16x16x32_bf16 v[60:63], a[252:255], v[156:159], v[60:63]\n"
    "v_exp_f32 v140, v140\n"
    "v_mfma_f32_16x16x32_bf16 v[180:183], v[208:211], v[152:155], v[180:183]\n"
    "v_cvt_pk_bf16_f32 v149, v138, v139\n"
    "v_mfma_f32_16x16x32_bf16 v[180:183], v[208:211], v[156:159], v[180:183]\n"
    "v_exp_f32 v141, v141\n"
    "v_mfma_f32_16x16x32_bf16 v[128:131], a[64:67], a[48:51], v[172:175]\n"
    "v_mfma_f32_16x16x32_bf16 v[128:131], a[68:71], a[52:55], v[128:131]\n"
    "v_exp_f32 v142, v142\n"
    "v_mfma_f32_16x16x32_bf16 v[128:131], a[72:75], a[56:59], v[128:131]\n"
    "ds_read_b128 a[64:67], v212 offset:0\n"
    "v_cvt_pk_bf16_f32 v150, v140, v141\n"
    "v_mfma_f32_16x16x32_bf16 v[128:131], a[76:79], a[60:63], v[128:131]\n"
    "v_exp_f32 v143, v143\n"
    "v_mfma_f32_16x16x32_bf16 v[132:135], a[80:83], a[48:51], v[172:175]\n"
    "ds_read_b128 a[68:71], v213 offset:0\n"
    "v_mfma_f32_16x16x32_bf16 v[132:135], a[84:87], a[52:55], v[132:135]\n"
    "v_mfma_f32_16x16x32_bf16 v[132:135], a[88:91], a[56:59], v[132:135]\n"
    "ds_read_b128 a[72:75], v214 offset:0\n"
    "v_cvt_pk_bf16_f32 v151, v142, v143\n"
    "v_mfma_f32_16x16x32_bf16 v[132:135], a[92:95], a[60:63], v[132:135]\n"
    "v_exp_f32 v128, v128\n"
    "v_mfma_f32_16x16x32_bf16 v[136:139], a[96:99], a[48:51], v[172:175]\n"
    "ds_read_b128 a[76:79], v215 offset:0\n"
    "v_mfma_f32_16x16x32_bf16 v[136:139], a[100:103], a[52:55], v[136:139]\n"
    "v_exp_f32 v129, v129\n"
    "v_mfma_f32_16x16x32_bf16 v[136:139], a[104:107], a[56:59], v[136:139]\n"
    "ds_read_b128 a[80:83], v212 offset:4096\n"
    "v_mfma_f32_16x16x32_bf16 v[136:139], a[108:111], a[60:63], v[136:139]\n"
    "v_exp_f32 v130, v130\n"
    "v_mfma_f32_16x16x32_bf16 v[140:143], a[112:115], a[48:51], v[172:175]\n"
    "ds_read_b128 a[84:87], v213 offset:4096\n"
    "v_cvt_pk_bf16_f32 v152, v128, v129\n"
    "v_mfma_f32_16x16x32_bf16 v[140:143], a[116:119], a[52:55], v[140:143]\n"
    "v_exp_f32 v131, v131\n"
    "v_mfma_f32_16x16x32_bf16 v[140:143], a[120:123], a[56:59], v[140:143]\n"
    "ds_read_b128 a[88:91], v214 offset:4096\n"
    "v_mfma_f32_16x16x32_bf16 v[140:143], a[124:127], a[60:63], v[140:143]\n"
    "v_exp_f32 v132, v132\n"
    "v_mfma_f32_16x16x32_bf16 v[64:67], a[192:195], v[144:147], v[64:67]\n"
    "ds_read_b128 a[92:95], v215 offset:4096\n"
    "v_cvt_pk_bf16_f32 v153, v130, v131\n"
    "v_mfma_f32_16x16x32_bf16 v[64:67], a[196:199], v[148:151], v[64:67]\n"
    "v_exp_f32 v133, v133\n"
    "v_mfma_f32_16x16x32_bf16 v[68:71], a[200:203], v[144:147], v[68:71]\n"
    "ds_read_b128 a[96:99], v212 offset:8192\n"
    "v_mfma_f32_16x16x32_bf16 v[68:71], a[204:207], v[148:151], v[68:71]\n"
    "v_exp_f32 v134, v134\n"
    "v_mfma_f32_16x16x32_bf16 v[72:75], a[208:211], v[144:147], v[72:75]\n"
    "ds_read_b128 a[100:103], v213 offset:8192\n"
    "v_cvt_pk_bf16_f32 v154, v132, v133\n"
    "v_mfma_f32_16x16x32_bf16 v[72:75], a[212:215], v[148:151], v[72:75]\n"
    "v_exp_f32 v135, v135\n"
    "v_mfma_f32_16x16x32_bf16 v[76:79], a[216:219], v[144:147], v[76:79]\n"
    "ds_read_b128 a[104:107], v214 offset:8192\n"
    "v_mfma_f32_16x16x32_bf16 v[76:79], a[220:223], v[148:151], v[76:79]\n"
    "v_exp_f32 v136, v136\n"
    "v_mfma_f32_16x16x32_bf16 v[80:83], a[224:227], v[144:147], v[80:83]\n"
    "ds_read_b128 a[108:111], v215 offset:8192\n"
    "v_cvt_pk_bf16_f32 v155, v134, v135\n"
    "v_mfma_f32_16x16x32_bf16 v[80:83], a[228:231], v[148:151], v[80:83]\n"
    "v_exp_f32 v137, v137\n"
    "v_mfma_f32_16x16x32_bf16 v[84:87], a[232:235], v[144:147], v[84:87]\n"
    "ds_read_b128 a[112:115], v212 offset:12288\n"
    "v_mfma_f32_16x16x32_bf16 v[84:87], a[236:239], v[148:151], v[84:87]\n"
    "v_exp_f32 v138, v138\n"
    "v_mfma_f32_16x16x32_bf16 v[88:91], a[240:243], v[144:147], v[88:91]\n"
    "ds_read_b128 a[116:119], v213 offset:12288\n"
    "v_cvt_pk_bf16_f32 v156, v136, v137\n"
    "v_mfma_f32_16x16x32_bf16 v[88:91], a[244:247], v[148:151], v[88:91]\n"
    "v_exp_f32 v139, v139\n"
    "v_mfma_f32_16x16x32_bf16 v[92:95], a[248:251], v[144:147], v[92:95]\n"
    "ds_read_b128 a[120:123], v214 offset:12288\n"
    "v_mfma_f32_16x16x32_bf16 v[92:95], a[252:255], v[148:151], v[92:95]\n"
    "v_exp_f32 v140, v140\n"
    "v_mfma_f32_16x16x32_bf16 v[184:187], v[208:211], v[144:147], v[184:187]\n"
    "ds_read_b128 a[124:127], v215 offset:12288\n"
    "v_cvt_pk_bf16_f32 v157, v138, v139\n"
    "v_mfma_f32_16x16x32_bf16 v[184:187], v[208:211], v[148:151], v[184:187]\n"
    "s_waitcnt lgkmcnt(4)\n"
    "v_exp_f32 v141, v141\n"
    "v_mfma_f32_16x16x32_bf16 v[128:131], a[64:67], a[0:3], v[160:163]\n"
    "ds_read_b64_tr_b16 a[172:173], v221 offset:9216\n"
    "ds_read_b64_tr_b16 a[174:175], v221 offset:13312\n"
    "v_mfma_f32_16x16x32_bf16 v[128:131], a[68:71], a[4:7], v[128:131]\n"
    "v_exp_f32 v142, v142\n"
    "v_mfma_f32_16x16x32_bf16 v[128:131], a[72:75], a[8:11], v[128:131]\n"
    "ds_read_b64_tr_b16 a[176:177], v220 offset:1536\n"
    "v_cvt_pk_bf16_f32 v158, v140, v141\n"
    "v_mfma_f32_16x16x32_bf16 v[128:131], a[76:79], a[12:15], v[128:131]\n"
    "v_exp_f32 v143, v143\n"
    "v_mfma_f32_16x16x32_bf16 v[132:135], a[80:83], a[0:3], v[160:163]\n"
    "ds_read_b64_tr_b16 a[178:179], v220 offset:5632\n"
    "ds_read_b64_tr_b16 a[180:181], v220 offset:9728\n"
    "v_mfma_f32_16x16x32_bf16 v[132:135], a[84:87], a[4:7], v[132:135]\n"
    "v_mfma_f32_16x16x32_bf16 v[132:135], a[88:91], a[8:11], v[132:135]\n"
    "ds_read_b64_tr_b16 a[182:183], v220 offset:13824\n"
    "v_cvt_pk_bf16_f32 v159, v142, v143\n"
    "v_mfma_f32_16x16x32_bf16 v[132:135], a[92:95], a[12:15], v[132:135]\n"
    "s_waitcnt lgkmcnt(6)\n"
    "v_exp_f32 v128, v128\n"
    "v_mfma_f32_16x16x32_bf16 v[136:139], a[96:99], a[0:3], v[160:163]\n"
    "ds_read_b64_tr_b16 a[184:185], v221 offset:1536\n"
    "ds_read_b64_tr_b16 a[186:187], v221 offset:5632\n"
    "v_mfma_f32_16x16x32_bf16 v[136:139], a[100:103], a[4:7], v[136:139]\n"
    "v_exp_f32 v129, v129\n"
    "v_mfma_f32_16x16x32_bf16 v[136:139], a[104:107], a[8:11], v[136:139]\n"
    "ds_read_b64_tr_b16 a[188:189], v221 offset:9728\n"
    "v_mfma_f32_16x16x32_bf16 v[136:139], a[108:111], a[12:15], v[136:139]\n"
    "v_exp_f32 v130, v130\n"
    "v_mfma_f32_16x16x32_bf16 v[140:143], a[112:115], a[0:3], v[160:163]\n"
    "ds_read_b64_tr_b16 a[190:191], v221 offset:13824\n"
    "v_cvt_pk_bf16_f32 v144, v128, v129\n"
    "v_mfma_f32_16x16x32_bf16 v[140:143], a[116:119], a[4:7], v[140:143]\n"
    "s_add_u32 m0, s85, 50176\n"
    "v_exp_f32 v131, v131\n"
    "v_mfma_f32_16x16x32_bf16 v[140:143], a[120:123], a[8:11], v[140:143]\n"
    "buffer_load_dwordx4 v252, s[40:43], s84 offen offset:0 nt lds\n"
    "v_mfma_f32_16x16x32_bf16 v[140:143], a[124:127], a[12:15], v[140:143]\n"
    "v_exp_f32 v132, v132\n"
    "v_mfma_f32_16x16x32_bf16 v[96:99], a[192:195], v[152:155], v[96:99]\n"
    "v_cvt_pk_bf16_f32 v145, v130, v131\n"
    "v_mfma_f32_16x16x32_bf16 v[96:99], a[196:199], v[156:159], v[96:99]\n"
    "v_exp_f32 v133, v133\n"
    "v_mfma_f32_16x16x32_bf16 v[100:103], a[200:203], v[152:155], v[100:103]\n"
    "buffer_load_dwordx4 v252, s[40:43], s84 offen offset:2048 nt lds\n"
    "v_mfma_f32_16x16x32_bf16 v[100:103], a[204:207], v[156:159], v[100:103]\n"
    "v_exp_f32 v134, v134\n"
    "v_mfma_f32_16x16x32_bf16 v[104:107], a[208:211], v[152:155], v[104:107]\n"
    "v_cvt_pk_bf16_f32 v146, v132, v133\n"
    "v_mfma_f32_16x16x32_bf16 v[104:107], a[212:215], v[156:159], v[104:107]\n"
    "v_exp_f32 v135, v135\n"
    "v_mfma_f32_16x16x32_bf16 v[108:111], a[216:219], v[152:155], v[108:111]\n"
    "v_mfma_f32_16x16x32_bf16 v[108:111], a[220:223], v[156:159], v[108:111]\n"
    "v_exp_f32 v136, v136\n"
    "v_mfma_f32_16x16x32_bf16 v[112:115], a[224:227], v[152:155], v[112:115]\n"
    "v_cvt_pk_bf16_f32 v147, v134, v135\n"
    "v_mfma_f32_16x16x32_bf16 v[112:115], a[228:231], v[156:159], v[112:115]\n"
    "v_exp_f32 v137, v137\n"
    "v_mfma_f32_16x16x32_bf16 v[116:119], a[232:235], v[152:155], v[116:119]\n"
    "s_add_u32 s84, s84, 16384\n"
    "v_mfma_f32_16x16x32_bf16 v[116:119], a[236:239], v[156:159], v[116:119]\n"
    "s_add_u32 s85, s85, 32768\n"
    "v_exp_f32 v138, v138\n"
    "v_mfma_f32_16x16x32_bf16 v[120:123], a[240:243], v[152:155], v[120:123]\n"
    "s_and_b32 s85, s85, 0x1ffff\n"
    "v_cvt_pk_bf16_f32 v148, v136, v137\n"
    "v_mfma_f32_16x16x32_bf16 v[120:123], a[244:247], v[156:159], v[120:123]\n"
    "v_exp_f32 v139, v139\n"
    "v_mfma_f32_16x16x32_bf16 v[124:127], a[248:251], v[152:155], v[124:127]\n"
    "s_bitcmp1_b32 s6, s71\n"
    "s_cbranch_scc1 L_side_ev\n"
    "L_back_ev:\n"
    "v_mfma_f32_16x16x32_bf16 v[124:127], a[252:255], v[156:159], v[124:127]\n"
    "v_exp_f32 v140, v140\n"
    "v_mfma_f32_16x16x32_bf16 v[188:191], v[208:211], v[152:155], v[188:191]\n"
    "s_sub_u32 s71, s71, 1\n"
    "v_cvt_pk_bf16_f32 v149, v138, v139\n"
    "v_mfma_f32_16x16x32_bf16 v[188:191], v[208:211], v[156:159], v[188:191]\n"
    "s_cmp_lg_u32 s71, 0\n"
    "v_exp_f32 v141, v141\n"
    "s_cbranch_scc1 L_loop_att\n"
    "L_tail:\n"
    "v_mfma_f32_16x16x32_bf16 v[128:131], a[64:67], a[16:19], v[164:167]\n"
    "s_add_u32 s76, s76, 32768\n"
    "s_and_b32 s76, s76, 0x1ffff\n"
    "s_sub_u32 s4, 1, s10\n"
    "v_add_u32 v220, s76, v222\n"
    "v_mfma_f32_16x16x32_bf16 v[128:131], a[68:71], a[20:23], v[128:131]\n"
    "s_waitcnt vmcnt(0)\n"
    "s_xor_b32 s4, s4, s11\n"
    "v_exp_f32 v142, v142\n"
    "v_mfma_f32_16x16x32_bf16 v[128:131], a[72:75], a[24:27], v[128:131]\n"
    "s_barrier\n"
    "s_add_u32 s4, s4, s12\n"
    "v_cvt_pk_bf16_f32 v150, v140, v141\n"
    "v_add_u32 v221, s76, v223\n"
    "v_mfma_f32_16x16x32_bf16 v[128:131], a[76:79], a[28:31], v[128:131]\n"
    "s_lshl_b32 s8, s4, 12\n"
    "s_add_u32 s8, s8, s80\n"
    "v_exp_f32 v143, v143\n"
    "v_mfma_f32_16x16x32_bf16 v[132:135], a[80:83], a[16:19], v[164:167]\n"
    "s_mul_i32 s9, s4, 147456\n"
    "s_add_u32 s9, s9, s81\n"
    "s_mov_b32 s78, s9\n"
    "ds_read_b64_tr_b16 a[192:193], v220 offset:0\n"
    "v_mfma_f32_16x16x32_bf16 v[132:135], a[84:87], a[20:23], v[132:135]\n"
    "s_sub_u32 s32, s10, 1\n"
    "s_mov_b32 s33, s32\n"
    "s_mov_b32 s84, s8\n"
    "ds_read_b64_tr_b16 a[194:195], v220 offset:4096\n"
    "v_mfma_f32_16x16x32_bf16 v[132:135], a[88:91], a[24:27], v[132:135]\n"
    "s_add_u32 s85, s9, 73728\n"
    "s_lshl_b32 s6, s10, 1\n"
    "ds_read_b64_tr_b16 a[196:197], v220 offset:8192\n"
    "v_cvt_pk_bf16_f32 v151, v142, v143\n"
    "v_mfma_f32_16x16x32_bf16 v[132:135], a[92:95], a[28:31], v[132:135]\n"
    "s_sub_u32 s4, s6, s10\n"
    "s_xor_b32 s4, s4, s11\n"
    "v_exp_f32 v128, v128\n"
    "v_mfma_f32_16x16x32_bf16 v[136:139], a[96:99], a[16:19], v[164:167]\n"
    "s_add_u32 s4, s4, s12\n"
    "s_lshl_b32 s14, s4, 12\n"
    "s_add_u32 s14, s14, s80\n"
    "ds_read_b64_tr_b16 a[198:199], v220 offset:12288\n"
    "v_mfma_f32_16x16x32_bf16 v[136:139], a[100:103], a[20:23], v[136:139]\n"
    "s_add_u32 s15, s14, 2048\n"
    "s_mul_i32 s4, s4, 147456\n"
    "v_exp_f32 v129, v129\n"
    "v_mfma_f32_16x16x32_bf16 v[136:139], a[104:107], a[24:27], v[136:139]\n"
    "s_add_u32 s4, s4, s81\n"
    "s_add_u32 s6, s4, 73728\n"
    "ds_read_b64_tr_b16 a[200:201], v221 offset:0\n"
    "v_mfma_f32_16x16x32_bf16 v[136:139], a[108:111], a[28:31], v[136:139]\n"
    "v_exp_f32 v130, v130\n"
    "v_mfma_f32_16x16x32_bf16 v[140:143], a[112:115], a[16:19], v[164:167]\n"
    "ds_read_b64_tr_b16 a[202:203], v221 offset:4096\n"
    "v_cvt_pk_bf16_f32 v152, v128, v129\n"
    "v_mfma_f32_16x16x32_bf16 v[140:143], a[116:119], a[20:23], v[140:143]\n"
    "v_exp_f32 v131, v131\n"
    "v_mfma_f32_16x16x32_bf16 v[140:143], a[120:123], a[24:27], v[140:143]\n"
    "s_cmp_eq_u32 s10, 0\n"
    "s_cselect_b32 s85, s4, s85\n"
    "s_cselect_b32 s77, s6, s4\n"
    "v_mfma_f32_16x16x32_bf16 v[140:143], a[124:127], a[28:31], v[140:143]\n"
    "s_waitcnt lgkmcnt(6)\n"
    "v_exp_f32 v132, v132\n"
    "v_mfma_f32_16x16x32_bf16 v[0:3], a[128:131], v[144:147], v[0:3]\n"
    "ds_read_b64_tr_b16 a[204:205], v221 offset:8192\n"
    "v_cvt_pk_bf16_f32 v153, v130, v131\n"
    "v_mfma_f32_16x16x32_bf16 v[0:3], a[132:135], v[148:151], v[0:3]\n"
    "v_exp_f32 v133, v133\n"
    "v_mfma_f32_16x16x32_bf16 v[4:7], a[136:139], v[144:147], v[4:7]\n"
    "v_lshrrev_b32 v224, 2, v253\n"
    "v_and_b32 v224, 7, v224\n"
    "v_mfma_f32_16x16x32_bf16 v[4:7], a[140:143], v[148:151], v[4:7]\n"
    "v_exp_f32 v134, v134\n"
    "v_mfma_f32_16x16x32_bf16 v[8:11], a[144:147], v[144:147], v[8:11]\n"
    "ds_read_b64_tr_b16 a[206:207], v221 offset:12288\n"
    "v_cvt_pk_bf16_f32 v154, v132, v133\n"
    "v_mfma_f32_16x16x32_bf16 v[8:11], a[148:151], v[148:151], v[8:11]\n"
    "v_exp_f32 v135, v135\n"
    "v_mfma_f32_16x16x32_bf16 v[12:15], a[152:155], v[144:147], v[12:15]\n"
    "v_and_b32 v225, 3, v253\n"
    "v_lshrrev_b32 v226, 3, v253\n"
    "v_mfma_f32_16x16x32_bf16 v[12:15], a[156:159], v[148:151], v[12:15]\n"
    "v_exp_f32 v136, v136\n"
    "v_mfma_f32_16x16x32_bf16 v[16:19], a[160:163], v[144:147], v[16:19]\n"
    "ds_read_b64_tr_b16 a[208:209], v220 offset:512\n"
    "v_cvt_pk_bf16_f32 v155, v134, v135\n"
    "v_mfma_f32_16x16x32_bf16 v[16:19], a[164:167], v[148:151], v[16:19]\n"
    "v_exp_f32 v137, v137\n"
    "v_mfma_f32_16x16x32_bf16 v[20:23], a[168:171], v[144:147], v[20:23]\n"
    "v_and_b32 v226, 2, v226\n"
    "v_xor_b32 v226, v226, v225\n"
    "v_mfma_f32_16x16x32_bf16 v[20:23], a[172:175], v[148:151], v[20:23]\n"
    "v_exp_f32 v138, v138\n"
    "v_mfma_f32_16x16x32_bf16 v[24:27], a[176:179], v[144:147], v[24:27]\n"
    "ds_read_b64_tr_b16 a[210:211], v220 offset:4608\n"
    "v_cvt_pk_bf16_f32 v156, v136, v137\n"
    "v_mfma_f32_16x16x32_bf16 v[24:27], a[180:183], v[148:151], v[24:27]\n"
    "v_exp_f32 v139, v139\n"
    "v_mfma_f32_16x16x32_bf16 v[28:31], a[184:187], v[144:147], v[28:31]\n"
    "v_mul_u32_u24 v227, 1152, v224\n"
    "v_lshl_add_u32 v245, v226, 4, v227\n"
    "v_mfma_f32_16x16x32_bf16 v[28:31], a[188:191], v[148:151], v[28:31]\n"
    "v_exp_f32 v140, v140\n"
    "v_mfma_f32_16x16x32_bf16 v[176:179], v[208:211], v[144:147], v[176:179]\n"
    "ds_read_b64_tr_b16 a[212:213], v220 offset:8704\n"
    "v_cvt_pk_bf16_f32 v157, v138, v139\n"
    "v_mfma_f32_16x16x32_bf16 v[176:179], v[208:211], v[148:151], v[176:179]\n"
    "v_exp_f32 v141, v141\n"
    "v_mfma_f32_16x16x32_bf16 v[128:131], a[64:67], a[32:35], v[168:171]\n"
    "ds_read_b64_tr_b16 a[214:215], v220 offset:12800\n"
    "v_lshrrev_b32 v226, 5, v253\n"
    "v_mfma_f32_16x16x32_bf16 v[128:131], a[68:71], a[36:39], v[128:131]\n"
    "v_exp_f32 v142, v142\n"
    "v_mfma_f32_16x16x32_bf16 v[128:131], a[72:75], a[40:43], v[128:131]\n"
    "ds_read_b64_tr_b16 a[216:217], v221 offset:512\n"
    "v_cvt_pk_bf16_f32 v158, v140, v141\n"
    "v_mfma_f32_16x16x32_bf16 v[128:131], a[76:79], a[44:47], v[128:131]\n"
    "v_exp_f32 v143, v143\n"
    "v_mfma_f32_16x16x32_bf16 v[132:135], a[80:83], a[32:35], v[168:171]\n"
    "ds_read_b64_tr_b16 a[218:219], v221 offset:4608\n"
    "v_lshl_add_u32 v247, v226, 6, v245\n"
    "v_mfma_f32_16x16x32_bf16 v[132:135], a[84:87], a[36:39], v[132:135]\n"
    "s_lshl_b32 s73, s75, 6\n"
    "v_add_u32 v214, s76, v218\n"
    "v_add_u32 v215, s76, v219\n"
    "v_mfma_f32_16x16x32_bf16 v[132:135], a[88:91], a[40:43], v[132:135]\n"
    "ds_read_b64_tr_b16 a[220:221], v221 offset:8704\n"
    "v_cvt_pk_bf16_f32 v159, v142, v143\n"
    "v_mfma_f32_16x16x32_bf16 v[132:135], a[92:95], a[44:47], v[132:135]\n"
    "v_exp_f32 v128, v128\n"
    "v_mfma_f32_16x16x32_bf16 v[136:139], a[96:99], a[32:35], v[168:171]\n"
    "ds_read_b64_tr_b16 a[222:223], v221 offset:12800\n"
    "v_add_u32 v247, s73, v247\n"
    "v_mfma_f32_16x16x32_bf16 v[136:139], a[100:103], a[36:39], v[136:139]\n"
    "s_lshl_b32 s73, s75, 9\n"
    "v_exp_f32 v129, v129\n"
    "v_mfma_f32_16x16x32_bf16 v[136:139], a[104:107], a[40:43], v[136:139]\n"
    "s_sub_u32 s73, 512, s73\n"
    "ds_read_b64_tr_b16 a[224:225], v220 offset:1024\n"
    "v_add_u32 v245, s73, v245\n"
    "v_mfma_f32_16x16x32_bf16 v[136:139], a[108:111], a[44:47], v[136:139]\n"
    "s_add_u32 m0, s82, 0\n"
    "v_exp_f32 v130, v130\n"
    "v_mfma_f32_16x16x32_bf16 v[140:143], a[112:115], a[32:35], v[168:171]\n"
    "s_add_u32 s72, s14, 0\n"
    "ds_read_b64_tr_b16 a[226:227], v220 offset:5120\n"
    "v_cvt_pk_bf16_f32 v144, v128, v129\n"
    "v_mfma_f32_16x16x32_bf16 v[140:143], a[116:119], a[36:39], v[140:143]\n"
    "s_and_b32 s73, s68, 1\n"
    "v_exp_f32 v131, v131\n"
    "v_mfma_f32_16x16x32_bf16 v[140:143], a[120:123], a[40:43], v[140:143]\n"
    "s_lshl_b32 s86, s73, 5\n"
    "s_mov_b64 exec, s[32:33]\n"
    "buffer_load_dwordx4 v246, s[20:23], s72 offen sc1 lds\n"
    "s_mov_b64 exec, -1\n"
    "v_mfma_f32_16x16x32_bf16 v[140:143], a[124:127], a[44:47], v[140:143]\n"
    "v_exp_f32 v132, v132\n"
    "v_mfma_f32_16x16x32_bf16 v[32:35], a[128:131], v[152:155], v[32:35]\n"
    "ds_read_b64_tr_b16 a[228:229], v220 offset:9216\n"
    "v_cvt_pk_bf16_f32 v145, v130, v131\n"
    "v_mfma_f32_16x16x32_bf16 v[32:35], a[132:135], v[156:159], v[32:35]\n"
    "v_exp_f32 v133, v133\n"
    "v_mfma_f32_16x16x32_bf16 v[36:39], a[136:139], v[152:155], v[36:39]\n"
    "s_mov_b64 exec, s[32:33]\n"
    "buffer_load_dwordx4 v246, s[20:23], s72 offen offset:1024 sc1 lds\n"
    "s_mov_b64 exec, -1\n"
    "v_mfma_f32_16x16x32_bf16 v[36:39], a[140:143], v[156:159], v[36:39]\n"
    "s_add_u32 m0, s82, 2048\n"
    "v_exp_f32 v134, v134\n"
    "v_mfma_f32_16x16x32_bf16 v[40:43], a[144:147], v[152:155], v[40:43]\n"
    "s_add_u32 s72, s72, s13\n"
    "ds_read_b64_tr_b16 a[230:231], v220 offset:13312\n"
    "v_cvt_pk_bf16_f32 v146, v132, v133\n"
    "v_mfma_f32_16x16x32_bf16 v[40:43], a[148:151], v[156:159], v[40:43]\n"
    "v_exp_f32 v135, v135\n"
    "v_mfma_f32_16x16x32_bf16 v[44:47], a[152:155], v[152:155], v[44:47]\n"
    "s_mov_b64 exec, s[32:33]\n"
    "buffer_load_dwordx4 v246, s[20:23], s72 offen sc1 lds\n"
    "s_mov_b64 exec, -1\n"
    "v_mfma_f32_16x16x32_bf16 v[44:47], a[156:159], v[156:159], v[44:47]\n"
    "v_exp_f32 v136, v136\n"
    "v_mfma_f32_16x16x32_bf16 v[48:51], a[160:163], v[152:155], v[48:51]\n"
    "ds_read_b64_tr_b16 a[232:233], v221 offset:1024\n"
    "v_cvt_pk_bf16_f32 v147, v134, v135\n"
    "v_mfma_f32_16x16x32_bf16 v[48:51], a[164:167], v[156:159], v[48:51]\n"
    "v_exp_f32 v137, v137\n"
    "v_mfma_f32_16x16x32_bf16 v[52:55], a[168:171], v[152:155], v[52:55]\n"
    "s_mov_b64 exec, s[32:33]\n"
    "buffer_load_dwordx4 v246, s[20:23], s72 offen offset:1024 sc1 lds\n"
    "s_mov_b64 exec, -1\n"
    "v_mfma_f32_16x16x32_bf16 v[52:55], a[172:175], v[156:159], v[52:55]\n"
    "s_add_u32 m0, s83, 16384\n"
    "v_exp_f32 v138, v138\n"
    "v_mfma_f32_16x16x32_bf16 v[56:59], a[176:179], v[152:155], v[56:59]\n"
    "s_add_u32 s72, s85, 0\n"
    "ds_read_b64_tr_b16 a[234:235], v221 offset:5120\n"
    "v_cvt_pk_bf16_f32 v148, v136, v137\n"
    "v_mfma_f32_16x16x32_bf16 v[56:59], a[180:183], v[156:159], v[56:59]\n"
    "v_exp_f32 v139, v139\n"
    "v_mfma_f32_16x16x32_bf16 v[60:63], a[184:187], v[152:155], v[60:63]\n"
    "v_add_u32 v212, s76, v216\n"
    "v_add_u32 v213, s76, v217\n"
    "v_mfma_f32_16x16x32_bf16 v[60:63], a[188:191], v[156:159], v[60:63]\n"
    "v_exp_f32 v140, v140\n"
    "v_mfma_f32_16x16x32_bf16 v[180:183], v[208:211], v[152:155], v[180:183]\n"
    "v_cvt_pk_bf16_f32 v149, v138, v139\n"
    "v_lshlrev_b32 v224, 6, v224\n"
    "v_mfma_f32_16x16x32_bf16 v[180:183], v[208:211], v[156:159], v[180:183]\n"
    "v_exp_f32 v141, v141\n"
    "v_mfma_f32_16x16x32_bf16 v[128:131], a[64:67], a[48:51], v[172:175]\n"
    "buffer_load_dwordx4 v247, s[24:27], s72 offen lds\n"
    "v_mfma_f32_16x16x32_bf16 v[128:131], a[68:71], a[52:55], v[128:131]\n"
    "s_add_u32 m0, s83, 17408\n"
    "v_exp_f32 v142, v142\n"
    "v_mfma_f32_16x16x32_bf16 v[128:131], a[72:75], a[56:59], v[128:131]\n"
    "s_add_u32 s72, s85, 128\n"
    "ds_read_b128 a[64:67], v212 offset:0\n"
    "v_cvt_pk_bf16_f32 v150, v140, v141\n"
    "v_mfma_f32_16x16x32_bf16 v[128:131], a[76:79], a[60:63], v[128:131]\n"
    "v_exp_f32 v143, v143\n"
    "v_mfma_f32_16x16x32_bf16 v[132:135], a[80:83], a[48:51], v[172:175]\n"
    "ds_read_b128 a[68:71], v213 offset:0\n"
    "v_mul_u32_u24 v226, 4608, v226\n"
    "v_mfma_f32_16x16x32_bf16 v[132:135], a[84:87], a[52:55], v[132:135]\n"
    "buffer_load_dwordx4 v247, s[24:27], s72 offen lds\n"
    "v_mfma_f32_16x16x32_bf16 v[132:135], a[88:91], a[56:59], v[132:135]\n"
    "s_add_u32 m0, s83, 18432\n"
    "ds_read_b128 a[72:75], v214 offset:0\n"
    "v_cvt_pk_bf16_f32 v151, v142, v143\n"
    "v_mfma_f32_16x16x32_bf16 v[132:135], a[92:95], a[60:63], v[132:135]\n"
    "s_add_u32 s72, s85, 256\n"
    "v_exp_f32 v128, v128\n"
    "v_mfma_f32_16x16x32_bf16 v[136:139], a[96:99], a[48:51], v[172:175]\n"
    "ds_read_b128 a[76:79], v215 offset:0\n"
    "v_add_u32 v224, v224, v226\n"
    "v_mfma_f32_16x16x32_bf16 v[136:139], a[100:103], a[52:55], v[136:139]\n"
    "v_exp_f32 v129, v129\n"
    "v_mfma_f32_16x16x32_bf16 v[136:139], a[104:107], a[56:59], v[136:139]\n"
    "ds_read_b128 a[80:83], v212 offset:4096\n"
    "v_lshl_add_u32 v224, v225, 3, v224\n"
    "v_mfma_f32_16x16x32_bf16 v[136:139], a[108:111], a[60:63], v[136:139]\n"
    "v_exp_f32 v130, v130\n"
    "v_mfma_f32_16x16x32_bf16 v[140:143], a[112:115], a[48:51], v[172:175]\n"
    "ds_read_b128 a[84:87], v213 offset:4096\n"
    "v_cvt_pk_bf16_f32 v152, v128, v129\n"
    "v_mfma_f32_16x16x32_bf16 v[140:143], a[116:119], a[52:55], v[140:143]\n"
    "v_exp_f32 v131, v131\n"
    "v_mfma_f32_16x16x32_bf16 v[140:143], a[120:123], a[56:59], v[140:143]\n"
    "ds_read_b128 a[88:91], v214 offset:4096\n"
    "v_lshrrev_b32 v225, 4, v253\n"
    "v_mfma_f32_16x16x32_bf16 v[140:143], a[124:127], a[60:63], v[140:143]\n"
    "v_exp_f32 v132, v132\n"
    "v_mfma_f32_16x16x32_bf16 v[64:67], a[128:131], v[144:147], v[64:67]\n"
    "ds_read_b128 a[92:95], v215 offset:4096\n"
    "v_cvt_pk_bf16_f32 v153, v130, v131\n"
    "v_mfma_f32_16x16x32_bf16 v[64:67], a[132:135], v[148:151], v[64:67]\n"
    "v_exp_f32 v133, v133\n"
    "v_mfma_f32_16x16x32_bf16 v[68:71], a[136:139], v[144:147], v[68:71]\n"
    "ds_read_b128 a[96:99], v212 offset:8192\n"
    "v_and_b32 v225, 1, v225\n"
    "v_mfma_f32_16x16x32_bf16 v[68:71], a[140:143], v[148:151], v[68:71]\n"
    "v_exp_f32 v134, v134\n"
    "v_mfma_f32_16x16x32_bf16 v[72:75], a[144:147], v[144:147], v[72:75]\n"
    "ds_read_b128 a[100:103], v213 offset:8192\n"
    "v_cvt_pk_bf16_f32 v154, v132, v133\n"
    "v_mfma_f32_16x16x32_bf16 v[72:75], a[148:151], v[148:151], v[72:75]\n"
    "v_exp_f32 v135, v135\n"
    "v_mfma_f32_16x16x32_bf16 v[76:79], a[152:155], v[144:147], v[76:79]\n"
    "ds_read_b128 a[104:107], v214 offset:8192\n"
    "v_lshlrev_b32 v225, 5, v225\n"
    "v_mfma_f32_16x16x32_bf16 v[76:79], a[156:159], v[148:151], v[76:79]\n"
    "v_exp_f32 v136, v136\n"
    "v_mfma_f32_16x16x32_bf16 v[80:83], a[160:163], v[144:147], v[80:83]\n"
    "ds_read_b128 a[108:111], v215 offset:8192\n"
    "v_cvt_pk_bf16_f32 v155, v134, v135\n"
    "v_mfma_f32_16x16x32_bf16 v[80:83], a[164:167], v[148:151], v[80:83]\n"
    "v_exp_f32 v137, v137\n"
    "v_mfma_f32_16x16x32_bf16 v[84:87], a[168:171], v[144:147], v[84:87]\n"
    "ds_read_b128 a[112:115], v212 offset:12288\n"
    "v_xor_b32 v225, s86, v225\n"
    "v_mfma_f32_16x16x32_bf16 v[84:87], a[172:175], v[148:151], v[84:87]\n"
    "v_exp_f32 v138, v138\n"
    "v_mfma_f32_16x16x32_bf16 v[88:91], a[176:179], v[144:147], v[88:91]\n"
    "ds_read_b128 a[116:119], v213 offset:12288\n"
    "v_cvt_pk_bf16_f32 v156, v136, v137\n"
    "v_mfma_f32_16x16x32_bf16 v[88:91], a[180:183], v[148:151], v[88:91]\n"
    "v_exp_f32 v139, v139\n"
    "v_mfma_f32_16x16x32_bf16 v[92:95], a[184:187], v[144:147], v[92:95]\n"
    "ds_read_b128 a[120:123], v214 offset:12288\n"
    "v_add_u32 v248, v224, v225\n"
    "v_mfma_f32_16x16x32_bf16 v[92:95], a[188:191], v[148:151], v[92:95]\n"
    "v_exp_f32 v140, v140\n"
    "v_mfma_f32_16x16x32_bf16 v[184:187], v[208:211], v[144:147], v[184:187]\n"
    "ds_read_b128 a[124:127], v215 offset:12288\n"
    "v_cvt_pk_bf16_f32 v157, v138, v139\n"
    "v_mfma_f32_16x16x32_bf16 v[184:187], v[208:211], v[148:151], v[184:187]\n"
    "s_waitcnt lgkmcnt(4)\n"
    "v_exp_f32 v141, v141\n"
    "v_mfma_f32_16x16x32_bf16 v[128:131], a[64:67], a[0:3], v[160:163]\n"
    "ds_read_b64_tr_b16 a[236:237], v221 offset:9216\n"
    "ds_read_b64_tr_b16 a[238:239], v221 offset:13312\n"
    "v_mfma_f32_16x16x32_bf16 v[128:131], a[68:71], a[4:7], v[128:131]\n"
    "v_exp_f32 v142, v142\n"
    "v_mfma_f32_16x16x32_bf16 v[128:131], a[72:75], a[8:11], v[128:131]\n"
    "ds_read_b64_tr_b16 a[240:241], v220 offset:1536\n"
    "v_cvt_pk_bf16_f32 v158, v140, v141\n"
    "v_mfma_f32_16x16x32_bf16 v[128:131], a[76:79], a[12:15], v[128:131]\n"
    "v_exp_f32 v143, v143\n"
    "v_mfma_f32_16x16x32_bf16 v[132:135], a[80:83], a[0:3], v[160:163]\n"
    "ds_read_b64_tr_b16 a[242:243], v220 offset:5632\n"
    "ds_read_b64_tr_b16 a[244:245], v220 offset:9728\n"
    "v_mfma_f32_16x16x32_bf16 v[132:135], a[84:87], a[4:7], v[132:135]\n"
    "buffer_load_dwordx4 v247, s[24:27], s72 offen lds\n"
    "v_mfma_f32_16x16x32_bf16 v[132:135], a[88:91], a[8:11], v[132:135]\n"
    "s_add_u32 m0, s83, 19456\n"
    "ds_read_b64_tr_b16 a[246:247], v220 offset:13824\n"
    "v_cvt_pk_bf16_f32 v159, v142, v143\n"
    "v_mfma_f32_16x16x32_bf16 v[132:135], a[92:95], a[12:15], v[132:135]\n"
    "s_waitcnt lgkmcnt(6)\n"
    "v_exp_f32 v128, v128\n"
    "v_mfma_f32_16x16x32_bf16 v[136:139], a[96:99], a[0:3], v[160:163]\n"
    "s_add_u32 s72, s85, 384\n"
    "ds_read_b64_tr_b16 a[248:249], v221 offset:1536\n"
    "ds_read_b64_tr_b16 a[250:251], v221 offset:5632\n"
    "v_mfma_f32_16x16x32_bf16 v[136:139], a[100:103], a[4:7], v[136:139]\n"
    "v_exp_f32 v129, v129\n"
    "v_mfma_f32_16x16x32_bf16 v[136:139], a[104:107], a[8:11], v[136:139]\n"
    "ds_read_b64_tr_b16 a[252:253], v221 offset:9728\n"
    "v_xor_b32 v225, 32, v225\n"
    "v_mfma_f32_16x16x32_bf16 v[136:139], a[108:111], a[12:15], v[136:139]\n"
    "v_exp_f32 v130, v130\n"
    "v_mfma_f32_16x16x32_bf16 v[140:143], a[112:115], a[0:3], v[160:163]\n"
    "ds_read_b64_tr_b16 a[254:255], v221 offset:13824\n"
    "v_cvt_pk_bf16_f32 v144, v128, v129\n"
    "v_mfma_f32_16x16x32_bf16 v[140:143], a[116:119], a[4:7], v[140:143]\n"
    "v_exp_f32 v131, v131\n"
    "v_mfma_f32_16x16x32_bf16 v[140:143], a[120:123], a[8:11], v[140:143]\n"
    "buffer_load_dwordx4 v247, s[24:27], s72 offen lds\n"
    "v_mfma_f32_16x16x32_bf16 v[140:143], a[124:127], a[12:15], v[140:143]\n"
    "s_add_u32 m0, s66, 16384\n"
    "v_exp_f32 v132, v132\n"
    "v_mfma_f32_16x16x32_bf16 v[96:99], a[128:131], v[152:155], v[96:99]\n"
    "s_add_u32 s72, s85, 0\n"
    "v_cvt_pk_bf16_f32 v145, v130, v131\n"
    "v_add_u32 v249, v224, v225\n"
    "v_mfma_f32_16x16x32_bf16 v[96:99], a[132:135], v[156:159], v[96:99]\n"
    "s_mul_i32 s86, s73, 2048\n"
    "v_exp_f32 v133, v133\n"
    "v_mfma_f32_16x16x32_bf16 v[100:103], a[136:139], v[152:155], v[100:103]\n"
    "s_add_u32 s86, s86, 16384\n"
    "s_mov_b32 exec_hi, 0\n"
    "buffer_load_dwordx4 v245, s[24:27], s72 offen lds\n"
    "s_mov_b32 exec_hi, -1\n"
    "v_mfma_f32_16x16x32_bf16 v[100:103], a[140:143], v[156:159], v[100:103]\n"
    "s_add_u32 m0, s83, 20992\n"
    "v_exp_f32 v134, v134\n"
    "v_mfma_f32_16x16x32_bf16 v[104:107], a[144:147], v[152:155], v[104:107]\n"
    "s_add_u32 s72, s85, 9216\n"
    "v_cvt_pk_bf16_f32 v146, v132, v133\n"
    "v_add_u32 v248, s86, v248\n"
    "v_mfma_f32_16x16x32_bf16 v[104:107], a[148:151], v[156:159], v[104:107]\n"
    "s_mul_i32 s86, s73, 2560\n"
    "v_exp_f32 v135, v135\n"
    "v_mfma_f32_16x16x32_bf16 v[108:111], a[152:155], v[152:155], v[108:111]\n"
    "s_add_u32 s86, s86, 16384\n"
    "buffer_load_dwordx4 v247, s[24:27], s72 offen lds\n"
    "v_mfma_f32_16x16x32_bf16 v[108:111], a[156:159], v[156:159], v[108:111]\n"
    "s_add_u32 m0, s83, 22016\n"
    "v_exp_f32 v136, v136\n"
    "v_mfma_f32_16x16x32_bf16 v[112:115], a[160:163], v[152:155], v[112:115]\n"
    "s_add_u32 s72, s85, 9344\n"
    "v_cvt_pk_bf16_f32 v147, v134, v135\n"
    "v_add_u32 v249, s86, v249\n"
    "v_mfma_f32_16x16x32_bf16 v[112:115], a[164:167], v[156:159], v[112:115]\n"
    "s_lshr_b32 s86, s68, 1\n"
    "v_exp_f32 v137, v137\n"
    "v_mfma_f32_16x16x32_bf16 v[116:119], a[168:171], v[152:155], v[116:119]\n"
    "s_lshl_b32 s86, s86, 13\n"
    "buffer_load_dwordx4 v247, s[24:27], s72 offen lds\n"
    "v_mfma_f32_16x16x32_bf16 v[116:119], a[172:175], v[156:159], v[116:119]\n"
    "s_add_u32 m0, s83, 23040\n"
    "v_exp_f32 v138, v138\n"
    "v_mfma_f32_16x16x32_bf16 v[120:123], a[176:179], v[152:155], v[120:123]\n"
    "s_add_u32 s72, s85, 9472\n"
    "v_cvt_pk_bf16_f32 v148, v136, v137\n"
    "v_add_u32 v250, s86, v246\n"
    "v_mfma_f32_16x16x32_bf16 v[120:123], a[180:183], v[156:159], v[120:123]\n"
    "v_exp_f32 v139, v139\n"
    "v_mfma_f32_16x16x32_bf16 v[124:127], a[184:187], v[152:155], v[124:127]\n"
    "buffer_load_dwordx4 v247, s[24:27], s72 offen lds\n"
    "v_mfma_f32_16x16x32_bf16 v[124:127], a[188:191], v[156:159], v[124:127]\n"
    "s_add_u32 m0, s83, 24064\n"
    "v_exp_f32 v140, v140\n"
    "v_mfma_f32_16x16x32_bf16 v[188:191], v[208:211], v[152:155], v[188:191]\n"
    "s_add_u32 s72, s85, 9600\n"
    "v_cvt_pk_bf16_f32 v149, v138, v139\n"
    "v_lshlrev_b32 v251, 3, v253\n"
    "v_mfma_f32_16x16x32_bf16 v[188:191], v[208:211], v[156:159], v[188:191]\n"
    "v_exp_f32 v141, v141\n"
    "v_mfma_f32_16x16x32_bf16 v[128:131], a[64:67], a[16:19], v[164:167]\n"
    "buffer_load_dwordx4 v247, s[24:27], s72 offen lds\n"
    "v_mfma_f32_16x16x32_bf16 v[128:131], a[68:71], a[20:23], v[128:131]\n"
    "s_waitcnt lgkmcnt(0)\n"
    "v_exp_f32 v142, v142\n"
    "v_mfma_f32_16x16x32_bf16 v[128:131], a[72:75], a[24:27], v[128:131]\n"
    "s_barrier\n"
    "s_add_u32 m0, s66, 20992\n"
    "v_cvt_pk_bf16_f32 v150, v140, v141\n"
    "v_mfma_f32_16x16x32_bf16 v[128:131], a[76:79], a[28:31], v[128:131]\n"
    "s_add_u32 s72, s85, 9216\n"
    "v_exp_f32 v143, v143\n"
    "v_mfma_f32_16x16x32_bf16 v[132:135], a[80:83], a[16:19], v[164:167]\n"
    "s_mov_b32 exec_hi, 0\n"
    "buffer_load_dwordx4 v245, s[24:27], s72 offen lds\n"
    "s_mov_b32 exec_hi, -1\n"
    "v_mfma_f32_16x16x32_bf16 v[132:135], a[84:87], a[20:23], v[132:135]\n"
    "s_add_u32 m0, s82, 53248\n"
    "s_add_u32 s72, s15, 0\n"
    "v_and_b32 v252, 7, v253\n"
    "v_mfma_f32_16x16x32_bf16 v[132:135], a[88:91], a[24:27], v[132:135]\n"
    "v_cvt_pk_bf16_f32 v151, v142, v143\n"
    "v_lshlrev_b32 v252, 9, v252\n"
    "v_mfma_f32_16x16x32_bf16 v[132:135], a[92:95], a[28:31], v[132:135]\n"
    "v_exp_f32 v128, v128\n"
    "v_mfma_f32_16x16x32_bf16 v[136:139], a[96:99], a[16:19], v[164:167]\n"
    "s_mov_b64 exec, s[32:33]\n"
    "buffer_load_dwordx4 v246, s[20:23], s72 offen sc1 lds\n"
    "s_mov_b64 exec, -1\n"
    "v_mfma_f32_16x16x32_bf16 v[136:139], a[100:103], a[20:23], v[136:139]\n"
    "v_exp_f32 v129, v129\n"
    "v_mfma_f32_16x16x32_bf16 v[136:139], a[104:107], a[24:27], v[136:139]\n"
    "s_mov_b64 exec, s[32:33]\n"
    "buffer_load_dwordx4 v246, s[20:23], s72 offen offset:1024 sc1 lds\n"
    "s_mov_b64 exec, -1\n"
    "v_mfma_f32_16x16x32_bf16 v[136:139], a[108:111], a[28:31], v[136:139]\n"
    "s_add_u32 m0, s82, 55296\n"
    "v_exp_f32 v130, v130\n"
    "v_mfma_f32_16x16x32_bf16 v[140:143], a[112:115], a[16:19], v[164:167]\n"
    "s_add_u32 s72, s72, s13\n"
    "v_cvt_pk_bf16_f32 v152, v128, v129\n"
    "v_mfma_f32_16x16x32_bf16 v[140:143], a[116:119], a[20:23], v[140:143]\n"
    "v_exp_f32 v131, v131\n"
    "v_mfma_f32_16x16x32_bf16 v[140:143], a[120:123], a[24:27], v[140:143]\n"
    "s_mov_b64 exec, s[32:33]\n"
    "buffer_load_dwordx4 v246, s[20:23], s72 offen sc1 lds\n"
    "s_mov_b64 exec, -1\n"
    "v_mfma_f32_16x16x32_bf16 v[140:143], a[124:127], a[28:31], v[140:143]\n"
    "v_exp_f32 v132, v132\n"
    "v_mfma_f32_16x16x32_bf16 v[0:3], a[192:195], v[144:147], v[0:3]\n"
    "v_cvt_pk_bf16_f32 v153, v130, v131\n"
    "v_mfma_f32_16x16x32_bf16 v[0:3], a[196:199], v[148:151], v[0:3]\n"
    "v_exp_f32 v133, v133\n"
    "v_mfma_f32_16x16x32_bf16 v[4:7], a[200:203], v[144:147], v[4:7]\n"
    "s_mov_b64 exec, s[32:33]\n"
    "buffer_load_dwordx4 v246, s[20:23], s72 offen offset:1024 sc1 lds\n"
    "s_mov_b64 exec, -1\n"
    "v_mfma_f32_16x16x32_bf16 v[4:7], a[204:207], v[148:151], v[4:7]\n"
    "s_add_u32 m0, s83, 69632\n"
    "v_exp_f32 v134, v134\n"
    "v_mfma_f32_16x16x32_bf16 v[8:11], a[208:211], v[144:147], v[8:11]\n"
    "s_add_u32 s72, s77, 0\n"
    "v_cvt_pk_bf16_f32 v154, v132, v133\n"
    "v_mfma_f32_16x16x32_bf16 v[8:11], a[212:215], v[148:151], v[8:11]\n"
    "v_exp_f32 v135, v135\n"
    "v_mfma_f32_16x16x32_bf16 v[12:15], a[216:219], v[144:147], v[12:15]\n"
    "buffer_load_dwordx4 v247, s[24:27], s72 offen lds\n"
    "v_mfma_f32_16x16x32_bf16 v[12:15], a[220:223], v[148:151], v[12:15]\n"
    "s_add_u32 m0, s83, 70656\n"
    "v_exp_f32 v136, v136\n"
    "v_mfma_f32_16x16x32_bf16 v[16:19], a[224:227], v[144:147], v[16:19]\n"
    "s_add_u32 s72, s77, 128\n"
    "v_cvt_pk_bf16_f32 v155, v134, v135\n"
    "v_mfma_f32_16x16x32_bf16 v[16:19], a[228:231], v[148:151], v[16:19]\n"
    "v_exp_f32 v137, v137\n"
    "v_mfma_f32_16x16x32_bf16 v[20:23], a[232:235], v[144:147], v[20:23]\n"
    "buffer_load_dwordx4 v247, s[24:27], s72 offen lds\n"
    "v_mfma_f32_16x16x32_bf16 v[20:23], a[236:239], v[148:151], v[20:23]\n"
    "s_add_u32 m0, s83, 71680\n"
    "v_exp_f32 v138, v138\n"
    "v_mfma_f32_16x16x32_bf16 v[24:27], a[240:243], v[144:147], v[24:27]\n"
    "s_add_u32 s72, s77, 256\n"
    "v_cvt_pk_bf16_f32 v156, v136, v137\n"
    "v_mfma_f32_16x16x32_bf16 v[24:27], a[244:247], v[148:151], v[24:27]\n"
    "v_exp_f32 v139, v139\n"
    "v_mfma_f32_16x16x32_bf16 v[28:31], a[248:251], v[144:147], v[28:31]\n"
    "buffer_load_dwordx4 v247, s[24:27], s72 offen lds\n"
    "v_mfma_f32_16x16x32_bf16 v[28:31], a[252:255], v[148:151], v[28:31]\n"
    "s_add_u32 m0, s83, 72704\n"
    "v_exp_f32 v140, v140\n"
    "v_mfma_f32_16x16x32_bf16 v[176:179], v[208:211], v[144:147], v[176:179]\n"
    "s_add_u32 s72, s77, 384\n"
    "v_cvt_pk_bf16_f32 v157, v138, v139\n"
    "v_mfma_f32_16x16x32_bf16 v[176:179], v[208:211], v[148:151], v[176:179]\n"
    "v_exp_f32 v141, v141\n"
    "v_mfma_f32_16x16x32_bf16 v[128:131], a[64:67], a[32:35], v[168:171]\n"
    "buffer_load_dwordx4 v247, s[24:27], s72 offen lds\n"
    "v_mfma_f32_16x16x32_bf16 v[128:131], a[68:71], a[36:39], v[128:131]\n"
    "s_add_u32 m0, s66, 69632\n"
    "v_exp_f32 v142, v142\n"
    "v_mfma_f32_16x16x32_bf16 v[128:131], a[72:75], a[40:43], v[128:131]\n"
    "s_add_u32 s72, s77, 0\n"
    "v_cvt_pk_bf16_f32 v158, v140, v141\n"
    "v_mfma_f32_16x16x32_bf16 v[128:131], a[76:79], a[44:47], v[128:131]\n"
    "v_exp_f32 v143, v143\n"
    "v_mfma_f32_16x16x32_bf16 v[132:135], a[80:83], a[32:35], v[168:171]\n"
    "s_mov_b32 exec_hi, 0\n"
    "buffer_load_dwordx4 v245, s[24:27], s72 offen lds\n"
    "s_mov_b32 exec_hi, -1\n"
    "v_mfma_f32_16x16x32_bf16 v[132:135], a[84:87], a[36:39], v[132:135]\n"
    "s_add_u32 m0, s83, 74240\n"
    "s_add_u32 s72, s77, 9216\n"
    "v_mfma_f32_16x16x32_bf16 v[132:135], a[88:91], a[40:43], v[132:135]\n"
    "v_cvt_pk_bf16_f32 v159, v142, v143\n"
    "v_mfma_f32_16x16x32_bf16 v[132:135], a[92:95], a[44:47], v[132:135]\n"
    "v_exp_f32 v128, v128\n"
    "v_mfma_f32_16x16x32_bf16 v[136:139], a[96:99], a[32:35], v[168:171]\n"
    "buffer_load_dwordx4 v247, s[24:27], s72 offen lds\n"
    "v_mfma_f32_16x16x32_bf16 v[136:139], a[100:103], a[36:39], v[136:139]\n"
    "s_add_u32 m0, s83, 75264\n"
    "v_exp_f32 v129, v129\n"
    "v_mfma_f32_16x16x32_bf16 v[136:139], a[104:107], a[40:43], v[136:139]\n"
    "s_add_u32 s72, s77, 9344\n"
    "buffer_load_dwordx4 v247, s[24:27], s72 offen lds\n"
    "v_mfma_f32_16x16x32_bf16 v[136:139], a[108:111], a[44:47], v[136:139]\n"
    "s_add_u32 m0, s83, 76288\n"
    "v_exp_f32 v130, v130\n"
    "v_mfma_f32_16x16x32_bf16 v[140:143], a[112:115], a[32:35], v[168:171]\n"
    "s_add_u32 s72, s77, 9472\n"
    "v_cvt_pk_bf16_f32 v144, v128, v129\n"
    "v_mfma_f32_16x16x32_bf16 v[140:143], a[116:119], a[36:39], v[140:143]\n"
    "v_exp_f32 v131, v131\n"
    "v_mfma_f32_16x16x32_bf16 v[140:143], a[120:123], a[40:43], v[140:143]\n"
    "buffer_load_dwordx4 v247, s[24:27], s72 offen lds\n"
    "v_mfma_f32_16x16x32_bf16 v[140:143], a[124:127], a[44:47], v[140:143]\n"
    "s_add_u32 m0, s83, 77312\n"
    "v_exp_f32 v132, v132\n"
    "v_mfma_f32_16x16x32_bf16 v[32:35], a[192:195], v[152:155], v[32:35]\n"
    "s_add_u32 s72, s77, 9600\n"
    "v_cvt_pk_bf16_f32 v145, v130, v131\n"
    "v_mfma_f32_16x16x32_bf16 v[32:35], a[196:199], v[156:159], v[32:35]\n"
    "v_exp_f32 v133, v133\n"
    "v_mfma_f32_16x16x32_bf16 v[36:39], a[200:203], v[152:155], v[36:39]\n"
    "buffer_load_dwordx4 v247, s[24:27], s72 offen lds\n"
    "v_mfma_f32_16x16x32_bf16 v[36:39], a[204:207], v[156:159], v[36:39]\n"
    "s_add_u32 m0, s66, 74240\n"
    "v_exp_f32 v134, v134\n"
    "v_mfma_f32_16x16x32_bf16 v[40:43], a[208:211], v[152:155], v[40:43]\n"
    "s_add_u32 s72, s77, 9216\n"
    "v_cvt_pk_bf16_f32 v146, v132, v133\n"
    "v_mfma_f32_16x16x32_bf16 v[40:43], a[212:215], v[156:159], v[40:43]\n"
    "v_exp_f32 v135, v135\n"
    "v_mfma_f32_16x16x32_bf16 v[44:47], a[216:219], v[152:155], v[44:47]\n"
    "s_mov_b32 exec_hi, 0\n"
    "buffer_load_dwordx4 v245, s[24:27], s72 offen lds\n"
    "s_mov_b32 exec_hi, -1\n"
    "v_mfma_f32_16x16x32_bf16 v[44:47], a[220:223], v[156:159], v[44:47]\n"
    "s_add_u32 m0, s82, 106496\n"
    "v_exp_f32 v136, v136\n"
    "v_mfma_f32_16x16x32_bf16 v[48:51], a[224:227], v[152:155], v[48:51]\n"
    "s_add_u32 s72, s84, 0\n"
    "v_cvt_pk_bf16_f32 v147, v134, v135\n"
    "v_mfma_f32_16x16x32_bf16 v[48:51], a[228:231], v[156:159], v[48:51]\n"
    "v_exp_f32 v137, v137\n"
    "v_mfma_f32_16x16x32_bf16 v[52:55], a[232:235], v[152:155], v[52:55]\n"
    "s_mov_b64 exec, s[32:33]\n"
    "buffer_load_dwordx4 v246, s[20:23], s72 offen sc1 lds\n"
    "s_mov_b64 exec, -1\n"
    "v_mfma_f32_16x16x32_bf16 v[52:55], a[236:239], v[156:159], v[52:55]\n"
    "v_exp_f32 v138, v138\n"
    "v_mfma_f32_16x16x32_bf16 v[56:59], a[240:243], v[152:155], v[56:59]\n"
    "v_cvt_pk_bf16_f32 v148, v136, v137\n"
    "v_mfma_f32_16x16x32_bf16 v[56:59], a[244:247], v[156:159], v[56:59]\n"
    "v_exp_f32 v139, v139\n"
    "v_mfma_f32_16x16x32_bf16 v[60:63], a[248:251], v[152:155], v[60:63]\n"
    "s_mov_b64 exec, s[32:33]\n"
    "buffer_load_dwordx4 v246, s[20:23], s72 offen offset:1024 sc1 lds\n"
    "s_mov_b64 exec, -1\n"
    "v_mfma_f32_16x16x32_bf16 v[60:63], a[252:255], v[156:159], v[60:63]\n"
    "s_add_u32 m0, s82, 108544\n"
    "v_exp_f32 v140, v140\n"
    "v_mfma_f32_16x16x32_bf16 v[180:183], v[208:211], v[152:155], v[180:183]\n"
    "s_add_u32 s72, s72, s13\n"
    "v_cvt_pk_bf16_f32 v149, v138, v139\n"
    "v_mfma_f32_16x16x32_bf16 v[180:183], v[208:211], v[156:159], v[180:183]\n"
    "v_exp_f32 v141, v141\n"
    "v_mfma_f32_16x16x32_bf16 v[128:131], a[64:67], a[48:51], v[172:175]\n"
    "s_mov_b64 exec, s[32:33]\n"
    "buffer_load_dwordx4 v246, s[20:23], s72 offen sc1 lds\n"
    "s_mov_b64 exec, -1\n"
    "v_mfma_f32_16x16x32_bf16 v[128:131], a[68:71], a[52:55], v[128:131]\n"
    "v_exp_f32 v142, v142\n"
    "v_mfma_f32_16x16x32_bf16 v[128:131], a[72:75], a[56:59], v[128:131]\n"
    "v_cvt_pk_bf16_f32 v150, v140, v141\n"
    "v_mfma_f32_16x16x32_bf16 v[128:131], a[76:79], a[60:63], v[128:131]\n"
    "v_exp_f32 v143, v143\n"
    "v_mfma_f32_16x16x32_bf16 v[132:135], a[80:83], a[48:51], v[172:175]\n"
    "s_mov_b64 exec, s[32:33]\n"
    "buffer_load_dwordx4 v246, s[20:23], s72 offen offset:1024 sc1 lds\n"
    "s_mov_b64 exec, -1\n"
    "v_mfma_f32_16x16x32_bf16 v[132:135], a[84:87], a[52:55], v[132:135]\n"
    "s_add_u32 m0, s83, 122880\n"
    "s_add_u32 s72, s78, 0\n"
    "v_mfma_f32_16x16x32_bf16 v[132:135], a[88:91], a[56:59], v[132:135]\n"
    "v_cvt_pk_bf16_f32 v151, v142, v143\n"
    "v_mfma_f32_16x16x32_bf16 v[132:135], a[92:95], a[60:63], v[132:135]\n"
    "v_exp_f32 v128, v128\n"
    "v_mfma_f32_16x16x32_bf16 v[136:139], a[96:99], a[48:51], v[172:175]\n"
    "buffer_load_dwordx4 v247, s[24:27], s72 offen lds\n"
    "v_mfma_f32_16x16x32_bf16 v[136:139], a[100:103], a[52:55], v[136:139]\n"
    "s_add_u32 m0, s83, 123904\n"
    "v_exp_f32 v129, v129\n"
    "v_mfma_f32_16x16x32_bf16 v[136:139], a[104:107], a[56:59], v[136:139]\n"
    "s_add_u32 s72, s78, 128\n"
    "buffer_load_dwordx4 v247, s[24:27], s72 offen lds\n"
    "v_mfma_f32_16x16x32_bf16 v[136:139], a[108:111], a[60:63], v[136:139]\n"
    "s_add_u32 m0, s83, 124928\n"
    "v_exp_f32 v130, v130\n"
    "v_mfma_f32_16x16x32_bf16 v[140:143], a[112:115], a[48:51], v[172:175]\n"
    "s_add_u32 s72, s78, 256\n"
    "v_cvt_pk_bf16_f32 v152, v128, v129\n"
    "v_mfma_f32_16x16x32_bf16 v[140:143], a[116:119], a[52:55], v[140:143]\n"
    "v_exp_f32 v131, v131\n"
    "v_mfma_f32_16x16x32_bf16 v[140:143], a[120:123], a[56:59], v[140:143]\n"
    "buffer_load_dwordx4 v247, s[24:27], s72 offen lds\n"
    "v_mfma_f32_16x16x32_bf16 v[140:143], a[124:127], a[60:63], v[140:143]\n"
    "s_add_u32 m0, s83, 125952\n"
    "v_exp_f32 v132, v132\n"
    "v_mfma_f32_16x16x32_bf16 v[64:67], a[192:195], v[144:147], v[64:67]\n"
    "s_add_u32 s72, s78, 384\n"
    "v_cvt_pk_bf16_f32 v153, v130, v131\n"
    "v_mfma_f32_16x16x32_bf16 v[64:67], a[196:199], v[148:151], v[64:67]\n"
    "v_exp_f32 v133, v133\n"
    "v_mfma_f32_16x16x32_bf16 v[68:71], a[200:203], v[144:147], v[68:71]\n"
    "buffer_load_dwordx4 v247, s[24:27], s72 offen lds\n"
    "v_mfma_f32_16x16x32_bf16 v[68:71], a[204:207], v[148:151], v[68:71]\n"
    "s_add_u32 m0, s66, 122880\n"
    "v_exp_f32 v134, v134\n"
    "v_mfma_f32_16x16x32_bf16 v[72:75], a[208:211], v[144:147], v[72:75]\n"
    "s_add_u32 s72, s78, 0\n"
    "v_cvt_pk_bf16_f32 v154, v132, v133\n"
    "v_mfma_f32_16x16x32_bf16 v[72:75], a[212:215], v[148:151], v[72:75]\n"
    "v_exp_f32 v135, v135\n"
    "v_mfma_f32_16x16x32_bf16 v[76:79], a[216:219], v[144:147], v[76:79]\n"
    "s_mov_b32 exec_hi, 0\n"
    "buffer_load_dwordx4 v245, s[24:27], s72 offen lds\n"
    "s_mov_b32 exec_hi, -1\n"
    "v_mfma_f32_16x16x32_bf16 v[76:79], a[220:223], v[148:151], v[76:79]\n"
    "s_add_u32 m0, s83, 127488\n"
    "v_exp_f32 v136, v136\n"
    "s_waitcnt vmcnt(23)\n"
    "s_barrier\n"
    "v_mfma_f32_16x16x32_bf16 v[80:83], a[224:227], v[144:147], v[80:83]\n"
    "s_add_u32 s72, s78, 9216\n"
    "ds_read_b64_tr_b16 a[176:177], v248 offset:0\n"
    "v_cvt_pk_bf16_f32 v155, v134, v135\n"
    "v_mfma_f32_16x16x32_bf16 v[80:83], a[228:231], v[148:151], v[80:83]\n"
    "v_exp_f32 v137, v137\n"
    "v_mfma_f32_16x16x32_bf16 v[84:87], a[232:235], v[144:147], v[84:87]\n"
    "buffer_load_dwordx4 v247, s[24:27], s72 offen lds\n"
    "v_mfma_f32_16x16x32_bf16 v[84:87], a[236:239], v[148:151], v[84:87]\n"
    "s_add_u32 m0, s83, 128512\n"
    "v_exp_f32 v138, v138\n"
    "v_mfma_f32_16x16x32_bf16 v[88:91], a[240:243], v[144:147], v[88:91]\n"
    "s_add_u32 s72, s78, 9344\n"
    "ds_read_b64_tr_b16 a[178:179], v248 offset:9216\n"
    "v_cvt_pk_bf16_f32 v156, v136, v137\n"
    "v_mfma_f32_16x16x32_bf16 v[88:91], a[244:247], v[148:151], v[88:91]\n"
    "v_exp_f32 v139, v139\n"
    "v_mfma_f32_16x16x32_bf16 v[92:95], a[248:251], v[144:147], v[92:95]\n"
    "buffer_load_dwordx4 v247, s[24:27], s72 offen lds\n"
    "v_mfma_f32_16x16x32_bf16 v[92:95], a[252:255], v[148:151], v[92:95]\n"
    "s_add_u32 m0, s83, 129536\n"
    "v_exp_f32 v140, v140\n"
    "v_mfma_f32_16x16x32_bf16 v[184:187], v[208:211], v[144:147], v[184:187]\n"
    "s_add_u32 s72, s78, 9472\n"
    "ds_read_b64_tr_b16 a[180:181], v248 offset:18432\n"
    "v_cvt_pk_bf16_f32 v157, v138, v139\n"
    "v_mfma_f32_16x16x32_bf16 v[184:187], v[208:211], v[148:151], v[184:187]\n"
    "v_exp_f32 v141, v141\n"
    "ds_read_b64_tr_b16 a[182:183], v248 offset:27648\n"
    "v_cvt_pk_bf16_f32 v158, v140, v141\n"
    "v_exp_f32 v142, v142\n"
    "buffer_load_dwordx4 v247, s[24:27], s72 offen lds\n"
    "v_exp_f32 v143, v143\n"
    "s_nop 0\n"
    "s_add_u32 m0, s83, 130560\n"
    "s_add_u32 s72, s78, 9600\n"
    "v_cvt_pk_bf16_f32 v159, v142, v143\n"
    "s_nop 1\n"
    "buffer_load_dwordx4 v247, s[24:27], s72 offen lds\n"
    "v_mfma_f32_16x16x32_bf16 v[96:99], a[192:195], v[152:155], v[96:99]\n"
    "s_add_u32 m0, s66, 127488\n"
    "s_add_u32 s72, s78, 9216\n"
    "ds_read_b128 a[144:147], v250 offset:0\n"
    "v_mfma_f32_16x16x32_bf16 v[96:99], a[196:199], v[156:159], v[96:99]\n"
    "s_mov_b32 exec_hi, 0\n"
    "buffer_load_dwordx4 v245, s[24:27], s72 offen lds\n"
    "s_mov_b32 exec_hi, -1\n"
    "v_mfma_f32_16x16x32_bf16 v[100:103], a[200:203], v[152:155], v[100:103]\n"
    "ds_read_b128 a[148:151], v250 offset:1024\n"
    "ds_read_b128 a[152:155], v250 offset:2048\n"
    "v_mfma_f32_16x16x32_bf16 v[100:103], a[204:207], v[156:159], v[100:103]\n"
    "ds_read_b128 a[156:159], v250 offset:3072\n"
    "ds_read_b128 a[160:163], v250 offset:4096\n"
    "v_mfma_f32_16x16x32_bf16 v[104:107], a[208:211], v[152:155], v[104:107]\n"
    "ds_read_b128 a[164:167], v250 offset:5120\n"
    "ds_read_b128 a[168:171], v250 offset:6144\n"
    "v_mfma_f32_16x16x32_bf16 v[104:107], a[212:215], v[156:159], v[104:107]\n"
    "ds_read_b128 a[172:175], v250 offset:7168\n"
    "ds_read_b64_tr_b16 a[184:185], v249 offset:0\n"
    "v_mfma_f32_16x16x32_bf16 v[108:111], a[216:219], v[152:155], v[108:111]\n"
    "ds_read_b64_tr_b16 a[186:187], v249 offset:9216\n"
    "ds_read_b64_tr_b16 a[188:189], v249 offset:18432\n"
    "v_mfma_f32_16x16x32_bf16 v[108:111], a[220:223], v[156:159], v[108:111]\n"
    "ds_read_b64_tr_b16 a[190:191], v249 offset:27648\n"
    "v_mfma_f32_16x16x32_bf16 v[112:115], a[224:227], v[152:155], v[112:115]\n"
    "v_mfma_f32_16x16x32_bf16 v[112:115], a[228:231], v[156:159], v[112:115]\n"
    "v_mfma_f32_16x16x32_bf16 v[116:119], a[232:235], v[152:155], v[116:119]\n"
    "v_mfma_f32_16x16x32_bf16 v[116:119], a[236:239], v[156:159], v[116:119]\n"
    "v_mfma_f32_16x16x32_bf16 v[120:123], a[240:243], v[152:155], v[120:123]\n"
    "v_mfma_f32_16x16x32_bf16 v[120:123], a[244:247], v[156:159], v[120:123]\n"
    "v_mfma_f32_16x16x32_bf16 v[124:127], a[248:251], v[152:155], v[124:127]\n"
    "v_mfma_f32_16x16x32_bf16 v[124:127], a[252:255], v[156:159], v[124:127]\n"
    "v_mfma_f32_16x16x32_bf16 v[188:191], v[208:211], v[152:155], v[188:191]\n"
    "v_mfma_f32_16x16x32_bf16 v[188:191], v[208:211], v[156:159], v[188:191]\n"
    "s_nop 6\n"
    "v_mov_b32 v160, 0x5f800000\n"
    "v_cmp_nle_f32 s[72:73], v176, v160\n"
    "v_cmp_nle_f32 vcc, v180, v160\n"
    "s_nop 0\n"
    "s_or_b64 s[72:73], s[72:73], vcc\n"
    "v_cmp_nle_f32 vcc, v184, v160\n"
    "s_nop 0\n"
    "s_or_b64 s[72:73], s[72:73], vcc\n"
    "v_cmp_nle_f32 vcc, v188, v160\n"
    "s_nop 0\n"
    "s_or_b64 s[72:73], s[72:73], vcc\n"
    "s_cbranch_scc1 L_safe\n"
    "L_safe_done:\n"
    "v_rcp_f32 v177, v176\n"
    "v_rcp_f32 v181, v180\n"
    "v_rcp_f32 v185, v184\n"
    "v_rcp_f32 v189, v188\n"
    "s_lshr_b32 s86, s69, 2\n"
    "s_lshr_b32 s87, s68, 1\n"
    "s_xor_b32 s86, s86, s87\n"
    "s_and_b32 s86, s86, 1\n"
    "s_sub_u32 s32, s86, 1\n"
    "s_mov_b32 s33, s32\n"
    "s_lshl_b32 s84, s7, 2\n"
    "s_lshl_b32 s86, s68, 7\n"
    "s_add_u32 s84, s84, s86\n"
    "s_cmp_eq_u32 s10, 1\n"
    "s_cbranch_scc1 L_spath\n"
    "s_waitcnt lgkmcnt(4)\n"
    "v_mfma_f32_16x16x32_bf16 a[0:3], a[176:179], a[144:147], 0\n"
    "ds_read_b64_tr_b16 a[224:225], v248 offset:512\n"
    "v_mul_f32 v0, v0, v177\n"
    "v_mul_f32 v1, v1, v177\n"
    "v_mul_f32 v2, v2, v177\n"
    "v_mul_f32 v3, v3, v177\n"
    "v_mfma_f32_16x16x32_bf16 a[0:3], a[180:183], a[148:151], a[0:3]\n"
    "ds_read_b64_tr_b16 a[226:227], v248 offset:9728\n"
    "v_mul_f32 v4, v4, v177\n"
    "v_mul_f32 v5, v5, v177\n"
    "v_mul_f32 v6, v6, v177\n"
    "v_mul_f32 v7, v7, v177\n"
    "v_mfma_f32_16x16x32_bf16 a[4:7], a[176:179], a[152:155], 0\n"
    "ds_read_b64_tr_b16 a[228:229], v248 offset:18944\n"
    "v_cvt_pk_bf16_f32 v0, v0, v1\n"
    "v_cvt_pk_bf16_f32 v1, v2, v3\n"
    "v_cvt_pk_bf16_f32 v2, v4, v5\n"
    "v_cvt_pk_bf16_f32 v3, v6, v7\n"
    "v_mfma_f32_16x16x32_bf16 a[4:7], a[180:183], a[156:159], a[4:7]\n"
    "s_add_u32 s86, s79, 0\n"
    "ds_read_b64_tr_b16 a[230:231], v248 offset:28160\n"
    "s_mov_b64 exec, s[32:33]\n"
    "buffer_store_dwordx4 v[0:3], v246, s[48:51], s86 offen offset:0 sc1\n"
    "s_mov_b64 exec, -1\n"
    "v_mul_f32 v8, v8, v177\n"
    "v_mul_f32 v9, v9, v177\n"
    "v_mfma_f32_16x16x32_bf16 a[8:11], a[176:179], a[160:163], 0\n"
    "v_mul_f32 v10, v10, v177\n"
    "v_mul_f32 v11, v11, v177\n"
    "v_mul_f32 v12, v12, v177\n"
    "v_mul_f32 v13, v13, v177\n"
    "v_mul_f32 v14, v14, v177\n"
    "v_mfma_f32_16x16x32_bf16 a[8:11], a[180:183], a[164:167], a[8:11]\n"
    "v_mul_f32 v15, v15, v177\n"
    "v_cvt_pk_bf16_f32 v8, v8, v9\n"
    "v_cvt_pk_bf16_f32 v9, v10, v11\n"
    "v_cvt_pk_bf16_f32 v10, v12, v13\n"
    "v_cvt_pk_bf16_f32 v11, v14, v15\n"
    "v_mfma_f32_16x16x32_bf16 a[12:15], a[176:179], a[168:171], 0\n"
    "s_mov_b64 exec, s[32:33]\n"
    "buffer_store_dwordx4 v[8:11], v246, s[48:51], s86 offen offset:1024 sc1\n"
    "s_mov_b64 exec, -1\n"
    "v_mul_f32 v16, v16, v177\n"
    "v_mul_f32 v17, v17, v177\n"
    "v_mul_f32 v18, v18, v177\n"
    "v_mfma_f32_16x16x32_bf16 a[12:15], a[180:183], a[172:175], a[12:15]\n"
    "s_waitcnt lgkmcnt(4)\n"
    "v_mul_f32 v19, v19, v177\n"
    "v_mul_f32 v20, v20, v177\n"
    "v_mul_f32 v21, v21, v177\n"
    "v_mul_f32 v22, v22, v177\n"
    "v_mul_f32 v23, v23, v177\n"
    "v_mfma_f32_16x16x32_bf16 a[16:19], a[184:187], a[144:147], 0\n"
    "ds_read_b64_tr_b16 a[176:177], v249 offset:512\n"
    "v_cvt_pk_bf16_f32 v16, v16, v17\n"
    "v_cvt_pk_bf16_f32 v17, v18, v19\n"
    "v_cvt_pk_bf16_f32 v18, v20, v21\n"
    "v_cvt_pk_bf16_f32 v19, v22, v23\n"
    "v_mfma_f32_16x16x32_bf16 a[16:19], a[188:191], a[148:151], a[16:19]\n"
    "ds_read_b64_tr_b16 a[178:179], v249 offset:9728\n"
    "s_mov_b64 exec, s[32:33]\n"
    "buffer_store_dwordx4 v[16:19], v246, s[48:51], s86 offen offset:2048 sc1\n"
    "s_mov_b64 exec, -1\n"
    "v_mul_f32 v24, v24, v177\n"
    "v_mul_f32 v25, v25, v177\n"
    "v_mfma_f32_16x16x32_bf16 a[20:23], a[184:187], a[152:155], 0\n"
    "ds_read_b64_tr_b16 a[180:181], v249 offset:18944\n"
    "v_mul_f32 v26, v26, v177\n"
    "v_mul_f32 v27, v27, v177\n"
    "v_mul_f32 v28, v28, v177\n"
    "v_mul_f32 v29, v29, v177\n"
    "v_mfma_f32_16x16x32_bf16 a[20:23], a[188:191], a[156:159], a[20:23]\n"
    "ds_read_b64_tr_b16 a[182:183], v249 offset:28160\n"
    "v_mul_f32 v30, v30, v177\n"
    "v_mul_f32 v31, v31, v177\n"
    "v_cvt_pk_bf16_f32 v24, v24, v25\n"
    "v_cvt_pk_bf16_f32 v25, v26, v27\n"
    "v_mfma_f32_16x16x32_bf16 a[24:27], a[184:187], a[160:163], 0\n"
    "v_cvt_pk_bf16_f32 v26, v28, v29\n"
    "v_cvt_pk_bf16_f32 v27, v30, v31\n"
    "v_mfma_f32_16x16x32_bf16 a[24:27], a[188:191], a[164:167], a[24:27]\n"
    "s_mov_b64 exec, s[32:33]\n"
    "buffer_store_dwordx4 v[24:27], v246, s[48:51], s86 offen offset:3072 sc1\n"
    "s_mov_b64 exec, -1\n"
    "v_mul_f32 v32, v32, v181\n"
    "v_mul_f32 v33, v33, v181\n"
    "v_mul_f32 v34, v34, v181\n"
    "v_mfma_f32_16x16x32_bf16 a[28:31], a[184:187], a[168:171], 0\n"
    "v_mul_f32 v35, v35, v181\n"
    "v_mul_f32 v36, v36, v181\n"
    "v_mul_f32 v37, v37, v181\n"
    "v_mul_f32 v38, v38, v181\n"
    "v_mul_f32 v39, v39, v181\n"
    "v_mfma_f32_16x16x32_bf16 a[28:31], a[188:191], a[172:175], a[28:31]\n"
    "s_waitcnt lgkmcnt(4)\n"
    "v_cvt_pk_bf16_f32 v32, v32, v33\n"
    "v_cvt_pk_bf16_f32 v33, v34, v35\n"
    "v_cvt_pk_bf16_f32 v34, v36, v37\n"
    "v_cvt_pk_bf16_f32 v35, v38, v39\n"
    "v_mfma_f32_16x16x32_bf16 a[32:35], a[224:227], a[144:147], 0\n"
    "s_add_u32 s86, s79, 131072\n"
    "ds_read_b64_tr_b16 a[184:185], v248 offset:1024\n"
    "s_mov_b64 exec, s[32:33]\n"
    "buffer_store_dwordx4 v[32:35], v246, s[48:51], s86 offen offset:0 sc1\n"
    "s_mov_b64 exec, -1\n"
    "v_mul_f32 v40, v40, v181\n"
    "v_mul_f32 v41, v41, v181\n"
    "v_mfma_f32_16x16x32_bf16 a[32:35], a[228:231], a[148:151], a[32:35]\n"
    "ds_read_b64_tr_b16 a[186:187], v248 offset:10240\n"
    "v_mul_f32 v42, v42, v181\n"
    "v_mul_f32 v43, v43, v181\n"
    "v_mul_f32 v44, v44, v181\n"
    "v_mul_f32 v45, v45, v181\n"
    "v_mfma_f32_16x16x32_bf16 a[36:39], a[224:227], a[152:155], 0\n"
    "ds_read_b64_tr_b16 a[188:189], v248 offset:19456\n"
    "v_mul_f32 v46, v46, v181\n"
    "v_mul_f32 v47, v47, v181\n"
    "v_cvt_pk_bf16_f32 v40, v40, v41\n"
    "v_cvt_pk_bf16_f32 v41, v42, v43\n"
    "v_mfma_f32_16x16x32_bf16 a[36:39], a[228:231], a[156:159], a[36:39]\n"
    "ds_read_b64_tr_b16 a[190:191], v248 offset:28672\n"
    "v_cvt_pk_bf16_f32 v42, v44, v45\n"
    "v_cvt_pk_bf16_f32 v43, v46, v47\n"
    "v_mfma_f32_16x16x32_bf16 a[40:43], a[224:227], a[160:163], 0\n"
    "s_mov_b64 exec, s[32:33]\n"
    "buffer_store_dwordx4 v[40:43], v246, s[48:51], s86 offen offset:1024 sc1\n"
    "s_mov_b64 exec, -1\n"
    "v_mul_f32 v48, v48, v181\n"
    "v_mul_f32 v49, v49, v181\n"
    "v_mul_f32 v50, v50, v181\n"
    "v_mfma_f32_16x16x32_bf16 a[40:43], a[228:231], a[164:167], a[40:43]\n"
    "v_mul_f32 v51, v51, v181\n"
    "v_mul_f32 v52, v52, v181\n"
    "v_mul_f32 v53, v53, v181\n"
    "v_mul_f32 v54, v54, v181\n"
    "v_mul_f32 v55, v55, v181\n"
    "v_mfma_f32_16x16x32_bf16 a[44:47], a[224:227], a[168:171], 0\n"
    "v_cvt_pk_bf16_f32 v48, v48, v49\n"
    "v_cvt_pk_bf16_f32 v49, v50, v51\n"
    "v_cvt_pk_bf16_f32 v50, v52, v53\n"
    "v_cvt_pk_bf16_f32 v51, v54, v55\n"
    "v_mfma_f32_16x16x32_bf16 a[44:47], a[228:231], a[172:175], a[44:47]\n"
    "s_waitcnt lgkmcnt(4)\n"
    "s_mov_b64 exec, s[32:33]\n"
    "buffer_store_dwordx4 v[48:51], v246, s[48:51], s86 offen offset:2048 sc1\n"
    "s_mov_b64 exec, -1\n"
    "v_mul_f32 v56, v56, v181\n"
    "v_mul_f32 v57, v57, v181\n"
    "v_mul_f32 v58, v58, v181\n"
    "v_mfma_f32_16x16x32_bf16 a[48:51], a[176:179], a[144:147], 0\n"
    "ds_read_b64_tr_b16 a[224:225], v249 offset:1024\n"
    "v_mul_f32 v59, v59, v181\n"
    "v_mul_f32 v60, v60, v181\n"
    "v_mul_f32 v61, v61, v181\n"
    "v_mul_f32 v62, v62, v181\n"
    "v_mfma_f32_16x16x32_bf16 a[48:51], a[180:183], a[148:151], a[48:51]\n"
    "ds_read_b64_tr_b16 a[226:227], v249 offset:10240\n"
    "v_mul_f32 v63, v63, v181\n"
    "v_cvt_pk_bf16_f32 v56, v56, v57\n"
    "v_cvt_pk_bf16_f32 v57, v58, v59\n"
    "v_cvt_pk_bf16_f32 v58, v60, v61\n"
    "v_mfma_f32_16x16x32_bf16 a[52:55], a[176:179], a[152:155], 0\n"
    "ds_read_b64_tr_b16 a[228:229], v249 offset:19456\n"
    "v_cvt_pk_bf16_f32 v59, v62, v63\n"
    "v_mfma_f32_16x16x32_bf16 a[52:55], a[180:183], a[156:159], a[52:55]\n"
    "ds_read_b64_tr_b16 a[230:231], v249 offset:28672\n"
    "s_mov_b64 exec, s[32:33]\n"
    "buffer_store_dwordx4 v[56:59], v246, s[48:51], s86 offen offset:3072 sc1\n"
    "s_mov_b64 exec, -1\n"
    "v_mul_f32 v64, v64, v185\n"
    "v_mul_f32 v65, v65, v185\n"
    "v_mfma_f32_16x16x32_bf16 a[56:59], a[176:179], a[160:163], 0\n"
    "v_mul_f32 v66, v66, v185\n"
    "v_mul_f32 v67, v67, v185\n"
    "v_mul_f32 v68, v68, v185\n"
    "v_mul_f32 v69, v69, v185\n"
    "v_mul_f32 v70, v70, v185\n"
    "v_mfma_f32_16x16x32_bf16 a[56:59], a[180:183], a[164:167], a[56:59]\n"
    "v_mul_f32 v71, v71, v185\n"
    "v_cvt_pk_bf16_f32 v64, v64, v65\n"
    "v_cvt_pk_bf16_f32 v65, v66, v67\n"
    "v_cvt_pk_bf16_f32 v66, v68, v69\n"
    "v_cvt_pk_bf16_f32 v67, v70, v71\n"
    "v_mfma_f32_16x16x32_bf16 a[60:63], a[176:179], a[168:171], 0\n"
    "s_add_u32 s86, s79, 262144\n"
    "s_mov_b64 exec, s[32:33]\n"
    "buffer_store_dwordx4 v[64:67], v246, s[48:51], s86 offen offset:0 sc1\n"
    "s_mov_b64 exec, -1\n"
    "v_mul_f32 v72, v72, v185\n"
    "v_mul_f32 v73, v73, v185\n"
    "v_mul_f32 v74, v74, v185\n"
    "v_mfma_f32_16x16x32_bf16 a[60:63], a[180:183], a[172:175], a[60:63]\n"
    "s_waitcnt lgkmcnt(4)\n"
    "v_mul_f32 v75, v75, v185\n"
    "v_mul_f32 v76, v76, v185\n"
    "v_mul_f32 v77, v77, v185\n"
    "v_mul_f32 v78, v78, v185\n"
    "v_mul_f32 v79, v79, v185\n"
    "v_mfma_f32_16x16x32_bf16 a[64:67], a[184:187], a[144:147], 0\n"
    "ds_read_b64_tr_b16 a[176:177], v248 offset:1536\n"
    "v_cvt_pk_bf16_f32 v72, v72, v73\n"
    "v_cvt_pk_bf16_f32 v73, v74, v75\n"
    "v_cvt_pk_bf16_f32 v74, v76, v77\n"
    "v_cvt_pk_bf16_f32 v75, v78, v79\n"
    "v_mfma_f32_16x16x32_bf16 a[64:67], a[188:191], a[148:151], a[64:67]\n"
    "ds_read_b64_tr_b16 a[178:179], v248 offset:10752\n"
    "s_mov_b64 exec, s[32:33]\n"
    "buffer_store_dwordx4 v[72:75], v246, s[48:51], s86 offen offset:1024 sc1\n"
    "s_mov_b64 exec, -1\n"
    "v_mul_f32 v80, v80, v185\n"
    "v_mul_f32 v81, v81, v185\n"
    "v_mfma_f32_16x16x32_bf16 a[68:71], a[184:187], a[152:155], 0\n"
    "ds_read_b64_tr_b16 a[180:181], v248 offset:19968\n"
    "v_mul_f32 v82, v82, v185\n"
    "v_mul_f32 v83, v83, v185\n"
    "v_mul_f32 v84, v84, v185\n"
    "v_mul_f32 v85, v85, v185\n"
    "v_mfma_f32_16x16x32_bf16 a[68:71], a[188:191], a[156:159], a[68:71]\n"
    "ds_read_b64_tr_b16 a[182:183], v248 offset:29184\n"
    "v_mul_f32 v86, v86, v185\n"
    "v_mul_f32 v87, v87, v185\n"
    "v_cvt_pk_bf16_f32 v80, v80, v81\n"
    "v_cvt_pk_bf16_f32 v81, v82, v83\n"
    "v_mfma_f32_16x16x32_bf16 a[72:75], a[184:187], a[160:163], 0\n"
    "v_cvt_pk_bf16_f32 v82, v84, v85\n"
    "v_cvt_pk_bf16_f32 v83, v86, v87\n"
    "v_mfma_f32_16x16x32_bf16 a[72:75], a[188:191], a[164:167], a[72:75]\n"
    "s_mov_b64 exec, s[32:33]\n"
    "buffer_store_dwordx4 v[80:83], v246, s[48:51], s86 offen offset:2048 sc1\n"
    "s_mov_b64 exec, -1\n"
    "v_mul_f32 v88, v88, v185\n"
    "v_mul_f32 v89, v89, v185\n"
    "v_mul_f32 v90, v90, v185\n"
    "v_mfma_f32_16x16x32_bf16 a[76:79], a[184:187], a[168:171], 0\n"
    "v_mul_f32 v91, v91, v185\n"
    "v_mul_f32 v92, v92, v185\n"
    "v_mul_f32 v93, v93, v185\n"
    "v_mul_f32 v94, v94, v185\n"
    "v_mul_f32 v95, v95, v185\n"
    "v_mfma_f32_16x16x32_bf16 a[76:79], a[188:191], a[172:175], a[76:79]\n"
    "s_waitcnt lgkmcnt(4)\n"
    "v_cvt_pk_bf16_f32 v88, v88, v89\n"
    "v_cvt_pk_bf16_f32 v89, v90, v91\n"
    "v_cvt_pk_bf16_f32 v90, v92, v93\n"
    "v_cvt_pk_bf16_f32 v91, v94, v95\n"
    "v_mfma_f32_16x16x32_bf16 a[80:83], a[224:227], a[144:147], 0\n"
    "ds_read_b64_tr_b16 a[184:185], v249 offset:1536\n"
    "s_mov_b64 exec, s[32:33]\n"
    "buffer_store_dwordx4 v[88:91], v246, s[48:51], s86 offen offset:3072 sc1\n"
    "s_mov_b64 exec, -1\n"
    "v_mul_f32 v96, v96, v189\n"
    "v_mul_f32 v97, v97, v189\n"
    "v_mfma_f32_16x16x32_bf16 a[80:83], a[228:231], a[148:151], a[80:83]\n"
    "ds_read_b64_tr_b16 a[186:187], v249 offset:10752\n"
    "v_mul_f32 v98, v98, v189\n"
    "v_mul_f32 v99, v99, v189\n"
    "v_mul_f32 v100, v100, v189\n"
    "v_mul_f32 v101, v101, v189\n"
    "v_mfma_f32_16x16x32_bf16 a[84:87], a[224:227], a[152:155], 0\n"
    "ds_read_b64_tr_b16 a[188:189], v249 offset:19968\n"
    "v_mul_f32 v102, v102, v189\n"
    "v_mul_f32 v103, v103, v189\n"
    "v_cvt_pk_bf16_f32 v96, v96, v97\n"
    "v_cvt_pk_bf16_f32 v97, v98, v99\n"
    "v_mfma_f32_16x16x32_bf16 a[84:87], a[228:231], a[156:159], a[84:87]\n"
    "ds_read_b64_tr_b16 a[190:191], v249 offset:29184\n"
    "v_cvt_pk_bf16_f32 v98, v100, v101\n"
    "v_cvt_pk_bf16_f32 v99, v102, v103\n"
    "v_mfma_f32_16x16x32_bf16 a[88:91], a[224:227], a[160:163], 0\n"
    "s_add_u32 s86, s79, 393216\n"
    "s_mov_b64 exec, s[32:33]\n"
    "buffer_store_dwordx4 v[96:99], v246, s[48:51], s86 offen offset:0 sc1\n"
    "s_mov_b64 exec, -1\n"
    "v_mul_f32 v104, v104, v189\n"
    "v_mul_f32 v105, v105, v189\n"
    "v_mul_f32 v106, v106, v189\n"
    "v_mfma_f32_16x16x32_bf16 a[88:91], a[228:231], a[164:167], a[88:91]\n"
    "v_mul_f32 v107, v107, v189\n"
    "v_mul_f32 v108, v108, v189\n"
    "v_mul_f32 v109, v109, v189\n"
    "v_mul_f32 v110, v110, v189\n"
    "v_mul_f32 v111, v111, v189\n"
    "v_mfma_f32_16x16x32_bf16 a[92:95], a[224:227], a[168:171], 0\n"
    "v_cvt_pk_bf16_f32 v104, v104, v105\n"
    "v_cvt_pk_bf16_f32 v105, v106, v107\n"
    "v_cvt_pk_bf16_f32 v106, v108, v109\n"
    "v_cvt_pk_bf16_f32 v107, v110, v111\n"
    "v_mfma_f32_16x16x32_bf16 a[92:95], a[228:231], a[172:175], a[92:95]\n"
    "s_waitcnt lgkmcnt(4)\n"
    "s_mov_b64 exec, s[32:33]\n"
    "buffer_store_dwordx4 v[104:107], v246, s[48:51], s86 offen offset:1024 sc1\n"
    "s_mov_b64 exec, -1\n"
    "v_mul_f32 v112, v112, v189\n"
    "v_mul_f32 v113, v113, v189\n"
    "v_mul_f32 v114, v114, v189\n"
    "v_mfma_f32_16x16x32_bf16 a[96:99], a[176:179], a[144:147], 0\n"
    "ds_read_b64_tr_b16 a[224:225], v248 offset:2048\n"
    "v_mul_f32 v115, v115, v189\n"
    "v_mul_f32 v116, v116, v189\n"
    "v_mul_f32 v117, v117, v189\n"
    "v_mul_f32 v118, v118, v189\n"
    "v_mfma_f32_16x16x32_bf16 a[96:99], a[180:183], a[148:151], a[96:99]\n"
    "ds_read_b64_tr_b16 a[226:227], v248 offset:11264\n"
    "v_mul_f32 v119, v119, v189\n"
    "v_cvt_pk_bf16_f32 v112, v112, v113\n"
    "v_cvt_pk_bf16_f32 v113, v114, v115\n"
    "v_cvt_pk_bf16_f32 v114, v116, v117\n"
    "v_mfma_f32_16x16x32_bf16 a[100:103], a[176:179], a[152:155], 0\n"
    "ds_read_b64_tr_b16 a[228:229], v248 offset:20480\n"
    "v_cvt_pk_bf16_f32 v115, v118, v119\n"
    "v_mfma_f32_16x16x32_bf16 a[100:103], a[180:183], a[156:159], a[100:103]\n"
    "ds_read_b64_tr_b16 a[230:231], v248 offset:29696\n"
    "s_mov_b64 exec, s[32:33]\n"
    "buffer_store_dwordx4 v[112:115], v246, s[48:51], s86 offen offset:2048 sc1\n"
    "s_mov_b64 exec, -1\n"
    "v_mul_f32 v120, v120, v189\n"
    "v_mul_f32 v121, v121, v189\n"
    "v_mfma_f32_16x16x32_bf16 a[104:107], a[176:179], a[160:163], 0\n"
    "v_mul_f32 v122, v122, v189\n"
    "v_mul_f32 v123, v123, v189\n"
    "v_mul_f32 v124, v124, v189\n"
    "v_mul_f32 v125, v125, v189\n"
    "v_mul_f32 v126, v126, v189\n"
    "v_mfma_f32_16x16x32_bf16 a[104:107], a[180:183], a[164:167], a[104:107]\n"
    "v_mul_f32 v127, v127, v189\n"
    "v_cvt_pk_bf16_f32 v120, v120, v121\n"
    "v_cvt_pk_bf16_f32 v121, v122, v123\n"
    "v_cvt_pk_bf16_f32 v122, v124, v125\n"
    "v_cvt_pk_bf16_f32 v123, v126, v127\n"
    "v_mfma_f32_16x16x32_bf16 a[108:111], a[176:179], a[168:171], 0\n"
    "s_mov_b64 exec, s[32:33]\n"
    "buffer_store_dwordx4 v[120:123], v246, s[48:51], s86 offen offset:3072 sc1\n"
    "s_mov_b64 exec, -1\n"
    "v_add_u32 v248, 53248, v248\n"
    "v_add_u32 v249, 53248, v249\n"
    "v_mfma_f32_16x16x32_bf16 a[108:111], a[180:183], a[172:175], a[108:111]\n"
    "s_waitcnt lgkmcnt(4)\n"
    "v_add_u32 v250, 53248, v250\n"
    "v_mfma_f32_16x16x32_bf16 a[112:115], a[184:187], a[144:147], 0\n"
    "v_mfma_f32_16x16x32_bf16 a[112:115], a[188:191], a[148:151], a[112:115]\n"
    "s_waitcnt vmcnt(30) lgkmcnt(0)\n"
    "s_barrier\n"
    "v_mfma_f32_16x16x32_bf16 a[116:119], a[184:187], a[152:155], 0\n"
    "ds_read_b64_tr_b16 a[176:177], v248 offset:0\n"
    "v_mfma_f32_16x16x32_bf16 a[116:119], a[188:191], a[156:159], a[116:119]\n"
    "ds_read_b64_tr_b16 a[178:179], v248 offset:9216\n"
    "v_mfma_f32_16x16x32_bf16 a[120:123], a[184:187], a[160:163], 0\n"
    "ds_read_b64_tr_b16 a[180:181], v248 offset:18432\n"
    "ds_read_b128 a[192:195], v250 offset:0\n"
    "v_mfma_f32_16x16x32_bf16 a[120:123], a[188:191], a[164:167], a[120:123]\n"
    "ds_read_b64_tr_b16 a[182:183], v248 offset:27648\n"
    "ds_read_b128 a[196:199], v250 offset:1024\n"
    "v_mfma_f32_16x16x32_bf16 a[124:127], a[184:187], a[168:171], 0\n"
    "s_add_u32 s86, s79, 0\n"
    "ds_read_b128 a[200:203], v250 offset:2048\n"
    "v_mfma_f32_16x16x32_bf16 a[124:127], a[188:191], a[172:175], a[124:127]\n"
    "ds_read_b128 a[204:207], v250 offset:3072\n"
    "v_mfma_f32_16x16x32_bf16 a[128:131], a[224:227], a[144:147], 0\n"
    "ds_read_b128 a[208:211], v250 offset:4096\n"
    "ds_read_b128 a[212:215], v250 offset:5120\n"
    "v_mfma_f32_16x16x32_bf16 a[128:131], a[228:231], a[148:151], a[128:131]\n"
    "ds_read_b128 a[216:219], v250 offset:6144\n"
    "ds_read_b128 a[220:223], v250 offset:7168\n"
    "v_mfma_f32_16x16x32_bf16 a[132:135], a[224:227], a[152:155], 0\n"
    "ds_read_b64_tr_b16 a[184:185], v249 offset:0\n"
    "v_mfma_f32_16x16x32_bf16 a[132:135], a[228:231], a[156:159], a[132:135]\n"
    "ds_read_b64_tr_b16 a[186:187], v249 offset:9216\n"
    "v_mfma_f32_16x16x32_bf16 a[136:139], a[224:227], a[160:163], 0\n"
    "ds_read_b64_tr_b16 a[188:189], v249 offset:18432\n"
    "v_mfma_f32_16x16x32_bf16 a[136:139], a[228:231], a[164:167], a[136:139]\n"
    "ds_read_b64_tr_b16 a[190:191], v249 offset:27648\n"
    "v_mfma_f32_16x16x32_bf16 a[140:143], a[224:227], a[168:171], 0\n"
    "s_not_b64 exec, s[32:33]\n"
    "buffer_store_dwordx4 v[0:3], v246, s[48:51], s86 offen offset:0 sc1\n"
    "s_mov_b64 exec, -1\n"
    "v_mfma_f32_16x16x32_bf16 a[140:143], a[228:231], a[172:175], a[140:143]\n"
    "s_waitcnt lgkmcnt(4)\n"
    "v_mfma_f32_16x16x32_bf16 a[0:3], a[176:179], a[192:195], a[0:3]\n"
    "ds_read_b64_tr_b16 a[224:225], v248 offset:512\n"
    "v_mfma_f32_16x16x32_bf16 a[0:3], a[180:183], a[196:199], a[0:3]\n"
    "ds_read_b64_tr_b16 a[226:227], v248 offset:9728\n"
    "v_mfma_f32_16x16x32_bf16 a[4:7], a[176:179], a[200:203], a[4:7]\n"
    "ds_read_b64_tr_b16 a[228:229], v248 offset:18944\n"
    "v_mfma_f32_16x16x32_bf16 a[4:7], a[180:183], a[204:207], a[4:7]\n"
    "ds_read_b64_tr_b16 a[230:231], v248 offset:28160\n"
    "v_mfma_f32_16x16x32_bf16 a[8:11], a[176:179], a[208:211], a[8:11]\n"
    "s_add_u32 m0, s82, 0\n"
    "s_not_b64 exec, s[32:33]\n"
    "buffer_store_dwordx4 v[8:11], v246, s[48:51], s86 offen offset:1024 sc1\n"
    "s_mov_b64 exec, -1\n"
    "v_mfma_f32_16x16x32_bf16 a[8:11], a[180:183], a[212:215], a[8:11]\n"
    "s_add_u32 s72, s8, 2048\n"
    "buffer_load_dwordx4 v246, s[20:23], s72 offen sc1 lds\n"
    "v_mfma_f32_16x16x32_bf16 a[12:15], a[176:179], a[216:219], a[12:15]\n"
    "v_mfma_f32_16x16x32_bf16 a[12:15], a[180:183], a[220:223], a[12:15]\n"
    "s_waitcnt lgkmcnt(4)\n"
    "buffer_load_dwordx4 v246, s[20:23], s72 offen offset:1024 sc1 lds\n"
    "v_mfma_f32_16x16x32_bf16 a[16:19], a[184:187], a[192:195], a[16:19]\n"
    "ds_read_b64_tr_b16 a[176:177], v249 offset:512\n"
    "v_mfma_f32_16x16x32_bf16 a[16:19], a[188:191], a[196:199], a[16:19]\n"
    "ds_read_b64_tr_b16 a[178:179], v249 offset:9728\n"
    "v_mfma_f32_16x16x32_bf16 a[20:23], a[184:187], a[200:203], a[20:23]\n"
    "ds_read_b64_tr_b16 a[180:181], v249 offset:18944\n"
    "v_mfma_f32_16x16x32_bf16 a[20:23], a[188:191], a[204:207], a[20:23]\n"
    "ds_read_b64_tr_b16 a[182:183], v249 offset:28160\n"
    "v_mfma_f32_16x16x32_bf16 a[24:27], a[184:187], a[208:211], a[24:27]\n"
    "s_add_u32 m0, s82, 2048\n"
    "v_mfma_f32_16x16x32_bf16 a[24:27], a[188:191], a[212:215], a[24:27]\n"
    "s_add_u32 s72, s72, s13\n"
    "buffer_load_dwordx4 v246, s[20:23], s72 offen sc1 lds\n"
    "v_mfma_f32_16x16x32_bf16 a[28:31], a[184:187], a[216:219], a[28:31]\n"
    "v_mfma_f32_16x16x32_bf16 a[28:31], a[188:191], a[220:223], a[28:31]\n"
    "s_waitcnt lgkmcnt(4)\n"
    "buffer_load_dwordx4 v246, s[20:23], s72 offen offset:1024 sc1 lds\n"
    "v_mfma_f32_16x16x32_bf16 a[32:35], a[224:227], a[192:195], a[32:35]\n"
    "ds_read_b64_tr_b16 a[184:185], v248 offset:1024\n"
    "v_mfma_f32_16x16x32_bf16 a[32:35], a[228:231], a[196:199], a[32:35]\n"
    "ds_read_b64_tr_b16 a[186:187], v248 offset:10240\n"
    "s_cmp_eq_u32 s32, 0\n"
    "s_cbranch_scc1 L_opub1\n"
    "s_waitcnt vmcnt(6)\n"
    "s_add_u32 s73, s84, 32768\n"
    "v_mov_b32 v176, s74\n"
    "s_mov_b64 exec, 1\n"
    "buffer_store_dword v176, off, s[52:55], s73 sc1\n"
    "s_mov_b64 exec, -1\n"
    "L_opub1:\n"
    "v_mfma_f32_16x16x32_bf16 a[36:39], a[224:227], a[200:203], a[36:39]\n"
    "ds_read_b64_tr_b16 a[188:189], v248 offset:19456\n"
    "v_mfma_f32_16x16x32_bf16 a[36:39], a[228:231], a[204:207], a[36:39]\n"
    "ds_read_b64_tr_b16 a[190:191], v248 offset:28672\n"
    "v_mfma_f32_16x16x32_bf16 a[40:43], a[224:227], a[208:211], a[40:43]\n"
    "s_add_u32 m0, s83, 16384\n"
    "s_not_b64 exec, s[32:33]\n"
    "buffer_store_dwordx4 v[16:19], v246, s[48:51], s86 offen offset:2048 sc1\n"
    "s_mov_b64 exec, -1\n"
    "v_mfma_f32_16x16x32_bf16 a[40:43], a[228:231], a[212:215], a[40:43]\n"
    "s_add_u32 s72, s9, 73728\n"
    "buffer_load_dwordx4 v247, s[24:27], s72 offen lds\n"
    "v_mfma_f32_16x16x32_bf16 a[44:47], a[224:227], a[216:219], a[44:47]\n"
    "s_add_u32 m0, s83, 17408\n"
    "v_mfma_f32_16x16x32_bf16 a[44:47], a[228:231], a[220:223], a[44:47]\n"
    "s_waitcnt lgkmcnt(4)\n"
    "s_add_u32 s72, s9, 73856\n"
    "buffer_load_dwordx4 v247, s[24:27], s72 offen lds\n"
    "v_mfma_f32_16x16x32_bf16 a[48:51], a[176:179], a[192:195], a[48:51]\n"
    "ds_read_b64_tr_b16 a[224:225], v249 offset:1024\n"
    "v_mfma_f32_16x16x32_bf16 a[48:51], a[180:183], a[196:199], a[48:51]\n"
    "ds_read_b64_tr_b16 a[226:227], v249 offset:10240\n"
    "v_mfma_f32_16x16x32_bf16 a[52:55], a[176:179], a[200:203], a[52:55]\n"
    "ds_read_b64_tr_b16 a[228:229], v249 offset:19456\n"
    "v_mfma_f32_16x16x32_bf16 a[52:55], a[180:183], a[204:207], a[52:55]\n"
    "ds_read_b64_tr_b16 a[230:231], v249 offset:28672\n"
    "v_mfma_f32_16x16x32_bf16 a[56:59], a[176:179], a[208:211], a[56:59]\n"
    "s_add_u32 m0, s83, 18432\n"
    "v_mfma_f32_16x16x32_bf16 a[56:59], a[180:183], a[212:215], a[56:59]\n"
    "s_add_u32 s72, s9, 73984\n"
    "buffer_load_dwordx4 v247, s[24:27], s72 offen lds\n"
    "v_mfma_f32_16x16x32_bf16 a[60:63], a[176:179], a[216:219], a[60:63]\n"
    "s_add_u32 m0, s83, 19456\n"
    "v_mfma_f32_16x16x32_bf16 a[60:63], a[180:183], a[220:223], a[60:63]\n"
    "s_waitcnt lgkmcnt(4)\n"
    "s_add_u32 s72, s9, 74112\n"
    "buffer_load_dwordx4 v247, s[24:27], s72 offen lds\n"
    "v_mfma_f32_16x16x32_bf16 a[64:67], a[184:187], a[192:195], a[64:67]\n"
    "ds_read_b64_tr_b16 a[176:177], v248 offset:1536\n"
    "v_mfma_f32_16x16x32_bf16 a[64:67], a[188:191], a[196:199], a[64:67]\n"
    "ds_read_b64_tr_b16 a[178:179], v248 offset:10752\n"
    "v_mfma_f32_16x16x32_bf16 a[68:71], a[184:187], a[200:203], a[68:71]\n"
    "ds_read_b64_tr_b16 a[180:181], v248 offset:19968\n"
    "v_mfma_f32_16x16x32_bf16 a[68:71], a[188:191], a[204:207], a[68:71]\n"
    "ds_read_b64_tr_b16 a[182:183], v248 offset:29184\n"
    "v_mfma_f32_16x16x32_bf16 a[72:75], a[184:187], a[208:211], a[72:75]\n"
    "s_add_u32 m0, s66, 16384\n"
    "s_not_b64 exec, s[32:33]\n"
    "buffer_store_dwordx4 v[24:27], v246, s[48:51], s86 offen offset:3072 sc1\n"
    "s_mov_b64 exec, -1\n"
    "v_mfma_f32_16x16x32_bf16 a[72:75], a[188:191], a[212:215], a[72:75]\n"
    "s_add_u32 s72, s9, 73728\n"
    "s_mov_b32 exec_hi, 0\n"
    "buffer_load_dwordx4 v245, s[24:27], s72 offen lds\n"
    "s_mov_b32 exec_hi, -1\n"
    "v_mfma_f32_16x16x32_bf16 a[76:79], a[184:187], a[216:219], a[76:79]\n"
    "s_add_u32 m0, s83, 20992\n"
    "v_mfma_f32_16x16x32_bf16 a[76:79], a[188:191], a[220:223], a[76:79]\n"
    "s_waitcnt lgkmcnt(4)\n"
    "s_add_u32 s72, s9, 82944\n"
    "buffer_load_dwordx4 v247, s[24:27], s72 offen lds\n"
    "v_mfma_f32_16x16x32_bf16 a[80:83], a[224:227], a[192:195], a[80:83]\n"
    "ds_read_b64_tr_b16 a[184:185], v249 offset:1536\n"
    "v_mfma_f32_16x16x32_bf16 a[80:83], a[228:231], a[196:199], a[80:83]\n"
    "ds_read_b64_tr_b16 a[186:187], v249 offset:10752\n"
    "v_mfma_f32_16x16x32_bf16 a[84:87], a[224:227], a[200:203], a[84:87]\n"
    "ds_read_b64_tr_b16 a[188:189], v249 offset:19968\n"
    "v_mfma_f32_16x16x32_bf16 a[84:87], a[228:231], a[204:207], a[84:87]\n"
    "ds_read_b64_tr_b16 a[190:191], v249 offset:29184\n"
    "v_mfma_f32_16x16x32_bf16 a[88:91], a[224:227], a[208:211], a[88:91]\n"
    "s_add_u32 m0, s83, 22016\n"
    "v_mfma_f32_16x16x32_bf16 a[88:91], a[228:231], a[212:215], a[88:91]\n"
    "s_add_u32 s72, s9, 83072\n"
    "buffer_load_dwordx4 v247, s[24:27], s72 offen lds\n"
    "v_mfma_f32_16x16x32_bf16 a[92:95], a[224:227], a[216:219], a[92:95]\n"
    "s_add_u32 m0, s83, 23040\n"
    "s_add_u32 s86, s79, 131072\n"
    "v_mfma_f32_16x16x32_bf16 a[92:95], a[228:231], a[220:223], a[92:95]\n"
    "s_waitcnt lgkmcnt(4)\n"
    "s_add_u32 s72, s9, 83200\n"
    "buffer_load_dwordx4 v247, s[24:27], s72 offen lds\n"
    "v_mfma_f32_16x16x32_bf16 a[96:99], a[176:179], a[192:195], a[96:99]\n"
    "ds_read_b64_tr_b16 a[224:225], v248 offset:2048\n"
    "v_mfma_f32_16x16x32_bf16 a[96:99], a[180:183], a[196:199], a[96:99]\n"
    "ds_read_b64_tr_b16 a[226:227], v248 offset:11264\n"
    "v_mfma_f32_16x16x32_bf16 a[100:103], a[176:179], a[200:203], a[100:103]\n"
    "ds_read_b64_tr_b16 a[228:229], v248 offset:20480\n"
    "v_mfma_f32_16x16x32_bf16 a[100:103], a[180:183], a[204:207], a[100:103]\n"
    "ds_read_b64_tr_b16 a[230:231], v248 offset:29696\n"
    "v_mfma_f32_16x16x32_bf16 a[104:107], a[176:179], a[208:211], a[104:107]\n"
    "s_add_u32 m0, s83, 24064\n"
    "s_not_b64 exec, s[32:33]\n"
    "buffer_store_dwordx4 v[32:35], v246, s[48:51], s86 offen offset:0 sc1\n"
    "s_mov_b64 exec, -1\n"
    "v_mfma_f32_16x16x32_bf16 a[104:107], a[180:183], a[212:215], a[104:107]\n"
    "s_add_u32 s72, s9, 83328\n"
    "buffer_load_dwordx4 v247, s[24:27], s72 offen lds\n"
    "v_mfma_f32_16x16x32_bf16 a[108:111], a[176:179], a[216:219], a[108:111]\n"
    "v_add_u32 v248, 53248, v248\n"
    "v_add_u32 v249, 53248, v249\n"
    "v_mfma_f32_16x16x32_bf16 a[108:111], a[180:183], a[220:223], a[108:111]\n"
    "s_waitcnt lgkmcnt(4)\n"
    "s_add_u32 m0, s66, 20992\n"
    "v_add_u32 v250, 53248, v250\n"
    "v_mfma_f32_16x16x32_bf16 a[112:115], a[184:187], a[192:195], a[112:115]\n"
    "s_add_u32 s72, s9, 82944\n"
    "s_mov_b32 exec_hi, 0\n"
    "buffer_load_dwordx4 v245, s[24:27], s72 offen lds\n"
    "s_mov_b32 exec_hi, -1\n"
    "v_mfma_f32_16x16x32_bf16 a[112:115], a[188:191], a[196:199], a[112:115]\n"
    "s_waitcnt vmcnt(35) lgkmcnt(0)\n"
    "s_barrier\n"
    "v_mfma_f32_16x16x32_bf16 a[116:119], a[184:187], a[200:203], a[116:119]\n"
    "ds_read_b64_tr_b16 a[176:177], v248 offset:0\n"
    "v_mfma_f32_16x16x32_bf16 a[116:119], a[188:191], a[204:207], a[116:119]\n"
    "ds_read_b64_tr_b16 a[178:179], v248 offset:9216\n"
    "v_mfma_f32_16x16x32_bf16 a[120:123], a[184:187], a[208:211], a[120:123]\n"
    "s_sub_u32 s4, 2, s10\n"
    "ds_read_b64_tr_b16 a[180:181], v248 offset:18432\n"
    "ds_read_b128 a[144:147], v250 offset:0\n"
    "v_mfma_f32_16x16x32_bf16 a[120:123], a[188:191], a[212:215], a[120:123]\n"
    "s_xor_b32 s4, s4, s11\n"
    "ds_read_b64_tr_b16 a[182:183], v248 offset:27648\n"
    "ds_read_b128 a[148:151], v250 offset:1024\n"
    "v_mfma_f32_16x16x32_bf16 a[124:127], a[184:187], a[216:219], a[124:127]\n"
    "s_add_u32 s4, s4, s12\n"
    "ds_read_b128 a[152:155], v250 offset:2048\n"
    "v_mfma_f32_16x16x32_bf16 a[124:127], a[188:191], a[220:223], a[124:127]\n"
    "s_lshl_b32 s8, s4, 12\n"
    "ds_read_b128 a[156:159], v250 offset:3072\n"
    "v_mfma_f32_16x16x32_bf16 a[128:131], a[224:227], a[192:195], a[128:131]\n"
    "s_add_u32 s8, s8, s80\n"
    "ds_read_b128 a[160:163], v250 offset:4096\n"
    "ds_read_b128 a[164:167], v250 offset:5120\n"
    "v_mfma_f32_16x16x32_bf16 a[128:131], a[228:231], a[196:199], a[128:131]\n"
    "s_mul_i32 s9, s4, 147456\n"
    "ds_read_b128 a[168:171], v250 offset:6144\n"
    "ds_read_b128 a[172:175], v250 offset:7168\n"
    "v_mfma_f32_16x16x32_bf16 a[132:135], a[224:227], a[200:203], a[132:135]\n"
    "s_add_u32 s9, s9, s81\n"
    "ds_read_b64_tr_b16 a[184:185], v249 offset:0\n"
    "v_mfma_f32_16x16x32_bf16 a[132:135], a[228:231], a[204:207], a[132:135]\n"
    "ds_read_b64_tr_b16 a[186:187], v249 offset:9216\n"
    "v_mfma_f32_16x16x32_bf16 a[136:139], a[224:227], a[208:211], a[136:139]\n"
    "ds_read_b64_tr_b16 a[188:189], v249 offset:18432\n"
    "v_mfma_f32_16x16x32_bf16 a[136:139], a[228:231], a[212:215], a[136:139]\n"
    "ds_read_b64_tr_b16 a[190:191], v249 offset:27648\n"
    "v_mfma_f32_16x16x32_bf16 a[140:143], a[224:227], a[216:219], a[140:143]\n"
    "s_not_b64 exec, s[32:33]\n"
    "buffer_store_dwordx4 v[40:43], v246, s[48:51], s86 offen offset:1024 sc1\n"
    "s_mov_b64 exec, -1\n"
    "v_mfma_f32_16x16x32_bf16 a[140:143], a[228:231], a[220:223], a[140:143]\n"
    "s_waitcnt lgkmcnt(4)\n"
    "v_mfma_f32_16x16x32_bf16 a[0:3], a[176:179], a[144:147], a[0:3]\n"
    "ds_read_b64_tr_b16 a[224:225], v248 offset:512\n"
    "v_mfma_f32_16x16x32_bf16 a[0:3], a[180:183], a[148:151], a[0:3]\n"
    "ds_read_b64_tr_b16 a[226:227], v248 offset:9728\n"
    "v_mfma_f32_16x16x32_bf16 a[4:7], a[176:179], a[152:155], a[4:7]\n"
    "ds_read_b64_tr_b16 a[228:229], v248 offset:18944\n"
    "v_mfma_f32_16x16x32_bf16 a[4:7], a[180:183], a[156:159], a[4:7]\n"
    "ds_read_b64_tr_b16 a[230:231], v248 offset:28160\n"
    "v_mfma_f32_16x16x32_bf16 a[8:11], a[176:179], a[160:163], a[8:11]\n"
    "s_add_u32 m0, s82, 53248\n"
    "s_not_b64 exec, s[32:33]\n"
    "buffer_store_dwordx4 v[48:51], v246, s[48:51], s86 offen offset:2048 sc1\n"
    "s_mov_b64 exec, -1\n"
    "v_mfma_f32_16x16x32_bf16 a[8:11], a[180:183], a[164:167], a[8:11]\n"
    "s_add_u32 s72, s8, 0\n"
    "buffer_load_dwordx4 v246, s[20:23], s72 offen sc1 lds\n"
    "v_mfma_f32_16x16x32_bf16 a[12:15], a[176:179], a[168:171], a[12:15]\n"
    "v_mfma_f32_16x16x32_bf16 a[12:15], a[180:183], a[172:175], a[12:15]\n"
    "s_waitcnt lgkmcnt(4)\n"
    "buffer_load_dwordx4 v246, s[20:23], s72 offen offset:1024 sc1 lds\n"
    "v_mfma_f32_16x16x32_bf16 a[16:19], a[184:187], a[144:147], a[16:19]\n"
    "ds_read_b64_tr_b16 a[176:177], v249 offset:512\n"
    "v_mfma_f32_16x16x32_bf16 a[16:19], a[188:191], a[148:151], a[16:19]\n"
    "ds_read_b64_tr_b16 a[178:179], v249 offset:9728\n"
    "v_mfma_f32_16x16x32_bf16 a[20:23], a[184:187], a[152:155], a[20:23]\n"
    "ds_read_b64_tr_b16 a[180:181], v249 offset:18944\n"
    "v_mfma_f32_16x16x32_bf16 a[20:23], a[188:191], a[156:159], a[20:23]\n"
    "ds_read_b64_tr_b16 a[182:183], v249 offset:28160\n"
    "v_mfma_f32_16x16x32_bf16 a[24:27], a[184:187], a[160:163], a[24:27]\n"
    "s_add_u32 m0, s82, 55296\n"
    "v_mfma_f32_16x16x32_bf16 a[24:27], a[188:191], a[164:167], a[24:27]\n"
    "s_add_u32 s72, s72, s13\n"
    "buffer_load_dwordx4 v246, s[20:23], s72 offen sc1 lds\n"
    "v_mfma_f32_16x16x32_bf16 a[28:31], a[184:187], a[168:171], a[28:31]\n"
    "v_mfma_f32_16x16x32_bf16 a[28:31], a[188:191], a[172:175], a[28:31]\n"
    "s_waitcnt lgkmcnt(4)\n"
    "buffer_load_dwordx4 v246, s[20:23], s72 offen offset:1024 sc1 lds\n"
    "v_mfma_f32_16x16x32_bf16 a[32:35], a[224:227], a[144:147], a[32:35]\n"
    "ds_read_b64_tr_b16 a[184:185], v248 offset:1024\n"
    "v_mfma_f32_16x16x32_bf16 a[32:35], a[228:231], a[148:151], a[32:35]\n"
    "ds_read_b64_tr_b16 a[186:187], v248 offset:10240\n"
    "v_mfma_f32_16x16x32_bf16 a[36:39], a[224:227], a[152:155], a[36:39]\n"
    "ds_read_b64_tr_b16 a[188:189], v248 offset:19456\n"
    "v_mfma_f32_16x16x32_bf16 a[36:39], a[228:231], a[156:159], a[36:39]\n"
    "ds_read_b64_tr_b16 a[190:191], v248 offset:28672\n"
    "v_mfma_f32_16x16x32_bf16 a[40:43], a[224:227], a[160:163], a[40:43]\n"
    "s_add_u32 m0, s83, 69632\n"
    "s_not_b64 exec, s[32:33]\n"
    "buffer_store_dwordx4 v[56:59], v246, s[48:51], s86 offen offset:3072 sc1\n"
    "s_mov_b64 exec, -1\n"
    "v_mfma_f32_16x16x32_bf16 a[40:43], a[228:231], a[164:167], a[40:43]\n"
    "s_add_u32 s72, s9, 0\n"
    "buffer_load_dwordx4 v247, s[24:27], s72 offen lds\n"
    "v_mfma_f32_16x16x32_bf16 a[44:47], a[224:227], a[168:171], a[44:47]\n"
    "s_add_u32 m0, s83, 70656\n"
    "v_mfma_f32_16x16x32_bf16 a[44:47], a[228:231], a[172:175], a[44:47]\n"
    "s_waitcnt lgkmcnt(4)\n"
    "s_add_u32 s72, s9, 128\n"
    "buffer_load_dwordx4 v247, s[24:27], s72 offen lds\n"
    "v_mfma_f32_16x16x32_bf16 a[48:51], a[176:179], a[144:147], a[48:51]\n"
    "ds_read_b64_tr_b16 a[224:225], v249 offset:1024\n"
    "v_mfma_f32_16x16x32_bf16 a[48:51], a[180:183], a[148:151], a[48:51]\n"
    "ds_read_b64_tr_b16 a[226:227], v249 offset:10240\n"
    "v_mfma_f32_16x16x32_bf16 a[52:55], a[176:179], a[152:155], a[52:55]\n"
    "ds_read_b64_tr_b16 a[228:229], v249 offset:19456\n"
    "v_mfma_f32_16x16x32_bf16 a[52:55], a[180:183], a[156:159], a[52:55]\n"
    "ds_read_b64_tr_b16 a[230:231], v249 offset:28672\n"
    "v_mfma_f32_16x16x32_bf16 a[56:59], a[176:179], a[160:163], a[56:59]\n"
    "s_add_u32 m0, s83, 71680\n"
    "v_mfma_f32_16x16x32_bf16 a[56:59], a[180:183], a[164:167], a[56:59]\n"
    "s_add_u32 s72, s9, 256\n"
    "buffer_load_dwordx4 v247, s[24:27], s72 offen lds\n"
    "v_mfma_f32_16x16x32_bf16 a[60:63], a[176:179], a[168:171], a[60:63]\n"
    "s_add_u32 m0, s83, 72704\n"
    "s_add_u32 s86, s79, 262144\n"
    "v_mfma_f32_16x16x32_bf16 a[60:63], a[180:183], a[172:175], a[60:63]\n"
    "s_waitcnt lgkmcnt(4)\n"
    "s_add_u32 s72, s9, 384\n"
    "buffer_load_dwordx4 v247, s[24:27], s72 offen lds\n"
    "v_mfma_f32_16x16x32_bf16 a[64:67], a[184:187], a[144:147], a[64:67]\n"
    "ds_read_b64_tr_b16 a[176:177], v248 offset:1536\n"
    "v_mfma_f32_16x16x32_bf16 a[64:67], a[188:191], a[148:151], a[64:67]\n"
    "ds_read_b64_tr_b16 a[178:179], v248 offset:10752\n"
    "v_mfma_f32_16x16x32_bf16 a[68:71], a[184:187], a[152:155], a[68:71]\n"
    "ds_read_b64_tr_b16 a[180:181], v248 offset:19968\n"
    "v_mfma_f32_16x16x32_bf16 a[68:71], a[188:191], a[156:159], a[68:71]\n"
    "ds_read_b64_tr_b16 a[182:183], v248 offset:29184\n"
    "v_mfma_f32_16x16x32_bf16 a[72:75], a[184:187], a[160:163], a[72:75]\n"
    "s_add_u32 m0, s66, 69632\n"
    "s_not_b64 exec, s[32:33]\n"
    "buffer_store_dwordx4 v[64:67], v246, s[48:51], s86 offen offset:0 sc1\n"
    "s_mov_b64 exec, -1\n"
    "v_mfma_f32_16x16x32_bf16 a[72:75], a[188:191], a[164:167], a[72:75]\n"
    "s_add_u32 s72, s9, 0\n"
    "s_mov_b32 exec_hi, 0\n"
    "buffer_load_dwordx4 v245, s[24:27], s72 offen lds\n"
    "s_mov_b32 exec_hi, -1\n"
    "v_mfma_f32_16x16x32_bf16 a[76:79], a[184:187], a[168:171], a[76:79]\n"
    "s_add_u32 m0, s83, 74240\n"
    "v_mfma_f32_16x16x32_bf16 a[76:79], a[188:191], a[172:175], a[76:79]\n"
    "s_waitcnt lgkmcnt(4)\n"
    "s_add_u32 s72, s9, 9216\n"
    "buffer_load_dwordx4 v247, s[24:27], s72 offen lds\n"
    "v_mfma_f32_16x16x32_bf16 a[80:83], a[224:227], a[144:147], a[80:83]\n"
    "ds_read_b64_tr_b16 a[184:185], v249 offset:1536\n"
    "v_mfma_f32_16x16x32_bf16 a[80:83], a[228:231], a[148:151], a[80:83]\n"
    "ds_read_b64_tr_b16 a[186:187], v249 offset:10752\n"
    "v_mfma_f32_16x16x32_bf16 a[84:87], a[224:227], a[152:155], a[84:87]\n"
    "ds_read_b64_tr_b16 a[188:189], v249 offset:19968\n"
    "v_mfma_f32_16x16x32_bf16 a[84:87], a[228:231], a[156:159], a[84:87]\n"
    "ds_read_b64_tr_b16 a[190:191], v249 offset:29184\n"
    "v_mfma_f32_16x16x32_bf16 a[88:91], a[224:227], a[160:163], a[88:91]\n"
    "s_add_u32 m0, s83, 75264\n"
    "s_not_b64 exec, s[32:33]\n"
    "buffer_store_dwordx4 v[72:75], v246, s[48:51], s86 offen offset:1024 sc1\n"
    "s_mov_b64 exec, -1\n"
    "v_mfma_f32_16x16x32_bf16 a[88:91], a[228:231], a[164:167], a[88:91]\n"
    "s_add_u32 s72, s9, 9344\n"
    "buffer_load_dwordx4 v247, s[24:27], s72 offen lds\n"
    "v_mfma_f32_16x16x32_bf16 a[92:95], a[224:227], a[168:171], a[92:95]\n"
    "s_add_u32 m0, s83, 76288\n"
    "v_mfma_f32_16x16x32_bf16 a[92:95], a[228:231], a[172:175], a[92:95]\n"
    "s_waitcnt lgkmcnt(4)\n"
    "s_add_u32 s72, s9, 9472\n"
    "buffer_load_dwordx4 v247, s[24:27], s72 offen lds\n"
    "v_mfma_f32_16x16x32_bf16 a[96:99], a[176:179], a[144:147], a[96:99]\n"
    "ds_read_b64_tr_b16 a[224:225], v248 offset:2048\n"
    "v_mfma_f32_16x16x32_bf16 a[96:99], a[180:183], a[148:151], a[96:99]\n"
    "ds_read_b64_tr_b16 a[226:227], v248 offset:11264\n"
    "v_mfma_f32_16x16x32_bf16 a[100:103], a[176:179], a[152:155], a[100:103]\n"
    "ds_read_b64_tr_b16 a[228:229], v248 offset:20480\n"
    "v_mfma_f32_16x16x32_bf16 a[100:103], a[180:183], a[156:159], a[100:103]\n"
    "ds_read_b64_tr_b16 a[230:231], v248 offset:29696\n"
    "v_mfma_f32_16x16x32_bf16 a[104:107], a[176:179], a[160:163], a[104:107]\n"
    "s_add_u32 m0, s83, 77312\n"
    "v_mfma_f32_16x16x32_bf16 a[104:107], a[180:183], a[164:167], a[104:107]\n"
    "s_add_u32 s72, s9, 9600\n"
    "buffer_load_dwordx4 v247, s[24:27], s72 offen lds\n"
    "v_mfma_f32_16x16x32_bf16 a[108:111], a[176:179], a[168:171], a[108:111]\n"
    "v_add_u32 v248, 4294860800, v248\n"
    "v_add_u32 v249, 4294860800, v249\n"
    "v_mfma_f32_16x16x32_bf16 a[108:111], a[180:183], a[172:175], a[108:111]\n"
    "s_waitcnt lgkmcnt(4)\n"
    "s_add_u32 m0, s66, 74240\n"
    "v_add_u32 v250, 4294860800, v250\n"
    "v_mfma_f32_16x16x32_bf16 a[112:115], a[184:187], a[144:147], a[112:115]\n"
    "s_add_u32 s72, s9, 9216\n"
    "s_mov_b32 exec_hi, 0\n"
    "buffer_load_dwordx4 v245, s[24:27], s72 offen lds\n"
    "s_mov_b32 exec_hi, -1\n"
    "v_mfma_f32_16x16x32_bf16 a[112:115], a[188:191], a[148:151], a[112:115]\n"
    "s_waitcnt vmcnt(19) lgkmcnt(0)\n"
    "s_barrier\n"
    "v_mfma_f32_16x16x32_bf16 a[116:119], a[184:187], a[152:155], a[116:119]\n"
    "ds_read_b64_tr_b16 a[176:177], v248 offset:0\n"
    "v_mfma_f32_16x16x32_bf16 a[116:119], a[188:191], a[156:159], a[116:119]\n"
    "ds_read_b64_tr_b16 a[178:179], v248 offset:9216\n"
    "v_mfma_f32_16x16x32_bf16 a[120:123], a[184:187], a[160:163], a[120:123]\n"
    "ds_read_b64_tr_b16 a[180:181], v248 offset:18432\n"
    "ds_read_b128 a[192:195], v250 offset:0\n"
    "v_mfma_f32_16x16x32_bf16 a[120:123], a[188:191], a[164:167], a[120:123]\n"
    "ds_read_b64_tr_b16 a[182:183], v248 offset:27648\n"
    "ds_read_b128 a[196:199], v250 offset:1024\n"
    "v_mfma_f32_16x16x32_bf16 a[124:127], a[184:187], a[168:171], a[124:127]\n"
    "ds_read_b128 a[200:203], v250 offset:2048\n"
    "v_mfma_f32_16x16x32_bf16 a[124:127], a[188:191], a[172:175], a[124:127]\n"
    "ds_read_b128 a[204:207], v250 offset:3072\n"
    "v_mfma_f32_16x16x32_bf16 a[128:131], a[224:227], a[144:147], a[128:131]\n"
    "ds_read_b128 a[208:211], v250 offset:4096\n"
    "ds_read_b128 a[212:215], v250 offset:5120\n"
    "v_mfma_f32_16x16x32_bf16 a[128:131], a[228:231], a[148:151], a[128:131]\n"
    "ds_read_b128 a[216:219], v250 offset:6144\n"
    "ds_read_b128 a[220:223], v250 offset:7168\n"
    "v_mfma_f32_16x16x32_bf16 a[132:135], a[224:227], a[152:155], a[132:135]\n"
    "ds_read_b64_tr_b16 a[184:185], v249 offset:0\n"
    "v_mfma_f32_16x16x32_bf16 a[132:135], a[228:231], a[156:159], a[132:135]\n"
    "ds_read_b64_tr_b16 a[186:187], v249 offset:9216\n"
    "v_mfma_f32_16x16x32_bf16 a[136:139], a[224:227], a[160:163], a[136:139]\n"
    "ds_read_b64_tr_b16 a[188:189], v249 offset:18432\n"
    "v_mfma_f32_16x16x32_bf16 a[136:139], a[228:231], a[164:167], a[136:139]\n"
    "ds_read_b64_tr_b16 a[190:191], v249 offset:27648\n"
    "v_mfma_f32_16x16x32_bf16 a[140:143], a[224:227], a[168:171], a[140:143]\n"
    "s_not_b64 exec, s[32:33]\n"
    "buffer_store_dwordx4 v[80:83], v246, s[48:51], s86 offen offset:2048 sc1\n"
    "s_mov_b64 exec, -1\n"
    "v_mfma_f32_16x16x32_bf16 a[140:143], a[228:231], a[172:175], a[140:143]\n"
    "s_waitcnt lgkmcnt(4)\n"
    "v_mfma_f32_16x16x32_bf16 a[0:3], a[176:179], a[192:195], a[0:3]\n"
    "ds_read_b64_tr_b16 a[224:225], v248 offset:512\n"
    "v_mfma_f32_16x16x32_bf16 a[0:3], a[180:183], a[196:199], a[0:3]\n"
    "ds_read_b64_tr_b16 a[226:227], v248 offset:9728\n"
    "v_mfma_f32_16x16x32_bf16 a[4:7], a[176:179], a[200:203], a[4:7]\n"
    "ds_read_b64_tr_b16 a[228:229], v248 offset:18944\n"
    "v_mfma_f32_16x16x32_bf16 a[4:7], a[180:183], a[204:207], a[4:7]\n"
    "ds_read_b64_tr_b16 a[230:231], v248 offset:28160\n"
    "v_mfma_f32_16x16x32_bf16 a[8:11], a[176:179], a[208:211], a[8:11]\n"
    "s_add_u32 m0, s82, 106496\n"
    "s_not_b64 exec, s[32:33]\n"
    "buffer_store_dwordx4 v[88:91], v246, s[48:51], s86 offen offset:3072 sc1\n"
    "s_mov_b64 exec, -1\n"
    "v_mfma_f32_16x16x32_bf16 a[8:11], a[180:183], a[212:215], a[8:11]\n"
    "s_add_u32 s72, s8, 2048\n"
    "buffer_load_dwordx4 v246, s[20:23], s72 offen sc1 lds\n"
    "v_mfma_f32_16x16x32_bf16 a[12:15], a[176:179], a[216:219], a[12:15]\n"
    "v_mfma_f32_16x16x32_bf16 a[12:15], a[180:183], a[220:223], a[12:15]\n"
    "s_waitcnt lgkmcnt(4)\n"
    "buffer_load_dwordx4 v246, s[20:23], s72 offen offset:1024 sc1 lds\n"
    "v_mfma_f32_16x16x32_bf16 a[16:19], a[184:187], a[192:195], a[16:19]\n"
    "ds_read_b64_tr_b16 a[176:177], v249 offset:512\n"
    "v_mfma_f32_16x16x32_bf16 a[16:19], a[188:191], a[196:199], a[16:19]\n"
    "ds_read_b64_tr_b16 a[178:179], v249 offset:9728\n"
    "v_mfma_f32_16x16x32_bf16 a[20:23], a[184:187], a[200:203], a[20:23]\n"
    "ds_read_b64_tr_b16 a[180:181], v249 offset:18944\n"
    "v_mfma_f32_16x16x32_bf16 a[20:23], a[188:191], a[204:207], a[20:23]\n"
    "ds_read_b64_tr_b16 a[182:183], v249 offset:28160\n"
    "v_mfma_f32_16x16x32_bf16 a[24:27], a[184:187], a[208:211], a[24:27]\n"
    "s_add_u32 m0, s82, 108544\n"
    "v_mfma_f32_16x16x32_bf16 a[24:27], a[188:191], a[212:215], a[24:27]\n"
    "s_add_u32 s72, s72, s13\n"
    "buffer_load_dwordx4 v246, s[20:23], s72 offen sc1 lds\n"
    "v_mfma_f32_16x16x32_bf16 a[28:31], a[184:187], a[216:219], a[28:31]\n"
    "s_add_u32 s86, s79, 393216\n"
    "s_not_b64 exec, s[32:33]\n"
    "buffer_store_dwordx4 v[96:99], v246, s[48:51], s86 offen offset:0 sc1\n"
    "s_mov_b64 exec, -1\n"
    "v_mfma_f32_16x16x32_bf16 a[28:31], a[188:191], a[220:223], a[28:31]\n"
    "s_waitcnt lgkmcnt(4)\n"
    "buffer_load_dwordx4 v246, s[20:23], s72 offen offset:1024 sc1 lds\n"
    "v_mfma_f32_16x16x32_bf16 a[32:35], a[224:227], a[192:195], a[32:35]\n"
    "ds_read_b64_tr_b16 a[184:185], v248 offset:1024\n"
    "v_mfma_f32_16x16x32_bf16 a[32:35], a[228:231], a[196:199], a[32:35]\n"
    "ds_read_b64_tr_b16 a[186:187], v248 offset:10240\n"
    "v_mfma_f32_16x16x32_bf16 a[36:39], a[224:227], a[200:203], a[36:39]\n"
    "ds_read_b64_tr_b16 a[188:189], v248 offset:19456\n"
    "v_mfma_f32_16x16x32_bf16 a[36:39], a[228:231], a[204:207], a[36:39]\n"
    "ds_read_b64_tr_b16 a[190:191], v248 offset:28672\n"
    "v_mfma_f32_16x16x32_bf16 a[40:43], a[224:227], a[208:211], a[40:43]\n"
    "s_add_u32 m0, s83, 122880\n"
    "v_mfma_f32_16x16x32_bf16 a[40:43], a[228:231], a[212:215], a[40:43]\n"
    "s_add_u32 s72, s9, 73728\n"
    "buffer_load_dwordx4 v247, s[24:27], s72 offen lds\n"
    "v_mfma_f32_16x16x32_bf16 a[44:47], a[224:227], a[216:219], a[44:47]\n"
    "s_add_u32 m0, s83, 123904\n"
    "v_mfma_f32_16x16x32_bf16 a[44:47], a[228:231], a[220:223], a[44:47]\n"
    "s_waitcnt lgkmcnt(4)\n"
    "s_add_u32 s72, s9, 73856\n"
    "buffer_load_dwordx4 v247, s[24:27], s72 offen lds\n"
    "v_mfma_f32_16x16x32_bf16 a[48:51], a[176:179], a[192:195], a[48:51]\n"
    "ds_read_b64_tr_b16 a[224:225], v249 offset:1024\n"
    "v_mfma_f32_16x16x32_bf16 a[48:51], a[180:183], a[196:199], a[48:51]\n"
    "ds_read_b64_tr_b16 a[226:227], v249 offset:10240\n"
    "v_mfma_f32_16x16x32_bf16 a[52:55], a[176:179], a[200:203], a[52:55]\n"
    "ds_read_b64_tr_b16 a[228:229], v249 offset:19456\n"
    "v_mfma_f32_16x16x32_bf16 a[52:55], a[180:183], a[204:207], a[52:55]\n"
    "ds_read_b64_tr_b16 a[230:231], v249 offset:28672\n"
    "v_mfma_f32_16x16x32_bf16 a[56:59], a[176:179], a[208:211], a[56:59]\n"
    "s_add_u32 m0, s83, 124928\n"
    "s_not_b64 exec, s[32:33]\n"
    "buffer_store_dwordx4 v[104:107], v246, s[48:51], s86 offen offset:1024 sc1\n"
    "s_mov_b64 exec, -1\n"
    "v_mfma_f32_16x16x32_bf16 a[56:59], a[180:183], a[212:215], a[56:59]\n"
    "s_add_u32 s72, s9, 73984\n"
    "buffer_load_dwordx4 v247, s[24:27], s72 offen lds\n"
    "v_mfma_f32_16x16x32_bf16 a[60:63], a[176:179], a[216:219], a[60:63]\n"
    "s_add_u32 m0, s83, 125952\n"
    "v_mfma_f32_16x16x32_bf16 a[60:63], a[180:183], a[220:223], a[60:63]\n"
    "s_waitcnt lgkmcnt(4)\n"
    "s_add_u32 s72, s9, 74112\n"
    "buffer_load_dwordx4 v247, s[24:27], s72 offen lds\n"
    "v_mfma_f32_16x16x32_bf16 a[64:67], a[184:187], a[192:195], a[64:67]\n"
    "ds_read_b64_tr_b16 a[176:177], v248 offset:1536\n"
    "v_mfma_f32_16x16x32_bf16 a[64:67], a[188:191], a[196:199], a[64:67]\n"
    "ds_read_b64_tr_b16 a[178:179], v248 offset:10752\n"
    "v_mfma_f32_16x16x32_bf16 a[68:71], a[184:187], a[200:203], a[68:71]\n"
    "ds_read_b64_tr_b16 a[180:181], v248 offset:19968\n"
    "v_mfma_f32_16x16x32_bf16 a[68:71], a[188:191], a[204:207], a[68:71]\n"
    "ds_read_b64_tr_b16 a[182:183], v248 offset:29184\n"
    "v_mfma_f32_16x16x32_bf16 a[72:75], a[184:187], a[208:211], a[72:75]\n"
    "s_add_u32 m0, s66, 122880\n"
    "v_mfma_f32_16x16x32_bf16 a[72:75], a[188:191], a[212:215], a[72:75]\n"
    "s_add_u32 s72, s9, 73728\n"
    "s_mov_b32 exec_hi, 0\n"
    "buffer_load_dwordx4 v245, s[24:27], s72 offen lds\n"
    "s_mov_b32 exec_hi, -1\n"
    "v_mfma_f32_16x16x32_bf16 a[76:79], a[184:187], a[216:219], a[76:79]\n"
    "s_add_u32 m0, s83, 127488\n"
    "v_mfma_f32_16x16x32_bf16 a[76:79], a[188:191], a[220:223], a[76:79]\n"
    "s_waitcnt lgkmcnt(4)\n"
    "s_add_u32 s72, s9, 82944\n"
    "buffer_load_dwordx4 v247, s[24:27], s72 offen lds\n"
    "v_mfma_f32_16x16x32_bf16 a[80:83], a[224:227], a[192:195], a[80:83]\n"
    "ds_read_b64_tr_b16 a[184:185], v249 offset:1536\n"
    "v_mfma_f32_16x16x32_bf16 a[80:83], a[228:231], a[196:199], a[80:83]\n"
    "ds_read_b64_tr_b16 a[186:187], v249 offset:10752\n"
    "v_mfma_f32_16x16x32_bf16 a[84:87], a[224:227], a[200:203], a[84:87]\n"
    "ds_read_b64_tr_b16 a[188:189], v249 offset:19968\n"
    "v_mfma_f32_16x16x32_bf16 a[84:87], a[228:231], a[204:207], a[84:87]\n"
    "ds_read_b64_tr_b16 a[190:191], v249 offset:29184\n"
    "v_mfma_f32_16x16x32_bf16 a[88:91], a[224:227], a[208:211], a[88:91]\n"
    "s_add_u32 m0, s83, 128512\n"
    "s_not_b64 exec, s[32:33]\n"
    "buffer_store_dwordx4 v[112:115], v246, s[48:51], s86 offen offset:2048 sc1\n"
    "s_mov_b64 exec, -1\n"
    "v_mfma_f32_16x16x32_bf16 a[88:91], a[228:231], a[212:215], a[88:91]\n"
    "s_add_u32 s72, s9, 83072\n"
    "buffer_load_dwordx4 v247, s[24:27], s72 offen lds\n"
    "v_mfma_f32_16x16x32_bf16 a[92:95], a[224:227], a[216:219], a[92:95]\n"
    "s_add_u32 m0, s83, 129536\n"
    "v_mfma_f32_16x16x32_bf16 a[92:95], a[228:231], a[220:223], a[92:95]\n"
    "s_waitcnt lgkmcnt(4)\n"
    "s_add_u32 s72, s9, 83200\n"
    "buffer_load_dwordx4 v247, s[24:27], s72 offen lds\n"
    "v_mfma_f32_16x16x32_bf16 a[96:99], a[176:179], a[192:195], a[96:99]\n"
    "ds_read_b64_tr_b16 a[224:225], v248 offset:2048\n"
    "v_mfma_f32_16x16x32_bf16 a[96:99], a[180:183], a[196:199], a[96:99]\n"
    "ds_read_b64_tr_b16 a[226:227], v248 offset:11264\n"
    "v_mfma_f32_16x16x32_bf16 a[100:103], a[176:179], a[200:203], a[100:103]\n"
    "ds_read_b64_tr_b16 a[228:229], v248 offset:20480\n"
    "v_mfma_f32_16x16x32_bf16 a[100:103], a[180:183], a[204:207], a[100:103]\n"
    "ds_read_b64_tr_b16 a[230:231], v248 offset:29696\n"
    "v_mfma_f32_16x16x32_bf16 a[104:107], a[176:179], a[208:211], a[104:107]\n"
    "s_add_u32 m0, s83, 130560\n"
    "v_mfma_f32_16x16x32_bf16 a[104:107], a[180:183], a[212:215], a[104:107]\n"
    "s_add_u32 s72, s9, 83328\n"
    "buffer_load_dwordx4 v247, s[24:27], s72 offen lds\n"
    "v_mfma_f32_16x16x32_bf16 a[108:111], a[176:179], a[216:219], a[108:111]\n"
    "v_add_u32 v248, 53248, v248\n"
    "v_add_u32 v249, 53248, v249\n"
    "v_mfma_f32_16x16x32_bf16 a[108:111], a[180:183], a[220:223], a[108:111]\n"
    "s_waitcnt lgkmcnt(4)\n"
    "s_add_u32 m0, s66, 127488\n"
    "v_add_u32 v250, 53248, v250\n"
    "v_mfma_f32_16x16x32_bf16 a[112:115], a[184:187], a[192:195], a[112:115]\n"
    "s_add_u32 s72, s9, 82944\n"
    "s_mov_b32 exec_hi, 0\n"
    "buffer_load_dwordx4 v245, s[24:27], s72 offen lds\n"
    "s_mov_b32 exec_hi, -1\n"
    "v_mfma_f32_16x16x32_bf16 a[112:115], a[188:191], a[196:199], a[112:115]\n"
    "s_waitcnt vmcnt(19) lgkmcnt(0)\n"
    "s_barrier\n"
    "v_mfma_f32_16x16x32_bf16 a[116:119], a[184:187], a[200:203], a[116:119]\n"
    "ds_read_b64_tr_b16 a[176:177], v248 offset:0\n"
    "v_mfma_f32_16x16x32_bf16 a[116:119], a[188:191], a[204:207], a[116:119]\n"
    "ds_read_b64_tr_b16 a[178:179], v248 offset:9216\n"
    "v_mfma_f32_16x16x32_bf16 a[120:123], a[184:187], a[208:211], a[120:123]\n"
    "s_sub_u32 s4, 3, s10\n"
    "ds_read_b64_tr_b16 a[180:181], v248 offset:18432\n"
    "ds_read_b128 a[144:147], v250 offset:0\n"
    "v_mfma_f32_16x16x32_bf16 a[120:123], a[188:191], a[212:215], a[120:123]\n"
    "s_xor_b32 s4, s4, s11\n"
    "ds_read_b64_tr_b16 a[182:183], v248 offset:27648\n"
    "ds_read_b128 a[148:151], v250 offset:1024\n"
    "v_mfma_f32_16x16x32_bf16 a[124:127], a[184:187], a[216:219], a[124:127]\n"
    "s_add_u32 s4, s4, s12\n"
    "ds_read_b128 a[152:155], v250 offset:2048\n"
    "v_mfma_f32_16x16x32_bf16 a[124:127], a[188:191], a[220:223], a[124:127]\n"
    "s_lshl_b32 s8, s4, 12\n"
    "ds_read_b128 a[156:159], v250 offset:3072\n"
    "v_mfma_f32_16x16x32_bf16 a[128:131], a[224:227], a[192:195], a[128:131]\n"
    "s_add_u32 s8, s8, s80\n"
    "ds_read_b128 a[160:163], v250 offset:4096\n"
    "ds_read_b128 a[164:167], v250 offset:5120\n"
    "v_mfma_f32_16x16x32_bf16 a[128:131], a[228:231], a[196:199], a[128:131]\n"
    "s_mul_i32 s9, s4, 147456\n"
    "ds_read_b128 a[168:171], v250 offset:6144\n"
    "ds_read_b128 a[172:175], v250 offset:7168\n"
    "v_mfma_f32_16x16x32_bf16 a[132:135], a[224:227], a[200:203], a[132:135]\n"
    "s_add_u32 s9, s9, s81\n"
    "ds_read_b64_tr_b16 a[184:185], v249 offset:0\n"
    "v_mfma_f32_16x16x32_bf16 a[132:135], a[228:231], a[204:207], a[132:135]\n"
    "ds_read_b64_tr_b16 a[186:187], v249 offset:9216\n"
    "v_mfma_f32_16x16x32_bf16 a[136:139], a[224:227], a[208:211], a[136:139]\n"
    "ds_read_b64_tr_b16 a[188:189], v249 offset:18432\n"
    "v_mfma_f32_16x16x32_bf16 a[136:139], a[228:231], a[212:215], a[136:139]\n"
    "ds_read_b64_tr_b16 a[190:191], v249 offset:27648\n"
    "v_mfma_f32_16x16x32_bf16 a[140:143], a[224:227], a[216:219], a[140:143]\n"
    "s_not_b64 exec, s[32:33]\n"
    "buffer_store_dwordx4 v[120:123], v246, s[48:51], s86 offen offset:3072 sc1\n"
    "s_mov_b64 exec, -1\n"
    "v_mfma_f32_16x16x32_bf16 a[140:143], a[228:231], a[220:223], a[140:143]\n"
    "s_waitcnt lgkmcnt(4)\n"
    "v_mfma_f32_16x16x32_bf16 a[0:3], a[176:179], a[144:147], a[0:3]\n"
    "ds_read_b64_tr_b16 a[224:225], v248 offset:512\n"
    "v_mfma_f32_16x16x32_bf16 a[0:3], a[180:183], a[148:151], a[0:3]\n"
    "ds_read_b64_tr_b16 a[226:227], v248 offset:9728\n"
    "v_mfma_f32_16x16x32_bf16 a[4:7], a[176:179], a[152:155], a[4:7]\n"
    "ds_read_b64_tr_b16 a[228:229], v248 offset:18944\n"
    "v_mfma_f32_16x16x32_bf16 a[4:7], a[180:183], a[156:159], a[4:7]\n"
    "ds_read_b64_tr_b16 a[230:231], v248 offset:28160\n"
    "v_mfma_f32_16x16x32_bf16 a[8:11], a[176:179], a[160:163], a[8:11]\n"
    "s_add_u32 m0, s82, 0\n"
    "v_mfma_f32_16x16x32_bf16 a[8:11], a[180:183], a[164:167], a[8:11]\n"
    "s_add_u32 s72, s8, 0\n"
    "buffer_load_dwordx4 v246, s[20:23], s72 offen sc1 lds\n"
    "v_mfma_f32_16x16x32_bf16 a[12:15], a[176:179], a[168:171], a[12:15]\n"
    "v_mfma_f32_16x16x32_bf16 a[12:15], a[180:183], a[172:175], a[12:15]\n"
    "s_waitcnt lgkmcnt(4)\n"
    "buffer_load_dwordx4 v246, s[20:23], s72 offen offset:1024 sc1 lds\n"
    "v_mfma_f32_16x16x32_bf16 a[16:19], a[184:187], a[144:147], a[16:19]\n"
    "ds_read_b64_tr_b16 a[176:177], v249 offset:512\n"
    "v_mfma_f32_16x16x32_bf16 a[16:19], a[188:191], a[148:151], a[16:19]\n"
    "ds_read_b64_tr_b16 a[178:179], v249 offset:9728\n"
    "v_mfma_f32_16x16x32_bf16 a[20:23], a[184:187], a[152:155], a[20:23]\n"
    "ds_read_b64_tr_b16 a[180:181], v249 offset:18944\n"
    "v_mfma_f32_16x16x32_bf16 a[20:23], a[188:191], a[156:159], a[20:23]\n"
    "ds_read_b64_tr_b16 a[182:183], v249 offset:28160\n"
    "v_mfma_f32_16x16x32_bf16 a[24:27], a[184:187], a[160:163], a[24:27]\n"
    "s_add_u32 m0, s82, 2048\n"
    "v_mfma_f32_16x16x32_bf16 a[24:27], a[188:191], a[164:167], a[24:27]\n"
    "s_add_u32 s72, s72, s13\n"
    "buffer_load_dwordx4 v246, s[20:23], s72 offen sc1 lds\n"
    "v_mfma_f32_16x16x32_bf16 a[28:31], a[184:187], a[168:171], a[28:31]\n"
    "v_mfma_f32_16x16x32_bf16 a[28:31], a[188:191], a[172:175], a[28:31]\n"
    "s_waitcnt lgkmcnt(4)\n"
    "buffer_load_dwordx4 v246, s[20:23], s72 offen offset:1024 sc1 lds\n"
    "v_mfma_f32_16x16x32_bf16 a[32:35], a[224:227], a[144:147], a[32:35]\n"
    "ds_read_b64_tr_b16 a[184:185], v248 offset:1024\n"
    "v_mfma_f32_16x16x32_bf16 a[32:35], a[228:231], a[148:151], a[32:35]\n"
    "ds_read_b64_tr_b16 a[186:187], v248 offset:10240\n"
    "v_mfma_f32_16x16x32_bf16 a[36:39], a[224:227], a[152:155], a[36:39]\n"
    "ds_read_b64_tr_b16 a[188:189], v248 offset:19456\n"
    "v_mfma_f32_16x16x32_bf16 a[36:39], a[228:231], a[156:159], a[36:39]\n"
    "ds_read_b64_tr_b16 a[190:191], v248 offset:28672\n"
    "v_mfma_f32_16x16x32_bf16 a[40:43], a[224:227], a[160:163], a[40:43]\n"
    "s_add_u32 m0, s83, 16384\n"
    "v_mfma_f32_16x16x32_bf16 a[40:43], a[228:231], a[164:167], a[40:43]\n"
    "s_add_u32 s72, s9, 0\n"
    "buffer_load_dwordx4 v247, s[24:27], s72 offen lds\n"
    "v_mfma_f32_16x16x32_bf16 a[44:47], a[224:227], a[168:171], a[44:47]\n"
    "s_add_u32 m0, s83, 17408\n"
    "v_mfma_f32_16x16x32_bf16 a[44:47], a[228:231], a[172:175], a[44:47]\n"
    "s_waitcnt lgkmcnt(4)\n"
    "s_add_u32 s72, s9, 128\n"
    "buffer_load_dwordx4 v247, s[24:27], s72 offen lds\n"
    "v_mfma_f32_16x16x32_bf16 a[48:51], a[176:179], a[144:147], a[48:51]\n"
    "ds_read_b64_tr_b16 a[224:225], v249 offset:1024\n"
    "v_mfma_f32_16x16x32_bf16 a[48:51], a[180:183], a[148:151], a[48:51]\n"
    "ds_read_b64_tr_b16 a[226:227], v249 offset:10240\n"
    "v_mfma_f32_16x16x32_bf16 a[52:55], a[176:179], a[152:155], a[52:55]\n"
    "ds_read_b64_tr_b16 a[228:229], v249 offset:19456\n"
    "v_mfma_f32_16x16x32_bf16 a[52:55], a[180:183], a[156:159], a[52:55]\n"
    "ds_read_b64_tr_b16 a[230:231], v249 offset:28672\n"
    "v_mfma_f32_16x16x32_bf16 a[56:59], a[176:179], a[160:163], a[56:59]\n"
    "s_add_u32 m0, s83, 18432\n"
    "v_mfma_f32_16x16x32_bf16 a[56:59], a[180:183], a[164:167], a[56:59]\n"
    "s_add_u32 s72, s9, 256\n"
    "buffer_load_dwordx4 v247, s[24:27], s72 offen lds\n"
    "v_mfma_f32_16x16x32_bf16 a[60:63], a[176:179], a[168:171], a[60:63]\n"
    "s_add_u32 m0, s83, 19456\n"
    "v_mfma_f32_16x16x32_bf16 a[60:63], a[180:183], a[172:175], a[60:63]\n"
    "s_waitcnt lgkmcnt(4)\n"
    "s_add_u32 s72, s9, 384\n"
    "buffer_load_dwordx4 v247, s[24:27], s72 offen lds\n"
    "v_mfma_f32_16x16x32_bf16 a[64:67], a[184:187], a[144:147], a[64:67]\n"
    "ds_read_b64_tr_b16 a[176:177], v248 offset:1536\n"
    "v_mfma_f32_16x16x32_bf16 a[64:67], a[188:191], a[148:151], a[64:67]\n"
    "ds_read_b64_tr_b16 a[178:179], v248 offset:10752\n"
    "v_mfma_f32_16x16x32_bf16 a[68:71], a[184:187], a[152:155], a[68:71]\n"
    "ds_read_b64_tr_b16 a[180:181], v248 offset:19968\n"
    "v_mfma_f32_16x16x32_bf16 a[68:71], a[188:191], a[156:159], a[68:71]\n"
    "ds_read_b64_tr_b16 a[182:183], v248 offset:29184\n"
    "v_mfma_f32_16x16x32_bf16 a[72:75], a[184:187], a[160:163], a[72:75]\n"
    "s_add_u32 m0, s66, 16384\n"
    "v_mfma_f32_16x16x32_bf16 a[72:75], a[188:191], a[164:167], a[72:75]\n"
    "s_add_u32 s72, s9, 0\n"
    "s_mov_b32 exec_hi, 0\n"
    "buffer_load_dwordx4 v245, s[24:27], s72 offen lds\n"
    "s_mov_b32 exec_hi, -1\n"
    "v_mfma_f32_16x16x32_bf16 a[76:79], a[184:187], a[168:171], a[76:79]\n"
    "s_add_u32 m0, s83, 20992\n"
    "v_mfma_f32_16x16x32_bf16 a[76:79], a[188:191], a[172:175], a[76:79]\n"
    "s_waitcnt lgkmcnt(4)\n"
    "s_add_u32 s72, s9, 9216\n"
    "buffer_load_dwordx4 v247, s[24:27], s72 offen lds\n"
    "v_mfma_f32_16x16x32_bf16 a[80:83], a[224:227], a[144:147], a[80:83]\n"
    "ds_read_b64_tr_b16 a[184:185], v249 offset:1536\n"
    "v_mfma_f32_16x16x32_bf16 a[80:83], a[228:231], a[148:151], a[80:83]\n"
    "ds_read_b64_tr_b16 a[186:187], v249 offset:10752\n"
    "v_mfma_f32_16x16x32_bf16 a[84:87], a[224:227], a[152:155], a[84:87]\n"
    "ds_read_b64_tr_b16 a[188:189], v249 offset:19968\n"
    "v_mfma_f32_16x16x32_bf16 a[84:87], a[228:231], a[156:159], a[84:87]\n"
    "ds_read_b64_tr_b16 a[190:191], v249 offset:29184\n"
    "v_mfma_f32_16x16x32_bf16 a[88:91], a[224:227], a[160:163], a[88:91]\n"
    "s_add_u32 m0, s83, 22016\n"
    "v_mfma_f32_16x16x32_bf16 a[88:91], a[228:231], a[164:167], a[88:91]\n"
    "s_add_u32 s72, s9, 9344\n"
    "buffer_load_dwordx4 v247, s[24:27], s72 offen lds\n"
    "v_mfma_f32_16x16x32_bf16 a[92:95], a[224:227], a[168:171], a[92:95]\n"
    "s_add_u32 m0, s83, 23040\n"
    "v_mfma_f32_16x16x32_bf16 a[92:95], a[228:231], a[172:175], a[92:95]\n"
    "s_waitcnt lgkmcnt(4)\n"
    "s_add_u32 s72, s9, 9472\n"
    "buffer_load_dwordx4 v247, s[24:27], s72 offen lds\n"
    "s_cmp_lg_u32 s32, 0\n"
    "s_cbranch_scc1 L_opub2\n"
    "s_waitcnt vmcnt(12)\n"
    "s_add_u32 s73, s84, 32768\n"
    "v_mov_b32 v176, s74\n"
    "s_mov_b64 exec, 1\n"
    "buffer_store_dword v176, off, s[52:55], s73 sc1\n"
    "s_mov_b64 exec, -1\n"
    "L_opub2:\n"
    "v_mfma_f32_16x16x32_bf16 a[96:99], a[176:179], a[144:147], a[96:99]\n"
    "ds_read_b64_tr_b16 a[224:225], v248 offset:2048\n"
    "v_mfma_f32_16x16x32_bf16 a[96:99], a[180:183], a[148:151], a[96:99]\n"
    "ds_read_b64_tr_b16 a[226:227], v248 offset:11264\n"
    "v_mfma_f32_16x16x32_bf16 a[100:103], a[176:179], a[152:155], a[100:103]\n"
    "ds_read_b64_tr_b16 a[228:229], v248 offset:20480\n"
    "v_mfma_f32_16x16x32_bf16 a[100:103], a[180:183], a[156:159], a[100:103]\n"
    "ds_read_b64_tr_b16 a[230:231], v248 offset:29696\n"
    "v_mfma_f32_16x16x32_bf16 a[104:107], a[176:179], a[160:163], a[104:107]\n"
    "s_add_u32 m0, s83, 24064\n"
    "v_mfma_f32_16x16x32_bf16 a[104:107], a[180:183], a[164:167], a[104:107]\n"
    "s_add_u32 s72, s9, 9600\n"
    "buffer_load_dwordx4 v247, s[24:27], s72 offen lds\n"
    "v_mfma_f32_16x16x32_bf16 a[108:111], a[176:179], a[168:171], a[108:111]\n"
    "v_add_u32 v248, 53248, v248\n"
    "v_add_u32 v249, 53248, v249\n"
    "v_mfma_f32_16x16x32_bf16 a[108:111], a[180:183], a[172:175], a[108:111]\n"
    "s_waitcnt lgkmcnt(4)\n"
    "s_add_u32 m0, s66, 20992\n"
    "v_add_u32 v250, 53248, v250\n"
    "v_mfma_f32_16x16x32_bf16 a[112:115], a[184:187], a[144:147], a[112:115]\n"
    "s_add_u32 s72, s9, 9216\n"
    "s_mov_b32 exec_hi, 0\n"
    "buffer_load_dwordx4 v245, s[24:27], s72 offen lds\n"
    "s_mov_b32 exec_hi, -1\n"
    "v_mfma_f32_16x16x32_bf16 a[112:115], a[188:191], a[148:151], a[112:115]\n"
    "s_waitcnt vmcnt(15) lgkmcnt(0)\n"
    "s_barrier\n"
    "v_mfma_f32_16x16x32_bf16 a[116:119], a[184:187], a[152:155], a[116:119]\n"
    "ds_read_b64_tr_b16 a[176:177], v248 offset:0\n"
    "v_mfma_f32_16x16x32_bf16 a[116:119], a[188:191], a[156:159], a[116:119]\n"
    "ds_read_b64_tr_b16 a[178:179], v248 offset:9216\n"
    "v_mfma_f32_16x16x32_bf16 a[120:123], a[184:187], a[160:163], a[120:123]\n"
    "ds_read_b64_tr_b16 a[180:181], v248 offset:18432\n"
    "ds_read_b128 a[192:195], v250 offset:0\n"
    "v_mfma_f32_16x16x32_bf16 a[120:123], a[188:191], a[164:167], a[120:123]\n"
    "ds_read_b64_tr_b16 a[182:183], v248 offset:27648\n"
    "ds_read_b128 a[196:199], v250 offset:1024\n"
    "v_mfma_f32_16x16x32_bf16 a[124:127], a[184:187], a[168:171], a[124:127]\n"
    "ds_read_b128 a[200:203], v250 offset:2048\n"
    "v_mfma_f32_16x16x32_bf16 a[124:127], a[188:191], a[172:175], a[124:127]\n"
    "ds_read_b128 a[204:207], v250 offset:3072\n"
    "v_mfma_f32_16x16x32_bf16 a[128:131], a[224:227], a[144:147], a[128:131]\n"
    "ds_read_b128 a[208:211], v250 offset:4096\n"
    "ds_read_b128 a[212:215], v250 offset:5120\n"
    "v_mfma_f32_16x16x32_bf16 a[128:131], a[228:231], a[148:151], a[128:131]\n"
    "ds_read_b128 a[216:219], v250 offset:6144\n"
    "ds_read_b128 a[220:223], v250 offset:7168\n"
    "v_mfma_f32_16x16x32_bf16 a[132:135], a[224:227], a[152:155], a[132:135]\n"
    "ds_read_b64_tr_b16 a[184:185], v249 offset:0\n"
    "v_mfma_f32_16x16x32_bf16 a[132:135], a[228:231], a[156:159], a[132:135]\n"
    "ds_read_b64_tr_b16 a[186:187], v249 offset:9216\n"
    "v_mfma_f32_16x16x32_bf16 a[136:139], a[224:227], a[160:163], a[136:139]\n"
    "ds_read_b64_tr_b16 a[188:189], v249 offset:18432\n"
    "v_mfma_f32_16x16x32_bf16 a[136:139], a[228:231], a[164:167], a[136:139]\n"
    "ds_read_b64_tr_b16 a[190:191], v249 offset:27648\n"
    "v_mfma_f32_16x16x32_bf16 a[140:143], a[224:227], a[168:171], a[140:143]\n"
    "v_mfma_f32_16x16x32_bf16 a[140:143], a[228:231], a[172:175], a[140:143]\n"
    "s_waitcnt lgkmcnt(4)\n"
    "v_mfma_f32_16x16x32_bf16 a[0:3], a[176:179], a[192:195], a[0:3]\n"
    "ds_read_b64_tr_b16 a[224:225], v248 offset:512\n"
    "v_mfma_f32_16x16x32_bf16 a[0:3], a[180:183], a[196:199], a[0:3]\n"
    "ds_read_b64_tr_b16 a[226:227], v248 offset:9728\n"
    "v_mfma_f32_16x16x32_bf16 a[4:7], a[176:179], a[200:203], a[4:7]\n"
    "ds_read_b64_tr_b16 a[228:229], v248 offset:18944\n"
    "v_mfma_f32_16x16x32_bf16 a[4:7], a[180:183], a[204:207], a[4:7]\n"
    "ds_read_b64_tr_b16 a[230:231], v248 offset:28160\n"
    "v_mfma_f32_16x16x32_bf16 a[8:11], a[176:179], a[208:211], a[8:11]\n"
    "s_add_u32 m0, s82, 53248\n"
    "v_mfma_f32_16x16x32_bf16 a[8:11], a[180:183], a[212:215], a[8:11]\n"
    "s_add_u32 s72, s8, 2048\n"
    "buffer_load_dwordx4 v246, s[20:23], s72 offen sc1 lds\n"
    "v_mfma_f32_16x16x32_bf16 a[12:15], a[176:179], a[216:219], a[12:15]\n"
    "v_mfma_f32_16x16x32_bf16 a[12:15], a[180:183], a[220:223], a[12:15]\n"
    "s_waitcnt lgkmcnt(4)\n"
    "buffer_load_dwordx4 v246, s[20:23], s72 offen offset:1024 sc1 lds\n"
    "v_mfma_f32_16x16x32_bf16 a[16:19], a[184:187], a[192:195], a[16:19]\n"
    "ds_read_b64_tr_b16 a[176:177], v249 offset:512\n"
    "v_mfma_f32_16x16x32_bf16 a[16:19], a[188:191], a[196:199], a[16:19]\n"
    "ds_read_b64_tr_b16 a[178:179], v249 offset:9728\n"
    "v_mfma_f32_16x16x32_bf16 a[20:23], a[184:187], a[200:203], a[20:23]\n"
    "ds_read_b64_tr_b16 a[180:181], v249 offset:18944\n"
    "v_mfma_f32_16x16x32_bf16 a[20:23], a[188:191], a[204:207], a[20:23]\n"
    "ds_read_b64_tr_b16 a[182:183], v249 offset:28160\n"
    "v_mfma_f32_16x16x32_bf16 a[24:27], a[184:187], a[208:211], a[24:27]\n"
    "s_add_u32 m0, s82, 55296\n"
    "v_mfma_f32_16x16x32_bf16 a[24:27], a[188:191], a[212:215], a[24:27]\n"
    "s_add_u32 s72, s72, s13\n"
    "buffer_load_dwordx4 v246, s[20:23], s72 offen sc1 lds\n"
    "v_mfma_f32_16x16x32_bf16 a[28:31], a[184:187], a[216:219], a[28:31]\n"
    "v_mfma_f32_16x16x32_bf16 a[28:31], a[188:191], a[220:223], a[28:31]\n"
    "s_waitcnt lgkmcnt(4)\n"
    "buffer_load_dwordx4 v246, s[20:23], s72 offen offset:1024 sc1 lds\n"
    "v_mfma_f32_16x16x32_bf16 a[32:35], a[224:227], a[192:195], a[32:35]\n"
    "ds_read_b64_tr_b16 a[184:185], v248 offset:1024\n"
    "v_mfma_f32_16x16x32_bf16 a[32:35], a[228:231], a[196:199], a[32:35]\n"
    "ds_read_b64_tr_b16 a[186:187], v248 offset:10240\n"
    "v_mfma_f32_16x16x32_bf16 a[36:39], a[224:227], a[200:203], a[36:39]\n"
    "ds_read_b64_tr_b16 a[188:189], v248 offset:19456\n"
    "v_mfma_f32_16x16x32_bf16 a[36:39], a[228:231], a[204:207], a[36:39]\n"
    "ds_read_b64_tr_b16 a[190:191], v248 offset:28672\n"
    "v_mfma_f32_16x16x32_bf16 a[40:43], a[224:227], a[208:211], a[40:43]\n"
    "s_add_u32 m0, s83, 69632\n"
    "v_mfma_f32_16x16x32_bf16 a[40:43], a[228:231], a[212:215], a[40:43]\n"
    "s_add_u32 s72, s9, 73728\n"
    "buffer_load_dwordx4 v247, s[24:27], s72 offen lds\n"
    "v_mfma_f32_16x16x32_bf16 a[44:47], a[224:227], a[216:219], a[44:47]\n"
    "s_add_u32 m0, s83, 70656\n"
    "v_mfma_f32_16x16x32_bf16 a[44:47], a[228:231], a[220:223], a[44:47]\n"
    "s_waitcnt lgkmcnt(4)\n"
    "s_add_u32 s72, s9, 73856\n"
    "buffer_load_dwordx4 v247, s[24:27], s72 offen lds\n"
    "v_mfma_f32_16x16x32_bf16 a[48:51], a[176:179], a[192:195], a[48:51]\n"
    "ds_read_b64_tr_b16 a[224:225], v249 offset:1024\n"
    "v_mfma_f32_16x16x32_bf16 a[48:51], a[180:183], a[196:199], a[48:51]\n"
    "ds_read_b64_tr_b16 a[226:227], v249 offset:10240\n"
    "v_mfma_f32_16x16x32_bf16 a[52:55], a[176:179], a[200:203], a[52:55]\n"
    "ds_read_b64_tr_b16 a[228:229], v249 offset:19456\n"
    "v_mfma_f32_16x16x32_bf16 a[52:55], a[180:183], a[204:207], a[52:55]\n"
    "ds_read_b64_tr_b16 a[230:231], v249 offset:28672\n"
    "v_mfma_f32_16x16x32_bf16 a[56:59], a[176:179], a[208:211], a[56:59]\n"
    "s_add_u32 m0, s83, 71680\n"
    "v_mfma_f32_16x16x32_bf16 a[56:59], a[180:183], a[212:215], a[56:59]\n"
    "s_add_u32 s72, s9, 73984\n"
    "buffer_load_dwordx4 v247, s[24:27], s72 offen lds\n"
    "v_mfma_f32_16x16x32_bf16 a[60:63], a[176:179], a[216:219], a[60:63]\n"
    "s_add_u32 m0, s83, 72704\n"
    "v_mfma_f32_16x16x32_bf16 a[60:63], a[180:183], a[220:223], a[60:63]\n"
    "s_waitcnt lgkmcnt(4)\n"
    "s_add_u32 s72, s9, 74112\n"
    "buffer_load_dwordx4 v247, s[24:27], s72 offen lds\n"
    "v_mfma_f32_16x16x32_bf16 a[64:67], a[184:187], a[192:195], a[64:67]\n"
    "ds_read_b64_tr_b16 a[176:177], v248 offset:1536\n"
    "v_mfma_f32_16x16x32_bf16 a[64:67], a[188:191], a[196:199], a[64:67]\n"
    "ds_read_b64_tr_b16 a[178:179], v248 offset:10752\n"
    "v_mfma_f32_16x16x32_bf16 a[68:71], a[184:187], a[200:203], a[68:71]\n"
    "ds_read_b64_tr_b16 a[180:181], v248 offset:19968\n"
    "v_mfma_f32_16x16x32_bf16 a[68:71], a[188:191], a[204:207], a[68:71]\n"
    "ds_read_b64_tr_b16 a[182:183], v248 offset:29184\n"
    "v_mfma_f32_16x16x32_bf16 a[72:75], a[184:187], a[208:211], a[72:75]\n"
    "s_add_u32 m0, s66, 69632\n"
    "v_mfma_f32_16x16x32_bf16 a[72:75], a[188:191], a[212:215], a[72:75]\n"
    "s_add_u32 s72, s9, 73728\n"
    "s_mov_b32 exec_hi, 0\n"
    "buffer_load_dwordx4 v245, s[24:27], s72 offen lds\n"
    "s_mov_b32 exec_hi, -1\n"
    "v_mfma_f32_16x16x32_bf16 a[76:79], a[184:187], a[216:219], a[76:79]\n"
    "s_add_u32 m0, s83, 74240\n"
    "v_mfma_f32_16x16x32_bf16 a[76:79], a[188:191], a[220:223], a[76:79]\n"
    "s_waitcnt lgkmcnt(4)\n"
    "s_add_u32 s72, s9, 82944\n"
    "buffer_load_dwordx4 v247, s[24:27], s72 offen lds\n"
    "v_mfma_f32_16x16x32_bf16 a[80:83], a[224:227], a[192:195], a[80:83]\n"
    "ds_read_b64_tr_b16 a[184:185], v249 offset:1536\n"
    "v_mfma_f32_16x16x32_bf16 a[80:83], a[228:231], a[196:199], a[80:83]\n"
    "ds_read_b64_tr_b16 a[186:187], v249 offset:10752\n"
    "v_mfma_f32_16x16x32_bf16 a[84:87], a[224:227], a[200:203], a[84:87]\n"
    "ds_read_b64_tr_b16 a[188:189], v249 offset:19968\n"
    "v_mfma_f32_16x16x32_bf16 a[84:87], a[228:231], a[204:207], a[84:87]\n"
    "ds_read_b64_tr_b16 a[190:191], v249 offset:29184\n"
    "v_mfma_f32_16x16x32_bf16 a[88:91], a[224:227], a[208:211], a[88:91]\n"
    "s_add_u32 m0, s83, 75264\n"
    "v_mfma_f32_16x16x32_bf16 a[88:91], a[228:231], a[212:215], a[88:91]\n"
    "s_add_u32 s72, s9, 83072\n"
    "buffer_load_dwordx4 v247, s[24:27], s72 offen lds\n"
    "v_mfma_f32_16x16x32_bf16 a[92:95], a[224:227], a[216:219], a[92:95]\n"
    "s_add_u32 m0, s83, 76288\n"
    "v_mfma_f32_16x16x32_bf16 a[92:95], a[228:231], a[220:223], a[92:95]\n"
    "s_waitcnt lgkmcnt(4)\n"
    "s_add_u32 s72, s9, 83200\n"
    "buffer_load_dwordx4 v247, s[24:27], s72 offen lds\n"
    "v_mfma_f32_16x16x32_bf16 a[96:99], a[176:179], a[192:195], a[96:99]\n"
    "ds_read_b64_tr_b16 a[224:225], v248 offset:2048\n"
    "v_mfma_f32_16x16x32_bf16 a[96:99], a[180:183], a[196:199], a[96:99]\n"
    "ds_read_b64_tr_b16 a[226:227], v248 offset:11264\n"
    "v_mfma_f32_16x16x32_bf16 a[100:103], a[176:179], a[200:203], a[100:103]\n"
    "ds_read_b64_tr_b16 a[228:229], v248 offset:20480\n"
    "v_mfma_f32_16x16x32_bf16 a[100:103], a[180:183], a[204:207], a[100:103]\n"
    "ds_read_b64_tr_b16 a[230:231], v248 offset:29696\n"
    "v_mfma_f32_16x16x32_bf16 a[104:107], a[176:179], a[208:211], a[104:107]\n"
    "s_add_u32 m0, s83, 77312\n"
    "v_mfma_f32_16x16x32_bf16 a[104:107], a[180:183], a[212:215], a[104:107]\n"
    "s_add_u32 s72, s9, 83328\n"
    "buffer_load_dwordx4 v247, s[24:27], s72 offen lds\n"
    "v_mfma_f32_16x16x32_bf16 a[108:111], a[176:179], a[216:219], a[108:111]\n"
    "v_add_u32 v248, 4294860800, v248\n"
    "v_add_u32 v249, 4294860800, v249\n"
    "v_mfma_f32_16x16x32_bf16 a[108:111], a[180:183], a[220:223], a[108:111]\n"
    "s_waitcnt lgkmcnt(4)\n"
    "s_add_u32 m0, s66, 74240\n"
    "v_add_u32 v250, 4294860800, v250\n"
    "v_mfma_f32_16x16x32_bf16 a[112:115], a[184:187], a[192:195], a[112:115]\n"
    "s_add_u32 s72, s9, 82944\n"
    "s_mov_b32 exec_hi, 0\n"
    "buffer_load_dwordx4 v245, s[24:27], s72 offen lds\n"
    "s_mov_b32 exec_hi, -1\n"
    "v_mfma_f32_16x16x32_bf16 a[112:115], a[188:191], a[196:199], a[112:115]\n"
    "s_waitcnt vmcnt(14) lgkmcnt(0)\n"
    "s_barrier\n"
    "v_mfma_f32_16x16x32_bf16 a[116:119], a[184:187], a[200:203], a[116:119]\n"
    "ds_read_b64_tr_b16 a[176:177], v248 offset:0\n"
    "v_mfma_f32_16x16x32_bf16 a[116:119], a[188:191], a[204:207], a[116:119]\n"
    "ds_read_b64_tr_b16 a[178:179], v248 offset:9216\n"
    "v_mfma_f32_16x16x32_bf16 a[120:123], a[184:187], a[208:211], a[120:123]\n"
    "s_sub_u32 s4, 4, s10\n"
    "ds_read_b64_tr_b16 a[180:181], v248 offset:18432\n"
    "ds_read_b128 a[144:147], v250 offset:0\n"
    "v_mfma_f32_16x16x32_bf16 a[120:123], a[188:191], a[212:215], a[120:123]\n"
    "s_xor_b32 s4, s4, s11\n"
    "ds_read_b64_tr_b16 a[182:183], v248 offset:27648\n"
    "ds_read_b128 a[148:151], v250 offset:1024\n"
    "v_mfma_f32_16x16x32_bf16 a[124:127], a[184:187], a[216:219], a[124:127]\n"
    "s_add_u32 s4, s4, s12\n"
    "ds_read_b128 a[152:155], v250 offset:2048\n"
    "v_mfma_f32_16x16x32_bf16 a[124:127], a[188:191], a[220:223], a[124:127]\n"
    "s_lshl_b32 s8, s4, 12\n"
    "ds_read_b128 a[156:159], v250 offset:3072\n"
    "v_mfma_f32_16x16x32_bf16 a[128:131], a[224:227], a[192:195], a[128:131]\n"
    "s_add_u32 s8, s8, s80\n"
    "ds_read_b128 a[160:163], v250 offset:4096\n"
    "ds_read_b128 a[164:167], v250 offset:5120\n"
    "v_mfma_f32_16x16x32_bf16 a[128:131], a[228:231], a[196:199], a[128:131]\n"
    "s_mul_i32 s9, s4, 147456\n"
    "ds_read_b128 a[168:171], v250 offset:6144\n"
    "ds_read_b128 a[172:175], v250 offset:7168\n"
    "v_mfma_f32_16x16x32_bf16 a[132:135], a[224:227], a[200:203], a[132:135]\n"
    "s_add_u32 s9, s9, s81\n"
    "ds_read_b64_tr_b16 a[184:185], v249 offset:0\n"
    "v_mfma_f32_16x16x32_bf16 a[132:135], a[228:231], a[204:207], a[132:135]\n"
    "ds_read_b64_tr_b16 a[186:187], v249 offset:9216\n"
    "v_mfma_f32_16x16x32_bf16 a[136:139], a[224:227], a[208:211], a[136:139]\n"
    "ds_read_b64_tr_b16 a[188:189], v249 offset:18432\n"
    "v_mfma_f32_16x16x32_bf16 a[136:139], a[228:231], a[212:215], a[136:139]\n"
    "ds_read_b64_tr_b16 a[190:191], v249 offset:27648\n"
    "v_mfma_f32_16x16x32_bf16 a[140:143], a[224:227], a[216:219], a[140:143]\n"
    "v_mfma_f32_16x16x32_bf16 a[140:143], a[228:231], a[220:223], a[140:143]\n"
    "L_common:\n"
    "s_waitcnt lgkmcnt(4)\n"
    "v_mfma_f32_16x16x32_bf16 a[0:3], a[176:179], a[144:147], a[0:3]\n"
    "ds_read_b64_tr_b16 a[224:225], v248 offset:512\n"
    "v_lshrrev_b32 v178, 1, v253\n"
    "v_mfma_f32_16x16x32_bf16 a[0:3], a[180:183], a[148:151], a[0:3]\n"
    "ds_read_b64_tr_b16 a[226:227], v248 offset:9728\n"
    "v_and_b32 v178, 3, v178\n"
    "v_mfma_f32_16x16x32_bf16 a[4:7], a[176:179], a[152:155], a[4:7]\n"
    "s_xor_b32 s78, s69, 4\n"
    "ds_read_b64_tr_b16 a[228:229], v248 offset:18944\n"
    "v_xor_b32 v178, s78, v178\n"
    "v_mfma_f32_16x16x32_bf16 a[4:7], a[180:183], a[156:159], a[4:7]\n"
    "ds_read_b64_tr_b16 a[230:231], v248 offset:28160\n"
    "v_lshlrev_b32 v178, 2, v178\n"
    "v_mfma_f32_16x16x32_bf16 a[8:11], a[176:179], a[160:163], a[8:11]\n"
    "s_add_u32 m0, s82, 106496\n"
    "v_and_b32 v179, 1, v253\n"
    "v_add_u32 v178, v178, v179\n"
    "v_mfma_f32_16x16x32_bf16 a[8:11], a[180:183], a[164:167], a[8:11]\n"
    "s_add_u32 s72, s8, 0\n"
    "buffer_load_dwordx4 v246, s[20:23], s72 offen sc1 lds\n"
    "v_mfma_f32_16x16x32_bf16 a[12:15], a[176:179], a[168:171], a[12:15]\n"
    "s_lshr_b32 s78, s69, 1\n"
    "s_and_b32 s78, s78, 2\n"
    "s_lshl_b32 s78, s78, 7\n"
    "v_mfma_f32_16x16x32_bf16 a[12:15], a[180:183], a[172:175], a[12:15]\n"
    "s_waitcnt lgkmcnt(4)\n"
    "buffer_load_dwordx4 v246, s[20:23], s72 offen offset:1024 sc1 lds\n"
    "v_mfma_f32_16x16x32_bf16 a[16:19], a[184:187], a[144:147], a[16:19]\n"
    "s_add_u32 s78, s78, 32768\n"
    "ds_read_b64_tr_b16 a[176:177], v249 offset:512\n"
    "v_lshlrev_b32 v178, 7, v178\n"
    "v_mfma_f32_16x16x32_bf16 a[16:19], a[188:191], a[148:151], a[16:19]\n"
    "ds_read_b64_tr_b16 a[178:179], v249 offset:9728\n"
    "v_mfma_f32_16x16x32_bf16 a[20:23], a[184:187], a[152:155], a[20:23]\n"
    "ds_read_b64_tr_b16 a[180:181], v249 offset:18944\n"
    "v_mfma_f32_16x16x32_bf16 a[20:23], a[188:191], a[156:159], a[20:23]\n"
    "ds_read_b64_tr_b16 a[182:183], v249 offset:28160\n"
    "v_mfma_f32_16x16x32_bf16 a[24:27], a[184:187], a[160:163], a[24:27]\n"
    "s_add_u32 m0, s82, 108544\n"
    "v_mfma_f32_16x16x32_bf16 a[24:27], a[188:191], a[164:167], a[24:27]\n"
    "s_add_u32 s72, s72, s13\n"
    "buffer_load_dwordx4 v246, s[20:23], s72 offen sc1 lds\n"
    "v_mfma_f32_16x16x32_bf16 a[28:31], a[184:187], a[168:171], a[28:31]\n"
    "v_mfma_f32_16x16x32_bf16 a[28:31], a[188:191], a[172:175], a[28:31]\n"
    "s_waitcnt lgkmcnt(4)\n"
    "buffer_load_dwordx4 v246, s[20:23], s72 offen offset:1024 sc1 lds\n"
    "v_mfma_f32_16x16x32_bf16 a[32:35], a[224:227], a[144:147], a[32:35]\n"
    "ds_read_b64_tr_b16 a[184:185], v248 offset:1024\n"
    "v_mfma_f32_16x16x32_bf16 a[32:35], a[228:231], a[148:151], a[32:35]\n"
    "ds_read_b64_tr_b16 a[186:187], v248 offset:10240\n"
    "v_mfma_f32_16x16x32_bf16 a[36:39], a[224:227], a[152:155], a[36:39]\n"
    "ds_read_b64_tr_b16 a[188:189], v248 offset:19456\n"
    "v_mfma_f32_16x16x32_bf16 a[36:39], a[228:231], a[156:159], a[36:39]\n"
    "ds_read_b64_tr_b16 a[190:191], v248 offset:28672\n"
    "v_mfma_f32_16x16x32_bf16 a[40:43], a[224:227], a[160:163], a[40:43]\n"
    "s_add_u32 m0, s83, 122880\n"
    "v_mfma_f32_16x16x32_bf16 a[40:43], a[228:231], a[164:167], a[40:43]\n"
    "s_add_u32 s72, s9, 0\n"
    "buffer_load_dwordx4 v247, s[24:27], s72 offen lds\n"
    "v_mfma_f32_16x16x32_bf16 a[44:47], a[224:227], a[168:171], a[44:47]\n"
    "s_add_u32 m0, s83, 123904\n"
    "v_mfma_f32_16x16x32_bf16 a[44:47], a[228:231], a[172:175], a[44:47]\n"
    "s_waitcnt lgkmcnt(4)\n"
    "s_add_u32 s72, s9, 128\n"
    "buffer_load_dwordx4 v247, s[24:27], s72 offen lds\n"
    "v_mfma_f32_16x16x32_bf16 a[48:51], a[176:179], a[144:147], a[48:51]\n"
    "ds_read_b64_tr_b16 a[224:225], v249 offset:1024\n"
    "v_mfma_f32_16x16x32_bf16 a[48:51], a[180:183], a[148:151], a[48:51]\n"
    "ds_read_b64_tr_b16 a[226:227], v249 offset:10240\n"
    "v_mfma_f32_16x16x32_bf16 a[52:55], a[176:179], a[152:155], a[52:55]\n"
    "ds_read_b64_tr_b16 a[228:229], v249 offset:19456\n"
    "v_mfma_f32_16x16x32_bf16 a[52:55], a[180:183], a[156:159], a[52:55]\n"
    "ds_read_b64_tr_b16 a[230:231], v249 offset:28672\n"
    "v_mfma_f32_16x16x32_bf16 a[56:59], a[176:179], a[160:163], a[56:59]\n"
    "s_add_u32 m0, s83, 124928\n"
    "v_mfma_f32_16x16x32_bf16 a[56:59], a[180:183], a[164:167], a[56:59]\n"
    "s_add_u32 s72, s9, 256\n"
    "buffer_load_dwordx4 v247, s[24:27], s72 offen lds\n"
    "v_mfma_f32_16x16x32_bf16 a[60:63], a[176:179], a[168:171], a[60:63]\n"
    "s_add_u32 m0, s83, 125952\n"
    "v_mfma_f32_16x16x32_bf16 a[60:63], a[180:183], a[172:175], a[60:63]\n"
    "s_waitcnt lgkmcnt(4)\n"
    "s_add_u32 s72, s9, 384\n"
    "buffer_load_dwordx4 v247, s[24:27], s72 offen lds\n"
    "v_mfma_f32_16x16x32_bf16 a[64:67], a[184:187], a[144:147], a[64:67]\n"
    "ds_read_b64_tr_b16 a[176:177], v248 offset:1536\n"
    "v_mfma_f32_16x16x32_bf16 a[64:67], a[188:191], a[148:151], a[64:67]\n"
    "ds_read_b64_tr_b16 a[178:179], v248 offset:10752\n"
    "v_mfma_f32_16x16x32_bf16 a[68:71], a[184:187], a[152:155], a[68:71]\n"
    "ds_read_b64_tr_b16 a[180:181], v248 offset:19968\n"
    "v_mfma_f32_16x16x32_bf16 a[68:71], a[188:191], a[156:159], a[68:71]\n"
    "ds_read_b64_tr_b16 a[182:183], v248 offset:29184\n"
    "v_mfma_f32_16x16x32_bf16 a[72:75], a[184:187], a[160:163], a[72:75]\n"
    "s_add_u32 m0, s66, 122880\n"
    "v_mfma_f32_16x16x32_bf16 a[72:75], a[188:191], a[164:167], a[72:75]\n"
    "s_add_u32 s72, s9, 0\n"
    "s_mov_b32 exec_hi, 0\n"
    "buffer_load_dwordx4 v245, s[24:27], s72 offen lds\n"
    "s_mov_b32 exec_hi, -1\n"
    "v_mfma_f32_16x16x32_bf16 a[76:79], a[184:187], a[168:171], a[76:79]\n"
    "s_add_u32 m0, s83, 127488\n"
    "v_mfma_f32_16x16x32_bf16 a[76:79], a[188:191], a[172:175], a[76:79]\n"
    "s_waitcnt lgkmcnt(4)\n"
    "s_add_u32 s72, s9, 9216\n"
    "buffer_load_dwordx4 v247, s[24:27], s72 offen lds\n"
    "v_mfma_f32_16x16x32_bf16 a[80:83], a[224:227], a[144:147], a[80:83]\n"
    "ds_read_b64_tr_b16 a[184:185], v249 offset:1536\n"
    "v_mfma_f32_16x16x32_bf16 a[80:83], a[228:231], a[148:151], a[80:83]\n"
    "ds_read_b64_tr_b16 a[186:187], v249 offset:10752\n"
    "v_mfma_f32_16x16x32_bf16 a[84:87], a[224:227], a[152:155], a[84:87]\n"
    "ds_read_b64_tr_b16 a[188:189], v249 offset:19968\n"
    "v_mfma_f32_16x16x32_bf16 a[84:87], a[228:231], a[156:159], a[84:87]\n"
    "ds_read_b64_tr_b16 a[190:191], v249 offset:29184\n"
    "v_mfma_f32_16x16x32_bf16 a[88:91], a[224:227], a[160:163], a[88:91]\n"
    "s_add_u32 m0, s83, 128512\n"
    "v_mfma_f32_16x16x32_bf16 a[88:91], a[228:231], a[164:167], a[88:91]\n"
    "s_add_u32 s72, s9, 9344\n"
    "buffer_load_dwordx4 v247, s[24:27], s72 offen lds\n"
    "v_mfma_f32_16x16x32_bf16 a[92:95], a[224:227], a[168:171], a[92:95]\n"
    "s_add_u32 m0, s83, 129536\n"
    "v_mfma_f32_16x16x32_bf16 a[92:95], a[228:231], a[172:175], a[92:95]\n"
    "s_waitcnt lgkmcnt(4)\n"
    "s_add_u32 s72, s9, 9472\n"
    "buffer_load_dwordx4 v247, s[24:27], s72 offen lds\n"
    "v_mfma_f32_16x16x32_bf16 a[96:99], a[176:179], a[144:147], a[96:99]\n"
    "ds_read_b64_tr_b16 a[224:225], v248 offset:2048\n"
    "v_mfma_f32_16x16x32_bf16 a[96:99], a[180:183], a[148:151], a[96:99]\n"
    "ds_read_b64_tr_b16 a[226:227], v248 offset:11264\n"
    "v_mfma_f32_16x16x32_bf16 a[100:103], a[176:179], a[152:155], a[100:103]\n"
    "ds_read_b64_tr_b16 a[228:229], v248 offset:20480\n"
    "v_mfma_f32_16x16x32_bf16 a[100:103], a[180:183], a[156:159], a[100:103]\n"
    "ds_read_b64_tr_b16 a[230:231], v248 offset:29696\n"
    "v_mfma_f32_16x16x32_bf16 a[104:107], a[176:179], a[160:163], a[104:107]\n"
    "s_add_u32 m0, s83, 130560\n"
    "v_mfma_f32_16x16x32_bf16 a[104:107], a[180:183], a[164:167], a[104:107]\n"
    "s_add_u32 s72, s9, 9600\n"
    "buffer_load_dwordx4 v247, s[24:27], s72 offen lds\n"
    "v_mfma_f32_16x16x32_bf16 a[108:111], a[176:179], a[168:171], a[108:111]\n"
    "v_add_u32 v248, 53248, v248\n"
    "v_add_u32 v249, 53248, v249\n"
    "v_mfma_f32_16x16x32_bf16 a[108:111], a[180:183], a[172:175], a[108:111]\n"
    "s_waitcnt lgkmcnt(4)\n"
    "s_add_u32 m0, s66, 127488\n"
    "v_add_u32 v250, 53248, v250\n"
    "v_mfma_f32_16x16x32_bf16 a[112:115], a[184:187], a[144:147], a[112:115]\n"
    "s_add_u32 s72, s9, 9216\n"
    "s_mov_b32 exec_hi, 0\n"
    "buffer_load_dwordx4 v245, s[24:27], s72 offen lds\n"
    "s_mov_b32 exec_hi, -1\n"
    "v_mfma_f32_16x16x32_bf16 a[112:115], a[188:191], a[148:151], a[112:115]\n"
    "s_waitcnt vmcnt(14) lgkmcnt(0)\n"
    "s_barrier\n"
    "v_mfma_f32_16x16x32_bf16 a[116:119], a[184:187], a[152:155], a[116:119]\n"
    "ds_read_b64_tr_b16 a[176:177], v248 offset:0\n"
    "v_mfma_f32_16x16x32_bf16 a[116:119], a[188:191], a[156:159], a[116:119]\n"
    "ds_read_b64_tr_b16 a[178:179], v248 offset:9216\n"
    "v_mfma_f32_16x16x32_bf16 a[120:123], a[184:187], a[160:163], a[120:123]\n"
    "ds_read_b64_tr_b16 a[180:181], v248 offset:18432\n"
    "ds_read_b128 a[192:195], v250 offset:0\n"
    "v_mfma_f32_16x16x32_bf16 a[120:123], a[188:191], a[164:167], a[120:123]\n"
    "ds_read_b64_tr_b16 a[182:183], v248 offset:27648\n"
    "ds_read_b128 a[196:199], v250 offset:1024\n"
    "v_mfma_f32_16x16x32_bf16 a[124:127], a[184:187], a[168:171], a[124:127]\n"
    "ds_read_b128 a[200:203], v250 offset:2048\n"
    "v_mfma_f32_16x16x32_bf16 a[124:127], a[188:191], a[172:175], a[124:127]\n"
    "ds_read_b128 a[204:207], v250 offset:3072\n"
    "v_mfma_f32_16x16x32_bf16 a[128:131], a[224:227], a[144:147], a[128:131]\n"
    "ds_read_b128 a[208:211], v250 offset:4096\n"
    "ds_read_b128 a[212:215], v250 offset:5120\n"
    "v_mfma_f32_16x16x32_bf16 a[128:131], a[228:231], a[148:151], a[128:131]\n"
    "ds_read_b128 a[216:219], v250 offset:6144\n"
    "ds_read_b128 a[220:223], v250 offset:7168\n"
    "v_mfma_f32_16x16x32_bf16 a[132:135], a[224:227], a[152:155], a[132:135]\n"
    "ds_read_b64_tr_b16 a[184:185], v249 offset:0\n"
    "v_mfma_f32_16x16x32_bf16 a[132:135], a[228:231], a[156:159], a[132:135]\n"
    "ds_read_b64_tr_b16 a[186:187], v249 offset:9216\n"
    "v_mfma_f32_16x16x32_bf16 a[136:139], a[224:227], a[160:163], a[136:139]\n"
    "ds_read_b64_tr_b16 a[188:189], v249 offset:18432\n"
    "v_mfma_f32_16x16x32_bf16 a[136:139], a[228:231], a[164:167], a[136:139]\n"
    "ds_read_b64_tr_b16 a[190:191], v249 offset:27648\n"
    "v_mfma_f32_16x16x32_bf16 a[140:143], a[224:227], a[168:171], a[140:143]\n"
    "v_mfma_f32_16x16x32_bf16 a[140:143], a[228:231], a[172:175], a[140:143]\n"
    "s_waitcnt lgkmcnt(4)\n"
    "v_mfma_f32_16x16x32_bf16 a[0:3], a[176:179], a[192:195], a[0:3]\n"
    "ds_read_b64_tr_b16 a[224:225], v248 offset:512\n"
    "v_mfma_f32_16x16x32_bf16 a[0:3], a[180:183], a[196:199], a[0:3]\n"
    "ds_read_b64_tr_b16 a[226:227], v248 offset:9728\n"
    "v_mfma_f32_16x16x32_bf16 a[4:7], a[176:179], a[200:203], a[4:7]\n"
    "ds_read_b64_tr_b16 a[228:229], v248 offset:18944\n"
    "v_mfma_f32_16x16x32_bf16 a[4:7], a[180:183], a[204:207], a[4:7]\n"
    "ds_read_b64_tr_b16 a[230:231], v248 offset:28160\n"
    "v_mfma_f32_16x16x32_bf16 a[8:11], a[176:179], a[208:211], a[8:11]\n"
    "s_add_u32 m0, s82, 0\n"
    "v_mfma_f32_16x16x32_bf16 a[8:11], a[180:183], a[212:215], a[8:11]\n"
    "s_add_u32 s72, s8, 2048\n"
    "buffer_load_dwordx4 v246, s[20:23], s72 offen sc1 lds\n"
    "v_mfma_f32_16x16x32_bf16 a[12:15], a[176:179], a[216:219], a[12:15]\n"
    "v_mfma_f32_16x16x32_bf16 a[12:15], a[180:183], a[220:223], a[12:15]\n"
    "s_waitcnt lgkmcnt(4)\n"
    "buffer_load_dwordx4 v246, s[20:23], s72 offen offset:1024 sc1 lds\n"
    "v_mfma_f32_16x16x32_bf16 a[16:19], a[184:187], a[192:195], a[16:19]\n"
    "ds_read_b64_tr_b16 a[176:177], v249 offset:512\n"
    "v_mfma_f32_16x16x32_bf16 a[16:19], a[188:191], a[196:199], a[16:19]\n"
    "ds_read_b64_tr_b16 a[178:179], v249 offset:9728\n"
    "v_mfma_f32_16x16x32_bf16 a[20:23], a[184:187], a[200:203], a[20:23]\n"
    "ds_read_b64_tr_b16 a[180:181], v249 offset:18944\n"
    "v_mfma_f32_16x16x32_bf16 a[20:23], a[188:191], a[204:207], a[20:23]\n"
    "ds_read_b64_tr_b16 a[182:183], v249 offset:28160\n"
    "v_mfma_f32_16x16x32_bf16 a[24:27], a[184:187], a[208:211], a[24:27]\n"
    "s_add_u32 m0, s82, 2048\n"
    "buffer_load_dword v179, v178, s[52:55], s78 offen sc1\n"
    "v_mfma_f32_16x16x32_bf16 a[24:27], a[188:191], a[212:215], a[24:27]\n"
    "s_add_u32 s72, s72, s13\n"
    "buffer_load_dwordx4 v246, s[20:23], s72 offen sc1 lds\n"
    "v_mfma_f32_16x16x32_bf16 a[28:31], a[184:187], a[216:219], a[28:31]\n"
    "v_mfma_f32_16x16x32_bf16 a[28:31], a[188:191], a[220:223], a[28:31]\n"
    "s_waitcnt lgkmcnt(4)\n"
    "buffer_load_dwordx4 v246, s[20:23], s72 offen offset:1024 sc1 lds\n"
    "v_mfma_f32_16x16x32_bf16 a[32:35], a[224:227], a[192:195], a[32:35]\n"
    "ds_read_b64_tr_b16 a[184:185], v248 offset:1024\n"
    "v_mfma_f32_16x16x32_bf16 a[32:35], a[228:231], a[196:199], a[32:35]\n"
    "ds_read_b64_tr_b16 a[186:187], v248 offset:10240\n"
    "v_mfma_f32_16x16x32_bf16 a[36:39], a[224:227], a[200:203], a[36:39]\n"
    "ds_read_b64_tr_b16 a[188:189], v248 offset:19456\n"
    "v_mfma_f32_16x16x32_bf16 a[36:39], a[228:231], a[204:207], a[36:39]\n"
    "ds_read_b64_tr_b16 a[190:191], v248 offset:28672\n"
    "v_mfma_f32_16x16x32_bf16 a[40:43], a[224:227], a[208:211], a[40:43]\n"
    "s_add_u32 m0, s83, 16384\n"
    "v_mfma_f32_16x16x32_bf16 a[40:43], a[228:231], a[212:215], a[40:43]\n"
    "s_add_u32 s72, s9, 73728\n"
    "buffer_load_dwordx4 v247, s[24:27], s72 offen lds\n"
    "v_mfma_f32_16x16x32_bf16 a[44:47], a[224:227], a[216:219], a[44:47]\n"
    "s_add_u32 m0, s83, 17408\n"
    "v_mfma_f32_16x16x32_bf16 a[44:47], a[228:231], a[220:223], a[44:47]\n"
    "s_waitcnt lgkmcnt(4)\n"
    "s_add_u32 s72, s9, 73856\n"
    "buffer_load_dwordx4 v247, s[24:27], s72 offen lds\n"
    "v_mfma_f32_16x16x32_bf16 a[48:51], a[176:179], a[192:195], a[48:51]\n"
    "ds_read_b64_tr_b16 a[224:225], v249 offset:1024\n"
    "v_mfma_f32_16x16x32_bf16 a[48:51], a[180:183], a[196:199], a[48:51]\n"
    "ds_read_b64_tr_b16 a[226:227], v249 offset:10240\n"
    "v_mfma_f32_16x16x32_bf16 a[52:55], a[176:179], a[200:203], a[52:55]\n"
    "ds_read_b64_tr_b16 a[228:229], v249 offset:19456\n"
    "v_mfma_f32_16x16x32_bf16 a[52:55], a[180:183], a[204:207], a[52:55]\n"
    "ds_read_b64_tr_b16 a[230:231], v249 offset:28672\n"
    "v_mfma_f32_16x16x32_bf16 a[56:59], a[176:179], a[208:211], a[56:59]\n"
    "s_add_u32 m0, s83, 18432\n"
    "v_mfma_f32_16x16x32_bf16 a[56:59], a[180:183], a[212:215], a[56:59]\n"
    "s_add_u32 s72, s9, 73984\n"
    "buffer_load_dwordx4 v247, s[24:27], s72 offen lds\n"
    "v_mfma_f32_16x16x32_bf16 a[60:63], a[176:179], a[216:219], a[60:63]\n"
    "s_add_u32 m0, s83, 19456\n"
    "v_mfma_f32_16x16x32_bf16 a[60:63], a[180:183], a[220:223], a[60:63]\n"
    "s_waitcnt lgkmcnt(4)\n"
    "s_add_u32 s72, s9, 74112\n"
    "buffer_load_dwordx4 v247, s[24:27], s72 offen lds\n"
    "v_mfma_f32_16x16x32_bf16 a[64:67], a[184:187], a[192:195], a[64:67]\n"
    "ds_read_b64_tr_b16 a[176:177], v248 offset:1536\n"
    "v_mfma_f32_16x16x32_bf16 a[64:67], a[188:191], a[196:199], a[64:67]\n"
    "ds_read_b64_tr_b16 a[178:179], v248 offset:10752\n"
    "v_mfma_f32_16x16x32_bf16 a[68:71], a[184:187], a[200:203], a[68:71]\n"
    "ds_read_b64_tr_b16 a[180:181], v248 offset:19968\n"
    "v_mfma_f32_16x16x32_bf16 a[68:71], a[188:191], a[204:207], a[68:71]\n"
    "ds_read_b64_tr_b16 a[182:183], v248 offset:29184\n"
    "v_mfma_f32_16x16x32_bf16 a[72:75], a[184:187], a[208:211], a[72:75]\n"
    "s_add_u32 m0, s66, 16384\n"
    "v_mfma_f32_16x16x32_bf16 a[72:75], a[188:191], a[212:215], a[72:75]\n"
    "s_add_u32 s72, s9, 73728\n"
    "s_mov_b32 exec_hi, 0\n"
    "buffer_load_dwordx4 v245, s[24:27], s72 offen lds\n"
    "s_mov_b32 exec_hi, -1\n"
    "v_mfma_f32_16x16x32_bf16 a[76:79], a[184:187], a[216:219], a[76:79]\n"
    "s_add_u32 m0, s83, 20992\n"
    "v_mfma_f32_16x16x32_bf16 a[76:79], a[188:191], a[220:223], a[76:79]\n"
    "s_waitcnt lgkmcnt(4)\n"
    "s_add_u32 s72, s9, 82944\n"
    "buffer_load_dwordx4 v247, s[24:27], s72 offen lds\n"
    "v_mfma_f32_16x16x32_bf16 a[80:83], a[224:227], a[192:195], a[80:83]\n"
    "ds_read_b64_tr_b16 a[184:185], v249 offset:1536\n"
    "v_mfma_f32_16x16x32_bf16 a[80:83], a[228:231], a[196:199], a[80:83]\n"
    "ds_read_b64_tr_b16 a[186:187], v249 offset:10752\n"
    "v_mfma_f32_16x16x32_bf16 a[84:87], a[224:227], a[200:203], a[84:87]\n"
    "ds_read_b64_tr_b16 a[188:189], v249 offset:19968\n"
    "v_mfma_f32_16x16x32_bf16 a[84:87], a[228:231], a[204:207], a[84:87]\n"
    "ds_read_b64_tr_b16 a[190:191], v249 offset:29184\n"
    "v_mfma_f32_16x16x32_bf16 a[88:91], a[224:227], a[208:211], a[88:91]\n"
    "s_add_u32 m0, s83, 22016\n"
    "v_mfma_f32_16x16x32_bf16 a[88:91], a[228:231], a[212:215], a[88:91]\n"
    "s_add_u32 s72, s9, 83072\n"
    "buffer_load_dwordx4 v247, s[24:27], s72 offen lds\n"
    "v_mfma_f32_16x16x32_bf16 a[92:95], a[224:227], a[216:219], a[92:95]\n"
    "s_add_u32 m0, s83, 23040\n"
    "v_mfma_f32_16x16x32_bf16 a[92:95], a[228:231], a[220:223], a[92:95]\n"
    "s_waitcnt lgkmcnt(4)\n"
    "s_add_u32 s72, s9, 83200\n"
    "buffer_load_dwordx4 v247, s[24:27], s72 offen lds\n"
    "v_mfma_f32_16x16x32_bf16 a[96:99], a[176:179], a[192:195], a[96:99]\n"
    "ds_read_b64_tr_b16 a[224:225], v248 offset:2048\n"
    "v_mfma_f32_16x16x32_bf16 a[96:99], a[180:183], a[196:199], a[96:99]\n"
    "ds_read_b64_tr_b16 a[226:227], v248 offset:11264\n"
    "v_mfma_f32_16x16x32_bf16 a[100:103], a[176:179], a[200:203], a[100:103]\n"
    "ds_read_b64_tr_b16 a[228:229], v248 offset:20480\n"
    "v_mfma_f32_16x16x32_bf16 a[100:103], a[180:183], a[204:207], a[100:103]\n"
    "ds_read_b64_tr_b16 a[230:231], v248 offset:29696\n"
    "v_mfma_f32_16x16x32_bf16 a[104:107], a[176:179], a[208:211], a[104:107]\n"
    "s_add_u32 m0, s83, 24064\n"
    "v_mfma_f32_16x16x32_bf16 a[104:107], a[180:183], a[212:215], a[104:107]\n"
    "s_add_u32 s72, s9, 83328\n"
    "buffer_load_dwordx4 v247, s[24:27], s72 offen lds\n"
    "v_mfma_f32_16x16x32_bf16 a[108:111], a[176:179], a[216:219], a[108:111]\n"
    "v_add_u32 v248, 53248, v248\n"
    "v_add_u32 v249, 53248, v249\n"
    "v_mfma_f32_16x16x32_bf16 a[108:111], a[180:183], a[220:223], a[108:111]\n"
    "s_waitcnt lgkmcnt(4)\n"
    "s_add_u32 m0, s66, 20992\n"
    "v_add_u32 v250, 53248, v250\n"
    "v_mfma_f32_16x16x32_bf16 a[112:115], a[184:187], a[192:195], a[112:115]\n"
    "s_add_u32 s72, s9, 82944\n"
    "s_mov_b32 exec_hi, 0\n"
    "buffer_load_dwordx4 v245, s[24:27], s72 offen lds\n"
    "s_mov_b32 exec_hi, -1\n"
    "v_mfma_f32_16x16x32_bf16 a[112:115], a[188:191], a[196:199], a[112:115]\n"
    "s_waitcnt vmcnt(15) lgkmcnt(0)\n"
    "s_barrier\n"
    "v_mfma_f32_16x16x32_bf16 a[116:119], a[184:187], a[200:203], a[116:119]\n"
    "ds_read_b64_tr_b16 a[176:177], v248 offset:0\n"
    "v_mfma_f32_16x16x32_bf16 a[116:119], a[188:191], a[204:207], a[116:119]\n"
    "ds_read_b64_tr_b16 a[178:179], v248 offset:9216\n"
    "v_mfma_f32_16x16x32_bf16 a[120:123], a[184:187], a[208:211], a[120:123]\n"
    "s_sub_u32 s4, 5, s10\n"
    "ds_read_b64_tr_b16 a[180:181], v248 offset:18432\n"
    "ds_read_b128 a[144:147], v250 offset:0\n"
    "v_mfma_f32_16x16x32_bf16 a[120:123], a[188:191], a[212:215], a[120:123]\n"
    "s_xor_b32 s4, s4, s11\n"
    "ds_read_b64_tr_b16 a[182:183], v248 offset:27648\n"
    "ds_read_b128 a[148:151], v250 offset:1024\n"
    "v_mfma_f32_16x16x32_bf16 a[124:127], a[184:187], a[216:219], a[124:127]\n"
    "s_add_u32 s4, s4, s12\n"
    "ds_read_b128 a[152:155], v250 offset:2048\n"
    "v_mfma_f32_16x16x32_bf16 a[124:127], a[188:191], a[220:223], a[124:127]\n"
    "s_lshl_b32 s8, s4, 12\n"
    "ds_read_b128 a[156:159], v250 offset:3072\n"
    "v_mfma_f32_16x16x32_bf16 a[128:131], a[224:227], a[192:195], a[128:131]\n"
    "s_add_u32 s8, s8, s80\n"
    "ds_read_b128 a[160:163], v250 offset:4096\n"
    "ds_read_b128 a[164:167], v250 offset:5120\n"
    "v_mfma_f32_16x16x32_bf16 a[128:131], a[228:231], a[196:199], a[128:131]\n"
    "s_mul_i32 s9, s4, 147456\n"
    "ds_read_b128 a[168:171], v250 offset:6144\n"
    "ds_read_b128 a[172:175], v250 offset:7168\n"
    "v_mfma_f32_16x16x32_bf16 a[132:135], a[224:227], a[200:203], a[132:135]\n"
    "s_add_u32 s9, s9, s81\n"
    "ds_read_b64_tr_b16 a[184:185], v249 offset:0\n"
    "v_mfma_f32_16x16x32_bf16 a[132:135], a[228:231], a[204:207], a[132:135]\n"
    "ds_read_b64_tr_b16 a[186:187], v249 offset:9216\n"
    "v_mfma_f32_16x16x32_bf16 a[136:139], a[224:227], a[208:211], a[136:139]\n"
    "ds_read_b64_tr_b16 a[188:189], v249 offset:18432\n"
    "v_mfma_f32_16x16x32_bf16 a[136:139], a[228:231], a[212:215], a[136:139]\n"
    "ds_read_b64_tr_b16 a[190:191], v249 offset:27648\n"
    "v_mfma_f32_16x16x32_bf16 a[140:143], a[224:227], a[216:219], a[140:143]\n"
    "v_mfma_f32_16x16x32_bf16 a[140:143], a[228:231], a[220:223], a[140:143]\n"
    "s_waitcnt lgkmcnt(4)\n"
    "v_mfma_f32_16x16x32_bf16 a[0:3], a[176:179], a[144:147], a[0:3]\n"
    "ds_read_b64_tr_b16 a[224:225], v248 offset:512\n"
    "v_mfma_f32_16x16x32_bf16 a[0:3], a[180:183], a[148:151], a[0:3]\n"
    "ds_read_b64_tr_b16 a[226:227], v248 offset:9728\n"
    "s_cmp_eq_u32 s10, 0\n"
    "s_cbranch_scc1 L_spun_o3\n"
    "s_waitcnt vmcnt(12)\n"
    "v_cmp_ne_u32 vcc, s74, v179\n"
    "s_cbranch_vccz L_spun_o3\n"
    "s_mov_b32 s87, 0x40000\n"
    "L_spin_o3:\n"
    "buffer_load_dword v179, v178, s[52:55], s78 offen sc1\n"
    "s_waitcnt vmcnt(0)\n"
    "v_cmp_ne_u32 vcc, s74, v179\n"
    "s_cbranch_vccz L_spun_o3\n"
    "s_sleep 1\n"
    "s_sub_u32 s87, s87, 1\n"
    "s_cbranch_scc0 L_spin_o3\n"
    "L_spun_o3:\n"
    "v_mfma_f32_16x16x32_bf16 a[4:7], a[176:179], a[152:155], a[4:7]\n"
    "ds_read_b64_tr_b16 a[228:229], v248 offset:18944\n"
    "v_mfma_f32_16x16x32_bf16 a[4:7], a[180:183], a[156:159], a[4:7]\n"
    "ds_read_b64_tr_b16 a[230:231], v248 offset:28160\n"
    "v_mfma_f32_16x16x32_bf16 a[8:11], a[176:179], a[160:163], a[8:11]\n"
    "s_add_u32 m0, s82, 53248\n"
    "v_mfma_f32_16x16x32_bf16 a[8:11], a[180:183], a[164:167], a[8:11]\n"
    "s_add_u32 s72, s8, 0\n"
    "buffer_load_dwordx4 v246, s[20:23], s72 offen sc1 lds\n"
    "v_mfma_f32_16x16x32_bf16 a[12:15], a[176:179], a[168:171], a[12:15]\n"
    "v_mfma_f32_16x16x32_bf16 a[12:15], a[180:183], a[172:175], a[12:15]\n"
    "s_waitcnt lgkmcnt(4)\n"
    "buffer_load_dwordx4 v246, s[20:23], s72 offen offset:1024 sc1 lds\n"
    "v_mfma_f32_16x16x32_bf16 a[16:19], a[184:187], a[144:147], a[16:19]\n"
    "ds_read_b64_tr_b16 a[176:177], v249 offset:512\n"
    "v_mfma_f32_16x16x32_bf16 a[16:19], a[188:191], a[148:151], a[16:19]\n"
    "ds_read_b64_tr_b16 a[178:179], v249 offset:9728\n"
    "v_mfma_f32_16x16x32_bf16 a[20:23], a[184:187], a[152:155], a[20:23]\n"
    "ds_read_b64_tr_b16 a[180:181], v249 offset:18944\n"
    "v_mfma_f32_16x16x32_bf16 a[20:23], a[188:191], a[156:159], a[20:23]\n"
    "ds_read_b64_tr_b16 a[182:183], v249 offset:28160\n"
    "v_mfma_f32_16x16x32_bf16 a[24:27], a[184:187], a[160:163], a[24:27]\n"
    "s_add_u32 m0, s82, 55296\n"
    "v_mfma_f32_16x16x32_bf16 a[24:27], a[188:191], a[164:167], a[24:27]\n"
    "s_add_u32 s72, s72, s13\n"
    "buffer_load_dwordx4 v246, s[20:23], s72 offen sc1 lds\n"
    "v_mfma_f32_16x16x32_bf16 a[28:31], a[184:187], a[168:171], a[28:31]\n"
    "v_mfma_f32_16x16x32_bf16 a[28:31], a[188:191], a[172:175], a[28:31]\n"
    "s_waitcnt lgkmcnt(4)\n"
    "buffer_load_dwordx4 v246, s[20:23], s72 offen offset:1024 sc1 lds\n"
    "v_mfma_f32_16x16x32_bf16 a[32:35], a[224:227], a[144:147], a[32:35]\n"
    "ds_read_b64_tr_b16 a[184:185], v248 offset:1024\n"
    "v_mfma_f32_16x16x32_bf16 a[32:35], a[228:231], a[148:151], a[32:35]\n"
    "ds_read_b64_tr_b16 a[186:187], v248 offset:10240\n"
    "v_mfma_f32_16x16x32_bf16 a[36:39], a[224:227], a[152:155], a[36:39]\n"
    "ds_read_b64_tr_b16 a[188:189], v248 offset:19456\n"
    "v_mfma_f32_16x16x32_bf16 a[36:39], a[228:231], a[156:159], a[36:39]\n"
    "ds_read_b64_tr_b16 a[190:191], v248 offset:28672\n"
    "v_mfma_f32_16x16x32_bf16 a[40:43], a[224:227], a[160:163], a[40:43]\n"
    "s_add_u32 m0, s83, 69632\n"
    "v_mfma_f32_16x16x32_bf16 a[40:43], a[228:231], a[164:167], a[40:43]\n"
    "s_add_u32 s72, s9, 0\n"
    "buffer_load_dwordx4 v247, s[24:27], s72 offen lds\n"
    "v_mfma_f32_16x16x32_bf16 a[44:47], a[224:227], a[168:171], a[44:47]\n"
    "s_add_u32 m0, s83, 70656\n"
    "v_mfma_f32_16x16x32_bf16 a[44:47], a[228:231], a[172:175], a[44:47]\n"
    "s_waitcnt lgkmcnt(4)\n"
    "s_add_u32 s72, s9, 128\n"
    "buffer_load_dwordx4 v247, s[24:27], s72 offen lds\n"
    "v_mfma_f32_16x16x32_bf16 a[48:51], a[176:179], a[144:147], a[48:51]\n"
    "ds_read_b64_tr_b16 a[224:225], v249 offset:1024\n"
    "v_mfma_f32_16x16x32_bf16 a[48:51], a[180:183], a[148:151], a[48:51]\n"
    "ds_read_b64_tr_b16 a[226:227], v249 offset:10240\n"
    "v_mfma_f32_16x16x32_bf16 a[52:55], a[176:179], a[152:155], a[52:55]\n"
    "ds_read_b64_tr_b16 a[228:229], v249 offset:19456\n"
    "v_mfma_f32_16x16x32_bf16 a[52:55], a[180:183], a[156:159], a[52:55]\n"
    "ds_read_b64_tr_b16 a[230:231], v249 offset:28672\n"
    "v_mfma_f32_16x16x32_bf16 a[56:59], a[176:179], a[160:163], a[56:59]\n"
    "s_add_u32 m0, s83, 71680\n"
    "v_mfma_f32_16x16x32_bf16 a[56:59], a[180:183], a[164:167], a[56:59]\n"
    "s_add_u32 s72, s9, 256\n"
    "buffer_load_dwordx4 v247, s[24:27], s72 offen lds\n"
    "v_mfma_f32_16x16x32_bf16 a[60:63], a[176:179], a[168:171], a[60:63]\n"
    "s_add_u32 m0, s83, 72704\n"
    "v_mfma_f32_16x16x32_bf16 a[60:63], a[180:183], a[172:175], a[60:63]\n"
    "s_waitcnt lgkmcnt(4)\n"
    "s_add_u32 s72, s9, 384\n"
    "buffer_load_dwordx4 v247, s[24:27], s72 offen lds\n"
    "v_mfma_f32_16x16x32_bf16 a[64:67], a[184:187], a[144:147], a[64:67]\n"
    "ds_read_b64_tr_b16 a[176:177], v248 offset:1536\n"
    "v_mfma_f32_16x16x32_bf16 a[64:67], a[188:191], a[148:151], a[64:67]\n"
    "ds_read_b64_tr_b16 a[178:179], v248 offset:10752\n"
    "v_mfma_f32_16x16x32_bf16 a[68:71], a[184:187], a[152:155], a[68:71]\n"
    "ds_read_b64_tr_b16 a[180:181], v248 offset:19968\n"
    "v_mfma_f32_16x16x32_bf16 a[68:71], a[188:191], a[156:159], a[68:71]\n"
    "ds_read_b64_tr_b16 a[182:183], v248 offset:29184\n"
    "v_mfma_f32_16x16x32_bf16 a[72:75], a[184:187], a[160:163], a[72:75]\n"
    "s_add_u32 m0, s66, 69632\n"
    "v_mfma_f32_16x16x32_bf16 a[72:75], a[188:191], a[164:167], a[72:75]\n"
    "s_add_u32 s72, s9, 0\n"
    "s_mov_b32 exec_hi, 0\n"
    "buffer_load_dwordx4 v245, s[24:27], s72 offen lds\n"
    "s_mov_b32 exec_hi, -1\n"
    "v_mfma_f32_16x16x32_bf16 a[76:79], a[184:187], a[168:171], a[76:79]\n"
    "s_add_u32 m0, s83, 74240\n"
    "v_mfma_f32_16x16x32_bf16 a[76:79], a[188:191], a[172:175], a[76:79]\n"
    "s_waitcnt lgkmcnt(4)\n"
    "s_add_u32 s72, s9, 9216\n"
    "buffer_load_dwordx4 v247, s[24:27], s72 offen lds\n"
    "v_mfma_f32_16x16x32_bf16 a[80:83], a[224:227], a[144:147], a[80:83]\n"
    "ds_read_b64_tr_b16 a[184:185], v249 offset:1536\n"
    "v_mfma_f32_16x16x32_bf16 a[80:83], a[228:231], a[148:151], a[80:83]\n"
    "ds_read_b64_tr_b16 a[186:187], v249 offset:10752\n"
    "v_mfma_f32_16x16x32_bf16 a[84:87], a[224:227], a[152:155], a[84:87]\n"
    "ds_read_b64_tr_b16 a[188:189], v249 offset:19968\n"
    "v_mfma_f32_16x16x32_bf16 a[84:87], a[228:231], a[156:159], a[84:87]\n"
    "ds_read_b64_tr_b16 a[190:191], v249 offset:29184\n"
    "v_mfma_f32_16x16x32_bf16 a[88:91], a[224:227], a[160:163], a[88:91]\n"
    "s_add_u32 m0, s83, 75264\n"
    "v_mfma_f32_16x16x32_bf16 a[88:91], a[228:231], a[164:167], a[88:91]\n"
    "s_add_u32 s72, s9, 9344\n"
    "buffer_load_dwordx4 v247, s[24:27], s72 offen lds\n"
    "v_mfma_f32_16x16x32_bf16 a[92:95], a[224:227], a[168:171], a[92:95]\n"
    "s_add_u32 m0, s83, 76288\n"
    "v_mfma_f32_16x16x32_bf16 a[92:95], a[228:231], a[172:175], a[92:95]\n"
    "s_waitcnt lgkmcnt(4)\n"
    "s_add_u32 s72, s9, 9472\n"
    "buffer_load_dwordx4 v247, s[24:27], s72 offen lds\n"
    "v_mfma_f32_16x16x32_bf16 a[96:99], a[176:179], a[144:147], a[96:99]\n"
    "ds_read_b64_tr_b16 a[224:225], v248 offset:2048\n"
    "v_mfma_f32_16x16x32_bf16 a[96:99], a[180:183], a[148:151], a[96:99]\n"
    "ds_read_b64_tr_b16 a[226:227], v248 offset:11264\n"
    "v_mfma_f32_16x16x32_bf16 a[100:103], a[176:179], a[152:155], a[100:103]\n"
    "ds_read_b64_tr_b16 a[228:229], v248 offset:20480\n"
    "v_mfma_f32_16x16x32_bf16 a[100:103], a[180:183], a[156:159], a[100:103]\n"
    "ds_read_b64_tr_b16 a[230:231], v248 offset:29696\n"
    "v_mfma_f32_16x16x32_bf16 a[104:107], a[176:179], a[160:163], a[104:107]\n"
    "s_add_u32 m0, s83, 77312\n"
    "v_mfma_f32_16x16x32_bf16 a[104:107], a[180:183], a[164:167], a[104:107]\n"
    "s_add_u32 s72, s9, 9600\n"
    "buffer_load_dwordx4 v247, s[24:27], s72 offen lds\n"
    "v_mfma_f32_16x16x32_bf16 a[108:111], a[176:179], a[168:171], a[108:111]\n"
    "v_add_u32 v248, 4294860800, v248\n"
    "v_add_u32 v249, 4294860800, v249\n"
    "v_mfma_f32_16x16x32_bf16 a[108:111], a[180:183], a[172:175], a[108:111]\n"
    "s_waitcnt lgkmcnt(4)\n"
    "s_add_u32 m0, s66, 74240\n"
    "v_add_u32 v250, 4294860800, v250\n"
    "v_mfma_f32_16x16x32_bf16 a[112:115], a[184:187], a[144:147], a[112:115]\n"
    "s_add_u32 s72, s9, 9216\n"
    "s_mov_b32 exec_hi, 0\n"
    "buffer_load_dwordx4 v245, s[24:27], s72 offen lds\n"
    "s_mov_b32 exec_hi, -1\n"
    "v_mfma_f32_16x16x32_bf16 a[112:115], a[188:191], a[148:151], a[112:115]\n"
    "s_waitcnt vmcnt(14) lgkmcnt(0)\n"
    "s_barrier\n"
    "v_mfma_f32_16x16x32_bf16 a[116:119], a[184:187], a[152:155], a[116:119]\n"
    "ds_read_b64_tr_b16 a[176:177], v248 offset:0\n"
    "v_mfma_f32_16x16x32_bf16 a[116:119], a[188:191], a[156:159], a[116:119]\n"
    "ds_read_b64_tr_b16 a[178:179], v248 offset:9216\n"
    "v_mfma_f32_16x16x32_bf16 a[120:123], a[184:187], a[160:163], a[120:123]\n"
    "ds_read_b64_tr_b16 a[180:181], v248 offset:18432\n"
    "ds_read_b128 a[192:195], v250 offset:0\n"
    "v_mfma_f32_16x16x32_bf16 a[120:123], a[188:191], a[164:167], a[120:123]\n"
    "ds_read_b64_tr_b16 a[182:183], v248 offset:27648\n"
    "ds_read_b128 a[196:199], v250 offset:1024\n"
    "v_mfma_f32_16x16x32_bf16 a[124:127], a[184:187], a[168:171], a[124:127]\n"
    "ds_read_b128 a[200:203], v250 offset:2048\n"
    "v_mfma_f32_16x16x32_bf16 a[124:127], a[188:191], a[172:175], a[124:127]\n"
    "ds_read_b128 a[204:207], v250 offset:3072\n"
    "v_mfma_f32_16x16x32_bf16 a[128:131], a[224:227], a[144:147], a[128:131]\n"
    "ds_read_b128 a[208:211], v250 offset:4096\n"
    "ds_read_b128 a[212:215], v250 offset:5120\n"
    "v_mfma_f32_16x16x32_bf16 a[128:131], a[228:231], a[148:151], a[128:131]\n"
    "ds_read_b128 a[216:219], v250 offset:6144\n"
    "ds_read_b128 a[220:223], v250 offset:7168\n"
    "v_mfma_f32_16x16x32_bf16 a[132:135], a[224:227], a[152:155], a[132:135]\n"
    "ds_read_b64_tr_b16 a[184:185], v249 offset:0\n"
    "v_mfma_f32_16x16x32_bf16 a[132:135], a[228:231], a[156:159], a[132:135]\n"
    "ds_read_b64_tr_b16 a[186:187], v249 offset:9216\n"
    "v_mfma_f32_16x16x32_bf16 a[136:139], a[224:227], a[160:163], a[136:139]\n"
    "ds_read_b64_tr_b16 a[188:189], v249 offset:18432\n"
    "v_mfma_f32_16x16x32_bf16 a[136:139], a[228:231], a[164:167], a[136:139]\n"
    "ds_read_b64_tr_b16 a[190:191], v249 offset:27648\n"
    "v_mfma_f32_16x16x32_bf16 a[140:143], a[224:227], a[168:171], a[140:143]\n"
    "v_mfma_f32_16x16x32_bf16 a[140:143], a[228:231], a[172:175], a[140:143]\n"
    "s_waitcnt lgkmcnt(4)\n"
    "v_mfma_f32_16x16x32_bf16 a[0:3], a[176:179], a[192:195], a[0:3]\n"
    "ds_read_b64_tr_b16 a[224:225], v248 offset:512\n"
    "v_and_b32 v210, 15, v253\n"
    "v_mfma_f32_16x16x32_bf16 a[0:3], a[180:183], a[196:199], a[0:3]\n"
    "ds_read_b64_tr_b16 a[226:227], v248 offset:9728\n"
    "v_lshrrev_b32 v211, 4, v253\n"
    "v_mfma_f32_16x16x32_bf16 a[4:7], a[176:179], a[200:203], a[4:7]\n"
    "ds_read_b64_tr_b16 a[228:229], v248 offset:18944\n"
    "v_lshrrev_b32 v212, 2, v210\n"
    "v_mfma_f32_16x16x32_bf16 a[4:7], a[180:183], a[204:207], a[4:7]\n"
    "ds_read_b64_tr_b16 a[230:231], v248 offset:28160\n"
    "v_cmp_eq_u32 vcc, v211, v212\n"
    "v_mfma_f32_16x16x32_bf16 a[8:11], a[176:179], a[208:211], a[8:11]\n"
    "s_add_u32 m0, s82, 106496\n"
    "v_and_b32 v212, 1, v210\n"
    "v_lshlrev_b32 v212, 4, v212\n"
    "v_mfma_f32_16x16x32_bf16 a[8:11], a[180:183], a[212:215], a[8:11]\n"
    "s_add_u32 s72, s8, 2048\n"
    "buffer_load_dwordx4 v246, s[20:23], s72 offen sc1 lds\n"
    "v_mfma_f32_16x16x32_bf16 a[12:15], a[176:179], a[216:219], a[12:15]\n"
    "v_mov_b32 v213, 0x3f80\n"
    "v_lshlrev_b32 v212, v212, v213\n"
    "v_mfma_f32_16x16x32_bf16 a[12:15], a[180:183], a[220:223], a[12:15]\n"
    "s_waitcnt lgkmcnt(4)\n"
    "buffer_load_dwordx4 v246, s[20:23], s72 offen offset:1024 sc1 lds\n"
    "v_mfma_f32_16x16x32_bf16 a[16:19], a[184:187], a[192:195], a[16:19]\n"
    "ds_read_b64_tr_b16 a[176:177], v249 offset:512\n"
    "v_cndmask_b32 v212, 0, v212, vcc\n"
    "v_mfma_f32_16x16x32_bf16 a[16:19], a[188:191], a[196:199], a[16:19]\n"
    "ds_read_b64_tr_b16 a[178:179], v249 offset:9728\n"
    "v_and_b32 v213, 2, v210\n"
    "v_mfma_f32_16x16x32_bf16 a[20:23], a[184:187], a[200:203], a[20:23]\n"
    "ds_read_b64_tr_b16 a[180:181], v249 offset:18944\n"
    "v_cmp_eq_u32 vcc, 0, v213\n"
    "v_mfma_f32_16x16x32_bf16 a[20:23], a[188:191], a[204:207], a[20:23]\n"
    "ds_read_b64_tr_b16 a[182:183], v249 offset:28160\n"
    "v_mov_b32 v200, 0\n"
    "v_mfma_f32_16x16x32_bf16 a[24:27], a[184:187], a[208:211], a[24:27]\n"
    "s_add_u32 m0, s82, 108544\n"
    "v_mov_b32 v201, 0\n"
    "v_cndmask_b32 v202, 0, v212, vcc\n"
    "v_mfma_f32_16x16x32_bf16 a[24:27], a[188:191], a[212:215], a[24:27]\n"
    "s_add_u32 s72, s72, s13\n"
    "buffer_load_dwordx4 v246, s[20:23], s72 offen sc1 lds\n"
    "v_mfma_f32_16x16x32_bf16 a[28:31], a[184:187], a[216:219], a[28:31]\n"
    "v_sub_u32 v203, v212, v202\n"
    "v_mov_b32 v204, 0\n"
    "v_mfma_f32_16x16x32_bf16 a[28:31], a[188:191], a[220:223], a[28:31]\n"
    "s_waitcnt lgkmcnt(4)\n"
    "buffer_load_dwordx4 v246, s[20:23], s72 offen offset:1024 sc1 lds\n"
    "v_mfma_f32_16x16x32_bf16 a[32:35], a[224:227], a[192:195], a[32:35]\n"
    "ds_read_b64_tr_b16 a[184:185], v248 offset:1024\n"
    "v_mov_b32 v205, 0\n"
    "v_mfma_f32_16x16x32_bf16 a[32:35], a[228:231], a[196:199], a[32:35]\n"
    "ds_read_b64_tr_b16 a[186:187], v248 offset:10240\n"
    "v_mov_b32 v206, v202\n"
    "v_mfma_f32_16x16x32_bf16 a[36:39], a[224:227], a[200:203], a[36:39]\n"
    "ds_read_b64_tr_b16 a[188:189], v248 offset:19456\n"
    "v_mov_b32 v207, v203\n"
    "v_mfma_f32_16x16x32_bf16 a[36:39], a[228:231], a[204:207], a[36:39]\n"
    "ds_read_b64_tr_b16 a[190:191], v248 offset:28672\n"
    "v_mov_b32 v208, v202\n"
    "v_mfma_f32_16x16x32_bf16 a[40:43], a[224:227], a[208:211], a[40:43]\n"
    "s_add_u32 m0, s83, 122880\n"
    "v_mov_b32 v209, v203\n"
    "v_mul_u32_u24 v221, 1152, v210\n"
    "v_mfma_f32_16x16x32_bf16 a[40:43], a[228:231], a[212:215], a[40:43]\n"
    "s_add_u32 s72, s9, 73728\n"
    "buffer_load_dwordx4 v247, s[24:27], s72 offen lds\n"
    "v_mfma_f32_16x16x32_bf16 a[44:47], a[224:227], a[216:219], a[44:47]\n"
    "s_add_u32 m0, s83, 123904\n"
    "v_lshl_add_u32 v221, v211, 3, v221\n"
    "v_mfma_f32_16x16x32_bf16 a[44:47], a[228:231], a[220:223], a[44:47]\n"
    "s_waitcnt lgkmcnt(4)\n"
    "s_add_u32 s72, s9, 73856\n"
    "buffer_load_dwordx4 v247, s[24:27], s72 offen lds\n"
    "v_mfma_f32_16x16x32_bf16 a[48:51], a[176:179], a[192:195], a[48:51]\n"
    "s_and_b32 s77, s65, 0x18\n"
    "s_lshl_b32 s77, s77, 9\n"
    "ds_read_b64_tr_b16 a[224:225], v249 offset:1024\n"
    "v_mfma_f32_16x16x32_bf16 a[48:51], a[180:183], a[196:199], a[48:51]\n"
    "s_add_u32 s77, s77, 49152\n"
    "s_lshl_b32 s5, s10, 8\n"
    "ds_read_b64_tr_b16 a[226:227], v249 offset:10240\n"
    "v_mfma_f32_16x16x32_bf16 a[52:55], a[176:179], a[200:203], a[52:55]\n"
    "s_add_u32 s77, s77, s5\n"
    "s_lshr_b32 s5, s68, 1\n"
    "ds_read_b64_tr_b16 a[228:229], v249 offset:19456\n"
    "v_mfma_f32_16x16x32_bf16 a[52:55], a[180:183], a[204:207], a[52:55]\n"
    "s_lshl_b32 s5, s5, 7\n"
    "s_add_u32 s77, s77, s5\n"
    "ds_read_b64_tr_b16 a[230:231], v249 offset:28672\n"
    "v_mfma_f32_16x16x32_bf16 a[56:59], a[176:179], a[208:211], a[56:59]\n"
    "s_add_u32 m0, s83, 124928\n"
    "s_lshr_b32 s79, s65, 4\n"
    "s_lshl_b32 s79, s79, 3\n"
    "v_mfma_f32_16x16x32_bf16 a[56:59], a[180:183], a[212:215], a[56:59]\n"
    "s_add_u32 s72, s9, 73984\n"
    "buffer_load_dwordx4 v247, s[24:27], s72 offen lds\n"
    "v_mfma_f32_16x16x32_bf16 a[60:63], a[176:179], a[216:219], a[60:63]\n"
    "s_add_u32 m0, s83, 125952\n"
    "s_and_b32 s5, s65, 7\n"
    "s_add_u32 s79, s79, s5\n"
    "v_mfma_f32_16x16x32_bf16 a[60:63], a[180:183], a[220:223], a[60:63]\n"
    "s_waitcnt lgkmcnt(4)\n"
    "s_add_u32 s72, s9, 74112\n"
    "buffer_load_dwordx4 v247, s[24:27], s72 offen lds\n"
    "v_mfma_f32_16x16x32_bf16 a[64:67], a[184:187], a[192:195], a[64:67]\n"
    "s_mul_i32 s79, s79, 18432\n"
    "s_mul_i32 s5, s75, 576\n"
    "ds_read_b64_tr_b16 a[176:177], v248 offset:1536\n"
    "v_mfma_f32_16x16x32_bf16 a[64:67], a[188:191], a[196:199], a[64:67]\n"
    "s_add_u32 s79, s79, s5\n"
    "s_lshr_b32 s5, s68, 1\n"
    "ds_read_b64_tr_b16 a[178:179], v248 offset:10752\n"
    "v_mfma_f32_16x16x32_bf16 a[68:71], a[184:187], a[200:203], a[68:71]\n"
    "s_mul_i32 s5, s5, 288\n"
    "s_add_u32 s79, s79, s5\n"
    "ds_read_b64_tr_b16 a[180:181], v248 offset:19968\n"
    "v_mfma_f32_16x16x32_bf16 a[68:71], a[188:191], a[204:207], a[68:71]\n"
    "s_and_b32 s5, s68, 1\n"
    "s_lshl_b32 s6, s5, 30\n"
    "ds_read_b64_tr_b16 a[182:183], v248 offset:29184\n"
    "v_mfma_f32_16x16x32_bf16 a[72:75], a[184:187], a[208:211], a[72:75]\n"
    "s_add_u32 m0, s66, 122880\n"
    "s_add_u32 s78, s79, s6\n"
    "s_add_u32 s78, s78, 256\n"
    "v_mfma_f32_16x16x32_bf16 a[72:75], a[188:191], a[212:215], a[72:75]\n"
    "s_add_u32 s72, s9, 73728\n"
    "s_mov_b32 exec_hi, 0\n"
    "buffer_load_dwordx4 v245, s[24:27], s72 offen lds\n"
    "s_mov_b32 exec_hi, -1\n"
    "v_mfma_f32_16x16x32_bf16 a[76:79], a[184:187], a[216:219], a[76:79]\n"
    "s_add_u32 m0, s83, 127488\n"
    "s_lshl_b32 s5, s5, 7\n"
    "s_add_u32 s79, s79, s5\n"
    "v_mfma_f32_16x16x32_bf16 a[76:79], a[188:191], a[220:223], a[76:79]\n"
    "s_waitcnt lgkmcnt(4)\n"
    "s_add_u32 s72, s9, 82944\n"
    "buffer_load_dwordx4 v247, s[24:27], s72 offen lds\n"
    "v_mfma_f32_16x16x32_bf16 a[80:83], a[224:227], a[192:195], a[80:83]\n"
    "s_lshr_b32 s64, s65, 3\n"
    "s_lshl_b32 s64, s64, 1\n"
    "ds_read_b64_tr_b16 a[184:185], v249 offset:1536\n"
    "v_mfma_f32_16x16x32_bf16 a[80:83], a[228:231], a[196:199], a[80:83]\n"
    "s_lshr_b32 s5, s68, 1\n"
    "s_add_u32 s64, s64, s5\n"
    "ds_read_b64_tr_b16 a[186:187], v249 offset:10752\n"
    "v_mfma_f32_16x16x32_bf16 a[84:87], a[224:227], a[200:203], a[84:87]\n"
    "s_lshl_b32 s64, s64, 5\n"
    "s_and_b32 s5, s65, 7\n"
    "ds_read_b64_tr_b16 a[188:189], v249 offset:19968\n"
    "v_mfma_f32_16x16x32_bf16 a[84:87], a[228:231], a[204:207], a[84:87]\n"
    "s_add_u32 s64, s64, s5\n"
    "s_lshl_b32 s64, s64, 1\n"
    "ds_read_b64_tr_b16 a[190:191], v249 offset:29184\n"
    "v_mfma_f32_16x16x32_bf16 a[88:91], a[224:227], a[208:211], a[88:91]\n"
    "s_add_u32 m0, s83, 128512\n"
    "s_and_b32 s5, s68, 1\n"
    "s_add_u32 s64, s64, s5\n"
    "v_mfma_f32_16x16x32_bf16 a[88:91], a[228:231], a[212:215], a[88:91]\n"
    "s_add_u32 s72, s9, 83072\n"
    "buffer_load_dwordx4 v247, s[24:27], s72 offen lds\n"
    "v_mfma_f32_16x16x32_bf16 a[92:95], a[224:227], a[216:219], a[92:95]\n"
    "s_add_u32 m0, s83, 129536\n"
    "s_mul_i32 s64, s64, 4608\n"
    "v_mfma_f32_16x16x32_bf16 a[92:95], a[228:231], a[220:223], a[92:95]\n"
    "s_waitcnt lgkmcnt(4)\n"
    "s_add_u32 s72, s9, 83200\n"
    "buffer_load_dwordx4 v247, s[24:27], s72 offen lds\n"
    "v_mfma_f32_16x16x32_bf16 a[96:99], a[176:179], a[192:195], a[96:99]\n"
    "ds_read_b64_tr_b16 a[224:225], v248 offset:2048\n"
    "v_mfma_f32_16x16x32_bf16 a[96:99], a[180:183], a[196:199], a[96:99]\n"
    "ds_read_b64_tr_b16 a[226:227], v248 offset:11264\n"
    "v_mfma_f32_16x16x32_bf16 a[100:103], a[176:179], a[200:203], a[100:103]\n"
    "ds_read_b64_tr_b16 a[228:229], v248 offset:20480\n"
    "v_mfma_f32_16x16x32_bf16 a[100:103], a[180:183], a[204:207], a[100:103]\n"
    "ds_read_b64_tr_b16 a[230:231], v248 offset:29696\n"
    "v_mfma_f32_16x16x32_bf16 a[104:107], a[176:179], a[208:211], a[104:107]\n"
    "s_add_u32 m0, s83, 130560\n"
    "v_mfma_f32_16x16x32_bf16 a[104:107], a[180:183], a[212:215], a[104:107]\n"
    "s_add_u32 s72, s9, 83328\n"
    "buffer_load_dwordx4 v247, s[24:27], s72 offen lds\n"
    "v_mfma_f32_16x16x32_bf16 a[108:111], a[176:179], a[216:219], a[108:111]\n"
    "v_add_u32 v248, 53248, v248\n"
    "v_add_u32 v249, 53248, v249\n"
    "v_mfma_f32_16x16x32_bf16 a[108:111], a[180:183], a[220:223], a[108:111]\n"
    "s_waitcnt lgkmcnt(4)\n"
    "s_add_u32 m0, s66, 127488\n"
    "v_add_u32 v250, 53248, v250\n"
    "v_mfma_f32_16x16x32_bf16 a[112:115], a[184:187], a[192:195], a[112:115]\n"
    "s_add_u32 s72, s9, 82944\n"
    "s_mov_b32 exec_hi, 0\n"
    "buffer_load_dwordx4 v245, s[24:27], s72 offen lds\n"
    "s_mov_b32 exec_hi, -1\n"
    "v_mfma_f32_16x16x32_bf16 a[112:115], a[188:191], a[196:199], a[112:115]\n"
    "s_waitcnt vmcnt(14) lgkmcnt(0)\n"
    "s_barrier\n"
    "v_mfma_f32_16x16x32_bf16 a[116:119], a[184:187], a[200:203], a[116:119]\n"
    "ds_read_b64_tr_b16 a[176:177], v248 offset:0\n"
    "v_mfma_f32_16x16x32_bf16 a[116:119], a[188:191], a[204:207], a[116:119]\n"
    "ds_read_b64_tr_b16 a[178:179], v248 offset:9216\n"
    "v_mfma_f32_16x16x32_bf16 a[120:123], a[184:187], a[208:211], a[120:123]\n"
    "s_sub_u32 s4, 6, s10\n"
    "ds_read_b64_tr_b16 a[180:181], v248 offset:18432\n"
    "ds_read_b128 a[144:147], v250 offset:0\n"
    "v_mfma_f32_16x16x32_bf16 a[120:123], a[188:191], a[212:215], a[120:123]\n"
    "s_xor_b32 s4, s4, s11\n"
    "ds_read_b64_tr_b16 a[182:183], v248 offset:27648\n"
    "ds_read_b128 a[148:151], v250 offset:1024\n"
    "v_mfma_f32_16x16x32_bf16 a[124:127], a[184:187], a[216:219], a[124:127]\n"
    "s_add_u32 s4, s4, s12\n"
    "ds_read_b128 a[152:155], v250 offset:2048\n"
    "v_mfma_f32_16x16x32_bf16 a[124:127], a[188:191], a[220:223], a[124:127]\n"
    "s_lshl_b32 s8, s4, 12\n"
    "ds_read_b128 a[156:159], v250 offset:3072\n"
    "v_mfma_f32_16x16x32_bf16 a[128:131], a[224:227], a[192:195], a[128:131]\n"
    "s_add_u32 s8, s8, s80\n"
    "ds_read_b128 a[160:163], v250 offset:4096\n"
    "ds_read_b128 a[164:167], v250 offset:5120\n"
    "v_mfma_f32_16x16x32_bf16 a[128:131], a[228:231], a[196:199], a[128:131]\n"
    "s_mul_i32 s9, s4, 147456\n"
    "ds_read_b128 a[168:171], v250 offset:6144\n"
    "ds_read_b128 a[172:175], v250 offset:7168\n"
    "v_mfma_f32_16x16x32_bf16 a[132:135], a[224:227], a[200:203], a[132:135]\n"
    "s_add_u32 s9, s9, s81\n"
    "ds_read_b64_tr_b16 a[184:185], v249 offset:0\n"
    "v_mfma_f32_16x16x32_bf16 a[132:135], a[228:231], a[204:207], a[132:135]\n"
    "ds_read_b64_tr_b16 a[186:187], v249 offset:9216\n"
    "v_mfma_f32_16x16x32_bf16 a[136:139], a[224:227], a[208:211], a[136:139]\n"
    "ds_read_b64_tr_b16 a[188:189], v249 offset:18432\n"
    "v_mfma_f32_16x16x32_bf16 a[136:139], a[228:231], a[212:215], a[136:139]\n"
    "ds_read_b64_tr_b16 a[190:191], v249 offset:27648\n"
    "v_mfma_f32_16x16x32_bf16 a[140:143], a[224:227], a[216:219], a[140:143]\n"
    "v_mfma_f32_16x16x32_bf16 a[140:143], a[228:231], a[220:223], a[140:143]\n"
    "s_waitcnt lgkmcnt(4)\n"
    "v_mfma_f32_16x16x32_bf16 a[0:3], a[176:179], a[144:147], a[0:3]\n"
    "ds_read_b64_tr_b16 a[224:225], v248 offset:512\n"
    "v_mfma_f32_16x16x32_bf16 a[0:3], a[180:183], a[148:151], a[0:3]\n"
    "ds_read_b64_tr_b16 a[226:227], v248 offset:9728\n"
    "v_mfma_f32_16x16x32_bf16 a[4:7], a[176:179], a[152:155], a[4:7]\n"
    "ds_read_b64_tr_b16 a[228:229], v248 offset:18944\n"
    "v_mfma_f32_16x16x32_bf16 a[4:7], a[180:183], a[156:159], a[4:7]\n"
    "ds_read_b64_tr_b16 a[230:231], v248 offset:28160\n"
    "v_mfma_f32_16x16x32_bf16 a[8:11], a[176:179], a[160:163], a[8:11]\n"
    "s_add_u32 m0, s82, 0\n"
    "v_mfma_f32_16x16x32_bf16 a[8:11], a[180:183], a[164:167], a[8:11]\n"
    "s_add_u32 s72, s8, 0\n"
    "buffer_load_dwordx4 v246, s[20:23], s72 offen sc1 lds\n"
    "v_mfma_f32_16x16x32_bf16 a[12:15], a[176:179], a[168:171], a[12:15]\n"
    "v_mfma_f32_16x16x32_bf16 a[12:15], a[180:183], a[172:175], a[12:15]\n"
    "s_waitcnt lgkmcnt(4)\n"
    "buffer_load_dwordx4 v246, s[20:23], s72 offen offset:1024 sc1 lds\n"
    "v_mfma_f32_16x16x32_bf16 a[16:19], a[184:187], a[144:147], a[16:19]\n"
    "ds_read_b64_tr_b16 a[176:177], v249 offset:512\n"
    "v_mfma_f32_16x16x32_bf16 a[16:19], a[188:191], a[148:151], a[16:19]\n"
    "ds_read_b64_tr_b16 a[178:179], v249 offset:9728\n"
    "v_mfma_f32_16x16x32_bf16 a[20:23], a[184:187], a[152:155], a[20:23]\n"
    "ds_read_b64_tr_b16 a[180:181], v249 offset:18944\n"
    "v_mfma_f32_16x16x32_bf16 a[20:23], a[188:191], a[156:159], a[20:23]\n"
    "ds_read_b64_tr_b16 a[182:183], v249 offset:28160\n"
    "v_mfma_f32_16x16x32_bf16 a[24:27], a[184:187], a[160:163], a[24:27]\n"
    "s_add_u32 m0, s82, 2048\n"
    "v_mfma_f32_16x16x32_bf16 a[24:27], a[188:191], a[164:167], a[24:27]\n"
    "s_add_u32 s72, s72, s13\n"
    "buffer_load_dwordx4 v246, s[20:23], s72 offen sc1 lds\n"
    "v_mfma_f32_16x16x32_bf16 a[28:31], a[184:187], a[168:171], a[28:31]\n"
    "v_mfma_f32_16x16x32_bf16 a[28:31], a[188:191], a[172:175], a[28:31]\n"
    "s_waitcnt lgkmcnt(4)\n"
    "buffer_load_dwordx4 v246, s[20:23], s72 offen offset:1024 sc1 lds\n"
    "v_mfma_f32_16x16x32_bf16 a[32:35], a[224:227], a[144:147], a[32:35]\n"
    "ds_read_b64_tr_b16 a[184:185], v248 offset:1024\n"
    "v_mfma_f32_16x16x32_bf16 a[32:35], a[228:231], a[148:151], a[32:35]\n"
    "ds_read_b64_tr_b16 a[186:187], v248 offset:10240\n"
    "v_mfma_f32_16x16x32_bf16 a[36:39], a[224:227], a[152:155], a[36:39]\n"
    "ds_read_b64_tr_b16 a[188:189], v248 offset:19456\n"
    "v_mfma_f32_16x16x32_bf16 a[36:39], a[228:231], a[156:159], a[36:39]\n"
    "ds_read_b64_tr_b16 a[190:191], v248 offset:28672\n"
    "v_mfma_f32_16x16x32_bf16 a[40:43], a[224:227], a[160:163], a[40:43]\n"
    "s_add_u32 m0, s83, 16384\n"
    "v_mfma_f32_16x16x32_bf16 a[40:43], a[228:231], a[164:167], a[40:43]\n"
    "s_add_u32 s72, s9, 0\n"
    "buffer_load_dwordx4 v247, s[24:27], s72 offen lds\n"
    "v_mfma_f32_16x16x32_bf16 a[44:47], a[224:227], a[168:171], a[44:47]\n"
    "s_add_u32 m0, s83, 17408\n"
    "v_mfma_f32_16x16x32_bf16 a[44:47], a[228:231], a[172:175], a[44:47]\n"
    "s_waitcnt lgkmcnt(4)\n"
    "s_add_u32 s72, s9, 128\n"
    "buffer_load_dwordx4 v247, s[24:27], s72 offen lds\n"
    "v_mfma_f32_16x16x32_bf16 a[48:51], a[176:179], a[144:147], a[48:51]\n"
    "ds_read_b64_tr_b16 a[224:225], v249 offset:1024\n"
    "v_mfma_f32_16x16x32_bf16 a[48:51], a[180:183], a[148:151], a[48:51]\n"
    "ds_read_b64_tr_b16 a[226:227], v249 offset:10240\n"
    "v_mfma_f32_16x16x32_bf16 a[52:55], a[176:179], a[152:155], a[52:55]\n"
    "ds_read_b64_tr_b16 a[228:229], v249 offset:19456\n"
    "v_mfma_f32_16x16x32_bf16 a[52:55], a[180:183], a[156:159], a[52:55]\n"
    "ds_read_b64_tr_b16 a[230:231], v249 offset:28672\n"
    "v_mfma_f32_16x16x32_bf16 a[56:59], a[176:179], a[160:163], a[56:59]\n"
    "s_add_u32 m0, s83, 18432\n"
    "v_mfma_f32_16x16x32_bf16 a[56:59], a[180:183], a[164:167], a[56:59]\n"
    "s_add_u32 s72, s9, 256\n"
    "buffer_load_dwordx4 v247, s[24:27], s72 offen lds\n"
    "v_mfma_f32_16x16x32_bf16 a[60:63], a[176:179], a[168:171], a[60:63]\n"
    "s_add_u32 m0, s83, 19456\n"
    "v_mfma_f32_16x16x32_bf16 a[60:63], a[180:183], a[172:175], a[60:63]\n"
    "s_waitcnt lgkmcnt(4)\n"
    "s_add_u32 s72, s9, 384\n"
    "buffer_load_dwordx4 v247, s[24:27], s72 offen lds\n"
    "v_mfma_f32_16x16x32_bf16 a[64:67], a[184:187], a[144:147], a[64:67]\n"
    "ds_read_b64_tr_b16 a[176:177], v248 offset:1536\n"
    "v_mfma_f32_16x16x32_bf16 a[64:67], a[188:191], a[148:151], a[64:67]\n"
    "ds_read_b64_tr_b16 a[178:179], v248 offset:10752\n"
    "v_mfma_f32_16x16x32_bf16 a[68:71], a[184:187], a[152:155], a[68:71]\n"
    "ds_read_b64_tr_b16 a[180:181], v248 offset:19968\n"
    "v_mfma_f32_16x16x32_bf16 a[68:71], a[188:191], a[156:159], a[68:71]\n"
    "ds_read_b64_tr_b16 a[182:183], v248 offset:29184\n"
    "v_mfma_f32_16x16x32_bf16 a[72:75], a[184:187], a[160:163], a[72:75]\n"
    "s_add_u32 m0, s66, 16384\n"
    "v_mfma_f32_16x16x32_bf16 a[72:75], a[188:191], a[164:167], a[72:75]\n"
    "s_add_u32 s72, s9, 0\n"
    "s_mov_b32 exec_hi, 0\n"
    "buffer_load_dwordx4 v245, s[24:27], s72 offen lds\n"
    "s_mov_b32 exec_hi, -1\n"
    "v_mfma_f32_16x16x32_bf16 a[76:79], a[184:187], a[168:171], a[76:79]\n"
    "s_add_u32 m0, s83, 20992\n"
    "v_mfma_f32_16x16x32_bf16 a[76:79], a[188:191], a[172:175], a[76:79]\n"
    "s_waitcnt lgkmcnt(4)\n"
    "s_add_u32 s72, s9, 9216\n"
    "buffer_load_dwordx4 v247, s[24:27], s72 offen lds\n"
    "v_mfma_f32_16x16x32_bf16 a[80:83], a[224:227], a[144:147], a[80:83]\n"
    "ds_read_b64_tr_b16 a[184:185], v249 offset:1536\n"
    "v_mfma_f32_16x16x32_bf16 a[80:83], a[228:231], a[148:151], a[80:83]\n"
    "ds_read_b64_tr_b16 a[186:187], v249 offset:10752\n"
    "v_mfma_f32_16x16x32_bf16 a[84:87], a[224:227], a[152:155], a[84:87]\n"
    "ds_read_b64_tr_b16 a[188:189], v249 offset:19968\n"
    "v_mfma_f32_16x16x32_bf16 a[84:87], a[228:231], a[156:159], a[84:87]\n"
    "ds_read_b64_tr_b16 a[190:191], v249 offset:29184\n"
    "v_mfma_f32_16x16x32_bf16 a[88:91], a[224:227], a[160:163], a[88:91]\n"
    "s_add_u32 m0, s83, 22016\n"
    "v_mfma_f32_16x16x32_bf16 a[88:91], a[228:231], a[164:167], a[88:91]\n"
    "s_add_u32 s72, s9, 9344\n"
    "buffer_load_dwordx4 v247, s[24:27], s72 offen lds\n"
    "v_mfma_f32_16x16x32_bf16 a[92:95], a[224:227], a[168:171], a[92:95]\n"
    "s_add_u32 m0, s83, 23040\n"
    "v_mfma_f32_16x16x32_bf16 a[92:95], a[228:231], a[172:175], a[92:95]\n"
    "s_waitcnt lgkmcnt(4)\n"
    "s_add_u32 s72, s9, 9472\n"
    "buffer_load_dwordx4 v247, s[24:27], s72 offen lds\n"
    "v_mfma_f32_16x16x32_bf16 a[96:99], a[176:179], a[144:147], a[96:99]\n"
    "ds_read_b64_tr_b16 a[224:225], v248 offset:2048\n"
    "v_mfma_f32_16x16x32_bf16 a[96:99], a[180:183], a[148:151], a[96:99]\n"
    "ds_read_b64_tr_b16 a[226:227], v248 offset:11264\n"
    "v_mfma_f32_16x16x32_bf16 a[100:103], a[176:179], a[152:155], a[100:103]\n"
    "ds_read_b64_tr_b16 a[228:229], v248 offset:20480\n"
    "v_mfma_f32_16x16x32_bf16 a[100:103], a[180:183], a[156:159], a[100:103]\n"
    "ds_read_b64_tr_b16 a[230:231], v248 offset:29696\n"
    "v_mfma_f32_16x16x32_bf16 a[104:107], a[176:179], a[160:163], a[104:107]\n"
    "s_add_u32 m0, s83, 24064\n"
    "v_mfma_f32_16x16x32_bf16 a[104:107], a[180:183], a[164:167], a[104:107]\n"
    "s_add_u32 s72, s9, 9600\n"
    "buffer_load_dwordx4 v247, s[24:27], s72 offen lds\n"
    "v_mfma_f32_16x16x32_bf16 a[108:111], a[176:179], a[168:171], a[108:111]\n"
    "v_add_u32 v248, 53248, v248\n"
    "v_add_u32 v249, 53248, v249\n"
    "v_mfma_f32_16x16x32_bf16 a[108:111], a[180:183], a[172:175], a[108:111]\n"
    "s_waitcnt lgkmcnt(4)\n"
    "s_add_u32 m0, s66, 20992\n"
    "v_add_u32 v250, 53248, v250\n"
    "v_mfma_f32_16x16x32_bf16 a[112:115], a[184:187], a[144:147], a[112:115]\n"
    "s_add_u32 s72, s9, 9216\n"
    "s_mov_b32 exec_hi, 0\n"
    "buffer_load_dwordx4 v245, s[24:27], s72 offen lds\n"
    "s_mov_b32 exec_hi, -1\n"
    "v_mfma_f32_16x16x32_bf16 a[112:115], a[188:191], a[148:151], a[112:115]\n"
    "s_waitcnt vmcnt(14) lgkmcnt(0)\n"
    "s_barrier\n"
    "v_mfma_f32_16x16x32_bf16 a[116:119], a[184:187], a[152:155], a[116:119]\n"
    "ds_read_b64_tr_b16 a[176:177], v248 offset:0\n"
    "v_mfma_f32_16x16x32_bf16 a[116:119], a[188:191], a[156:159], a[116:119]\n"
    "ds_read_b64_tr_b16 a[178:179], v248 offset:9216\n"
    "v_mfma_f32_16x16x32_bf16 a[120:123], a[184:187], a[160:163], a[120:123]\n"
    "ds_read_b64_tr_b16 a[180:181], v248 offset:18432\n"
    "ds_read_b128 a[192:195], v250 offset:0\n"
    "v_mfma_f32_16x16x32_bf16 a[120:123], a[188:191], a[164:167], a[120:123]\n"
    "ds_read_b64_tr_b16 a[182:183], v248 offset:27648\n"
    "ds_read_b128 a[196:199], v250 offset:1024\n"
    "v_mfma_f32_16x16x32_bf16 a[124:127], a[184:187], a[168:171], a[124:127]\n"
    "ds_read_b128 a[200:203], v250 offset:2048\n"
    "v_mfma_f32_16x16x32_bf16 a[124:127], a[188:191], a[172:175], a[124:127]\n"
    "ds_read_b128 a[204:207], v250 offset:3072\n"
    "v_mfma_f32_16x16x32_bf16 a[128:131], a[224:227], a[144:147], a[128:131]\n"
    "ds_read_b128 a[208:211], v250 offset:4096\n"
    "ds_read_b128 a[212:215], v250 offset:5120\n"
    "v_mfma_f32_16x16x32_bf16 a[128:131], a[228:231], a[148:151], a[128:131]\n"
    "ds_read_b128 a[216:219], v250 offset:6144\n"
    "ds_read_b128 a[220:223], v250 offset:7168\n"
    "v_mfma_f32_16x16x32_bf16 a[132:135], a[224:227], a[152:155], a[132:135]\n"
    "ds_read_b64_tr_b16 a[184:185], v249 offset:0\n"
    "v_mfma_f32_16x16x32_bf16 a[132:135], a[228:231], a[156:159], a[132:135]\n"
    "ds_read_b64_tr_b16 a[186:187], v249 offset:9216\n"
    "v_mfma_f32_16x16x32_bf16 a[136:139], a[224:227], a[160:163], a[136:139]\n"
    "ds_read_b64_tr_b16 a[188:189], v249 offset:18432\n"
    "v_mfma_f32_16x16x32_bf16 a[136:139], a[228:231], a[164:167], a[136:139]\n"
    "ds_read_b64_tr_b16 a[190:191], v249 offset:27648\n"
    "v_mfma_f32_16x16x32_bf16 a[140:143], a[224:227], a[168:171], a[140:143]\n"
    "v_mfma_f32_16x16x32_bf16 a[140:143], a[228:231], a[172:175], a[140:143]\n"
    "s_waitcnt lgkmcnt(4)\n"
    "v_mfma_f32_16x16x32_bf16 a[0:3], a[176:179], a[192:195], a[0:3]\n"
    "ds_read_b64_tr_b16 a[224:225], v248 offset:512\n"
    "v_mfma_f32_16x16x32_bf16 a[0:3], a[180:183], a[196:199], a[0:3]\n"
    "ds_read_b64_tr_b16 a[226:227], v248 offset:9728\n"
    "v_mfma_f32_16x16x32_bf16 a[4:7], a[176:179], a[200:203], a[4:7]\n"
    "ds_read_b64_tr_b16 a[228:229], v248 offset:18944\n"
    "v_mfma_f32_16x16x32_bf16 a[4:7], a[180:183], a[204:207], a[4:7]\n"
    "ds_read_b64_tr_b16 a[230:231], v248 offset:28160\n"
    "v_mfma_f32_16x16x32_bf16 a[8:11], a[176:179], a[208:211], a[8:11]\n"
    "s_add_u32 m0, s82, 53248\n"
    "v_mfma_f32_16x16x32_bf16 a[8:11], a[180:183], a[212:215], a[8:11]\n"
    "s_add_u32 s72, s8, 2048\n"
    "buffer_load_dwordx4 v246, s[20:23], s72 offen sc1 lds\n"
    "v_mfma_f32_16x16x32_bf16 a[12:15], a[176:179], a[216:219], a[12:15]\n"
    "v_mfma_f32_16x16x32_bf16 a[12:15], a[180:183], a[220:223], a[12:15]\n"
    "s_waitcnt lgkmcnt(4)\n"
    "buffer_load_dwordx4 v246, s[20:23], s72 offen offset:1024 sc1 lds\n"
    "v_mfma_f32_16x16x32_bf16 a[16:19], a[184:187], a[192:195], a[16:19]\n"
    "ds_read_b64_tr_b16 a[176:177], v249 offset:512\n"
    "v_mfma_f32_16x16x32_bf16 a[16:19], a[188:191], a[196:199], a[16:19]\n"
    "ds_read_b64_tr_b16 a[178:179], v249 offset:9728\n"
    "v_mfma_f32_16x16x32_bf16 a[20:23], a[184:187], a[200:203], a[20:23]\n"
    "ds_read_b64_tr_b16 a[180:181], v249 offset:18944\n"
    "v_mfma_f32_16x16x32_bf16 a[20:23], a[188:191], a[204:207], a[20:23]\n"
    "ds_read_b64_tr_b16 a[182:183], v249 offset:28160\n"
    "v_mfma_f32_16x16x32_bf16 a[24:27], a[184:187], a[208:211], a[24:27]\n"
    "s_add_u32 m0, s82, 55296\n"
    "v_mfma_f32_16x16x32_bf16 a[24:27], a[188:191], a[212:215], a[24:27]\n"
    "s_add_u32 s72, s72, s13\n"
    "buffer_load_dwordx4 v246, s[20:23], s72 offen sc1 lds\n"
    "v_mfma_f32_16x16x32_bf16 a[28:31], a[184:187], a[216:219], a[28:31]\n"
    "v_mfma_f32_16x16x32_bf16 a[28:31], a[188:191], a[220:223], a[28:31]\n"
    "s_waitcnt lgkmcnt(4)\n"
    "buffer_load_dwordx4 v246, s[20:23], s72 offen offset:1024 sc1 lds\n"
    "v_mfma_f32_16x16x32_bf16 a[32:35], a[224:227], a[192:195], a[32:35]\n"
    "ds_read_b64_tr_b16 a[184:185], v248 offset:1024\n"
    "v_mfma_f32_16x16x32_bf16 a[32:35], a[228:231], a[196:199], a[32:35]\n"
    "ds_read_b64_tr_b16 a[186:187], v248 offset:10240\n"
    "v_mfma_f32_16x16x32_bf16 a[36:39], a[224:227], a[200:203], a[36:39]\n"
    "ds_read_b64_tr_b16 a[188:189], v248 offset:19456\n"
    "v_mfma_f32_16x16x32_bf16 a[36:39], a[228:231], a[204:207], a[36:39]\n"
    "ds_read_b64_tr_b16 a[190:191], v248 offset:28672\n"
    "v_mfma_f32_16x16x32_bf16 a[40:43], a[224:227], a[208:211], a[40:43]\n"
    "s_add_u32 m0, s83, 69632\n"
    "v_mfma_f32_16x16x32_bf16 a[40:43], a[228:231], a[212:215], a[40:43]\n"
    "s_add_u32 s72, s9, 73728\n"
    "buffer_load_dwordx4 v247, s[24:27], s72 offen lds\n"
    "v_mfma_f32_16x16x32_bf16 a[44:47], a[224:227], a[216:219], a[44:47]\n"
    "s_add_u32 m0, s83, 70656\n"
    "v_mfma_f32_16x16x32_bf16 a[44:47], a[228:231], a[220:223], a[44:47]\n"
    "s_waitcnt lgkmcnt(4)\n"
    "s_add_u32 s72, s9, 73856\n"
    "buffer_load_dwordx4 v247, s[24:27], s72 offen lds\n"
    "v_mfma_f32_16x16x32_bf16 a[48:51], a[176:179], a[192:195], a[48:51]\n"
    "ds_read_b64_tr_b16 a[224:225], v249 offset:1024\n"
    "v_mfma_f32_16x16x32_bf16 a[48:51], a[180:183], a[196:199], a[48:51]\n"
    "ds_read_b64_tr_b16 a[226:227], v249 offset:10240\n"
    "v_mfma_f32_16x16x32_bf16 a[52:55], a[176:179], a[200:203], a[52:55]\n"
    "ds_read_b64_tr_b16 a[228:229], v249 offset:19456\n"
    "v_mfma_f32_16x16x32_bf16 a[52:55], a[180:183], a[204:207], a[52:55]\n"
    "ds_read_b64_tr_b16 a[230:231], v249 offset:28672\n"
    "v_mfma_f32_16x16x32_bf16 a[56:59], a[176:179], a[208:211], a[56:59]\n"
    "s_add_u32 m0, s83, 71680\n"
    "v_mfma_f32_16x16x32_bf16 a[56:59], a[180:183], a[212:215], a[56:59]\n"
    "s_add_u32 s72, s9, 73984\n"
    "buffer_load_dwordx4 v247, s[24:27], s72 offen lds\n"
    "v_mfma_f32_16x16x32_bf16 a[60:63], a[176:179], a[216:219], a[60:63]\n"
    "s_add_u32 m0, s83, 72704\n"
    "v_mfma_f32_16x16x32_bf16 a[60:63], a[180:183], a[220:223], a[60:63]\n"
    "s_waitcnt lgkmcnt(4)\n"
    "s_add_u32 s72, s9, 74112\n"
    "buffer_load_dwordx4 v247, s[24:27], s72 offen lds\n"
    "v_mfma_f32_16x16x32_bf16 a[64:67], a[184:187], a[192:195], a[64:67]\n"
    "ds_read_b64_tr_b16 a[176:177], v248 offset:1536\n"
    "v_mfma_f32_16x16x32_bf16 a[64:67], a[188:191], a[196:199], a[64:67]\n"
    "ds_read_b64_tr_b16 a[178:179], v248 offset:10752\n"
    "v_mfma_f32_16x16x32_bf16 a[68:71], a[184:187], a[200:203], a[68:71]\n"
    "ds_read_b64_tr_b16 a[180:181], v248 offset:19968\n"
    "v_mfma_f32_16x16x32_bf16 a[68:71], a[188:191], a[204:207], a[68:71]\n"
    "ds_read_b64_tr_b16 a[182:183], v248 offset:29184\n"
    "v_mfma_f32_16x16x32_bf16 a[72:75], a[184:187], a[208:211], a[72:75]\n"
    "s_add_u32 m0, s66, 69632\n"
    "v_mfma_f32_16x16x32_bf16 a[72:75], a[188:191], a[212:215], a[72:75]\n"
    "s_add_u32 s72, s9, 73728\n"
    "s_mov_b32 exec_hi, 0\n"
    "buffer_load_dwordx4 v245, s[24:27], s72 offen lds\n"
    "s_mov_b32 exec_hi, -1\n"
    "v_mfma_f32_16x16x32_bf16 a[76:79], a[184:187], a[216:219], a[76:79]\n"
    "s_add_u32 m0, s83, 74240\n"
    "v_mfma_f32_16x16x32_bf16 a[76:79], a[188:191], a[220:223], a[76:79]\n"
    "s_waitcnt lgkmcnt(4)\n"
    "s_add_u32 s72, s9, 82944\n"
    "buffer_load_dwordx4 v247, s[24:27], s72 offen lds\n"
    "v_mfma_f32_16x16x32_bf16 a[80:83], a[224:227], a[192:195], a[80:83]\n"
    "ds_read_b64_tr_b16 a[184:185], v249 offset:1536\n"
    "v_mfma_f32_16x16x32_bf16 a[80:83], a[228:231], a[196:199], a[80:83]\n"
    "ds_read_b64_tr_b16 a[186:187], v249 offset:10752\n"
    "v_mfma_f32_16x16x32_bf16 a[84:87], a[224:227], a[200:203], a[84:87]\n"
    "ds_read_b64_tr_b16 a[188:189], v249 offset:19968\n"
    "v_mfma_f32_16x16x32_bf16 a[84:87], a[228:231], a[204:207], a[84:87]\n"
    "ds_read_b64_tr_b16 a[190:191], v249 offset:29184\n"
    "v_mfma_f32_16x16x32_bf16 a[88:91], a[224:227], a[208:211], a[88:91]\n"
    "s_add_u32 m0, s83, 75264\n"
    "v_mfma_f32_16x16x32_bf16 a[88:91], a[228:231], a[212:215], a[88:91]\n"
    "s_add_u32 s72, s9, 83072\n"
    "buffer_load_dwordx4 v247, s[24:27], s72 offen lds\n"
    "v_mfma_f32_16x16x32_bf16 a[92:95], a[224:227], a[216:219], a[92:95]\n"
    "s_add_u32 m0, s83, 76288\n"
    "v_mfma_f32_16x16x32_bf16 a[92:95], a[228:231], a[220:223], a[92:95]\n"
    "s_waitcnt lgkmcnt(4)\n"
    "s_add_u32 s72, s9, 83200\n"
    "buffer_load_dwordx4 v247, s[24:27], s72 offen lds\n"
    "v_mfma_f32_16x16x32_bf16 a[96:99], a[176:179], a[192:195], a[96:99]\n"
    "ds_read_b64_tr_b16 a[224:225], v248 offset:2048\n"
    "v_mfma_f32_16x16x32_bf16 a[96:99], a[180:183], a[196:199], a[96:99]\n"
    "ds_read_b64_tr_b16 a[226:227], v248 offset:11264\n"
    "v_mfma_f32_16x16x32_bf16 a[100:103], a[176:179], a[200:203], a[100:103]\n"
    "ds_read_b64_tr_b16 a[228:229], v248 offset:20480\n"
    "v_mfma_f32_16x16x32_bf16 a[100:103], a[180:183], a[204:207], a[100:103]\n"
    "ds_read_b64_tr_b16 a[230:231], v248 offset:29696\n"
    "v_mfma_f32_16x16x32_bf16 a[104:107], a[176:179], a[208:211], a[104:107]\n"
    "s_add_u32 m0, s83, 77312\n"
    "v_mfma_f32_16x16x32_bf16 a[104:107], a[180:183], a[212:215], a[104:107]\n"
    "s_add_u32 s72, s9, 83328\n"
    "buffer_load_dwordx4 v247, s[24:27], s72 offen lds\n"
    "v_mfma_f32_16x16x32_bf16 a[108:111], a[176:179], a[216:219], a[108:111]\n"
    "v_add_u32 v248, 4294860800, v248\n"
    "v_add_u32 v249, 4294860800, v249\n"
    "v_mfma_f32_16x16x32_bf16 a[108:111], a[180:183], a[220:223], a[108:111]\n"
    "s_waitcnt lgkmcnt(4)\n"
    "s_add_u32 m0, s66, 74240\n"
    "v_add_u32 v250, 4294860800, v250\n"
    "v_mfma_f32_16x16x32_bf16 a[112:115], a[184:187], a[192:195], a[112:115]\n"
    "s_add_u32 s72, s9, 82944\n"
    "s_mov_b32 exec_hi, 0\n"
    "buffer_load_dwordx4 v245, s[24:27], s72 offen lds\n"
    "s_mov_b32 exec_hi, -1\n"
    "v_mfma_f32_16x16x32_bf16 a[112:115], a[188:191], a[196:199], a[112:115]\n"
    "s_waitcnt vmcnt(14) lgkmcnt(0)\n"
    "s_barrier\n"
    "v_mfma_f32_16x16x32_bf16 a[116:119], a[184:187], a[200:203], a[116:119]\n"
    "ds_read_b64_tr_b16 a[176:177], v248 offset:0\n"
    "v_mfma_f32_16x16x32_bf16 a[116:119], a[188:191], a[204:207], a[116:119]\n"
    "ds_read_b64_tr_b16 a[178:179], v248 offset:9216\n"
    "v_mfma_f32_16x16x32_bf16 a[120:123], a[184:187], a[208:211], a[120:123]\n"
    "s_sub_u32 s4, 7, s10\n"
    "ds_read_b64_tr_b16 a[180:181], v248 offset:18432\n"
    "ds_read_b128 a[144:147], v250 offset:0\n"
    "v_mfma_f32_16x16x32_bf16 a[120:123], a[188:191], a[212:215], a[120:123]\n"
    "s_xor_b32 s4, s4, s11\n"
    "ds_read_b64_tr_b16 a[182:183], v248 offset:27648\n"
    "ds_read_b128 a[148:151], v250 offset:1024\n"
    "v_mfma_f32_16x16x32_bf16 a[124:127], a[184:187], a[216:219], a[124:127]\n"
    "s_add_u32 s4, s4, s12\n"
    "ds_read_b128 a[152:155], v250 offset:2048\n"
    "v_mfma_f32_16x16x32_bf16 a[124:127], a[188:191], a[220:223], a[124:127]\n"
    "s_lshl_b32 s8, s4, 12\n"
    "ds_read_b128 a[156:159], v250 offset:3072\n"
    "v_mfma_f32_16x16x32_bf16 a[128:131], a[224:227], a[192:195], a[128:131]\n"
    "s_add_u32 s8, s8, s80\n"
    "ds_read_b128 a[160:163], v250 offset:4096\n"
    "ds_read_b128 a[164:167], v250 offset:5120\n"
    "v_mfma_f32_16x16x32_bf16 a[128:131], a[228:231], a[196:199], a[128:131]\n"
    "s_mul_i32 s9, s4, 147456\n"
    "ds_read_b128 a[168:171], v250 offset:6144\n"
    "ds_read_b128 a[172:175], v250 offset:7168\n"
    "v_mfma_f32_16x16x32_bf16 a[132:135], a[224:227], a[200:203], a[132:135]\n"
    "s_add_u32 s9, s9, s81\n"
    "ds_read_b64_tr_b16 a[184:185], v249 offset:0\n"
    "v_mfma_f32_16x16x32_bf16 a[132:135], a[228:231], a[204:207], a[132:135]\n"
    "ds_read_b64_tr_b16 a[186:187], v249 offset:9216\n"
    "v_mfma_f32_16x16x32_bf16 a[136:139], a[224:227], a[208:211], a[136:139]\n"
    "ds_read_b64_tr_b16 a[188:189], v249 offset:18432\n"
    "v_mfma_f32_16x16x32_bf16 a[136:139], a[228:231], a[212:215], a[136:139]\n"
    "ds_read_b64_tr_b16 a[190:191], v249 offset:27648\n"
    "v_mfma_f32_16x16x32_bf16 a[140:143], a[224:227], a[216:219], a[140:143]\n"
    "v_mfma_f32_16x16x32_bf16 a[140:143], a[228:231], a[220:223], a[140:143]\n"
    "s_waitcnt lgkmcnt(4)\n"
    "v_mfma_f32_16x16x32_bf16 a[0:3], a[176:179], a[144:147], a[0:3]\n"
    "ds_read_b64_tr_b16 a[224:225], v248 offset:512\n"
    "v_mfma_f32_16x16x32_bf16 a[0:3], a[180:183], a[148:151], a[0:3]\n"
    "ds_read_b64_tr_b16 a[226:227], v248 offset:9728\n"
    "v_mfma_f32_16x16x32_bf16 a[4:7], a[176:179], a[152:155], a[4:7]\n"
    "ds_read_b64_tr_b16 a[228:229], v248 offset:18944\n"
    "v_mfma_f32_16x16x32_bf16 a[4:7], a[180:183], a[156:159], a[4:7]\n"
    "ds_read_b64_tr_b16 a[230:231], v248 offset:28160\n"
    "v_mfma_f32_16x16x32_bf16 a[8:11], a[176:179], a[160:163], a[8:11]\n"
    "s_add_u32 m0, s82, 106496\n"
    "v_mfma_f32_16x16x32_bf16 a[8:11], a[180:183], a[164:167], a[8:11]\n"
    "s_add_u32 s72, s8, 0\n"
    "buffer_load_dwordx4 v246, s[20:23], s72 offen sc1 lds\n"
    "v_mfma_f32_16x16x32_bf16 a[12:15], a[176:179], a[168:171], a[12:15]\n"
    "v_mfma_f32_16x16x32_bf16 a[12:15], a[180:183], a[172:175], a[12:15]\n"
    "s_waitcnt lgkmcnt(4)\n"
    "buffer_load_dwordx4 v246, s[20:23], s72 offen offset:1024 sc1 lds\n"
    "v_mfma_f32_16x16x32_bf16 a[16:19], a[184:187], a[144:147], a[16:19]\n"
    "ds_read_b64_tr_b16 a[176:177], v249 offset:512\n"
    "v_mfma_f32_16x16x32_bf16 a[16:19], a[188:191], a[148:151], a[16:19]\n"
    "ds_read_b64_tr_b16 a[178:179], v249 offset:9728\n"
    "v_mfma_f32_16x16x32_bf16 a[20:23], a[184:187], a[152:155], a[20:23]\n"
    "ds_read_b64_tr_b16 a[180:181], v249 offset:18944\n"
    "v_mfma_f32_16x16x32_bf16 a[20:23], a[188:191], a[156:159], a[20:23]\n"
    "ds_read_b64_tr_b16 a[182:183], v249 offset:28160\n"
    "v_mfma_f32_16x16x32_bf16 a[24:27], a[184:187], a[160:163], a[24:27]\n"
    "s_add_u32 m0, s82, 108544\n"
    "v_mfma_f32_16x16x32_bf16 a[24:27], a[188:191], a[164:167], a[24:27]\n"
    "s_add_u32 s72, s72, s13\n"
    "buffer_load_dwordx4 v246, s[20:23], s72 offen sc1 lds\n"
    "v_mfma_f32_16x16x32_bf16 a[28:31], a[184:187], a[168:171], a[28:31]\n"
    "v_mfma_f32_16x16x32_bf16 a[28:31], a[188:191], a[172:175], a[28:31]\n"
    "s_waitcnt lgkmcnt(4)\n"
    "buffer_load_dwordx4 v246, s[20:23], s72 offen offset:1024 sc1 lds\n"
    "v_mfma_f32_16x16x32_bf16 a[32:35], a[224:227], a[144:147], a[32:35]\n"
    "ds_read_b64_tr_b16 a[184:185], v248 offset:1024\n"
    "v_mfma_f32_16x16x32_bf16 a[32:35], a[228:231], a[148:151], a[32:35]\n"
    "ds_read_b64_tr_b16 a[186:187], v248 offset:10240\n"
    "v_mfma_f32_16x16x32_bf16 a[36:39], a[224:227], a[152:155], a[36:39]\n"
    "ds_read_b64_tr_b16 a[188:189], v248 offset:19456\n"
    "v_mfma_f32_16x16x32_bf16 a[36:39], a[228:231], a[156:159], a[36:39]\n"
    "ds_read_b64_tr_b16 a[190:191], v248 offset:28672\n"
    "v_mfma_f32_16x16x32_bf16 a[40:43], a[224:227], a[160:163], a[40:43]\n"
    "s_add_u32 m0, s83, 122880\n"
    "v_mfma_f32_16x16x32_bf16 a[40:43], a[228:231], a[164:167], a[40:43]\n"
    "s_add_u32 s72, s9, 0\n"
    "buffer_load_dwordx4 v247, s[24:27], s72 offen lds\n"
    "v_mfma_f32_16x16x32_bf16 a[44:47], a[224:227], a[168:171], a[44:47]\n"
    "s_add_u32 m0, s83, 123904\n"
    "v_mfma_f32_16x16x32_bf16 a[44:47], a[228:231], a[172:175], a[44:47]\n"
    "s_waitcnt lgkmcnt(4)\n"
    "s_add_u32 s72, s9, 128\n"
    "buffer_load_dwordx4 v247, s[24:27], s72 offen lds\n"
    "v_mfma_f32_16x16x32_bf16 a[48:51], a[176:179], a[144:147], a[48:51]\n"
    "ds_read_b64_tr_b16 a[224:225], v249 offset:1024\n"
    "v_mfma_f32_16x16x32_bf16 a[48:51], a[180:183], a[148:151], a[48:51]\n"
    "ds_read_b64_tr_b16 a[226:227], v249 offset:10240\n"
    "v_mfma_f32_16x16x32_bf16 a[52:55], a[176:179], a[152:155], a[52:55]\n"
    "ds_read_b64_tr_b16 a[228:229], v249 offset:19456\n"
    "v_mfma_f32_16x16x32_bf16 a[52:55], a[180:183], a[156:159], a[52:55]\n"
    "ds_read_b64_tr_b16 a[230:231], v249 offset:28672\n"
    "v_mfma_f32_16x16x32_bf16 a[56:59], a[176:179], a[160:163], a[56:59]\n"
    "s_add_u32 m0, s83, 124928\n"
    "v_mfma_f32_16x16x32_bf16 a[56:59], a[180:183], a[164:167], a[56:59]\n"
    "s_add_u32 s72, s9, 256\n"
    "buffer_load_dwordx4 v247, s[24:27], s72 offen lds\n"
    "v_mfma_f32_16x16x32_bf16 a[60:63], a[176:179], a[168:171], a[60:63]\n"
    "s_add_u32 m0, s83, 125952\n"
    "v_mfma_f32_16x16x32_bf16 a[60:63], a[180:183], a[172:175], a[60:63]\n"
    "s_waitcnt lgkmcnt(4)\n"
    "s_add_u32 s72, s9, 384\n"
    "buffer_load_dwordx4 v247, s[24:27], s72 offen lds\n"
    "v_mfma_f32_16x16x32_bf16 a[64:67], a[184:187], a[144:147], a[64:67]\n"
    "ds_read_b64_tr_b16 a[176:177], v248 offset:1536\n"
    "v_mfma_f32_16x16x32_bf16 a[64:67], a[188:191], a[148:151], a[64:67]\n"
    "ds_read_b64_tr_b16 a[178:179], v248 offset:10752\n"
    "v_mfma_f32_16x16x32_bf16 a[68:71], a[184:187], a[152:155], a[68:71]\n"
    "ds_read_b64_tr_b16 a[180:181], v248 offset:19968\n"
    "v_mfma_f32_16x16x32_bf16 a[68:71], a[188:191], a[156:159], a[68:71]\n"
    "ds_read_b64_tr_b16 a[182:183], v248 offset:29184\n"
    "v_mfma_f32_16x16x32_bf16 a[72:75], a[184:187], a[160:163], a[72:75]\n"
    "s_add_u32 m0, s66, 122880\n"
    "v_mfma_f32_16x16x32_bf16 a[72:75], a[188:191], a[164:167], a[72:75]\n"
    "s_add_u32 s72, s9, 0\n"
    "s_mov_b32 exec_hi, 0\n"
    "buffer_load_dwordx4 v245, s[24:27], s72 offen lds\n"
    "s_mov_b32 exec_hi, -1\n"
    "v_mfma_f32_16x16x32_bf16 a[76:79], a[184:187], a[168:171], a[76:79]\n"
    "s_add_u32 m0, s83, 127488\n"
    "v_mfma_f32_16x16x32_bf16 a[76:79], a[188:191], a[172:175], a[76:79]\n"
    "s_waitcnt lgkmcnt(4)\n"
    "s_add_u32 s72, s9, 9216\n"
    "buffer_load_dwordx4 v247, s[24:27], s72 offen lds\n"
    "v_mfma_f32_16x16x32_bf16 a[80:83], a[224:227], a[144:147], a[80:83]\n"
    "ds_read_b64_tr_b16 a[184:185], v249 offset:1536\n"
    "v_mfma_f32_16x16x32_bf16 a[80:83], a[228:231], a[148:151], a[80:83]\n"
    "ds_read_b64_tr_b16 a[186:187], v249 offset:10752\n"
    "v_mfma_f32_16x16x32_bf16 a[84:87], a[224:227], a[152:155], a[84:87]\n"
    "ds_read_b64_tr_b16 a[188:189], v249 offset:19968\n"
    "v_mfma_f32_16x16x32_bf16 a[84:87], a[228:231], a[156:159], a[84:87]\n"
    "ds_read_b64_tr_b16 a[190:191], v249 offset:29184\n"
    "v_mfma_f32_16x16x32_bf16 a[88:91], a[224:227], a[160:163], a[88:91]\n"
    "s_add_u32 m0, s83, 128512\n"
    "v_mfma_f32_16x16x32_bf16 a[88:91], a[228:231], a[164:167], a[88:91]\n"
    "s_add_u32 s72, s9, 9344\n"
    "buffer_load_dwordx4 v247, s[24:27], s72 offen lds\n"
    "v_mfma_f32_16x16x32_bf16 a[92:95], a[224:227], a[168:171], a[92:95]\n"
    "s_add_u32 m0, s83, 129536\n"
    "v_mfma_f32_16x16x32_bf16 a[92:95], a[228:231], a[172:175], a[92:95]\n"
    "s_waitcnt lgkmcnt(4)\n"
    "s_add_u32 s72, s9, 9472\n"
    "buffer_load_dwordx4 v247, s[24:27], s72 offen lds\n"
    "v_mfma_f32_16x16x32_bf16 a[96:99], a[176:179], a[144:147], a[96:99]\n"
    "ds_read_b64_tr_b16 a[224:225], v248 offset:2048\n"
    "v_mfma_f32_16x16x32_bf16 a[96:99], a[180:183], a[148:151], a[96:99]\n"
    "ds_read_b64_tr_b16 a[226:227], v248 offset:11264\n"
    "v_mfma_f32_16x16x32_bf16 a[100:103], a[176:179], a[152:155], a[100:103]\n"
    "ds_read_b64_tr_b16 a[228:229], v248 offset:20480\n"
    "v_mfma_f32_16x16x32_bf16 a[100:103], a[180:183], a[156:159], a[100:103]\n"
    "ds_read_b64_tr_b16 a[230:231], v248 offset:29696\n"
    "v_mfma_f32_16x16x32_bf16 a[104:107], a[176:179], a[160:163], a[104:107]\n"
    "s_add_u32 m0, s83, 130560\n"
    "v_mfma_f32_16x16x32_bf16 a[104:107], a[180:183], a[164:167], a[104:107]\n"
    "s_add_u32 s72, s9, 9600\n"
    "buffer_load_dwordx4 v247, s[24:27], s72 offen lds\n"
    "v_mfma_f32_16x16x32_bf16 a[108:111], a[176:179], a[168:171], a[108:111]\n"
    "v_add_u32 v248, 53248, v248\n"
    "v_add_u32 v249, 53248, v249\n"
    "v_mfma_f32_16x16x32_bf16 a[108:111], a[180:183], a[172:175], a[108:111]\n"
    "s_waitcnt lgkmcnt(4)\n"
    "s_add_u32 m0, s66, 127488\n"
    "v_add_u32 v250, 53248, v250\n"
    "v_mfma_f32_16x16x32_bf16 a[112:115], a[184:187], a[144:147], a[112:115]\n"
    "s_add_u32 s72, s9, 9216\n"
    "s_mov_b32 exec_hi, 0\n"
    "buffer_load_dwordx4 v245, s[24:27], s72 offen lds\n"
    "s_mov_b32 exec_hi, -1\n"
    "v_mfma_f32_16x16x32_bf16 a[112:115], a[188:191], a[148:151], a[112:115]\n"
    "s_waitcnt vmcnt(14) lgkmcnt(0)\n"
    "s_barrier\n"
    "v_mfma_f32_16x16x32_bf16 a[116:119], a[184:187], a[152:155], a[116:119]\n"
    "ds_read_b64_tr_b16 a[176:177], v248 offset:0\n"
    "v_mfma_f32_16x16x32_bf16 a[116:119], a[188:191], a[156:159], a[116:119]\n"
    "ds_read_b64_tr_b16 a[178:179], v248 offset:9216\n"
    "v_mfma_f32_16x16x32_bf16 a[120:123], a[184:187], a[160:163], a[120:123]\n"
    "ds_read_b64_tr_b16 a[180:181], v248 offset:18432\n"
    "ds_read_b128 a[192:195], v250 offset:0\n"
    "v_mfma_f32_16x16x32_bf16 a[120:123], a[188:191], a[164:167], a[120:123]\n"
    "ds_read_b64_tr_b16 a[182:183], v248 offset:27648\n"
    "ds_read_b128 a[196:199], v250 offset:1024\n"
    "v_mfma_f32_16x16x32_bf16 a[124:127], a[184:187], a[168:171], a[124:127]\n"
    "ds_read_b128 a[200:203], v250 offset:2048\n"
    "v_mfma_f32_16x16x32_bf16 a[124:127], a[188:191], a[172:175], a[124:127]\n"
    "ds_read_b128 a[204:207], v250 offset:3072\n"
    "v_mfma_f32_16x16x32_bf16 a[128:131], a[224:227], a[144:147], a[128:131]\n"
    "ds_read_b128 a[208:211], v250 offset:4096\n"
    "ds_read_b128 a[212:215], v250 offset:5120\n"
    "v_mfma_f32_16x16x32_bf16 a[128:131], a[228:231], a[148:151], a[128:131]\n"
    "ds_read_b128 a[216:219], v250 offset:6144\n"
    "ds_read_b128 a[220:223], v250 offset:7168\n"
    "v_mfma_f32_16x16x32_bf16 a[132:135], a[224:227], a[152:155], a[132:135]\n"
    "ds_read_b64_tr_b16 a[184:185], v249 offset:0\n"
    "v_mfma_f32_16x16x32_bf16 a[132:135], a[228:231], a[156:159], a[132:135]\n"
    "ds_read_b64_tr_b16 a[186:187], v249 offset:9216\n"
    "v_mfma_f32_16x16x32_bf16 a[136:139], a[224:227], a[160:163], a[136:139]\n"
    "ds_read_b64_tr_b16 a[188:189], v249 offset:18432\n"
    "v_mfma_f32_16x16x32_bf16 a[136:139], a[228:231], a[164:167], a[136:139]\n"
    "ds_read_b64_tr_b16 a[190:191], v249 offset:27648\n"
    "v_mfma_f32_16x16x32_bf16 a[140:143], a[224:227], a[168:171], a[140:143]\n"
    "v_mfma_f32_16x16x32_bf16 a[140:143], a[228:231], a[172:175], a[140:143]\n"
    "s_waitcnt lgkmcnt(4)\n"
    "v_mfma_f32_16x16x32_bf16 a[0:3], a[176:179], a[192:195], a[0:3]\n"
    "ds_read_b64_tr_b16 a[224:225], v248 offset:512\n"
    "v_mfma_f32_16x16x32_bf16 a[0:3], a[180:183], a[196:199], a[0:3]\n"
    "ds_read_b64_tr_b16 a[226:227], v248 offset:9728\n"
    "v_mfma_f32_16x16x32_bf16 a[4:7], a[176:179], a[200:203], a[4:7]\n"
    "ds_read_b64_tr_b16 a[228:229], v248 offset:18944\n"
    "v_mfma_f32_16x16x32_bf16 a[4:7], a[180:183], a[204:207], a[4:7]\n"
    "ds_read_b64_tr_b16 a[230:231], v248 offset:28160\n"
    "v_mfma_f32_16x16x32_bf16 a[8:11], a[176:179], a[208:211], a[8:11]\n"
    "s_add_u32 m0, s82, 0\n"
    "v_mfma_f32_16x16x32_bf16 a[8:11], a[180:183], a[212:215], a[8:11]\n"
    "s_add_u32 s72, s8, 2048\n"
    "buffer_load_dwordx4 v246, s[20:23], s72 offen sc1 lds\n"
    "v_mfma_f32_16x16x32_bf16 a[12:15], a[176:179], a[216:219], a[12:15]\n"
    "v_mfma_f32_16x16x32_bf16 a[12:15], a[180:183], a[220:223], a[12:15]\n"
    "s_waitcnt lgkmcnt(4)\n"
    "buffer_load_dwordx4 v246, s[20:23], s72 offen offset:1024 sc1 lds\n"
    "v_mfma_f32_16x16x32_bf16 a[16:19], a[184:187], a[192:195], a[16:19]\n"
    "ds_read_b64_tr_b16 a[176:177], v249 offset:512\n"
    "v_mfma_f32_16x16x32_bf16 a[16:19], a[188:191], a[196:199], a[16:19]\n"
    "ds_read_b64_tr_b16 a[178:179], v249 offset:9728\n"
    "v_mfma_f32_16x16x32_bf16 a[20:23], a[184:187], a[200:203], a[20:23]\n"
    "ds_read_b64_tr_b16 a[180:181], v249 offset:18944\n"
    "v_mfma_f32_16x16x32_bf16 a[20:23], a[188:191], a[204:207], a[20:23]\n"
    "ds_read_b64_tr_b16 a[182:183], v249 offset:28160\n"
    "v_mfma_f32_16x16x32_bf16 a[24:27], a[184:187], a[208:211], a[24:27]\n"
    "s_add_u32 m0, s82, 2048\n"
    "v_mfma_f32_16x16x32_bf16 a[24:27], a[188:191], a[212:215], a[24:27]\n"
    "s_add_u32 s72, s72, s13\n"
    "buffer_load_dwordx4 v246, s[20:23], s72 offen sc1 lds\n"
    "v_mfma_f32_16x16x32_bf16 a[28:31], a[184:187], a[216:219], a[28:31]\n"
    "v_mfma_f32_16x16x32_bf16 a[28:31], a[188:191], a[220:223], a[28:31]\n"
    "s_waitcnt lgkmcnt(4)\n"
    "buffer_load_dwordx4 v246, s[20:23], s72 offen offset:1024 sc1 lds\n"
    "v_mfma_f32_16x16x32_bf16 a[32:35], a[224:227], a[192:195], a[32:35]\n"
    "ds_read_b64_tr_b16 a[184:185], v248 offset:1024\n"
    "v_mfma_f32_16x16x32_bf16 a[32:35], a[228:231], a[196:199], a[32:35]\n"
    "ds_read_b64_tr_b16 a[186:187], v248 offset:10240\n"
    "v_mfma_f32_16x16x32_bf16 a[36:39], a[224:227], a[200:203], a[36:39]\n"
    "ds_read_b64_tr_b16 a[188:189], v248 offset:19456\n"
    "v_mfma_f32_16x16x32_bf16 a[36:39], a[228:231], a[204:207], a[36:39]\n"
    "ds_read_b64_tr_b16 a[190:191], v248 offset:28672\n"
    "v_mfma_f32_16x16x32_bf16 a[40:43], a[224:227], a[208:211], a[40:43]\n"
    "s_add_u32 m0, s83, 16384\n"
    "v_mfma_f32_16x16x32_bf16 a[40:43], a[228:231], a[212:215], a[40:43]\n"
    "s_add_u32 s72, s9, 73728\n"
    "buffer_load_dwordx4 v247, s[24:27], s72 offen lds\n"
    "v_mfma_f32_16x16x32_bf16 a[44:47], a[224:227], a[216:219], a[44:47]\n"
    "s_add_u32 m0, s83, 17408\n"
    "v_mfma_f32_16x16x32_bf16 a[44:47], a[228:231], a[220:223], a[44:47]\n"
    "s_waitcnt lgkmcnt(4)\n"
    "s_add_u32 s72, s9, 73856\n"
    "buffer_load_dwordx4 v247, s[24:27], s72 offen lds\n"
    "v_mfma_f32_16x16x32_bf16 a[48:51], a[176:179], a[192:195], a[48:51]\n"
    "ds_read_b64_tr_b16 a[224:225], v249 offset:1024\n"
    "v_mfma_f32_16x16x32_bf16 a[48:51], a[180:183], a[196:199], a[48:51]\n"
    "ds_read_b64_tr_b16 a[226:227], v249 offset:10240\n"
    "v_mfma_f32_16x16x32_bf16 a[52:55], a[176:179], a[200:203], a[52:55]\n"
    "ds_read_b64_tr_b16 a[228:229], v249 offset:19456\n"
    "v_mfma_f32_16x16x32_bf16 a[52:55], a[180:183], a[204:207], a[52:55]\n"
    "ds_read_b64_tr_b16 a[230:231], v249 offset:28672\n"
    "v_mfma_f32_16x16x32_bf16 a[56:59], a[176:179], a[208:211], a[56:59]\n"
    "s_add_u32 m0, s83, 18432\n"
    "v_mfma_f32_16x16x32_bf16 a[56:59], a[180:183], a[212:215], a[56:59]\n"
    "s_add_u32 s72, s9, 73984\n"
    "buffer_load_dwordx4 v247, s[24:27], s72 offen lds\n"
    "v_mfma_f32_16x16x32_bf16 a[60:63], a[176:179], a[216:219], a[60:63]\n"
    "s_add_u32 m0, s83, 19456\n"
    "v_mfma_f32_16x16x32_bf16 a[60:63], a[180:183], a[220:223], a[60:63]\n"
    "s_waitcnt lgkmcnt(4)\n"
    "s_add_u32 s72, s9, 74112\n"
    "buffer_load_dwordx4 v247, s[24:27], s72 offen lds\n"
    "v_mfma_f32_16x16x32_bf16 a[64:67], a[184:187], a[192:195], a[64:67]\n"
    "ds_read_b64_tr_b16 a[176:177], v248 offset:1536\n"
    "v_mfma_f32_16x16x32_bf16 a[64:67], a[188:191], a[196:199], a[64:67]\n"
    "ds_read_b64_tr_b16 a[178:179], v248 offset:10752\n"
    "v_mfma_f32_16x16x32_bf16 a[68:71], a[184:187], a[200:203], a[68:71]\n"
    "ds_read_b64_tr_b16 a[180:181], v248 offset:19968\n"
    "v_mfma_f32_16x16x32_bf16 a[68:71], a[188:191], a[204:207], a[68:71]\n"
    "ds_read_b64_tr_b16 a[182:183], v248 offset:29184\n"
    "v_mfma_f32_16x16x32_bf16 a[72:75], a[184:187], a[208:211], a[72:75]\n"
    "s_add_u32 m0, s66, 16384\n"
    "v_mfma_f32_16x16x32_bf16 a[72:75], a[188:191], a[212:215], a[72:75]\n"
    "s_add_u32 s72, s9, 73728\n"
    "s_mov_b32 exec_hi, 0\n"
    "buffer_load_dwordx4 v245, s[24:27], s72 offen lds\n"
    "s_mov_b32 exec_hi, -1\n"
    "v_mfma_f32_16x16x32_bf16 a[76:79], a[184:187], a[216:219], a[76:79]\n"
    "s_add_u32 m0, s83, 20992\n"
    "v_mfma_f32_16x16x32_bf16 a[76:79], a[188:191], a[220:223], a[76:79]\n"
    "s_waitcnt lgkmcnt(4)\n"
    "s_add_u32 s72, s9, 82944\n"
    "buffer_load_dwordx4 v247, s[24:27], s72 offen lds\n"
    "v_mfma_f32_16x16x32_bf16 a[80:83], a[224:227], a[192:195], a[80:83]\n"
    "ds_read_b64_tr_b16 a[184:185], v249 offset:1536\n"
    "v_mfma_f32_16x16x32_bf16 a[80:83], a[228:231], a[196:199], a[80:83]\n"
    "ds_read_b64_tr_b16 a[186:187], v249 offset:10752\n"
    "v_mfma_f32_16x16x32_bf16 a[84:87], a[224:227], a[200:203], a[84:87]\n"
    "ds_read_b64_tr_b16 a[188:189], v249 offset:19968\n"
    "v_mfma_f32_16x16x32_bf16 a[84:87], a[228:231], a[204:207], a[84:87]\n"
    "ds_read_b64_tr_b16 a[190:191], v249 offset:29184\n"
    "v_mfma_f32_16x16x32_bf16 a[88:91], a[224:227], a[208:211], a[88:91]\n"
    "s_add_u32 m0, s83, 22016\n"
    "v_mfma_f32_16x16x32_bf16 a[88:91], a[228:231], a[212:215], a[88:91]\n"
    "s_add_u32 s72, s9, 83072\n"
    "buffer_load_dwordx4 v247, s[24:27], s72 offen lds\n"
    "v_mfma_f32_16x16x32_bf16 a[92:95], a[224:227], a[216:219], a[92:95]\n"
    "s_add_u32 m0, s83, 23040\n"
    "v_mfma_f32_16x16x32_bf16 a[92:95], a[228:231], a[220:223], a[92:95]\n"
    "s_waitcnt lgkmcnt(4)\n"
    "s_add_u32 s72, s9, 83200\n"
    "buffer_load_dwordx4 v247, s[24:27], s72 offen lds\n"
    "v_mfma_f32_16x16x32_bf16 a[96:99], a[176:179], a[192:195], a[96:99]\n"
    "ds_read_b64_tr_b16 a[224:225], v248 offset:2048\n"
    "v_mfma_f32_16x16x32_bf16 a[96:99], a[180:183], a[196:199], a[96:99]\n"
    "ds_read_b64_tr_b16 a[226:227], v248 offset:11264\n"
    "v_mfma_f32_16x16x32_bf16 a[100:103], a[176:179], a[200:203], a[100:103]\n"
    "ds_read_b64_tr_b16 a[228:229], v248 offset:20480\n"
    "v_mfma_f32_16x16x32_bf16 a[100:103], a[180:183], a[204:207], a[100:103]\n"
    "ds_read_b64_tr_b16 a[230:231], v248 offset:29696\n"
    "v_mfma_f32_16x16x32_bf16 a[104:107], a[176:179], a[208:211], a[104:107]\n"
    "s_add_u32 m0, s83, 24064\n"
    "v_mfma_f32_16x16x32_bf16 a[104:107], a[180:183], a[212:215], a[104:107]\n"
    "s_add_u32 s72, s9, 83328\n"
    "buffer_load_dwordx4 v247, s[24:27], s72 offen lds\n"
    "v_mfma_f32_16x16x32_bf16 a[108:111], a[176:179], a[216:219], a[108:111]\n"
    "v_add_u32 v248, 53248, v248\n"
    "v_add_u32 v249, 53248, v249\n"
    "v_mfma_f32_16x16x32_bf16 a[108:111], a[180:183], a[220:223], a[108:111]\n"
    "s_waitcnt lgkmcnt(4)\n"
    "s_add_u32 m0, s66, 20992\n"
    "v_add_u32 v250, 53248, v250\n"
    "v_mfma_f32_16x16x32_bf16 a[112:115], a[184:187], a[192:195], a[112:115]\n"
    "s_add_u32 s72, s9, 82944\n"
    "s_mov_b32 exec_hi, 0\n"
    "buffer_load_dwordx4 v245, s[24:27], s72 offen lds\n"
    "s_mov_b32 exec_hi, -1\n"
    "v_mfma_f32_16x16x32_bf16 a[112:115], a[188:191], a[196:199], a[112:115]\n"
    "s_waitcnt vmcnt(14) lgkmcnt(0)\n"
    "s_barrier\n"
    "v_mfma_f32_16x16x32_bf16 a[116:119], a[184:187], a[200:203], a[116:119]\n"
    "ds_read_b64_tr_b16 a[176:177], v248 offset:0\n"
    "v_mfma_f32_16x16x32_bf16 a[116:119], a[188:191], a[204:207], a[116:119]\n"
    "ds_read_b64_tr_b16 a[178:179], v248 offset:9216\n"
    "v_mfma_f32_16x16x32_bf16 a[120:123], a[184:187], a[208:211], a[120:123]\n"
    "s_sub_u32 s4, 8, s10\n"
    "ds_read_b64_tr_b16 a[180:181], v248 offset:18432\n"
    "ds_read_b128 a[144:147], v250 offset:0\n"
    "v_mfma_f32_16x16x32_bf16 a[120:123], a[188:191], a[212:215], a[120:123]\n"
    "s_xor_b32 s4, s4, s11\n"
    "ds_read_b64_tr_b16 a[182:183], v248 offset:27648\n"
    "ds_read_b128 a[148:151], v250 offset:1024\n"
    "v_mfma_f32_16x16x32_bf16 a[124:127], a[184:187], a[216:219], a[124:127]\n"
    "s_add_u32 s4, s4, s12\n"
    "ds_read_b128 a[152:155], v250 offset:2048\n"
    "v_mfma_f32_16x16x32_bf16 a[124:127], a[188:191], a[220:223], a[124:127]\n"
    "s_lshl_b32 s8, s4, 12\n"
    "ds_read_b128 a[156:159], v250 offset:3072\n"
    "v_mfma_f32_16x16x32_bf16 a[128:131], a[224:227], a[192:195], a[128:131]\n"
    "s_add_u32 s8, s8, s80\n"
    "ds_read_b128 a[160:163], v250 offset:4096\n"
    "ds_read_b128 a[164:167], v250 offset:5120\n"
    "v_mfma_f32_16x16x32_bf16 a[128:131], a[228:231], a[196:199], a[128:131]\n"
    "s_mul_i32 s9, s4, 147456\n"
    "ds_read_b128 a[168:171], v250 offset:6144\n"
    "ds_read_b128 a[172:175], v250 offset:7168\n"
    "v_mfma_f32_16x16x32_bf16 a[132:135], a[224:227], a[200:203], a[132:135]\n"
    "s_add_u32 s9, s9, s81\n"
    "ds_read_b64_tr_b16 a[184:185], v249 offset:0\n"
    "v_mfma_f32_16x16x32_bf16 a[132:135], a[228:231], a[204:207], a[132:135]\n"
    "ds_read_b64_tr_b16 a[186:187], v249 offset:9216\n"
    "v_mfma_f32_16x16x32_bf16 a[136:139], a[224:227], a[208:211], a[136:139]\n"
    "ds_read_b64_tr_b16 a[188:189], v249 offset:18432\n"
    "v_mfma_f32_16x16x32_bf16 a[136:139], a[228:231], a[212:215], a[136:139]\n"
    "ds_read_b64_tr_b16 a[190:191], v249 offset:27648\n"
    "v_mfma_f32_16x16x32_bf16 a[140:143], a[224:227], a[216:219], a[140:143]\n"
    "v_mfma_f32_16x16x32_bf16 a[140:143], a[228:231], a[220:223], a[140:143]\n"
    "s_waitcnt lgkmcnt(4)\n"
    "v_mfma_f32_16x16x32_bf16 a[0:3], a[176:179], a[144:147], a[0:3]\n"
    "s_mul_i32 s80, s65, 73728\n"
    "s_lshr_b32 s5, s68, 1\n"
    "ds_read_b64_tr_b16 a[224:225], v248 offset:512\n"
    "v_mfma_f32_16x16x32_bf16 a[0:3], a[180:183], a[148:151], a[0:3]\n"
    "s_mul_i32 s5, s5, 4608\n"
    "s_add_u32 s80, s80, s5\n"
    "ds_read_b64_tr_b16 a[226:227], v248 offset:9728\n"
    "v_mfma_f32_16x16x32_bf16 a[4:7], a[176:179], a[152:155], a[4:7]\n"
    "s_and_b32 s5, s68, 1\n"
    "s_lshl_b32 s6, s5, 30\n"
    "ds_read_b64_tr_b16 a[228:229], v248 offset:18944\n"
    "v_mfma_f32_16x16x32_bf16 a[4:7], a[180:183], a[156:159], a[4:7]\n"
    "s_add_u32 s81, s80, s6\n"
    "s_add_u32 s81, s81, 4096\n"
    "ds_read_b64_tr_b16 a[230:231], v248 offset:28160\n"
    "v_mfma_f32_16x16x32_bf16 a[8:11], a[176:179], a[160:163], a[8:11]\n"
    "s_add_u32 m0, s82, 53248\n"
    "s_lshl_b32 s5, s5, 11\n"
    "s_add_u32 s80, s80, s5\n"
    "v_mfma_f32_16x16x32_bf16 a[8:11], a[180:183], a[164:167], a[8:11]\n"
    "s_add_u32 s72, s8, 0\n"
    "buffer_load_dwordx4 v246, s[20:23], s72 offen sc1 lds\n"
    "v_mfma_f32_16x16x32_bf16 a[12:15], a[176:179], a[168:171], a[12:15]\n"
    "s_mov_b32 s6, 1073741824\n"
    "s_and_b32 s65, s65, 31\n"
    "s_lshl_b32 s65, s65, 2\n"
    "v_mfma_f32_16x16x32_bf16 a[12:15], a[180:183], a[172:175], a[12:15]\n"
    "s_waitcnt lgkmcnt(4)\n"
    "buffer_load_dwordx4 v246, s[20:23], s72 offen offset:1024 sc1 lds\n"
    "v_mfma_f32_16x16x32_bf16 a[16:19], a[184:187], a[144:147], a[16:19]\n"
    "s_add_u32 s65, s65, s68\n"
    "s_lshl_b32 s65, s65, 7\n"
    "ds_read_b64_tr_b16 a[176:177], v249 offset:512\n"
    "v_mfma_f32_16x16x32_bf16 a[16:19], a[188:191], a[148:151], a[16:19]\n"
    "ds_read_b64_tr_b16 a[178:179], v249 offset:9728\n"
    "v_mfma_f32_16x16x32_bf16 a[20:23], a[184:187], a[152:155], a[20:23]\n"
    "ds_read_b64_tr_b16 a[180:181], v249 offset:18944\n"
    "v_mfma_f32_16x16x32_bf16 a[20:23], a[188:191], a[156:159], a[20:23]\n"
    "ds_read_b64_tr_b16 a[182:183], v249 offset:28160\n"
    "v_mfma_f32_16x16x32_bf16 a[24:27], a[184:187], a[160:163], a[24:27]\n"
    "s_add_u32 m0, s82, 55296\n"
    "v_mfma_f32_16x16x32_bf16 a[24:27], a[188:191], a[164:167], a[24:27]\n"
    "s_add_u32 s72, s72, s13\n"
    "buffer_load_dwordx4 v246, s[20:23], s72 offen sc1 lds\n"
    "v_mfma_f32_16x16x32_bf16 a[28:31], a[184:187], a[168:171], a[28:31]\n"
    "v_mfma_f32_16x16x32_bf16 a[28:31], a[188:191], a[172:175], a[28:31]\n"
    "s_waitcnt lgkmcnt(4)\n"
    "buffer_load_dwordx4 v246, s[20:23], s72 offen offset:1024 sc1 lds\n"
    "v_mfma_f32_16x16x32_bf16 a[32:35], a[224:227], a[144:147], a[32:35]\n"
    "ds_read_b64_tr_b16 a[184:185], v248 offset:1024\n"
    "v_mfma_f32_16x16x32_bf16 a[32:35], a[228:231], a[148:151], a[32:35]\n"
    "ds_read_b64_tr_b16 a[186:187], v248 offset:10240\n"
    "v_mfma_f32_16x16x32_bf16 a[36:39], a[224:227], a[152:155], a[36:39]\n"
    "ds_read_b64_tr_b16 a[188:189], v248 offset:19456\n"
    "v_mfma_f32_16x16x32_bf16 a[36:39], a[228:231], a[156:159], a[36:39]\n"
    "ds_read_b64_tr_b16 a[190:191], v248 offset:28672\n"
    "v_mfma_f32_16x16x32_bf16 a[40:43], a[224:227], a[160:163], a[40:43]\n"
    "s_add_u32 m0, s83, 69632\n"
    "v_mfma_f32_16x16x32_bf16 a[40:43], a[228:231], a[164:167], a[40:43]\n"
    "s_add_u32 s72, s9, 0\n"
    "buffer_load_dwordx4 v247, s[24:27], s72 offen lds\n"
    "v_mfma_f32_16x16x32_bf16 a[44:47], a[224:227], a[168:171], a[44:47]\n"
    "s_add_u32 m0, s83, 70656\n"
    "v_mfma_f32_16x16x32_bf16 a[44:47], a[228:231], a[172:175], a[44:47]\n"
    "s_waitcnt lgkmcnt(4)\n"
    "s_add_u32 s72, s9, 128\n"
    "buffer_load_dwordx4 v247, s[24:27], s72 offen lds\n"
    "v_mfma_f32_16x16x32_bf16 a[48:51], a[176:179], a[144:147], a[48:51]\n"
    "ds_read_b64_tr_b16 a[224:225], v249 offset:1024\n"
    "v_mfma_f32_16x16x32_bf16 a[48:51], a[180:183], a[148:151], a[48:51]\n"
    "ds_read_b64_tr_b16 a[226:227], v249 offset:10240\n"
    "v_mfma_f32_16x16x32_bf16 a[52:55], a[176:179], a[152:155], a[52:55]\n"
    "ds_read_b64_tr_b16 a[228:229], v249 offset:19456\n"
    "v_mfma_f32_16x16x32_bf16 a[52:55], a[180:183], a[156:159], a[52:55]\n"
    "ds_read_b64_tr_b16 a[230:231], v249 offset:28672\n"
    "v_mfma_f32_16x16x32_bf16 a[56:59], a[176:179], a[160:163], a[56:59]\n"
    "s_add_u32 m0, s83, 71680\n"
    "v_mfma_f32_16x16x32_bf16 a[56:59], a[180:183], a[164:167], a[56:59]\n"
    "s_add_u32 s72, s9, 256\n"
    "buffer_load_dwordx4 v247, s[24:27], s72 offen lds\n"
    "v_mfma_f32_16x16x32_bf16 a[60:63], a[176:179], a[168:171], a[60:63]\n"
    "s_add_u32 m0, s83, 72704\n"
    "v_mfma_f32_16x16x32_bf16 a[60:63], a[180:183], a[172:175], a[60:63]\n"
    "s_waitcnt lgkmcnt(4)\n"
    "s_add_u32 s72, s9, 384\n"
    "buffer_load_dwordx4 v247, s[24:27], s72 offen lds\n"
    "v_mfma_f32_16x16x32_bf16 a[64:67], a[184:187], a[144:147], a[64:67]\n"
    "ds_read_b64_tr_b16 a[176:177], v248 offset:1536\n"
    "v_mfma_f32_16x16x32_bf16 a[64:67], a[188:191], a[148:151], a[64:67]\n"
    "ds_read_b64_tr_b16 a[178:179], v248 offset:10752\n"
    "v_mfma_f32_16x16x32_bf16 a[68:71], a[184:187], a[152:155], a[68:71]\n"
    "ds_read_b64_tr_b16 a[180:181], v248 offset:19968\n"
    "v_mfma_f32_16x16x32_bf16 a[68:71], a[188:191], a[156:159], a[68:71]\n"
    "ds_read_b64_tr_b16 a[182:183], v248 offset:29184\n"
    "v_mfma_f32_16x16x32_bf16 a[72:75], a[184:187], a[160:163], a[72:75]\n"
    "s_add_u32 m0, s66, 69632\n"
    "v_mfma_f32_16x16x32_bf16 a[72:75], a[188:191], a[164:167], a[72:75]\n"
    "s_add_u32 s72, s9, 0\n"
    "s_mov_b32 exec_hi, 0\n"
    "buffer_load_dwordx4 v245, s[24:27], s72 offen lds\n"
    "s_mov_b32 exec_hi, -1\n"
    "v_mfma_f32_16x16x32_bf16 a[76:79], a[184:187], a[168:171], a[76:79]\n"
    "s_add_u32 m0, s83, 74240\n"
    "v_mfma_f32_16x16x32_bf16 a[76:79], a[188:191], a[172:175], a[76:79]\n"
    "s_waitcnt lgkmcnt(4)\n"
    "s_add_u32 s72, s9, 9216\n"
    "buffer_load_dwordx4 v247, s[24:27], s72 offen lds\n"
    "v_mfma_f32_16x16x32_bf16 a[80:83], a[224:227], a[144:147], a[80:83]\n"
    "ds_read_b64_tr_b16 a[184:185], v249 offset:1536\n"
    "v_mfma_f32_16x16x32_bf16 a[80:83], a[228:231], a[148:151], a[80:83]\n"
    "ds_read_b64_tr_b16 a[186:187], v249 offset:10752\n"
    "v_mfma_f32_16x16x32_bf16 a[84:87], a[224:227], a[152:155], a[84:87]\n"
    "ds_read_b64_tr_b16 a[188:189], v249 offset:19968\n"
    "v_mfma_f32_16x16x32_bf16 a[84:87], a[228:231], a[156:159], a[84:87]\n"
    "ds_read_b64_tr_b16 a[190:191], v249 offset:29184\n"
    "v_mfma_f32_16x16x32_bf16 a[88:91], a[224:227], a[160:163], a[88:91]\n"
    "s_add_u32 m0, s83, 75264\n"
    "v_mfma_f32_16x16x32_bf16 a[88:91], a[228:231], a[164:167], a[88:91]\n"
    "s_add_u32 s72, s9, 9344\n"
    "buffer_load_dwordx4 v247, s[24:27], s72 offen lds\n"
    "v_mfma_f32_16x16x32_bf16 a[92:95], a[224:227], a[168:171], a[92:95]\n"
    "s_add_u32 m0, s83, 76288\n"
    "v_mfma_f32_16x16x32_bf16 a[92:95], a[228:231], a[172:175], a[92:95]\n"
    "s_waitcnt lgkmcnt(4)\n"
    "s_add_u32 s72, s9, 9472\n"
    "buffer_load_dwordx4 v247, s[24:27], s72 offen lds\n"
    "v_mfma_f32_16x16x32_bf16 a[96:99], a[176:179], a[144:147], a[96:99]\n"
    "ds_read_b64_tr_b16 a[224:225], v248 offset:2048\n"
    "v_mfma_f32_16x16x32_bf16 a[96:99], a[180:183], a[148:151], a[96:99]\n"
    "ds_read_b64_tr_b16 a[226:227], v248 offset:11264\n"
    "v_mfma_f32_16x16x32_bf16 a[100:103], a[176:179], a[152:155], a[100:103]\n"
    "ds_read_b64_tr_b16 a[228:229], v248 offset:20480\n"
    "v_mfma_f32_16x16x32_bf16 a[100:103], a[180:183], a[156:159], a[100:103]\n"
    "ds_read_b64_tr_b16 a[230:231], v248 offset:29696\n"
    "v_mfma_f32_16x16x32_bf16 a[104:107], a[176:179], a[160:163], a[104:107]\n"
    "s_add_u32 m0, s83, 77312\n"
    "v_mfma_f32_16x16x32_bf16 a[104:107], a[180:183], a[164:167], a[104:107]\n"
    "s_add_u32 s72, s9, 9600\n"
    "buffer_load_dwordx4 v247, s[24:27], s72 offen lds\n"
    "v_mfma_f32_16x16x32_bf16 a[108:111], a[176:179], a[168:171], a[108:111]\n"
    "v_add_u32 v248, 4294860800, v248\n"
    "v_add_u32 v249, 4294860800, v249\n"
    "v_mfma_f32_16x16x32_bf16 a[108:111], a[180:183], a[172:175], a[108:111]\n"
    "s_waitcnt lgkmcnt(4)\n"
    "s_add_u32 m0, s66, 74240\n"
    "v_add_u32 v250, 4294860800, v250\n"
    "v_mfma_f32_16x16x32_bf16 a[112:115], a[184:187], a[144:147], a[112:115]\n"
    "s_add_u32 s72, s9, 9216\n"
    "s_mov_b32 exec_hi, 0\n"
    "buffer_load_dwordx4 v245, s[24:27], s72 offen lds\n"
    "s_mov_b32 exec_hi, -1\n"
    "v_mfma_f32_16x16x32_bf16 a[112:115], a[188:191], a[148:151], a[112:115]\n"
    "s_waitcnt vmcnt(14) lgkmcnt(0)\n"
    "s_barrier\n"
    "v_mfma_f32_16x16x32_bf16 a[116:119], a[184:187], a[152:155], a[116:119]\n"
    "ds_read_b64_tr_b16 a[176:177], v248 offset:0\n"
    "v_mfma_f32_16x16x32_bf16 a[116:119], a[188:191], a[156:159], a[116:119]\n"
    "ds_read_b64_tr_b16 a[178:179], v248 offset:9216\n"
    "v_mfma_f32_16x16x32_bf16 a[120:123], a[184:187], a[160:163], a[120:123]\n"
    "ds_read_b64_tr_b16 a[180:181], v248 offset:18432\n"
    "ds_read_b128 a[192:195], v250 offset:0\n"
    "v_mfma_f32_16x16x32_bf16 a[120:123], a[188:191], a[164:167], a[120:123]\n"
    "ds_read_b64_tr_b16 a[182:183], v248 offset:27648\n"
    "ds_read_b128 a[196:199], v250 offset:1024\n"
    "v_mfma_f32_16x16x32_bf16 a[124:127], a[184:187], a[168:171], a[124:127]\n"
    "ds_read_b128 a[200:203], v250 offset:2048\n"
    "v_mfma_f32_16x16x32_bf16 a[124:127], a[188:191], a[172:175], a[124:127]\n"
    "ds_read_b128 a[204:207], v250 offset:3072\n"
    "v_mfma_f32_16x16x32_bf16 a[128:131], a[224:227], a[144:147], a[128:131]\n"
    "ds_read_b128 a[208:211], v250 offset:4096\n"
    "ds_read_b128 a[212:215], v250 offset:5120\n"
    "v_mfma_f32_16x16x32_bf16 a[128:131], a[228:231], a[148:151], a[128:131]\n"
    "ds_read_b128 a[216:219], v250 offset:6144\n"
    "ds_read_b128 a[220:223], v250 offset:7168\n"
    "v_mfma_f32_16x16x32_bf16 a[132:135], a[224:227], a[152:155], a[132:135]\n"
    "ds_read_b64_tr_b16 a[184:185], v249 offset:0\n"
    "v_mfma_f32_16x16x32_bf16 a[132:135], a[228:231], a[156:159], a[132:135]\n"
    "ds_read_b64_tr_b16 a[186:187], v249 offset:9216\n"
    "v_mfma_f32_16x16x32_bf16 a[136:139], a[224:227], a[160:163], a[136:139]\n"
    "ds_read_b64_tr_b16 a[188:189], v249 offset:18432\n"
    "v_mfma_f32_16x16x32_bf16 a[136:139], a[228:231], a[164:167], a[136:139]\n"
    "ds_read_b64_tr_b16 a[190:191], v249 offset:27648\n"
    "v_mfma_f32_16x16x32_bf16 a[140:143], a[224:227], a[168:171], a[140:143]\n"
    "v_mfma_f32_16x16x32_bf16 a[140:143], a[228:231], a[172:175], a[140:143]\n"
    "s_waitcnt lgkmcnt(4)\n"
    "v_mfma_f32_16x16x32_bf16 a[0:3], a[176:179], a[192:195], a[0:3]\n"
    "ds_read_b64_tr_b16 a[224:225], v248 offset:512\n"
    "v_mfma_f32_16x16x32_bf16 a[0:3], a[180:183], a[196:199], a[0:3]\n"
    "ds_read_b64_tr_b16 a[226:227], v248 offset:9728\n"
    "v_mfma_f32_16x16x32_bf16 a[4:7], a[176:179], a[200:203], a[4:7]\n"
    "ds_read_b64_tr_b16 a[228:229], v248 offset:18944\n"
    "v_mfma_f32_16x16x32_bf16 a[4:7], a[180:183], a[204:207], a[4:7]\n"
    "ds_read_b64_tr_b16 a[230:231], v248 offset:28160\n"
    "v_mfma_f32_16x16x32_bf16 a[8:11], a[176:179], a[208:211], a[8:11]\n"
    "s_add_u32 m0, s82, 106496\n"
    "buffer_load_dwordx2 v[210:211], v221, s[56:59], s79 offen offset:0\n"
    "v_mfma_f32_16x16x32_bf16 a[8:11], a[180:183], a[212:215], a[8:11]\n"
    "s_add_u32 s72, s8, 2048\n"
    "buffer_load_dwordx4 v246, s[20:23], s72 offen sc1 lds\n"
    "v_mfma_f32_16x16x32_bf16 a[12:15], a[176:179], a[216:219], a[12:15]\n"
    "v_mfma_f32_16x16x32_bf16 a[12:15], a[180:183], a[220:223], a[12:15]\n"
    "s_waitcnt lgkmcnt(4)\n"
    "buffer_load_dwordx4 v246, s[20:23], s72 offen offset:1024 sc1 lds\n"
    "v_mfma_f32_16x16x32_bf16 a[16:19], a[184:187], a[192:195], a[16:19]\n"
    "ds_read_b64_tr_b16 a[176:177], v249 offset:512\n"
    "v_mfma_f32_16x16x32_bf16 a[16:19], a[188:191], a[196:199], a[16:19]\n"
    "ds_read_b64_tr_b16 a[178:179], v249 offset:9728\n"
    "v_mfma_f32_16x16x32_bf16 a[20:23], a[184:187], a[200:203], a[20:23]\n"
    "ds_read_b64_tr_b16 a[180:181], v249 offset:18944\n"
    "v_mfma_f32_16x16x32_bf16 a[20:23], a[188:191], a[204:207], a[20:23]\n"
    "ds_read_b64_tr_b16 a[182:183], v249 offset:28160\n"
    "v_mfma_f32_16x16x32_bf16 a[24:27], a[184:187], a[208:211], a[24:27]\n"
    "s_add_u32 m0, s82, 108544\n"
    "buffer_load_dwordx2 v[212:213], v221, s[56:59], s79 offen offset:32\n"
    "v_mfma_f32_16x16x32_bf16 a[24:27], a[188:191], a[212:215], a[24:27]\n"
    "s_add_u32 s72, s72, s13\n"
    "buffer_load_dwordx4 v246, s[20:23], s72 offen sc1 lds\n"
    "v_mfma_f32_16x16x32_bf16 a[28:31], a[184:187], a[216:219], a[28:31]\n"
    "v_mfma_f32_16x16x32_bf16 a[28:31], a[188:191], a[220:223], a[28:31]\n"
    "s_waitcnt lgkmcnt(4)\n"
    "buffer_load_dwordx4 v246, s[20:23], s72 offen offset:1024 sc1 lds\n"
    "v_mfma_f32_16x16x32_bf16 a[32:35], a[224:227], a[192:195], a[32:35]\n"
    "ds_read_b64_tr_b16 a[184:185], v248 offset:1024\n"
    "v_mfma_f32_16x16x32_bf16 a[32:35], a[228:231], a[196:199], a[32:35]\n"
    "ds_read_b64_tr_b16 a[186:187], v248 offset:10240\n"
    "v_mfma_f32_16x16x32_bf16 a[36:39], a[224:227], a[200:203], a[36:39]\n"
    "ds_read_b64_tr_b16 a[188:189], v248 offset:19456\n"
    "v_mfma_f32_16x16x32_bf16 a[36:39], a[228:231], a[204:207], a[36:39]\n"
    "ds_read_b64_tr_b16 a[190:191], v248 offset:28672\n"
    "v_mfma_f32_16x16x32_bf16 a[40:43], a[224:227], a[208:211], a[40:43]\n"
    "s_add_u32 m0, s83, 122880\n"
    "buffer_load_dwordx2 v[214:215], v221, s[56:59], s79 offen offset:64\n"
    "v_mfma_f32_16x16x32_bf16 a[40:43], a[228:231], a[212:215], a[40:43]\n"
    "s_add_u32 s72, s9, 73728\n"
    "buffer_load_dwordx4 v247, s[24:27], s72 offen lds\n"
    "v_mfma_f32_16x16x32_bf16 a[44:47], a[224:227], a[216:219], a[44:47]\n"
    "s_add_u32 m0, s83, 123904\n"
    "v_mfma_f32_16x16x32_bf16 a[44:47], a[228:231], a[220:223], a[44:47]\n"
    "s_waitcnt lgkmcnt(4)\n"
    "s_add_u32 s72, s9, 73856\n"
    "buffer_load_dwordx4 v247, s[24:27], s72 offen lds\n"
    "v_mfma_f32_16x16x32_bf16 a[48:51], a[176:179], a[192:195], a[48:51]\n"
    "ds_read_b64_tr_b16 a[224:225], v249 offset:1024\n"
    "v_mfma_f32_16x16x32_bf16 a[48:51], a[180:183], a[196:199], a[48:51]\n"
    "ds_read_b64_tr_b16 a[226:227], v249 offset:10240\n"
    "v_mfma_f32_16x16x32_bf16 a[52:55], a[176:179], a[200:203], a[52:55]\n"
    "ds_read_b64_tr_b16 a[228:229], v249 offset:19456\n"
    "v_mfma_f32_16x16x32_bf16 a[52:55], a[180:183], a[204:207], a[52:55]\n"
    "ds_read_b64_tr_b16 a[230:231], v249 offset:28672\n"
    "v_mfma_f32_16x16x32_bf16 a[56:59], a[176:179], a[208:211], a[56:59]\n"
    "s_add_u32 m0, s83, 124928\n"
    "buffer_load_dwordx2 v[216:217], v221, s[56:59], s79 offen offset:96\n"
    "v_mfma_f32_16x16x32_bf16 a[56:59], a[180:183], a[212:215], a[56:59]\n"
    "s_add_u32 s72, s9, 73984\n"
    "buffer_load_dwordx4 v247, s[24:27], s72 offen lds\n"
    "v_mfma_f32_16x16x32_bf16 a[60:63], a[176:179], a[216:219], a[60:63]\n"
    "s_add_u32 m0, s83, 125952\n"
    "v_mfma_f32_16x16x32_bf16 a[60:63], a[180:183], a[220:223], a[60:63]\n"
    "s_waitcnt lgkmcnt(4)\n"
    "s_add_u32 s72, s9, 74112\n"
    "buffer_load_dwordx4 v247, s[24:27], s72 offen lds\n"
    "v_mfma_f32_16x16x32_bf16 a[64:67], a[184:187], a[192:195], a[64:67]\n"
    "ds_read_b64_tr_b16 a[176:177], v248 offset:1536\n"
    "v_mfma_f32_16x16x32_bf16 a[64:67], a[188:191], a[196:199], a[64:67]\n"
    "ds_read_b64_tr_b16 a[178:179], v248 offset:10752\n"
    "v_mfma_f32_16x16x32_bf16 a[68:71], a[184:187], a[200:203], a[68:71]\n"
    "ds_read_b64_tr_b16 a[180:181], v248 offset:19968\n"
    "v_mfma_f32_16x16x32_bf16 a[68:71], a[188:191], a[204:207], a[68:71]\n"
    "ds_read_b64_tr_b16 a[182:183], v248 offset:29184\n"
    "v_mfma_f32_16x16x32_bf16 a[72:75], a[184:187], a[208:211], a[72:75]\n"
    "s_add_u32 m0, s66, 122880\n"
    "buffer_load_dwordx2 v[218:219], v221, s[56:59], s78 offen\n"
    "v_mfma_f32_16x16x32_bf16 a[72:75], a[188:191], a[212:215], a[72:75]\n"
    "s_add_u32 s72, s9, 73728\n"
    "s_mov_b32 exec_hi, 0\n"
    "buffer_load_dwordx4 v245, s[24:27], s72 offen lds\n"
    "s_mov_b32 exec_hi, -1\n"
    "v_mfma_f32_16x16x32_bf16 a[76:79], a[184:187], a[216:219], a[76:79]\n"
    "s_add_u32 m0, s83, 127488\n"
    "v_mfma_f32_16x16x32_bf16 a[76:79], a[188:191], a[220:223], a[76:79]\n"
    "s_waitcnt lgkmcnt(4)\n"
    "s_add_u32 s72, s9, 82944\n"
    "buffer_load_dwordx4 v247, s[24:27], s72 offen lds\n"
    "v_mfma_f32_16x16x32_bf16 a[80:83], a[224:227], a[192:195], a[80:83]\n"
    "ds_read_b64_tr_b16 a[184:185], v249 offset:1536\n"
    "v_mfma_f32_16x16x32_bf16 a[80:83], a[228:231], a[196:199], a[80:83]\n"
    "ds_read_b64_tr_b16 a[186:187], v249 offset:10752\n"
    "v_mfma_f32_16x16x32_bf16 a[84:87], a[224:227], a[200:203], a[84:87]\n"
    "ds_read_b64_tr_b16 a[188:189], v249 offset:19968\n"
    "v_mfma_f32_16x16x32_bf16 a[84:87], a[228:231], a[204:207], a[84:87]\n"
    "ds_read_b64_tr_b16 a[190:191], v249 offset:29184\n"
    "v_mfma_f32_16x16x32_bf16 a[88:91], a[224:227], a[208:211], a[88:91]\n"
    "s_add_u32 m0, s83, 128512\n"
    "v_mfma_f32_16x16x32_bf16 a[88:91], a[228:231], a[212:215], a[88:91]\n"
    "s_add_u32 s72, s9, 83072\n"
    "buffer_load_dwordx4 v247, s[24:27], s72 offen lds\n"
    "v_mfma_f32_16x16x32_bf16 a[92:95], a[224:227], a[216:219], a[92:95]\n"
    "s_add_u32 m0, s83, 129536\n"
    "v_mfma_f32_16x16x32_bf16 a[92:95], a[228:231], a[220:223], a[92:95]\n"
    "s_waitcnt lgkmcnt(4)\n"
    "s_add_u32 s72, s9, 83200\n"
    "buffer_load_dwordx4 v247, s[24:27], s72 offen lds\n"
    "v_mfma_f32_16x16x32_bf16 a[96:99], a[176:179], a[192:195], a[96:99]\n"
    "ds_read_b64_tr_b16 a[224:225], v248 offset:2048\n"
    "v_mfma_f32_16x16x32_bf16 a[96:99], a[180:183], a[196:199], a[96:99]\n"
    "ds_read_b64_tr_b16 a[226:227], v248 offset:11264\n"
    "v_mfma_f32_16x16x32_bf16 a[100:103], a[176:179], a[200:203], a[100:103]\n"
    "ds_read_b64_tr_b16 a[228:229], v248 offset:20480\n"
    "v_mfma_f32_16x16x32_bf16 a[100:103], a[180:183], a[204:207], a[100:103]\n"
    "ds_read_b64_tr_b16 a[230:231], v248 offset:29696\n"
    "v_mfma_f32_16x16x32_bf16 a[104:107], a[176:179], a[208:211], a[104:107]\n"
    "s_add_u32 m0, s83, 130560\n"
    "v_mfma_f32_16x16x32_bf16 a[104:107], a[180:183], a[212:215], a[104:107]\n"
    "s_add_u32 s72, s9, 83328\n"
    "buffer_load_dwordx4 v247, s[24:27], s72 offen lds\n"
    "v_mfma_f32_16x16x32_bf16 a[108:111], a[176:179], a[216:219], a[108:111]\n"
    "v_add_u32 v248, 53248, v248\n"
    "v_add_u32 v249, 53248, v249\n"
    "v_mfma_f32_16x16x32_bf16 a[108:111], a[180:183], a[220:223], a[108:111]\n"
    "s_waitcnt lgkmcnt(4)\n"
    "s_add_u32 m0, s66, 127488\n"
    "v_add_u32 v250, 53248, v250\n"
    "v_mfma_f32_16x16x32_bf16 a[112:115], a[184:187], a[192:195], a[112:115]\n"
    "s_add_u32 s72, s9, 82944\n"
    "s_mov_b32 exec_hi, 0\n"
    "buffer_load_dwordx4 v245, s[24:27], s72 offen lds\n"
    "s_mov_b32 exec_hi, -1\n"
    "v_mfma_f32_16x16x32_bf16 a[112:115], a[188:191], a[196:199], a[112:115]\n"
    "s_waitcnt vmcnt(19) lgkmcnt(0)\n"
    "s_barrier\n"
    "v_mfma_f32_16x16x32_bf16 a[116:119], a[184:187], a[200:203], a[116:119]\n"
    "ds_read_b64_tr_b16 a[176:177], v248 offset:0\n"
    "v_mfma_f32_16x16x32_bf16 a[116:119], a[188:191], a[204:207], a[116:119]\n"
    "ds_read_b64_tr_b16 a[178:179], v248 offset:9216\n"
    "v_mfma_f32_16x16x32_bf16 a[120:123], a[184:187], a[208:211], a[120:123]\n"
    "ds_read_b64_tr_b16 a[180:181], v248 offset:18432\n"
    "ds_read_b128 a[144:147], v250 offset:0\n"
    "v_mfma_f32_16x16x32_bf16 a[120:123], a[188:191], a[212:215], a[120:123]\n"
    "ds_read_b64_tr_b16 a[182:183], v248 offset:27648\n"
    "ds_read_b128 a[148:151], v250 offset:1024\n"
    "v_mfma_f32_16x16x32_bf16 a[124:127], a[184:187], a[216:219], a[124:127]\n"
    "ds_read_b128 a[152:155], v250 offset:2048\n"
    "v_mfma_f32_16x16x32_bf16 a[124:127], a[188:191], a[220:223], a[124:127]\n"
    "ds_read_b128 a[156:159], v250 offset:3072\n"
    "v_mfma_f32_16x16x32_bf16 a[128:131], a[224:227], a[192:195], a[128:131]\n"
    "ds_read_b128 a[160:163], v250 offset:4096\n"
    "ds_read_b128 a[164:167], v250 offset:5120\n"
    "v_mfma_f32_16x16x32_bf16 a[128:131], a[228:231], a[196:199], a[128:131]\n"
    "ds_read_b128 a[168:171], v250 offset:6144\n"
    "ds_read_b128 a[172:175], v250 offset:7168\n"
    "v_mfma_f32_16x16x32_bf16 a[132:135], a[224:227], a[200:203], a[132:135]\n"
    "ds_read_b64_tr_b16 a[184:185], v249 offset:0\n"
    "v_mfma_f32_16x16x32_bf16 a[132:135], a[228:231], a[204:207], a[132:135]\n"
    "ds_read_b64_tr_b16 a[186:187], v249 offset:9216\n"
    "v_mfma_f32_16x16x32_bf16 a[136:139], a[224:227], a[208:211], a[136:139]\n"
    "ds_read_b64_tr_b16 a[188:189], v249 offset:18432\n"
    "v_mfma_f32_16x16x32_bf16 a[136:139], a[228:231], a[212:215], a[136:139]\n"
    "ds_read_b64_tr_b16 a[190:191], v249 offset:27648\n"
    "v_mfma_f32_16x16x32_bf16 a[140:143], a[224:227], a[216:219], a[140:143]\n"
    "v_mfma_f32_16x16x32_bf16 a[140:143], a[228:231], a[220:223], a[140:143]\n"
    "s_waitcnt lgkmcnt(4)\n"
    "v_mfma_f32_16x16x32_bf16 a[0:3], a[176:179], a[144:147], a[0:3]\n"
    "ds_read_b64_tr_b16 a[224:225], v248 offset:512\n"
    "v_mfma_f32_16x16x32_bf16 a[0:3], a[180:183], a[148:151], a[0:3]\n"
    "ds_read_b64_tr_b16 a[226:227], v248 offset:9728\n"
    "v_mfma_f32_16x16x32_bf16 a[4:7], a[176:179], a[152:155], a[4:7]\n"
    "ds_read_b64_tr_b16 a[228:229], v248 offset:18944\n"
    "v_mfma_f32_16x16x32_bf16 a[4:7], a[180:183], a[156:159], a[4:7]\n"
    "ds_read_b64_tr_b16 a[230:231], v248 offset:28160\n"
    "v_mfma_f32_16x16x32_bf16 a[8:11], a[176:179], a[160:163], a[8:11]\n"
    "v_add_u32 v254, 53248, v248\n"
    "v_add_u32 v255, 53248, v249\n"
    "v_mfma_f32_16x16x32_bf16 a[8:11], a[180:183], a[164:167], a[8:11]\n"
    "v_add_u32 v250, 53248, v250\n"
    "v_mfma_f32_16x16x32_bf16 a[12:15], a[176:179], a[168:171], a[12:15]\n"
    "v_mfma_f32_16x16x32_bf16 a[12:15], a[180:183], a[172:175], a[12:15]\n"
    "s_waitcnt lgkmcnt(4)\n"
    "v_mfma_f32_16x16x32_bf16 a[16:19], a[184:187], a[144:147], a[16:19]\n"
    "ds_read_b64_tr_b16 a[176:177], v249 offset:512\n"
    "v_mfma_f32_16x16x32_bf16 a[16:19], a[188:191], a[148:151], a[16:19]\n"
    "ds_read_b64_tr_b16 a[178:179], v249 offset:9728\n"
    "v_mfma_f32_16x16x32_bf16 a[20:23], a[184:187], a[152:155], a[20:23]\n"
    "ds_read_b64_tr_b16 a[180:181], v249 offset:18944\n"
    "v_mfma_f32_16x16x32_bf16 a[20:23], a[188:191], a[156:159], a[20:23]\n"
    "ds_read_b64_tr_b16 a[182:183], v249 offset:28160\n"
    "v_mfma_f32_16x16x32_bf16 a[24:27], a[184:187], a[160:163], a[24:27]\n"
    "v_mfma_f32_16x16x32_bf16 a[24:27], a[188:191], a[164:167], a[24:27]\n"
    "v_mfma_f32_16x16x32_bf16 a[28:31], a[184:187], a[168:171], a[28:31]\n"
    "v_mfma_f32_16x16x32_bf16 a[28:31], a[188:191], a[172:175], a[28:31]\n"
    "s_waitcnt lgkmcnt(4)\n"
    "v_mfma_f32_16x16x32_bf16 a[32:35], a[224:227], a[144:147], a[32:35]\n"
    "v_mfma_f32_16x16x32_bf16 a[32:35], a[228:231], a[148:151], a[32:35]\n"
    "s_waitcnt vmcnt(0)\n"
    "s_barrier\n"
    "v_mfma_f32_16x16x32_bf16 a[36:39], a[224:227], a[152:155], a[36:39]\n"
    "ds_read_b64_tr_b16 a[184:185], v254 offset:0\n"
    "v_mfma_f32_16x16x32_bf16 a[36:39], a[228:231], a[156:159], a[36:39]\n"
    "ds_read_b64_tr_b16 a[186:187], v254 offset:9216\n"
    "v_mfma_f32_16x16x32_bf16 a[40:43], a[224:227], a[160:163], a[40:43]\n"
    "ds_read_b64_tr_b16 a[188:189], v254 offset:18432\n"
    "v_mfma_f32_16x16x32_bf16 a[40:43], a[228:231], a[164:167], a[40:43]\n"
    "ds_read_b64_tr_b16 a[190:191], v254 offset:27648\n"
    "v_mfma_f32_16x16x32_bf16 a[44:47], a[224:227], a[168:171], a[44:47]\n"
    "ds_read_b128 a[192:195], v250 offset:0\n"
    "ds_read_b128 a[196:199], v250 offset:1024\n"
    "v_mfma_f32_16x16x32_bf16 a[44:47], a[228:231], a[172:175], a[44:47]\n"
    "s_waitcnt lgkmcnt(6)\n"
    "ds_read_b128 a[200:203], v250 offset:2048\n"
    "ds_read_b128 a[204:207], v250 offset:3072\n"
    "v_mfma_f32_16x16x32_bf16 a[48:51], a[176:179], a[144:147], a[48:51]\n"
    "ds_read_b64_tr_b16 a[224:225], v255 offset:0\n"
    "v_mfma_f32_16x16x32_bf16 a[48:51], a[180:183], a[148:151], a[48:51]\n"
    "ds_read_b64_tr_b16 a[226:227], v255 offset:9216\n"
    "v_mfma_f32_16x16x32_bf16 a[52:55], a[176:179], a[152:155], a[52:55]\n"
    "ds_read_b64_tr_b16 a[228:229], v255 offset:18432\n"
    "v_mfma_f32_16x16x32_bf16 a[52:55], a[180:183], a[156:159], a[52:55]\n"
    "ds_read_b64_tr_b16 a[230:231], v255 offset:27648\n"
    "v_mfma_f32_16x16x32_bf16 a[56:59], a[176:179], a[160:163], a[56:59]\n"
    "ds_read_b128 a[208:211], v250 offset:4096\n"
    "ds_read_b128 a[212:215], v250 offset:5120\n"
    "v_mfma_f32_16x16x32_bf16 a[56:59], a[180:183], a[164:167], a[56:59]\n"
    "ds_read_b128 a[216:219], v250 offset:6144\n"
    "ds_read_b128 a[220:223], v250 offset:7168\n"
    "v_mfma_f32_16x16x32_bf16 a[60:63], a[176:179], a[168:171], a[60:63]\n"
    "v_mfma_f32_16x16x32_bf16 a[60:63], a[180:183], a[172:175], a[60:63]\n"
    "s_waitcnt lgkmcnt(0)\n"
    "v_mfma_f32_16x16x32_bf16 a[0:3], a[184:187], a[192:195], a[0:3]\n"
    "ds_read_b64_tr_b16 a[176:177], v254 offset:512\n"
    "v_mfma_f32_16x16x32_bf16 a[0:3], a[188:191], a[196:199], a[0:3]\n"
    "ds_read_b64_tr_b16 a[178:179], v254 offset:9728\n"
    "v_mfma_f32_16x16x32_bf16 a[4:7], a[184:187], a[200:203], a[4:7]\n"
    "ds_read_b64_tr_b16 a[180:181], v254 offset:18944\n"
    "v_mfma_f32_16x16x32_bf16 a[4:7], a[188:191], a[204:207], a[4:7]\n"
    "ds_read_b64_tr_b16 a[182:183], v254 offset:28160\n"
    "v_mfma_f32_16x16x32_bf16 a[8:11], a[184:187], a[208:211], a[8:11]\n"
    "v_mfma_f32_16x16x32_bf16 a[8:11], a[188:191], a[212:215], a[8:11]\n"
    "v_mfma_f32_16x16x32_bf16 a[12:15], a[184:187], a[216:219], a[12:15]\n"
    "v_mfma_f32_16x16x32_bf16 a[12:15], a[188:191], a[220:223], a[12:15]\n"
    "s_waitcnt lgkmcnt(4)\n"
    "v_mfma_f32_16x16x32_bf16 a[16:19], a[224:227], a[192:195], a[16:19]\n"
    "ds_read_b64_tr_b16 a[184:185], v255 offset:512\n"
    "v_accvgpr_read_b32 v0, a0\n"
    "v_mfma_f32_16x16x32_bf16 a[16:19], a[228:231], a[196:199], a[16:19]\n"
    "ds_read_b64_tr_b16 a[186:187], v255 offset:9728\n"
    "v_accvgpr_read_b32 v1, a1\n"
    "v_mfma_f32_16x16x32_bf16 a[20:23], a[224:227], a[200:203], a[20:23]\n"
    "ds_read_b64_tr_b16 a[188:189], v255 offset:18944\n"
    "v_accvgpr_read_b32 v2, a2\n"
    "v_mfma_f32_16x16x32_bf16 a[20:23], a[228:231], a[204:207], a[20:23]\n"
    "ds_read_b64_tr_b16 a[190:191], v255 offset:28160\n"
    "v_accvgpr_read_b32 v3, a3\n"
    "v_mfma_f32_16x16x32_bf16 a[24:27], a[224:227], a[208:211], a[24:27]\n"
    "v_cvt_pk_bf16_f32 v0, v0, v1\n"
    "v_cvt_pk_bf16_f32 v1, v2, v3\n"
    "v_mfma_f32_16x16x32_bf16 a[24:27], a[228:231], a[212:215], a[24:27]\n"
    "v_mfma_f32_16x16x32_bf16 a[28:31], a[224:227], a[216:219], a[28:31]\n"
    "v_accvgpr_read_b32 v4, a16\n"
    "v_accvgpr_read_b32 v5, a17\n"
    "v_mfma_f32_16x16x32_bf16 a[28:31], a[228:231], a[220:223], a[28:31]\n"
    "s_waitcnt lgkmcnt(4)\n"
    "v_accvgpr_read_b32 v6, a18\n"
    "v_accvgpr_read_b32 v7, a19\n"
    "v_mfma_f32_16x16x32_bf16 a[32:35], a[176:179], a[192:195], a[32:35]\n"
    "ds_read_b64_tr_b16 a[224:225], v248 offset:1024\n"
    "v_cvt_pk_bf16_f32 v2, v4, v5\n"
    "v_mfma_f32_16x16x32_bf16 a[32:35], a[180:183], a[196:199], a[32:35]\n"
    "ds_read_b64_tr_b16 a[226:227], v248 offset:10240\n"
    "v_cvt_pk_bf16_f32 v3, v6, v7\n"
    "v_mfma_f32_16x16x32_bf16 a[36:39], a[176:179], a[200:203], a[36:39]\n"
    "s_add_u32 s86, s64, 0\n"
    "ds_read_b64_tr_b16 a[228:229], v248 offset:19456\n"
    "v_mfma_f32_16x16x32_bf16 a[36:39], a[180:183], a[204:207], a[36:39]\n"
    "ds_read_b64_tr_b16 a[230:231], v248 offset:28672\n"
    "v_mfma_f32_16x16x32_bf16 a[40:43], a[176:179], a[208:211], a[40:43]\n"
    "buffer_store_dwordx4 v[0:3], v246, s[28:31], s86 offen offset:0 sc1\n"
    "v_mfma_f32_16x16x32_bf16 a[40:43], a[180:183], a[212:215], a[40:43]\n"
    "v_accvgpr_read_b32 v8, a4\n"
    "v_accvgpr_read_b32 v9, a5\n"
    "v_mfma_f32_16x16x32_bf16 a[44:47], a[176:179], a[216:219], a[44:47]\n"
    "v_accvgpr_read_b32 v10, a6\n"
    "v_accvgpr_read_b32 v11, a7\n"
    "v_mfma_f32_16x16x32_bf16 a[44:47], a[180:183], a[220:223], a[44:47]\n"
    "s_waitcnt lgkmcnt(4)\n"
    "v_cvt_pk_bf16_f32 v8, v8, v9\n"
    "v_cvt_pk_bf16_f32 v9, v10, v11\n"
    "v_mfma_f32_16x16x32_bf16 a[48:51], a[184:187], a[192:195], a[48:51]\n"
    "ds_read_b64_tr_b16 a[176:177], v254 offset:1024\n"
    "v_accvgpr_read_b32 v12, a20\n"
    "v_mfma_f32_16x16x32_bf16 a[48:51], a[188:191], a[196:199], a[48:51]\n"
    "ds_read_b64_tr_b16 a[178:179], v254 offset:10240\n"
    "v_accvgpr_read_b32 v13, a21\n"
    "v_mfma_f32_16x16x32_bf16 a[52:55], a[184:187], a[200:203], a[52:55]\n"
    "ds_read_b64_tr_b16 a[180:181], v254 offset:19456\n"
    "v_accvgpr_read_b32 v14, a22\n"
    "v_mfma_f32_16x16x32_bf16 a[52:55], a[188:191], a[204:207], a[52:55]\n"
    "ds_read_b64_tr_b16 a[182:183], v254 offset:28672\n"
    "v_accvgpr_read_b32 v15, a23\n"
    "v_mfma_f32_16x16x32_bf16 a[56:59], a[184:187], a[208:211], a[56:59]\n"
    "v_cvt_pk_bf16_f32 v10, v12, v13\n"
    "v_cvt_pk_bf16_f32 v11, v14, v15\n"
    "v_mfma_f32_16x16x32_bf16 a[56:59], a[188:191], a[212:215], a[56:59]\n"
    "s_add_u32 s86, s64, 73728\n"
    "buffer_store_dwordx4 v[8:11], v246, s[28:31], s86 offen offset:0 sc1\n"
    "v_mfma_f32_16x16x32_bf16 a[60:63], a[184:187], a[216:219], a[60:63]\n"
    "v_accvgpr_read_b32 v16, a8\n"
    "v_accvgpr_read_b32 v17, a9\n"
    "v_mfma_f32_16x16x32_bf16 a[60:63], a[188:191], a[220:223], a[60:63]\n"
    "s_waitcnt lgkmcnt(4)\n"
    "v_accvgpr_read_b32 v18, a10\n"
    "v_accvgpr_read_b32 v19, a11\n"
    "v_mfma_f32_16x16x32_bf16 a[64:67], a[224:227], a[144:147], a[64:67]\n"
    "ds_read_b64_tr_b16 a[184:185], v249 offset:1024\n"
    "v_cvt_pk_bf16_f32 v16, v16, v17\n"
    "v_mfma_f32_16x16x32_bf16 a[64:67], a[228:231], a[148:151], a[64:67]\n"
    "ds_read_b64_tr_b16 a[186:187], v249 offset:10240\n"
    "v_cvt_pk_bf16_f32 v17, v18, v19\n"
    "v_mfma_f32_16x16x32_bf16 a[68:71], a[224:227], a[152:155], a[68:71]\n"
    "ds_read_b64_tr_b16 a[188:189], v249 offset:19456\n"
    "v_accvgpr_read_b32 v20, a24\n"
    "v_mfma_f32_16x16x32_bf16 a[68:71], a[228:231], a[156:159], a[68:71]\n"
    "ds_read_b64_tr_b16 a[190:191], v249 offset:28672\n"
    "v_accvgpr_read_b32 v21, a25\n"
    "v_mfma_f32_16x16x32_bf16 a[72:75], a[224:227], a[160:163], a[72:75]\n"
    "v_accvgpr_read_b32 v22, a26\n"
    "v_accvgpr_read_b32 v23, a27\n"
    "v_mfma_f32_16x16x32_bf16 a[72:75], a[228:231], a[164:167], a[72:75]\n"
    "v_cvt_pk_bf16_f32 v18, v20, v21\n"
    "v_cvt_pk_bf16_f32 v19, v22, v23\n"
    "v_mfma_f32_16x16x32_bf16 a[76:79], a[224:227], a[168:171], a[76:79]\n"
    "s_add_u32 s86, s64, 147456\n"
    "buffer_store_dwordx4 v[16:19], v246, s[28:31], s86 offen offset:0 sc1\n"
    "v_mfma_f32_16x16x32_bf16 a[76:79], a[228:231], a[172:175], a[76:79]\n"
    "s_waitcnt lgkmcnt(4)\n"
    "v_accvgpr_read_b32 v24, a12\n"
    "v_accvgpr_read_b32 v25, a13\n"
    "v_mfma_f32_16x16x32_bf16 a[64:67], a[176:179], a[192:195], a[64:67]\n"
    "ds_read_b64_tr_b16 a[224:225], v255 offset:1024\n"
    "v_accvgpr_read_b32 v26, a14\n"
    "v_mfma_f32_16x16x32_bf16 a[64:67], a[180:183], a[196:199], a[64:67]\n"
    "ds_read_b64_tr_b16 a[226:227], v255 offset:10240\n"
    "v_accvgpr_read_b32 v27, a15\n"
    "v_mfma_f32_16x16x32_bf16 a[68:71], a[176:179], a[200:203], a[68:71]\n"
    "ds_read_b64_tr_b16 a[228:229], v255 offset:19456\n"
    "v_cvt_pk_bf16_f32 v24, v24, v25\n"
    "v_mfma_f32_16x16x32_bf16 a[68:71], a[180:183], a[204:207], a[68:71]\n"
    "ds_read_b64_tr_b16 a[230:231], v255 offset:28672\n"
    "v_cvt_pk_bf16_f32 v25, v26, v27\n"
    "v_mfma_f32_16x16x32_bf16 a[72:75], a[176:179], a[208:211], a[72:75]\n"
    "v_accvgpr_read_b32 v28, a28\n"
    "v_accvgpr_read_b32 v29, a29\n"
    "v_mfma_f32_16x16x32_bf16 a[72:75], a[180:183], a[212:215], a[72:75]\n"
    "v_accvgpr_read_b32 v30, a30\n"
    "v_accvgpr_read_b32 v31, a31\n"
    "v_mfma_f32_16x16x32_bf16 a[76:79], a[176:179], a[216:219], a[76:79]\n"
    "v_cvt_pk_bf16_f32 v26, v28, v29\n"
    "v_cvt_pk_bf16_f32 v27, v30, v31\n"
    "v_mfma_f32_16x16x32_bf16 a[76:79], a[180:183], a[220:223], a[76:79]\n"
    "s_waitcnt lgkmcnt(4)\n"
    "s_add_u32 s86, s64, 221184\n"
    "v_mfma_f32_16x16x32_bf16 a[80:83], a[184:187], a[144:147], a[80:83]\n"
    "ds_read_b64_tr_b16 a[176:177], v248 offset:1536\n"
    "v_mfma_f32_16x16x32_bf16 a[80:83], a[188:191], a[148:151], a[80:83]\n"
    "ds_read_b64_tr_b16 a[178:179], v248 offset:10752\n"
    "v_mfma_f32_16x16x32_bf16 a[84:87], a[184:187], a[152:155], a[84:87]\n"
    "ds_read_b64_tr_b16 a[180:181], v248 offset:19968\n"
    "v_mfma_f32_16x16x32_bf16 a[84:87], a[188:191], a[156:159], a[84:87]\n"
    "ds_read_b64_tr_b16 a[182:183], v248 offset:29184\n"
    "v_mfma_f32_16x16x32_bf16 a[88:91], a[184:187], a[160:163], a[88:91]\n"
    "buffer_store_dwordx4 v[24:27], v246, s[28:31], s86 offen offset:0 sc1\n"
    "v_mfma_f32_16x16x32_bf16 a[88:91], a[188:191], a[164:167], a[88:91]\n"
    "v_accvgpr_read_b32 v32, a32\n"
    "v_accvgpr_read_b32 v33, a33\n"
    "v_mfma_f32_16x16x32_bf16 a[92:95], a[184:187], a[168:171], a[92:95]\n"
    "v_accvgpr_read_b32 v34, a34\n"
    "v_accvgpr_read_b32 v35, a35\n"
    "v_mfma_f32_16x16x32_bf16 a[92:95], a[188:191], a[172:175], a[92:95]\n"
    "s_waitcnt lgkmcnt(4)\n"
    "v_cvt_pk_bf16_f32 v32, v32, v33\n"
    "v_cvt_pk_bf16_f32 v33, v34, v35\n"
    "v_mfma_f32_16x16x32_bf16 a[80:83], a[224:227], a[192:195], a[80:83]\n"
    "ds_read_b64_tr_b16 a[184:185], v254 offset:1536\n"
    "v_accvgpr_read_b32 v36, a48\n"
    "v_mfma_f32_16x16x32_bf16 a[80:83], a[228:231], a[196:199], a[80:83]\n"
    "ds_read_b64_tr_b16 a[186:187], v254 offset:10752\n"
    "v_accvgpr_read_b32 v37, a49\n"
    "v_mfma_f32_16x16x32_bf16 a[84:87], a[224:227], a[200:203], a[84:87]\n"
    "ds_read_b64_tr_b16 a[188:189], v254 offset:19968\n"
    "v_accvgpr_read_b32 v38, a50\n"
    "v_mfma_f32_16x16x32_bf16 a[84:87], a[228:231], a[204:207], a[84:87]\n"
    "ds_read_b64_tr_b16 a[190:191], v254 offset:29184\n"
    "v_accvgpr_read_b32 v39, a51\n"
    "v_mfma_f32_16x16x32_bf16 a[88:91], a[224:227], a[208:211], a[88:91]\n"
    "v_cvt_pk_bf16_f32 v34, v36, v37\n"
    "v_cvt_pk_bf16_f32 v35, v38, v39\n"
    "v_mfma_f32_16x16x32_bf16 a[88:91], a[228:231], a[212:215], a[88:91]\n"
    "s_add_u32 s86, s64, 0\n"
    "buffer_store_dwordx4 v[32:35], v246, s[28:31], s86 offen offset:1024 sc1\n"
    "v_mfma_f32_16x16x32_bf16 a[92:95], a[224:227], a[216:219], a[92:95]\n"
    "v_accvgpr_read_b32 v40, a36\n"
    "v_accvgpr_read_b32 v41, a37\n"
    "v_mfma_f32_16x16x32_bf16 a[92:95], a[228:231], a[220:223], a[92:95]\n"
    "s_waitcnt lgkmcnt(4)\n"
    "v_accvgpr_read_b32 v42, a38\n"
    "v_accvgpr_read_b32 v43, a39\n"
    "v_mfma_f32_16x16x32_bf16 a[96:99], a[176:179], a[144:147], a[96:99]\n"
    "ds_read_b64_tr_b16 a[224:225], v249 offset:1536\n"
    "v_cvt_pk_bf16_f32 v40, v40, v41\n"
    "v_mfma_f32_16x16x32_bf16 a[96:99], a[180:183], a[148:151], a[96:99]\n"
    "ds_read_b64_tr_b16 a[226:227], v249 offset:10752\n"
    "v_cvt_pk_bf16_f32 v41, v42, v43\n"
    "v_mfma_f32_16x16x32_bf16 a[100:103], a[176:179], a[152:155], a[100:103]\n"
    "ds_read_b64_tr_b16 a[228:229], v249 offset:19968\n"
    "v_accvgpr_read_b32 v44, a52\n"
    "v_mfma_f32_16x16x32_bf16 a[100:103], a[180:183], a[156:159], a[100:103]\n"
    "ds_read_b64_tr_b16 a[230:231], v249 offset:29184\n"
    "v_accvgpr_read_b32 v45, a53\n"
    "v_mfma_f32_16x16x32_bf16 a[104:107], a[176:179], a[160:163], a[104:107]\n"
    "v_accvgpr_read_b32 v46, a54\n"
    "v_accvgpr_read_b32 v47, a55\n"
    "v_mfma_f32_16x16x32_bf16 a[104:107], a[180:183], a[164:167], a[104:107]\n"
    "v_cvt_pk_bf16_f32 v42, v44, v45\n"
    "v_cvt_pk_bf16_f32 v43, v46, v47\n"
    "v_mfma_f32_16x16x32_bf16 a[108:111], a[176:179], a[168:171], a[108:111]\n"
    "s_add_u32 s86, s64, 73728\n"
    "buffer_store_dwordx4 v[40:43], v246, s[28:31], s86 offen offset:1024 sc1\n"
    "v_mfma_f32_16x16x32_bf16 a[108:111], a[180:183], a[172:175], a[108:111]\n"
    "s_waitcnt lgkmcnt(4)\n"
    "v_accvgpr_read_b32 v48, a40\n"
    "v_accvgpr_read_b32 v49, a41\n"
    "v_mfma_f32_16x16x32_bf16 a[96:99], a[184:187], a[192:195], a[96:99]\n"
    "ds_read_b64_tr_b16 a[176:177], v255 offset:1536\n"
    "v_accvgpr_read_b32 v50, a42\n"
    "v_mfma_f32_16x16x32_bf16 a[96:99], a[188:191], a[196:199], a[96:99]\n"
    "ds_read_b64_tr_b16 a[178:179], v255 offset:10752\n"
    "v_accvgpr_read_b32 v51, a43\n"
    "v_mfma_f32_16x16x32_bf16 a[100:103], a[184:187], a[200:203], a[100:103]\n"
    "ds_read_b64_tr_b16 a[180:181], v255 offset:19968\n"
    "v_cvt_pk_bf16_f32 v48, v48, v49\n"
    "v_mfma_f32_16x16x32_bf16 a[100:103], a[188:191], a[204:207], a[100:103]\n"
    "ds_read_b64_tr_b16 a[182:183], v255 offset:29184\n"
    "v_cvt_pk_bf16_f32 v49, v50, v51\n"
    "v_mfma_f32_16x16x32_bf16 a[104:107], a[184:187], a[208:211], a[104:107]\n"
    "v_accvgpr_read_b32 v52, a56\n"
    "v_accvgpr_read_b32 v53, a57\n"
    "v_mfma_f32_16x16x32_bf16 a[104:107], a[188:191], a[212:215], a[104:107]\n"
    "v_accvgpr_read_b32 v54, a58\n"
    "v_accvgpr_read_b32 v55, a59\n"
    "v_mfma_f32_16x16x32_bf16 a[108:111], a[184:187], a[216:219], a[108:111]\n"
    "v_cvt_pk_bf16_f32 v50, v52, v53\n"
    "v_cvt_pk_bf16_f32 v51, v54, v55\n"
    "v_mfma_f32_16x16x32_bf16 a[108:111], a[188:191], a[220:223], a[108:111]\n"
    "s_waitcnt lgkmcnt(4)\n"
    "s_add_u32 s86, s64, 147456\n"
    "v_mfma_f32_16x16x32_bf16 a[112:115], a[224:227], a[144:147], a[112:115]\n"
    "ds_read_b64_tr_b16 a[184:185], v248 offset:2048\n"
    "v_mfma_f32_16x16x32_bf16 a[112:115], a[228:231], a[148:151], a[112:115]\n"
    "ds_read_b64_tr_b16 a[186:187], v248 offset:11264\n"
    "v_mfma_f32_16x16x32_bf16 a[116:119], a[224:227], a[152:155], a[116:119]\n"
    "ds_read_b64_tr_b16 a[188:189], v248 offset:20480\n"
    "v_mfma_f32_16x16x32_bf16 a[116:119], a[228:231], a[156:159], a[116:119]\n"
    "ds_read_b64_tr_b16 a[190:191], v248 offset:29696\n"
    "v_mfma_f32_16x16x32_bf16 a[120:123], a[224:227], a[160:163], a[120:123]\n"
    "buffer_store_dwordx4 v[48:51], v246, s[28:31], s86 offen offset:1024 sc1\n"
    "v_mfma_f32_16x16x32_bf16 a[120:123], a[228:231], a[164:167], a[120:123]\n"
    "v_accvgpr_read_b32 v56, a44\n"
    "v_accvgpr_read_b32 v57, a45\n"
    "v_mfma_f32_16x16x32_bf16 a[124:127], a[224:227], a[168:171], a[124:127]\n"
    "v_accvgpr_read_b32 v58, a46\n"
    "v_accvgpr_read_b32 v59, a47\n"
    "v_mfma_f32_16x16x32_bf16 a[124:127], a[228:231], a[172:175], a[124:127]\n"
    "s_waitcnt lgkmcnt(4)\n"
    "v_cvt_pk_bf16_f32 v56, v56, v57\n"
    "v_cvt_pk_bf16_f32 v57, v58, v59\n"
    "v_mfma_f32_16x16x32_bf16 a[112:115], a[176:179], a[192:195], a[112:115]\n"
    "ds_read_b64_tr_b16 a[224:225], v254 offset:2048\n"
    "v_accvgpr_read_b32 v60, a60\n"
    "v_mfma_f32_16x16x32_bf16 a[112:115], a[180:183], a[196:199], a[112:115]\n"
    "ds_read_b64_tr_b16 a[226:227], v254 offset:11264\n"
    "v_accvgpr_read_b32 v61, a61\n"
    "v_mfma_f32_16x16x32_bf16 a[116:119], a[176:179], a[200:203], a[116:119]\n"
    "ds_read_b64_tr_b16 a[228:229], v254 offset:20480\n"
    "v_accvgpr_read_b32 v62, a62\n"
    "v_mfma_f32_16x16x32_bf16 a[116:119], a[180:183], a[204:207], a[116:119]\n"
    "ds_read_b64_tr_b16 a[230:231], v254 offset:29696\n"
    "v_accvgpr_read_b32 v63, a63\n"
    "v_mfma_f32_16x16x32_bf16 a[120:123], a[176:179], a[208:211], a[120:123]\n"
    "v_cvt_pk_bf16_f32 v58, v60, v61\n"
    "v_cvt_pk_bf16_f32 v59, v62, v63\n"
    "v_mfma_f32_16x16x32_bf16 a[120:123], a[180:183], a[212:215], a[120:123]\n"
    "s_add_u32 s86, s64, 221184\n"
    "buffer_store_dwordx4 v[56:59], v246, s[28:31], s86 offen offset:1024 sc1\n"
    "v_mfma_f32_16x16x32_bf16 a[124:127], a[176:179], a[216:219], a[124:127]\n"
    "v_accvgpr_read_b32 v0, a64\n"
    "v_accvgpr_read_b32 v1, a65\n"
    "v_mfma_f32_16x16x32_bf16 a[124:127], a[180:183], a[220:223], a[124:127]\n"
    "s_waitcnt lgkmcnt(4)\n"
    "v_accvgpr_read_b32 v2, a66\n"
    "v_accvgpr_read_b32 v3, a67\n"
    "v_mfma_f32_16x16x32_bf16 a[128:131], a[184:187], a[144:147], a[128:131]\n"
    "v_cvt_pk_bf16_f32 v0, v0, v1\n"
    "v_cvt_pk_bf16_f32 v1, v2, v3\n"
    "v_mfma_f32_16x16x32_bf16 a[128:131], a[188:191], a[148:151], a[128:131]\n"
    "v_accvgpr_read_b32 v4, a80\n"
    "v_accvgpr_read_b32 v5, a81\n"
    "v_mfma_f32_16x16x32_bf16 a[132:135], a[184:187], a[152:155], a[132:135]\n"
    "v_accvgpr_read_b32 v6, a82\n"
    "v_accvgpr_read_b32 v7, a83\n"
    "v_mfma_f32_16x16x32_bf16 a[132:135], a[188:191], a[156:159], a[132:135]\n"
    "v_cvt_pk_bf16_f32 v2, v4, v5\n"
    "v_cvt_pk_bf16_f32 v3, v6, v7\n"
    "v_mfma_f32_16x16x32_bf16 a[136:139], a[184:187], a[160:163], a[136:139]\n"
    "s_add_u32 s86, s64, 0\n"
    "buffer_store_dwordx4 v[0:3], v246, s[28:31], s86 offen offset:2048 sc1\n"
    "v_mfma_f32_16x16x32_bf16 a[136:139], a[188:191], a[164:167], a[136:139]\n"
    "v_accvgpr_read_b32 v8, a68\n"
    "v_accvgpr_read_b32 v9, a69\n"
    "v_mfma_f32_16x16x32_bf16 a[140:143], a[184:187], a[168:171], a[140:143]\n"
    "v_accvgpr_read_b32 v10, a70\n"
    "v_accvgpr_read_b32 v11, a71\n"
    "v_mfma_f32_16x16x32_bf16 a[140:143], a[188:191], a[172:175], a[140:143]\n"
    "s_waitcnt lgkmcnt(0)\n"
    "v_cvt_pk_bf16_f32 v8, v8, v9\n"
    "v_cvt_pk_bf16_f32 v9, v10, v11\n"
    "v_mfma_f32_16x16x32_bf16 a[128:131], a[224:227], a[192:195], a[128:131]\n"
    "v_accvgpr_read_b32 v12, a84\n"
    "v_accvgpr_read_b32 v13, a85\n"
    "v_mfma_f32_16x16x32_bf16 a[128:131], a[228:231], a[196:199], a[128:131]\n"
    "v_accvgpr_read_b32 v14, a86\n"
    "v_accvgpr_read_b32 v15, a87\n"
    "v_mfma_f32_16x16x32_bf16 a[132:135], a[224:227], a[200:203], a[132:135]\n"
    "v_cvt_pk_bf16_f32 v10, v12, v13\n"
    "v_cvt_pk_bf16_f32 v11, v14, v15\n"
    "v_mfma_f32_16x16x32_bf16 a[132:135], a[228:231], a[204:207], a[132:135]\n"
    "s_add_u32 s86, s64, 73728\n"
    "buffer_store_dwordx4 v[8:11], v246, s[28:31], s86 offen offset:2048 sc1\n"
    "v_mfma_f32_16x16x32_bf16 a[136:139], a[224:227], a[208:211], a[136:139]\n"
    "v_accvgpr_read_b32 v16, a72\n"
    "v_accvgpr_read_b32 v17, a73\n"
    "v_mfma_f32_16x16x32_bf16 a[136:139], a[228:231], a[212:215], a[136:139]\n"
    "v_accvgpr_read_b32 v18, a74\n"
    "v_accvgpr_read_b32 v19, a75\n"
    "v_mfma_f32_16x16x32_bf16 a[140:143], a[224:227], a[216:219], a[140:143]\n"
    "v_cvt_pk_bf16_f32 v16, v16, v17\n"
    "v_cvt_pk_bf16_f32 v17, v18, v19\n"
    "v_mfma_f32_16x16x32_bf16 a[140:143], a[228:231], a[220:223], a[140:143]\n"
    "v_accvgpr_read_b32 v20, a88\n"
    "v_accvgpr_read_b32 v21, a89\n"
    "s_nop 4\n"
    "v_accvgpr_read_b32 v22, a90\n"
    "v_accvgpr_read_b32 v23, a91\n"
    "v_cvt_pk_bf16_f32 v18, v20, v21\n"
    "v_cvt_pk_bf16_f32 v19, v22, v23\n"
    "s_add_u32 s86, s64, 147456\n"
    "buffer_store_dwordx4 v[16:19], v246, s[28:31], s86 offen offset:2048 sc1\n"
    "v_accvgpr_read_b32 v24, a76\n"
    "v_accvgpr_read_b32 v25, a77\n"
    "v_accvgpr_read_b32 v26, a78\n"
    "v_accvgpr_read_b32 v27, a79\n"
    "v_cvt_pk_bf16_f32 v24, v24, v25\n"
    "v_cvt_pk_bf16_f32 v25, v26, v27\n"
    "v_accvgpr_read_b32 v28, a92\n"
    "v_accvgpr_read_b32 v29, a93\n"
    "v_accvgpr_read_b32 v30, a94\n"
    "v_accvgpr_read_b32 v31, a95\n"
    "v_cvt_pk_bf16_f32 v26, v28, v29\n"
    "v_cvt_pk_bf16_f32 v27, v30, v31\n"
    "s_add_u32 s86, s64, 221184\n"
    "buffer_store_dwordx4 v[24:27], v246, s[28:31], s86 offen offset:2048 sc1\n"
    "v_accvgpr_read_b32 v32, a96\n"
    "v_accvgpr_read_b32 v33, a97\n"
    "v_accvgpr_read_b32 v34, a98\n"
    "v_accvgpr_read_b32 v35, a99\n"
    "v_cvt_pk_bf16_f32 v32, v32, v33\n"
    "v_cvt_pk_bf16_f32 v33, v34, v35\n"
    "v_accvgpr_read_b32 v36, a112\n"
    "v_accvgpr_read_b32 v37, a113\n"
    "v_accvgpr_read_b32 v38, a114\n"
    "v_accvgpr_read_b32 v39, a115\n"
    "v_cvt_pk_bf16_f32 v34, v36, v37\n"
    "v_cvt_pk_bf16_f32 v35, v38, v39\n"
    "s_add_u32 s86, s64, 0\n"
    "buffer_store_dwordx4 v[32:35], v246, s[28:31], s86 offen offset:3072 sc1\n"
    "v_accvgpr_read_b32 v40, a100\n"
    "v_accvgpr_read_b32 v41, a101\n"
    "v_accvgpr_read_b32 v42, a102\n"
    "v_accvgpr_read_b32 v43, a103\n"
    "v_cvt_pk_bf16_f32 v40, v40, v41\n"
    "v_cvt_pk_bf16_f32 v41, v42, v43\n"
    "v_accvgpr_read_b32 v44, a116\n"
    "v_accvgpr_read_b32 v45, a117\n"
    "v_accvgpr_read_b32 v46, a118\n"
    "v_accvgpr_read_b32 v47, a119\n"
    "v_cvt_pk_bf16_f32 v42, v44, v45\n"
    "v_cvt_pk_bf16_f32 v43, v46, v47\n"
    "s_add_u32 s86, s64, 73728\n"
    "buffer_store_dwordx4 v[40:43], v246, s[28:31], s86 offen offset:3072 sc1\n"
    "v_accvgpr_read_b32 v48, a104\n"
    "v_accvgpr_read_b32 v49, a105\n"
    "v_accvgpr_read_b32 v50, a106\n"
    "v_accvgpr_read_b32 v51, a107\n"
    "v_cvt_pk_bf16_f32 v48, v48, v49\n"
    "v_cvt_pk_bf16_f32 v49, v50, v51\n"
    "v_accvgpr_read_b32 v52, a120\n"
    "v_accvgpr_read_b32 v53, a121\n"
    "v_accvgpr_read_b32 v54, a122\n"
    "v_accvgpr_read_b32 v55, a123\n"
    "v_cvt_pk_bf16_f32 v50, v52, v53\n"
    "v_cvt_pk_bf16_f32 v51, v54, v55\n"
    "s_add_u32 s86, s64, 147456\n"
    "buffer_store_dwordx4 v[48:51], v246, s[28:31], s86 offen offset:3072 sc1\n"
    "v_accvgpr_read_b32 v56, a108\n"
    "v_accvgpr_read_b32 v57, a109\n"
    "v_accvgpr_read_b32 v58, a110\n"
    "v_accvgpr_read_b32 v59, a111\n"
    "v_cvt_pk_bf16_f32 v56, v56, v57\n"
    "v_cvt_pk_bf16_f32 v57, v58, v59\n"
    "v_accvgpr_read_b32 v60, a124\n"
    "v_accvgpr_read_b32 v61, a125\n"
    "v_accvgpr_read_b32 v62, a126\n"
    "v_accvgpr_read_b32 v63, a127\n"
    "v_cvt_pk_bf16_f32 v58, v60, v61\n"
    "v_cvt_pk_bf16_f32 v59, v62, v63\n"
    "s_add_u32 s86, s64, 221184\n"
    "buffer_store_dwordx4 v[56:59], v246, s[28:31], s86 offen offset:3072 sc1\n"
    "v_accvgpr_read_b32 v0, a128\n"
    "v_accvgpr_read_b32 v1, a129\n"
    "v_accvgpr_read_b32 v2, a130\n"
    "v_accvgpr_read_b32 v3, a131\n"
    "v_cvt_pk_bf16_f32 v0, v0, v1\n"
    "v_cvt_pk_bf16_f32 v1, v2, v3\n"
    "s_add_u32 s86, s64, 4096\n"
    "buffer_store_dwordx2 v[0:1], v251, s[28:31], s86 offen sc1\n"
    "v_accvgpr_read_b32 v8, a132\n"
    "v_accvgpr_read_b32 v9, a133\n"
    "v_accvgpr_read_b32 v10, a134\n"
    "v_accvgpr_read_b32 v11, a135\n"
    "v_cvt_pk_bf16_f32 v8, v8, v9\n"
    "v_cvt_pk_bf16_f32 v9, v10, v11\n"
    "s_add_u32 s86, s64, 77824\n"
    "buffer_store_dwordx2 v[8:9], v251, s[28:31], s86 offen sc1\n"
    "v_accvgpr_read_b32 v16, a136\n"
    "v_accvgpr_read_b32 v17, a137\n"
    "v_accvgpr_read_b32 v18, a138\n"
    "v_accvgpr_read_b32 v19, a139\n"
    "v_cvt_pk_bf16_f32 v16, v16, v17\n"
    "v_cvt_pk_bf16_f32 v17, v18, v19\n"
    "s_add_u32 s86, s64, 151552\n"
    "buffer_store_dwordx2 v[16:17], v251, s[28:31], s86 offen sc1\n"
    "v_accvgpr_read_b32 v24, a140\n"
    "v_accvgpr_read_b32 v25, a141\n"
    "v_accvgpr_read_b32 v26, a142\n"
    "v_accvgpr_read_b32 v27, a143\n"
    "v_cvt_pk_bf16_f32 v24, v24, v25\n"
    "v_cvt_pk_bf16_f32 v25, v26, v27\n"
    "s_add_u32 s86, s64, 225280\n"
    "buffer_store_dwordx2 v[24:25], v251, s[28:31], s86 offen sc1\n"
    "buffer_load_dword v222, v252, s[52:55], s77 offen sc1\n"
    "v_lshlrev_b32 v224, 16, v210\n"
    "v_and_b32 v225, 0xffff0000, v210\n"
    "v_lshlrev_b32 v226, 16, v211\n"
    "v_and_b32 v227, 0xffff0000, v211\n"
    "v_lshlrev_b32 v228, 16, v212\n"
    "v_and_b32 v229, 0xffff0000, v212\n"
    "v_lshlrev_b32 v230, 16, v213\n"
    "v_and_b32 v231, 0xffff0000, v213\n"
    "v_lshlrev_b32 v232, 16, v214\n"
    "v_and_b32 v233, 0xffff0000, v214\n"
    "v_lshlrev_b32 v234, 16, v215\n"
    "v_and_b32 v235, 0xffff0000, v215\n"
    "v_lshlrev_b32 v236, 16, v216\n"
    "v_and_b32 v237, 0xffff0000, v216\n"
    "v_lshlrev_b32 v238, 16, v217\n"
    "v_and_b32 v239, 0xffff0000, v217\n"
    "v_lshlrev_b32 v240, 16, v218\n"
    "v_and_b32 v241, 0xffff0000, v218\n"
    "v_lshlrev_b32 v242, 16, v219\n"
    "v_and_b32 v243, 0xffff0000, v219\n"
    "s_lshr_b32 s72, s65, 9\n"
    "s_and_b32 s72, s72, 7\n"
    "s_lshl_b32 s84, 1, s72\n"
    "s_lshl_b32 s72, s72, 5\n"
    "s_getpc_b64 s[86:87]\n"
    "L_pc2:\n"
    "s_add_u32 s86, s86, s72\n"
    "s_addc_u32 s87, s87, 0\n"
    "s_mul_i32 s72, s72, 288\n"
    "s_add_u32 s4, s80, s72\n"
    "s_add_u32 s5, s81, s72\n"
    "s_add_u32 s86, s86, L_own-L_pc2\n"
    "s_addc_u32 s87, s87, 0\n"
    "s_waitcnt vmcnt(0)\n"
    "s_add_u32 s73, s65, 49152\n"
    "v_mov_b32 v220, s74\n"
    "s_mov_b64 exec, 1\n"
    "buffer_store_dword v220, off, s[52:55], s73 sc1\n"
    "s_mov_b64 exec, -1\n"
    "v_cmp_eq_u32 vcc, s74, v222\n"
    "s_mov_b32 s72, s4\n"
    "s_mov_b32 s8, s5\n"
    "s_andn2_b32 s85, vcc_lo, s84\n"
    "s_and_b32 s85, s85, 0xff\n"
    "s_or_b32 s84, s84, s85\n"
    "s_bitcmp1_b32 s85, 0\n"
    "s_cbranch_scc0 L_p1_0\n"
    "s_add_u32 s4, s80, 0\n"
    "s_add_u32 s5, s81, 0\n"
    "buffer_load_dwordx4 v[64:67], v246, s[28:31], s4 offen sc1\n"
    "buffer_load_dwordx4 v[68:71], v246, s[28:31], s4 offen offset:1024 sc1\n"
    "buffer_load_dwordx2 v[128:129], v251, s[28:31], s5 offen sc1\n"
    "L_p1_0:\n"
    "s_bitcmp1_b32 s85, 1\n"
    "s_cbranch_scc0 L_p1_1\n"
    "s_add_u32 s4, s80, 9216\n"
    "s_add_u32 s5, s81, 9216\n"
    "buffer_load_dwordx4 v[72:75], v246, s[28:31], s4 offen sc1\n"
    "buffer_load_dwordx4 v[76:79], v246, s[28:31], s4 offen offset:1024 sc1\n"
    "buffer_load_dwordx2 v[130:131], v251, s[28:31], s5 offen sc1\n"
    "L_p1_1:\n"
    "s_bitcmp1_b32 s85, 2\n"
    "s_cbranch_scc0 L_p1_2\n"
    "s_add_u32 s4, s80, 18432\n"
    "s_add_u32 s5, s81, 18432\n"
    "buffer_load_dwordx4 v[80:83], v246, s[28:31], s4 offen sc1\n"
    "buffer_load_dwordx4 v[84:87], v246, s[28:31], s4 offen offset:1024 sc1\n"
    "buffer_load_dwordx2 v[132:133], v251, s[28:31], s5 offen sc1\n"
    "L_p1_2:\n"
    "s_bitcmp1_b32 s85, 3\n"
    "s_cbranch_scc0 L_p1_3\n"
    "s_add_u32 s4, s80, 27648\n"
    "s_add_u32 s5, s81, 27648\n"
    "buffer_load_dwordx4 v[88:91], v246, s[28:31], s4 offen sc1\n"
    "buffer_load_dwordx4 v[92:95], v246, s[28:31], s4 offen offset:1024 sc1\n"
    "buffer_load_dwordx2 v[134:135], v251, s[28:31], s5 offen sc1\n"
    "L_p1_3:\n"
    "s_bitcmp1_b32 s85, 4\n"
    "s_cbranch_scc0 L_p1_4\n"
    "s_add_u32 s4, s80, 36864\n"
    "s_add_u32 s5, s81, 36864\n"
    "buffer_load_dwordx4 v[96:99], v246, s[28:31], s4 offen sc1\n"
    "buffer_load_dwordx4 v[100:103], v246, s[28:31], s4 offen offset:1024 sc1\n"
    "buffer_load_dwordx2 v[136:137], v251, s[28:31], s5 offen sc1\n"
    "L_p1_4:\n"
    "s_bitcmp1_b32 s85, 5\n"
    "s_cbranch_scc0 L_p1_5\n"
    "s_add_u32 s4, s80, 46080\n"
    "s_add_u32 s5, s81, 46080\n"
    "buffer_load_dwordx4 v[104:107], v246, s[28:31], s4 offen sc1\n"
    "buffer_load_dwordx4 v[108:111], v246, s[28:31], s4 offen offset:1024 sc1\n"
    "buffer_load_dwordx2 v[138:139], v251, s[28:31], s5 offen sc1\n"
    "L_p1_5:\n"
    "s_bitcmp1_b32 s85, 6\n"
    "s_cbranch_scc0 L_p1_6\n"
    "s_add_u32 s4, s80, 55296\n"
    "s_add_u32 s5, s81, 55296\n"
    "buffer_load_dwordx4 v[112:115], v246, s[28:31], s4 offen sc1\n"
    "buffer_load_dwordx4 v[116:119], v246, s[28:31], s4 offen offset:1024 sc1\n"
    "buffer_load_dwordx2 v[140:141], v251, s[28:31], s5 offen sc1\n"
    "L_p1_6:\n"
    "s_bitcmp1_b32 s85, 7\n"
    "s_cbranch_scc0 L_p1_7\n"
    "s_add_u32 s4, s80, 64512\n"
    "s_add_u32 s5, s81, 64512\n"
    "buffer_load_dwordx4 v[120:123], v246, s[28:31], s4 offen sc1\n"
    "buffer_load_dwordx4 v[124:127], v246, s[28:31], s4 offen offset:1024 sc1\n"
    "buffer_load_dwordx2 v[142:143], v251, s[28:31], s5 offen sc1\n"
    "L_p1_7:\n"
    "s_mov_b32 s4, s72\n"
    "s_mov_b32 s5, s8\n"
    "s_barrier\n"
    "s_setpc_b64 s[86:87]\n"
    ".p2align 5\n"
    "L_own:\n"
    "buffer_load_dwordx4 v[64:67], v246, s[28:31], s4 offen sc1\n"
    "buffer_load_dwordx4 v[68:71], v246, s[28:31], s4 offen offset:1024 sc1\n"
    "buffer_load_dwordx2 v[128:129], v251, s[28:31], s5 offen sc1\n"
    "s_branch L_own_done\n"
    "s_nop 0\n"
    "buffer_load_dwordx4 v[72:75], v246, s[28:31], s4 offen sc1\n"
    "buffer_load_dwordx4 v[76:79], v246, s[28:31], s4 offen offset:1024 sc1\n"
    "buffer_load_dwordx2 v[130:131], v251, s[28:31], s5 offen sc1\n"
    "s_branch L_own_done\n"
    "s_nop 0\n"
    "buffer_load_dwordx4 v[80:83], v246, s[28:31], s4 offen sc1\n"
    "buffer_load_dwordx4 v[84:87], v246, s[28:31], s4 offen offset:1024 sc1\n"
    "buffer_load_dwordx2 v[132:133], v251, s[28:31], s5 offen sc1\n"
    "s_branch L_own_done\n"
    "s_nop 0\n"
    "buffer_load_dwordx4 v[88:91], v246, s[28:31], s4 offen sc1\n"
    "buffer_load_dwordx4 v[92:95], v246, s[28:31], s4 offen offset:1024 sc1\n"
    "buffer_load_dwordx2 v[134:135], v251, s[28:31], s5 offen sc1\n"
    "s_branch L_own_done\n"
    "s_nop 0\n"
    "buffer_load_dwordx4 v[96:99], v246, s[28:31], s4 offen sc1\n"
    "buffer_load_dwordx4 v[100:103], v246, s[28:31], s4 offen offset:1024 sc1\n"
    "buffer_load_dwordx2 v[136:137], v251, s[28:31], s5 offen sc1\n"
    "s_branch L_own_done\n"
    "s_nop 0\n"
    "buffer_load_dwordx4 v[104:107], v246, s[28:31], s4 offen sc1\n"
    "buffer_load_dwordx4 v[108:111], v246, s[28:31], s4 offen offset:1024 sc1\n"
    "buffer_load_dwordx2 v[138:139], v251, s[28:31], s5 offen sc1\n"
    "s_branch L_own_done\n"
    "s_nop 0\n"
    "buffer_load_dwordx4 v[112:115], v246, s[28:31], s4 offen sc1\n"
    "buffer_load_dwordx4 v[116:119], v246, s[28:31], s4 offen offset:1024 sc1\n"
    "buffer_load_dwordx2 v[140:141], v251, s[28:31], s5 offen sc1\n"
    "s_branch L_own_done\n"
    "s_nop 0\n"
    "buffer_load_dwordx4 v[120:123], v246, s[28:31], s4 offen sc1\n"
    "buffer_load_dwordx4 v[124:127], v246, s[28:31], s4 offen offset:1024 sc1\n"
    "buffer_load_dwordx2 v[142:143], v251, s[28:31], s5 offen sc1\n"
    "s_branch L_own_done\n"
    "s_nop 0\n"
    "L_own_done:\n"
    "s_mov_b32 s87, 0x40000\n"
    "L_ga:\n"
    "s_and_b32 s85, s84, 15\n"
    "s_cmp_eq_u32 s85, 15\n"
    "s_cbranch_scc1 L_gda\n"
    "buffer_load_dword v220, v252, s[52:55], s77 offen sc1\n"
    "s_waitcnt vmcnt(0)\n"
    "v_cmp_eq_u32 vcc, s74, v220\n"
    "s_nop 0\n"
    "s_andn2_b32 s85, vcc_lo, s84\n"
    "s_and_b32 s85, s85, 0xff\n"
    "s_or_b32 s84, s84, s85\n"
    "s_bitcmp1_b32 s85, 0\n"
    "s_cbranch_scc0 L_gsa_0\n"
    "s_add_u32 s4, s80, 0\n"
    "s_add_u32 s5, s81, 0\n"
    "buffer_load_dwordx4 v[64:67], v246, s[28:31], s4 offen sc1\n"
    "buffer_load_dwordx4 v[68:71], v246, s[28:31], s4 offen offset:1024 sc1\n"
    "buffer_load_dwordx2 v[128:129], v251, s[28:31], s5 offen sc1\n"
    "L_gsa_0:\n"
    "s_bitcmp1_b32 s85, 1\n"
    "s_cbranch_scc0 L_gsa_1\n"
    "s_add_u32 s4, s80, 9216\n"
    "s_add_u32 s5, s81, 9216\n"
    "buffer_load_dwordx4 v[72:75], v246, s[28:31], s4 offen sc1\n"
    "buffer_load_dwordx4 v[76:79], v246, s[28:31], s4 offen offset:1024 sc1\n"
    "buffer_load_dwordx2 v[130:131], v251, s[28:31], s5 offen sc1\n"
    "L_gsa_1:\n"
    "s_bitcmp1_b32 s85, 2\n"
    "s_cbranch_scc0 L_gsa_2\n"
    "s_add_u32 s4, s80, 18432\n"
    "s_add_u32 s5, s81, 18432\n"
    "buffer_load_dwordx4 v[80:83], v246, s[28:31], s4 offen sc1\n"
    "buffer_load_dwordx4 v[84:87], v246, s[28:31], s4 offen offset:1024 sc1\n"
    "buffer_load_dwordx2 v[132:133], v251, s[28:31], s5 offen sc1\n"
    "L_gsa_2:\n"
    "s_bitcmp1_b32 s85, 3\n"
    "s_cbranch_scc0 L_gsa_3\n"
    "s_add_u32 s4, s80, 27648\n"
    "s_add_u32 s5, s81, 27648\n"
    "buffer_load_dwordx4 v[88:91], v246, s[28:31], s4 offen sc1\n"
    "buffer_load_dwordx4 v[92:95], v246, s[28:31], s4 offen offset:1024 sc1\n"
    "buffer_load_dwordx2 v[134:135], v251, s[28:31], s5 offen sc1\n"
    "L_gsa_3:\n"
    "s_bitcmp1_b32 s85, 4\n"
    "s_cbranch_scc0 L_gsa_4\n"
    "s_add_u32 s4, s80, 36864\n"
    "s_add_u32 s5, s81, 36864\n"
    "buffer_load_dwordx4 v[96:99], v246, s[28:31], s4 offen sc1\n"
    "buffer_load_dwordx4 v[100:103], v246, s[28:31], s4 offen offset:1024 sc1\n"
    "buffer_load_dwordx2 v[136:137], v251, s[28:31], s5 offen sc1\n"
    "L_gsa_4:\n"
    "s_bitcmp1_b32 s85, 5\n"
    "s_cbranch_scc0 L_gsa_5\n"
    "s_add_u32 s4, s80, 46080\n"
    "s_add_u32 s5, s81, 46080\n"
    "buffer_load_dwordx4 v[104:107], v246, s[28:31], s4 offen sc1\n"
    "buffer_load_dwordx4 v[108:111], v246, s[28:31], s4 offen offset:1024 sc1\n"
    "buffer_load_dwordx2 v[138:139], v251, s[28:31], s5 offen sc1\n"
    "L_gsa_5:\n"
    "s_bitcmp1_b32 s85, 6\n"
    "s_cbranch_scc0 L_gsa_6\n"
    "s_add_u32 s4, s80, 55296\n"
    "s_add_u32 s5, s81, 55296\n"
    "buffer_load_dwordx4 v[112:115], v246, s[28:31], s4 offen sc1\n"
    "buffer_load_dwordx4 v[116:119], v246, s[28:31], s4 offen offset:1024 sc1\n"
    "buffer_load_dwordx2 v[140:141], v251, s[28:31], s5 offen sc1\n"
    "L_gsa_6:\n"
    "s_bitcmp1_b32 s85, 7\n"
    "s_cbranch_scc0 L_gsa_7\n"
    "s_add_u32 s4, s80, 64512\n"
    "s_add_u32 s5, s81, 64512\n"
    "buffer_load_dwordx4 v[120:123], v246, s[28:31], s4 offen sc1\n"
    "buffer_load_dwordx4 v[124:127], v246, s[28:31], s4 offen offset:1024 sc1\n"
    "buffer_load_dwordx2 v[142:143], v251, s[28:31], s5 offen sc1\n"
    "L_gsa_7:\n"
    "s_sub_u32 s87, s87, 1\n"
    "s_cbranch_scc0 L_ga\n"
    "L_gda:\n"
    "s_waitcnt vmcnt(0)\n"
    "v_mfma_f32_16x16x32_bf16 v[160:163], v[202:205], v[64:67], 0\n"
    "v_mfma_f32_16x16x32_bf16 v[164:167], v[200:203], v[64:67], 0\n"
    "v_mfma_f32_16x16x32_bf16 v[168:171], v[202:205], v[68:71], 0\n"
    "v_mfma_f32_16x16x32_bf16 v[172:175], v[200:203], v[68:71], 0\n"
    "v_mfma_f32_16x16x32_bf16 v[160:163], v[202:205], v[72:75], v[160:163]\n"
    "v_mfma_f32_16x16x32_bf16 v[164:167], v[200:203], v[72:75], v[164:167]\n"
    "v_mfma_f32_16x16x32_bf16 v[168:171], v[202:205], v[76:79], v[168:171]\n"
    "v_mfma_f32_16x16x32_bf16 v[172:175], v[200:203], v[76:79], v[172:175]\n"
    "v_mfma_f32_16x16x32_bf16 v[176:179], v[206:209], v[128:131], 0\n"
    "v_mfma_f32_16x16x32_bf16 v[160:163], v[202:205], v[80:83], v[160:163]\n"
    "v_mfma_f32_16x16x32_bf16 v[164:167], v[200:203], v[80:83], v[164:167]\n"
    "v_mfma_f32_16x16x32_bf16 v[168:171], v[202:205], v[84:87], v[168:171]\n"
    "v_mfma_f32_16x16x32_bf16 v[172:175], v[200:203], v[84:87], v[172:175]\n"
    "v_mfma_f32_16x16x32_bf16 v[160:163], v[202:205], v[88:91], v[160:163]\n"
    "v_mfma_f32_16x16x32_bf16 v[164:167], v[200:203], v[88:91], v[164:167]\n"
    "v_mfma_f32_16x16x32_bf16 v[168:171], v[202:205], v[92:95], v[168:171]\n"
    "v_mfma_f32_16x16x32_bf16 v[172:175], v[200:203], v[92:95], v[172:175]\n"
    "v_mfma_f32_16x16x32_bf16 v[176:179], v[206:209], v[132:135], v[176:179]\n"
    "s_nop 7\n"
    "v_mul_f32 v160, 0x4275fdf0, v160\n"
    "v_mul_f32 v0, 0xbfb8aa3b, v160\n"
    "v_exp_f32 v0, v0\n"
    "v_mul_f32 v161, 0x4275fdf0, v161\n"
    "v_mul_f32 v1, 0xbfb8aa3b, v161\n"
    "v_exp_f32 v1, v1\n"
    "v_mul_f32 v162, 0x4275fdf0, v162\n"
    "v_mul_f32 v2, 0xbfb8aa3b, v162\n"
    "v_exp_f32 v2, v2\n"
    "v_mul_f32 v163, 0x4275fdf0, v163\n"
    "v_mul_f32 v3, 0xbfb8aa3b, v163\n"
    "v_exp_f32 v3, v3\n"
    "v_mul_f32 v164, 0x4275fdf0, v164\n"
    "v_mul_f32 v4, 0xbfb8aa3b, v164\n"
    "v_exp_f32 v4, v4\n"
    "v_mul_f32 v165, 0x4275fdf0, v165\n"
    "v_mul_f32 v5, 0xbfb8aa3b, v165\n"
    "v_exp_f32 v5, v5\n"
    "v_mul_f32 v166, 0x4275fdf0, v166\n"
    "v_mul_f32 v6, 0xbfb8aa3b, v166\n"
    "v_exp_f32 v6, v6\n"
    "v_mul_f32 v167, 0x4275fdf0, v167\n"
    "v_mul_f32 v7, 0xbfb8aa3b, v167\n"
    "v_exp_f32 v7, v7\n"
    "v_mul_f32 v168, 0x4275fdf0, v168\n"
    "v_mul_f32 v8, 0xbfb8aa3b, v168\n"
    "v_exp_f32 v8, v8\n"
    "v_mul_f32 v169, 0x4275fdf0, v169\n"
    "v_mul_f32 v9, 0xbfb8aa3b, v169\n"
    "v_exp_f32 v9, v9\n"
    "v_mul_f32 v170, 0x4275fdf0, v170\n"
    "v_mul_f32 v10, 0xbfb8aa3b, v170\n"
    "v_exp_f32 v10, v10\n"
    "v_mul_f32 v171, 0x4275fdf0, v171\n"
    "v_mul_f32 v11, 0xbfb8aa3b, v171\n"
    "v_exp_f32 v11, v11\n"
    "v_mul_f32 v172, 0x4275fdf0, v172\n"
    "v_mul_f32 v12, 0xbfb8aa3b, v172\n"
    "v_exp_f32 v12, v12\n"
    "v_mul_f32 v173, 0x4275fdf0, v173\n"
    "v_mul_f32 v13, 0xbfb8aa3b, v173\n"
    "v_exp_f32 v13, v13\n"
    "v_mul_f32 v174, 0x4275fdf0, v174\n"
    "v_mul_f32 v14, 0xbfb8aa3b, v174\n"
    "v_exp_f32 v14, v14\n"
    "v_mul_f32 v175, 0x4275fdf0, v175\n"
    "v_mul_f32 v15, 0xbfb8aa3b, v175\n"
    "v_exp_f32 v15, v15\n"
    "v_mul_f32 v176, 0x4275fdf0, v176\n"
    "v_mul_f32 v16, 0xbfb8aa3b, v176\n"
    "v_exp_f32 v16, v16\n"
    "v_mul_f32 v177, 0x4275fdf0, v177\n"
    "v_mul_f32 v17, 0xbfb8aa3b, v177\n"
    "v_exp_f32 v17, v17\n"
    "v_mul_f32 v178, 0x4275fdf0, v178\n"
    "v_mul_f32 v18, 0xbfb8aa3b, v178\n"
    "v_exp_f32 v18, v18\n"
    "v_mul_f32 v179, 0x4275fdf0, v179\n"
    "v_mul_f32 v19, 0xbfb8aa3b, v179\n"
    "v_exp_f32 v19, v19\n"
    "v_add_f32 v0, 1.0, v0\n"
    "v_rcp_f32 v0, v0\n"
    "v_add_f32 v1, 1.0, v1\n"
    "v_rcp_f32 v1, v1\n"
    "v_add_f32 v2, 1.0, v2\n"
    "v_rcp_f32 v2, v2\n"
    "v_add_f32 v3, 1.0, v3\n"
    "v_rcp_f32 v3, v3\n"
    "v_add_f32 v4, 1.0, v4\n"
    "v_rcp_f32 v4, v4\n"
    "v_add_f32 v5, 1.0, v5\n"
    "v_rcp_f32 v5, v5\n"
    "v_add_f32 v6, 1.0, v6\n"
    "v_rcp_f32 v6, v6\n"
    "v_add_f32 v7, 1.0, v7\n"
    "v_rcp_f32 v7, v7\n"
    "v_add_f32 v8, 1.0, v8\n"
    "v_rcp_f32 v8, v8\n"
    "v_add_f32 v9, 1.0, v9\n"
    "v_rcp_f32 v9, v9\n"
    "v_add_f32 v10, 1.0, v10\n"
    "v_rcp_f32 v10, v10\n"
    "v_add_f32 v11, 1.0, v11\n"
    "v_rcp_f32 v11, v11\n"
    "v_add_f32 v12, 1.0, v12\n"
    "v_rcp_f32 v12, v12\n"
    "v_add_f32 v13, 1.0, v13\n"
    "v_rcp_f32 v13, v13\n"
    "v_add_f32 v14, 1.0, v14\n"
    "v_rcp_f32 v14, v14\n"
    "v_add_f32 v15, 1.0, v15\n"
    "v_rcp_f32 v15, v15\n"
    "v_add_f32 v16, 1.0, v16\n"
    "v_rcp_f32 v16, v16\n"
    "v_add_f32 v17, 1.0, v17\n"
    "v_rcp_f32 v17, v17\n"
    "v_add_f32 v18, 1.0, v18\n"
    "v_rcp_f32 v18, v18\n"
    "v_add_f32 v19, 1.0, v19\n"
    "v_rcp_f32 v19, v19\n"
    "v_mul_f32 v160, v160, v0\n"
    "v_mul_f32 v161, v161, v1\n"
    "v_mul_f32 v162, v162, v2\n"
    "v_mul_f32 v163, v163, v3\n"
    "v_mul_f32 v164, v164, v4\n"
    "v_mul_f32 v165, v165, v5\n"
    "v_mul_f32 v166, v166, v6\n"
    "v_mul_f32 v167, v167, v7\n"
    "v_mul_f32 v168, v168, v8\n"
    "v_mul_f32 v169, v169, v9\n"
    "v_mul_f32 v170, v170, v10\n"
    "v_mul_f32 v171, v171, v11\n"
    "v_mul_f32 v172, v172, v12\n"
    "v_mul_f32 v173, v173, v13\n"
    "v_mul_f32 v174, v174, v14\n"
    "v_mul_f32 v175, v175, v15\n"
    "v_mul_f32 v176, v176, v16\n"
    "v_mul_f32 v177, v177, v17\n"
    "v_mul_f32 v178, v178, v18\n"
    "v_mul_f32 v179, v179, v19\n"
    "s_mov_b32 s87, 0x40000\n"
    "L_gb:\n"
    "s_and_b32 s85, s84, 240\n"
    "s_cmp_eq_u32 s85, 240\n"
    "s_cbranch_scc1 L_gdb\n"
    "buffer_load_dword v220, v252, s[52:55], s77 offen sc1\n"
    "s_waitcnt vmcnt(0)\n"
    "v_cmp_eq_u32 vcc, s74, v220\n"
    "s_nop 0\n"
    "s_andn2_b32 s85, vcc_lo, s84\n"
    "s_and_b32 s85, s85, 0xff\n"
    "s_or_b32 s84, s84, s85\n"
    "s_bitcmp1_b32 s85, 0\n"
    "s_cbranch_scc0 L_gsb_0\n"
    "s_add_u32 s4, s80, 0\n"
    "s_add_u32 s5, s81, 0\n"
    "buffer_load_dwordx4 v[64:67], v246, s[28:31], s4 offen sc1\n"
    "buffer_load_dwordx4 v[68:71], v246, s[28:31], s4 offen offset:1024 sc1\n"
    "buffer_load_dwordx2 v[128:129], v251, s[28:31], s5 offen sc1\n"
    "L_gsb_0:\n"
    "s_bitcmp1_b32 s85, 1\n"
    "s_cbranch_scc0 L_gsb_1\n"
    "s_add_u32 s4, s80, 9216\n"
    "s_add_u32 s5, s81, 9216\n"
    "buffer_load_dwordx4 v[72:75], v246, s[28:31], s4 offen sc1\n"
    "buffer_load_dwordx4 v[76:79], v246, s[28:31], s4 offen offset:1024 sc1\n"
    "buffer_load_dwordx2 v[130:131], v251, s[28:31], s5 offen sc1\n"
    "L_gsb_1:\n"
    "s_bitcmp1_b32 s85, 2\n"
    "s_cbranch_scc0 L_gsb_2\n"
    "s_add_u32 s4, s80, 18432\n"
    "s_add_u32 s5, s81, 18432\n"
    "buffer_load_dwordx4 v[80:83], v246, s[28:31], s4 offen sc1\n"
    "buffer_load_dwordx4 v[84:87], v246, s[28:31], s4 offen offset:1024 sc1\n"
    "buffer_load_dwordx2 v[132:133], v251, s[28:31], s5 offen sc1\n"
    "L_gsb_2:\n"
    "s_bitcmp1_b32 s85, 3\n"
    "s_cbranch_scc0 L_gsb_3\n"
    "s_add_u32 s4, s80, 27648\n"
    "s_add_u32 s5, s81, 27648\n"
    "buffer_load_dwordx4 v[88:91], v246, s[28:31], s4 offen sc1\n"
    "buffer_load_dwordx4 v[92:95], v246, s[28:31], s4 offen offset:1024 sc1\n"
    "buffer_load_dwordx2 v[134:135], v251, s[28:31], s5 offen sc1\n"
    "L_gsb_3:\n"
    "s_bitcmp1_b32 s85, 4\n"
    "s_cbranch_scc0 L_gsb_4\n"
    "s_add_u32 s4, s80, 36864\n"
    "s_add_u32 s5, s81, 36864\n"
    "buffer_load_dwordx4 v[96:99], v246, s[28:31], s4 offen sc1\n"
    "buffer_load_dwordx4 v[100:103], v246, s[28:31], s4 offen offset:1024 sc1\n"
    "buffer_load_dwordx2 v[136:137], v251, s[28:31], s5 offen sc1\n"
    "L_gsb_4:\n"
    "s_bitcmp1_b32 s85, 5\n"
    "s_cbranch_scc0 L_gsb_5\n"
    "s_add_u32 s4, s80, 46080\n"
    "s_add_u32 s5, s81, 46080\n"
    "buffer_load_dwordx4 v[104:107], v246, s[28:31], s4 offen sc1\n"
    "buffer_load_dwordx4 v[108:111], v246, s[28:31], s4 offen offset:1024 sc1\n"
    "buffer_load_dwordx2 v[138:139], v251, s[28:31], s5 offen sc1\n"
    "L_gsb_5:\n"
    "s_bitcmp1_b32 s85, 6\n"
    "s_cbranch_scc0 L_gsb_6\n"
    "s_add_u32 s4, s80, 55296\n"
    "s_add_u32 s5, s81, 55296\n"
    "buffer_load_dwordx4 v[112:115], v246, s[28:31], s4 offen sc1\n"
    "buffer_load_dwordx4 v[116:119], v246, s[28:31], s4 offen offset:1024 sc1\n"
    "buffer_load_dwordx2 v[140:141], v251, s[28:31], s5 offen sc1\n"
    "L_gsb_6:\n"
    "s_bitcmp1_b32 s85, 7\n"
    "s_cbranch_scc0 L_gsb_7\n"
    "s_add_u32 s4, s80, 64512\n"
    "s_add_u32 s5, s81, 64512\n"
    "buffer_load_dwordx4 v[120:123], v246, s[28:31], s4 offen sc1\n"
    "buffer_load_dwordx4 v[124:127], v246, s[28:31], s4 offen offset:1024 sc1\n"
    "buffer_load_dwordx2 v[142:143], v251, s[28:31], s5 offen sc1\n"
    "L_gsb_7:\n"
    "s_sub_u32 s87, s87, 1\n"
    "s_cbranch_scc0 L_gb\n"
    "L_gdb:\n"
    "s_waitcnt vmcnt(0)\n"
    "v_mfma_f32_16x16x32_bf16 v[180:183], v[202:205], v[96:99], 0\n"
    "v_mfma_f32_16x16x32_bf16 v[184:187], v[200:203], v[96:99], 0\n"
    "v_mfma_f32_16x16x32_bf16 v[188:191], v[202:205], v[100:103], 0\n"
    "v_mfma_f32_16x16x32_bf16 v[192:195], v[200:203], v[100:103], 0\n"
    "v_mfma_f32_16x16x32_bf16 v[180:183], v[202:205], v[104:107], v[180:183]\n"
    "v_mfma_f32_16x16x32_bf16 v[184:187], v[200:203], v[104:107], v[184:187]\n"
    "v_mfma_f32_16x16x32_bf16 v[188:191], v[202:205], v[108:111], v[188:191]\n"
    "v_mfma_f32_16x16x32_bf16 v[192:195], v[200:203], v[108:111], v[192:195]\n"
    "v_mfma_f32_16x16x32_bf16 v[196:199], v[206:209], v[136:139], 0\n"
    "v_mfma_f32_16x16x32_bf16 v[180:183], v[202:205], v[112:115], v[180:183]\n"
    "v_mfma_f32_16x16x32_bf16 v[184:187], v[200:203], v[112:115], v[184:187]\n"
    "v_mfma_f32_16x16x32_bf16 v[188:191], v[202:205], v[116:119], v[188:191]\n"
    "v_mfma_f32_16x16x32_bf16 v[192:195], v[200:203], v[116:119], v[192:195]\n"
    "v_mfma_f32_16x16x32_bf16 v[180:183], v[202:205], v[120:123], v[180:183]\n"
    "v_mfma_f32_16x16x32_bf16 v[184:187], v[200:203], v[120:123], v[184:187]\n"
    "v_mfma_f32_16x16x32_bf16 v[188:191], v[202:205], v[124:127], v[188:191]\n"
    "v_mfma_f32_16x16x32_bf16 v[192:195], v[200:203], v[124:127], v[192:195]\n"
    "v_mfma_f32_16x16x32_bf16 v[196:199], v[206:209], v[140:143], v[196:199]\n"
    "s_nop 7\n"
    "v_fmac_f32 v224, v160, v180\n"
    "v_fmac_f32 v225, v161, v181\n"
    "v_fmac_f32 v226, v162, v182\n"
    "v_fmac_f32 v227, v163, v183\n"
    "v_fmac_f32 v228, v164, v184\n"
    "v_fmac_f32 v229, v165, v185\n"
    "v_fmac_f32 v230, v166, v186\n"
    "v_fmac_f32 v231, v167, v187\n"
    "v_fmac_f32 v232, v168, v188\n"
    "v_fmac_f32 v233, v169, v189\n"
    "v_fmac_f32 v234, v170, v190\n"
    "v_fmac_f32 v235, v171, v191\n"
    "v_fmac_f32 v236, v172, v192\n"
    "v_fmac_f32 v237, v173, v193\n"
    "v_fmac_f32 v238, v174, v194\n"
    "v_fmac_f32 v239, v175, v195\n"
    "v_fmac_f32 v240, v176, v196\n"
    "v_fmac_f32 v241, v177, v197\n"
    "v_fmac_f32 v242, v178, v198\n"
    "v_fmac_f32 v243, v179, v199\n"
    "v_cvt_pk_bf16_f32 v210, v224, v225\n"
    "v_cvt_pk_bf16_f32 v211, v226, v227\n"
    "v_cvt_pk_bf16_f32 v212, v228, v229\n"
    "v_cvt_pk_bf16_f32 v213, v230, v231\n"
    "v_cvt_pk_bf16_f32 v214, v232, v233\n"
    "v_cvt_pk_bf16_f32 v215, v234, v235\n"
    "v_cvt_pk_bf16_f32 v216, v236, v237\n"
    "v_cvt_pk_bf16_f32 v217, v238, v239\n"
    "v_cvt_pk_bf16_f32 v218, v240, v241\n"
    "v_cvt_pk_bf16_f32 v219, v242, v243\n"
    "buffer_store_dwordx2 v[210:211], v221, s[60:63], s79 offen offset:0\n"
    "buffer_store_dwordx2 v[212:213], v221, s[60:63], s79 offen offset:32\n"
    "buffer_store_dwordx2 v[214:215], v221, s[60:63], s79 offen offset:64\n"
    "buffer_store_dwordx2 v[216:217], v221, s[60:63], s79 offen offset:96\n"
    "buffer_store_dwordx2 v[218:219], v221, s[60:63], s78 offen\n"
    "s_endpgm\n"
    "L_spath:\n"
    "v_add_u32 v248, 106496, v248\n"
    "v_add_u32 v249, 106496, v249\n"
    "v_add_u32 v250, 106496, v250\n"
    "s_sub_u32 s4, 2, s10\n"
    "s_xor_b32 s4, s4, s11\n"
    "s_add_u32 s4, s4, s12\n"
    "s_lshl_b32 s8, s4, 12\n"
    "s_add_u32 s8, s8, s80\n"
    "s_mul_i32 s9, s4, 147456\n"
    "s_add_u32 s9, s9, s81\n"
    "s_and_b32 s72, s68, 1\n"
    "s_lshl_b32 s72, s72, 13\n"
    "v_add_u32 v160, s72, v246\n"
    "v_add_u32 v161, 106496, v160\n"
    "v_mul_f32 v0, v0, v177\n"
    "v_mul_f32 v1, v1, v177\n"
    "v_mul_f32 v2, v2, v177\n"
    "v_mul_f32 v3, v3, v177\n"
    "v_mul_f32 v4, v4, v177\n"
    "v_mul_f32 v5, v5, v177\n"
    "v_mul_f32 v6, v6, v177\n"
    "v_mul_f32 v7, v7, v177\n"
    "v_cvt_pk_bf16_f32 v0, v0, v1\n"
    "v_cvt_pk_bf16_f32 v1, v2, v3\n"
    "v_cvt_pk_bf16_f32 v2, v4, v5\n"
    "v_cvt_pk_bf16_f32 v3, v6, v7\n"
    "s_mov_b64 exec, s[32:33]\n"
    "ds_write_b128 v161, v[0:3] offset:0\n"
    "s_mov_b64 exec, -1\n"
    "v_mul_f32 v8, v8, v177\n"
    "v_mul_f32 v9, v9, v177\n"
    "v_mul_f32 v10, v10, v177\n"
    "v_mul_f32 v11, v11, v177\n"
    "v_mul_f32 v12, v12, v177\n"
    "v_mul_f32 v13, v13, v177\n"
    "v_mul_f32 v14, v14, v177\n"
    "v_mul_f32 v15, v15, v177\n"
    "v_cvt_pk_bf16_f32 v8, v8, v9\n"
    "v_cvt_pk_bf16_f32 v9, v10, v11\n"
    "v_cvt_pk_bf16_f32 v10, v12, v13\n"
    "v_cvt_pk_bf16_f32 v11, v14, v15\n"
    "s_mov_b64 exec, s[32:33]\n"
    "ds_write_b128 v161, v[8:11] offset:1024\n"
    "s_mov_b64 exec, -1\n"
    "s_add_u32 s86, s79, 0\n"
    "s_mov_b64 exec, s[32:33]\n"
    "buffer_store_dwordx4 v[0:3], v246, s[48:51], s86 offen offset:0 sc1\n"
    "s_mov_b64 exec, -1\n"
    "v_mul_f32 v32, v32, v181\n"
    "v_mul_f32 v33, v33, v181\n"
    "v_mul_f32 v34, v34, v181\n"
    "v_mul_f32 v35, v35, v181\n"
    "v_mul_f32 v36, v36, v181\n"
    "v_mul_f32 v37, v37, v181\n"
    "v_mul_f32 v38, v38, v181\n"
    "v_mul_f32 v39, v39, v181\n"
    "v_cvt_pk_bf16_f32 v32, v32, v33\n"
    "v_cvt_pk_bf16_f32 v33, v34, v35\n"
    "v_cvt_pk_bf16_f32 v34, v36, v37\n"
    "v_cvt_pk_bf16_f32 v35, v38, v39\n"
    "s_mov_b64 exec, s[32:33]\n"
    "ds_write_b128 v161, v[32:35] offset:2048\n"
    "s_mov_b64 exec, -1\n"
    "v_mul_f32 v40, v40, v181\n"
    "v_mul_f32 v41, v41, v181\n"
    "v_mul_f32 v42, v42, v181\n"
    "v_mul_f32 v43, v43, v181\n"
    "v_mul_f32 v44, v44, v181\n"
    "v_mul_f32 v45, v45, v181\n"
    "v_mul_f32 v46, v46, v181\n"
    "v_mul_f32 v47, v47, v181\n"
    "v_cvt_pk_bf16_f32 v40, v40, v41\n"
    "v_cvt_pk_bf16_f32 v41, v42, v43\n"
    "v_cvt_pk_bf16_f32 v42, v44, v45\n"
    "v_cvt_pk_bf16_f32 v43, v46, v47\n"
    "s_mov_b64 exec, s[32:33]\n"
    "ds_write_b128 v161, v[40:43] offset:3072\n"
    "s_mov_b64 exec, -1\n"
    "s_add_u32 s86, s79, 131072\n"
    "s_mov_b64 exec, s[32:33]\n"
    "buffer_store_dwordx4 v[32:35], v246, s[48:51], s86 offen offset:0 sc1\n"
    "s_mov_b64 exec, -1\n"
    "v_mul_f32 v64, v64, v185\n"
    "v_mul_f32 v65, v65, v185\n"
    "v_mul_f32 v66, v66, v185\n"
    "v_mul_f32 v67, v67, v185\n"
    "v_mul_f32 v68, v68, v185\n"
    "v_mul_f32 v69, v69, v185\n"
    "v_mul_f32 v70, v70, v185\n"
    "v_mul_f32 v71, v71, v185\n"
    "v_cvt_pk_bf16_f32 v64, v64, v65\n"
    "v_cvt_pk_bf16_f32 v65, v66, v67\n"
    "v_cvt_pk_bf16_f32 v66, v68, v69\n"
    "v_cvt_pk_bf16_f32 v67, v70, v71\n"
    "s_mov_b64 exec, s[32:33]\n"
    "ds_write_b128 v161, v[64:67] offset:4096\n"
    "s_mov_b64 exec, -1\n"
    "v_mul_f32 v72, v72, v185\n"
    "v_mul_f32 v73, v73, v185\n"
    "v_mul_f32 v74, v74, v185\n"
    "v_mul_f32 v75, v75, v185\n"
    "v_mul_f32 v76, v76, v185\n"
    "v_mul_f32 v77, v77, v185\n"
    "v_mul_f32 v78, v78, v185\n"
    "v_mul_f32 v79, v79, v185\n"
    "v_cvt_pk_bf16_f32 v72, v72, v73\n"
    "v_cvt_pk_bf16_f32 v73, v74, v75\n"
    "v_cvt_pk_bf16_f32 v74, v76, v77\n"
    "v_cvt_pk_bf16_f32 v75, v78, v79\n"
    "s_mov_b64 exec, s[32:33]\n"
    "ds_write_b128 v161, v[72:75] offset:5120\n"
    "s_mov_b64 exec, -1\n"
    "s_add_u32 s86, s79, 262144\n"
    "s_mov_b64 exec, s[32:33]\n"
    "buffer_store_dwordx4 v[64:67], v246, s[48:51], s86 offen offset:0 sc1\n"
    "s_mov_b64 exec, -1\n"
    "v_mul_f32 v96, v96, v189\n"
    "v_mul_f32 v97, v97, v189\n"
    "v_mul_f32 v98, v98, v189\n"
    "v_mul_f32 v99, v99, v189\n"
    "v_mul_f32 v100, v100, v189\n"
    "v_mul_f32 v101, v101, v189\n"
    "v_mul_f32 v102, v102, v189\n"
    "v_mul_f32 v103, v103, v189\n"
    "v_cvt_pk_bf16_f32 v96, v96, v97\n"
    "v_cvt_pk_bf16_f32 v97, v98, v99\n"
    "v_cvt_pk_bf16_f32 v98, v100, v101\n"
    "v_cvt_pk_bf16_f32 v99, v102, v103\n"
    "s_mov_b64 exec, s[32:33]\n"
    "ds_write_b128 v161, v[96:99] offset:6144\n"
    "s_mov_b64 exec, -1\n"
    "v_mul_f32 v104, v104, v189\n"
    "v_mul_f32 v105, v105, v189\n"
    "v_mul_f32 v106, v106, v189\n"
    "v_mul_f32 v107, v107, v189\n"
    "v_mul_f32 v108, v108, v189\n"
    "v_mul_f32 v109, v109, v189\n"
    "v_mul_f32 v110, v110, v189\n"
    "v_mul_f32 v111, v111, v189\n"
    "v_cvt_pk_bf16_f32 v104, v104, v105\n"
    "v_cvt_pk_bf16_f32 v105, v106, v107\n"
    "v_cvt_pk_bf16_f32 v106, v108, v109\n"
    "v_cvt_pk_bf16_f32 v107, v110, v111\n"
    "s_mov_b64 exec, s[32:33]\n"
    "ds_write_b128 v161, v[104:107] offset:7168\n"
    "s_mov_b64 exec, -1\n"
    "s_add_u32 s86, s79, 393216\n"
    "s_mov_b64 exec, s[32:33]\n"
    "buffer_store_dwordx4 v[96:99], v246, s[48:51], s86 offen offset:0 sc1\n"
    "s_mov_b64 exec, -1\n"
    "s_waitcnt vmcnt(4) lgkmcnt(0)\n"
    "s_barrier\n"
    "ds_read_b64_tr_b16 a[176:177], v248 offset:0\n"
    "ds_read_b64_tr_b16 a[178:179], v248 offset:9216\n"
    "ds_read_b64_tr_b16 a[180:181], v248 offset:18432\n"
    "ds_read_b64_tr_b16 a[182:183], v248 offset:27648\n"
    "ds_read_b128 a[144:147], v250 offset:0\n"
    "ds_read_b128 a[148:151], v250 offset:1024\n"
    "ds_read_b128 a[152:155], v250 offset:2048\n"
    "ds_read_b128 a[156:159], v250 offset:3072\n"
    "ds_read_b128 a[160:163], v250 offset:4096\n"
    "ds_read_b128 a[164:167], v250 offset:5120\n"
    "ds_read_b128 a[168:171], v250 offset:6144\n"
    "ds_read_b128 a[172:175], v250 offset:7168\n"
    "ds_read_b64_tr_b16 a[184:185], v249 offset:0\n"
    "ds_read_b64_tr_b16 a[186:187], v249 offset:9216\n"
    "ds_read_b64_tr_b16 a[188:189], v249 offset:18432\n"
    "ds_read_b64_tr_b16 a[190:191], v249 offset:27648\n"
    "s_waitcnt lgkmcnt(4)\n"
    "v_mfma_f32_16x16x32_bf16 a[0:3], a[176:179], a[144:147], 0\n"
    "s_add_u32 s86, s79, 0\n"
    "ds_read_b64_tr_b16 a[224:225], v248 offset:512\n"
    "s_mov_b64 exec, s[32:33]\n"
    "buffer_store_dwordx4 v[8:11], v246, s[48:51], s86 offen offset:1024 sc1\n"
    "s_mov_b64 exec, -1\n"
    "v_mul_f32 v16, v16, v177\n"
    "v_mul_f32 v17, v17, v177\n"
    "v_mfma_f32_16x16x32_bf16 a[0:3], a[180:183], a[148:151], a[0:3]\n"
    "ds_read_b64_tr_b16 a[226:227], v248 offset:9728\n"
    "v_mul_f32 v18, v18, v177\n"
    "v_mul_f32 v19, v19, v177\n"
    "v_mul_f32 v20, v20, v177\n"
    "v_mul_f32 v21, v21, v177\n"
    "v_mfma_f32_16x16x32_bf16 a[4:7], a[176:179], a[152:155], 0\n"
    "ds_read_b64_tr_b16 a[228:229], v248 offset:18944\n"
    "v_mul_f32 v22, v22, v177\n"
    "v_mul_f32 v23, v23, v177\n"
    "v_cvt_pk_bf16_f32 v16, v16, v17\n"
    "v_cvt_pk_bf16_f32 v17, v18, v19\n"
    "v_mfma_f32_16x16x32_bf16 a[4:7], a[180:183], a[156:159], a[4:7]\n"
    "ds_read_b64_tr_b16 a[230:231], v248 offset:28160\n"
    "v_cvt_pk_bf16_f32 v18, v20, v21\n"
    "v_cvt_pk_bf16_f32 v19, v22, v23\n"
    "v_mfma_f32_16x16x32_bf16 a[8:11], a[176:179], a[160:163], 0\n"
    "s_mov_b64 exec, s[32:33]\n"
    "ds_write_b128 v160, v[16:19] offset:0\n"
    "s_mov_b64 exec, -1\n"
    "v_mul_f32 v24, v24, v177\n"
    "v_mul_f32 v25, v25, v177\n"
    "v_mul_f32 v26, v26, v177\n"
    "v_mfma_f32_16x16x32_bf16 a[8:11], a[180:183], a[164:167], a[8:11]\n"
    "v_mul_f32 v27, v27, v177\n"
    "v_mul_f32 v28, v28, v177\n"
    "v_mul_f32 v29, v29, v177\n"
    "v_mul_f32 v30, v30, v177\n"
    "v_mul_f32 v31, v31, v177\n"
    "v_mfma_f32_16x16x32_bf16 a[12:15], a[176:179], a[168:171], 0\n"
    "v_cvt_pk_bf16_f32 v24, v24, v25\n"
    "v_cvt_pk_bf16_f32 v25, v26, v27\n"
    "v_cvt_pk_bf16_f32 v26, v28, v29\n"
    "v_cvt_pk_bf16_f32 v27, v30, v31\n"
    "v_mfma_f32_16x16x32_bf16 a[12:15], a[180:183], a[172:175], a[12:15]\n"
    "s_waitcnt lgkmcnt(4)\n"
    "s_mov_b64 exec, s[32:33]\n"
    "ds_write_b128 v160, v[24:27] offset:1024\n"
    "s_mov_b64 exec, -1\n"
    "v_mfma_f32_16x16x32_bf16 a[16:19], a[184:187], a[144:147], 0\n"
    "s_add_u32 s86, s79, 0\n"
    "ds_read_b64_tr_b16 a[176:177], v249 offset:512\n"
    "s_mov_b64 exec, s[32:33]\n"
    "buffer_store_dwordx4 v[16:19], v246, s[48:51], s86 offen offset:2048 sc1\n"
    "s_mov_b64 exec, -1\n"
    "v_mfma_f32_16x16x32_bf16 a[16:19], a[188:191], a[148:151], a[16:19]\n"
    "s_add_u32 s86, s79, 131072\n"
    "ds_read_b64_tr_b16 a[178:179], v249 offset:9728\n"
    "s_mov_b64 exec, s[32:33]\n"
    "buffer_store_dwordx4 v[40:43], v246, s[48:51], s86 offen offset:1024 sc1\n"
    "s_mov_b64 exec, -1\n"
    "v_mul_f32 v48, v48, v181\n"
    "v_mul_f32 v49, v49, v181\n"
    "v_mfma_f32_16x16x32_bf16 a[20:23], a[184:187], a[152:155], 0\n"
    "ds_read_b64_tr_b16 a[180:181], v249 offset:18944\n"
    "v_mul_f32 v50, v50, v181\n"
    "v_mul_f32 v51, v51, v181\n"
    "v_mul_f32 v52, v52, v181\n"
    "v_mul_f32 v53, v53, v181\n"
    "v_mfma_f32_16x16x32_bf16 a[20:23], a[188:191], a[156:159], a[20:23]\n"
    "ds_read_b64_tr_b16 a[182:183], v249 offset:28160\n"
    "v_mul_f32 v54, v54, v181\n"
    "v_mul_f32 v55, v55, v181\n"
    "v_cvt_pk_bf16_f32 v48, v48, v49\n"
    "v_cvt_pk_bf16_f32 v49, v50, v51\n"
    "v_mfma_f32_16x16x32_bf16 a[24:27], a[184:187], a[160:163], 0\n"
    "v_cvt_pk_bf16_f32 v50, v52, v53\n"
    "v_cvt_pk_bf16_f32 v51, v54, v55\n"
    "v_mfma_f32_16x16x32_bf16 a[24:27], a[188:191], a[164:167], a[24:27]\n"
    "s_mov_b64 exec, s[32:33]\n"
    "ds_write_b128 v160, v[48:51] offset:2048\n"
    "s_mov_b64 exec, -1\n"
    "v_mul_f32 v56, v56, v181\n"
    "v_mul_f32 v57, v57, v181\n"
    "v_mul_f32 v58, v58, v181\n"
    "v_mfma_f32_16x16x32_bf16 a[28:31], a[184:187], a[168:171], 0\n"
    "v_mul_f32 v59, v59, v181\n"
    "v_mul_f32 v60, v60, v181\n"
    "v_mul_f32 v61, v61, v181\n"
    "v_mul_f32 v62, v62, v181\n"
    "v_mul_f32 v63, v63, v181\n"
    "v_mfma_f32_16x16x32_bf16 a[28:31], a[188:191], a[172:175], a[28:31]\n"
    "s_waitcnt lgkmcnt(7)\n"
    "v_cvt_pk_bf16_f32 v56, v56, v57\n"
    "v_cvt_pk_bf16_f32 v57, v58, v59\n"
    "v_cvt_pk_bf16_f32 v58, v60, v61\n"
    "v_cvt_pk_bf16_f32 v59, v62, v63\n"
    "v_mfma_f32_16x16x32_bf16 a[32:35], a[224:227], a[144:147], 0\n"
    "ds_read_b64_tr_b16 a[184:185], v248 offset:1024\n"
    "s_mov_b64 exec, s[32:33]\n"
    "ds_write_b128 v160, v[56:59] offset:3072\n"
    "s_mov_b64 exec, -1\n"
    "v_mfma_f32_16x16x32_bf16 a[32:35], a[228:231], a[148:151], a[32:35]\n"
    "s_add_u32 s86, s79, 131072\n"
    "ds_read_b64_tr_b16 a[186:187], v248 offset:10240\n"
    "s_mov_b64 exec, s[32:33]\n"
    "buffer_store_dwordx4 v[48:51], v246, s[48:51], s86 offen offset:2048 sc1\n"
    "s_mov_b64 exec, -1\n"
    "v_mfma_f32_16x16x32_bf16 a[36:39], a[224:227], a[152:155], 0\n"
    "s_add_u32 s86, s79, 262144\n"
    "ds_read_b64_tr_b16 a[188:189], v248 offset:19456\n"
    "s_mov_b64 exec, s[32:33]\n"
    "buffer_store_dwordx4 v[72:75], v246, s[48:51], s86 offen offset:1024 sc1\n"
    "s_mov_b64 exec, -1\n"
    "v_mul_f32 v80, v80, v185\n"
    "v_mul_f32 v81, v81, v185\n"
    "v_mfma_f32_16x16x32_bf16 a[36:39], a[228:231], a[156:159], a[36:39]\n"
    "ds_read_b64_tr_b16 a[190:191], v248 offset:28672\n"
    "v_mul_f32 v82, v82, v185\n"
    "v_mul_f32 v83, v83, v185\n"
    "v_mul_f32 v84, v84, v185\n"
    "v_mul_f32 v85, v85, v185\n"
    "v_mfma_f32_16x16x32_bf16 a[40:43], a[224:227], a[160:163], 0\n"
    "v_mul_f32 v86, v86, v185\n"
    "v_mul_f32 v87, v87, v185\n"
    "v_cvt_pk_bf16_f32 v80, v80, v81\n"
    "v_cvt_pk_bf16_f32 v81, v82, v83\n"
    "v_cvt_pk_bf16_f32 v82, v84, v85\n"
    "v_mfma_f32_16x16x32_bf16 a[40:43], a[228:231], a[164:167], a[40:43]\n"
    "v_cvt_pk_bf16_f32 v83, v86, v87\n"
    "v_mfma_f32_16x16x32_bf16 a[44:47], a[224:227], a[168:171], 0\n"
    "s_mov_b64 exec, s[32:33]\n"
    "ds_write_b128 v160, v[80:83] offset:4096\n"
    "s_mov_b64 exec, -1\n"
    "v_mul_f32 v88, v88, v185\n"
    "v_mul_f32 v89, v89, v185\n"
    "v_mul_f32 v90, v90, v185\n"
    "v_mfma_f32_16x16x32_bf16 a[44:47], a[228:231], a[172:175], a[44:47]\n"
    "s_waitcnt lgkmcnt(7)\n"
    "v_mul_f32 v91, v91, v185\n"
    "v_mul_f32 v92, v92, v185\n"
    "v_mul_f32 v93, v93, v185\n"
    "v_mul_f32 v94, v94, v185\n"
    "v_mul_f32 v95, v95, v185\n"
    "v_mfma_f32_16x16x32_bf16 a[48:51], a[176:179], a[144:147], 0\n"
    "ds_read_b64_tr_b16 a[224:225], v249 offset:1024\n"
    "v_cvt_pk_bf16_f32 v88, v88, v89\n"
    "v_cvt_pk_bf16_f32 v89, v90, v91\n"
    "v_cvt_pk_bf16_f32 v90, v92, v93\n"
    "v_cvt_pk_bf16_f32 v91, v94, v95\n"
    "v_mfma_f32_16x16x32_bf16 a[48:51], a[180:183], a[148:151], a[48:51]\n"
    "ds_read_b64_tr_b16 a[226:227], v249 offset:10240\n"
    "s_mov_b64 exec, s[32:33]\n"
    "ds_write_b128 v160, v[88:91] offset:5120\n"
    "s_mov_b64 exec, -1\n"
    "v_mfma_f32_16x16x32_bf16 a[52:55], a[176:179], a[152:155], 0\n"
    "s_add_u32 s86, s79, 262144\n"
    "ds_read_b64_tr_b16 a[228:229], v249 offset:19456\n"
    "s_mov_b64 exec, s[32:33]\n"
    "buffer_store_dwordx4 v[80:83], v246, s[48:51], s86 offen offset:2048 sc1\n"
    "s_mov_b64 exec, -1\n"
    "v_mfma_f32_16x16x32_bf16 a[52:55], a[180:183], a[156:159], a[52:55]\n"
    "s_add_u32 s86, s79, 393216\n"
    "ds_read_b64_tr_b16 a[230:231], v249 offset:28672\n"
    "s_mov_b64 exec, s[32:33]\n"
    "buffer_store_dwordx4 v[104:107], v246, s[48:51], s86 offen offset:1024 sc1\n"
    "s_mov_b64 exec, -1\n"
    "v_mul_f32 v112, v112, v189\n"
    "v_mul_f32 v113, v113, v189\n"
    "v_mfma_f32_16x16x32_bf16 a[56:59], a[176:179], a[160:163], 0\n"
    "v_mul_f32 v114, v114, v189\n"
    "v_mul_f32 v115, v115, v189\n"
    "v_mul_f32 v116, v116, v189\n"
    "v_mul_f32 v117, v117, v189\n"
    "v_mul_f32 v118, v118, v189\n"
    "v_mfma_f32_16x16x32_bf16 a[56:59], a[180:183], a[164:167], a[56:59]\n"
    "v_mul_f32 v119, v119, v189\n"
    "v_cvt_pk_bf16_f32 v112, v112, v113\n"
    "v_cvt_pk_bf16_f32 v113, v114, v115\n"
    "v_cvt_pk_bf16_f32 v114, v116, v117\n"
    "v_cvt_pk_bf16_f32 v115, v118, v119\n"
    "v_mfma_f32_16x16x32_bf16 a[60:63], a[176:179], a[168:171], 0\n"
    "s_mov_b64 exec, s[32:33]\n"
    "ds_write_b128 v160, v[112:115] offset:6144\n"
    "s_mov_b64 exec, -1\n"
    "v_mul_f32 v120, v120, v189\n"
    "v_mul_f32 v121, v121, v189\n"
    "v_mul_f32 v122, v122, v189\n"
    "v_mfma_f32_16x16x32_bf16 a[60:63], a[180:183], a[172:175], a[60:63]\n"
    "s_waitcnt lgkmcnt(7)\n"
    "v_mul_f32 v123, v123, v189\n"
    "v_mul_f32 v124, v124, v189\n"
    "v_mul_f32 v125, v125, v189\n"
    "v_mul_f32 v126, v126, v189\n"
    "v_mul_f32 v127, v127, v189\n"
    "v_mfma_f32_16x16x32_bf16 a[64:67], a[184:187], a[144:147], 0\n"
    "ds_read_b64_tr_b16 a[176:177], v248 offset:1536\n"
    "v_cvt_pk_bf16_f32 v120, v120, v121\n"
    "v_cvt_pk_bf16_f32 v121, v122, v123\n"
    "v_cvt_pk_bf16_f32 v122, v124, v125\n"
    "v_cvt_pk_bf16_f32 v123, v126, v127\n"
    "v_mfma_f32_16x16x32_bf16 a[64:67], a[188:191], a[148:151], a[64:67]\n"
    "ds_read_b64_tr_b16 a[178:179], v248 offset:10752\n"
    "s_mov_b64 exec, s[32:33]\n"
    "ds_write_b128 v160, v[120:123] offset:7168\n"
    "s_mov_b64 exec, -1\n"
    "v_mfma_f32_16x16x32_bf16 a[68:71], a[184:187], a[152:155], 0\n"
    "s_add_u32 s86, s79, 393216\n"
    "ds_read_b64_tr_b16 a[180:181], v248 offset:19968\n"
    "s_mov_b64 exec, s[32:33]\n"
    "buffer_store_dwordx4 v[112:115], v246, s[48:51], s86 offen offset:2048 sc1\n"
    "s_mov_b64 exec, -1\n"
    "v_mfma_f32_16x16x32_bf16 a[68:71], a[188:191], a[156:159], a[68:71]\n"
    "ds_read_b64_tr_b16 a[182:183], v248 offset:29184\n"
    "v_mfma_f32_16x16x32_bf16 a[72:75], a[184:187], a[160:163], 0\n"
    "v_mfma_f32_16x16x32_bf16 a[72:75], a[188:191], a[164:167], a[72:75]\n"
    "v_mfma_f32_16x16x32_bf16 a[76:79], a[184:187], a[168:171], 0\n"
    "s_add_u32 s86, s79, 0\n"
    "s_mov_b64 exec, s[32:33]\n"
    "buffer_store_dwordx4 v[24:27], v246, s[48:51], s86 offen offset:3072 sc1\n"
    "s_mov_b64 exec, -1\n"
    "v_mfma_f32_16x16x32_bf16 a[76:79], a[188:191], a[172:175], a[76:79]\n"
    "s_waitcnt lgkmcnt(6)\n"
    "v_mfma_f32_16x16x32_bf16 a[80:83], a[224:227], a[144:147], 0\n"
    "ds_read_b64_tr_b16 a[184:185], v249 offset:1536\n"
    "v_mfma_f32_16x16x32_bf16 a[80:83], a[228:231], a[148:151], a[80:83]\n"
    "ds_read_b64_tr_b16 a[186:187], v249 offset:10752\n"
    "v_mfma_f32_16x16x32_bf16 a[84:87], a[224:227], a[152:155], 0\n"
    "ds_read_b64_tr_b16 a[188:189], v249 offset:19968\n"
    "v_mfma_f32_16x16x32_bf16 a[84:87], a[228:231], a[156:159], a[84:87]\n"
    "ds_read_b64_tr_b16 a[190:191], v249 offset:29184\n"
    "v_mfma_f32_16x16x32_bf16 a[88:91], a[224:227], a[160:163], 0\n"
    "v_mfma_f32_16x16x32_bf16 a[88:91], a[228:231], a[164:167], a[88:91]\n"
    "s_add_u32 s86, s79, 131072\n"
    "s_mov_b64 exec, s[32:33]\n"
    "buffer_store_dwordx4 v[56:59], v246, s[48:51], s86 offen offset:3072 sc1\n"
    "s_mov_b64 exec, -1\n"
    "v_mfma_f32_16x16x32_bf16 a[92:95], a[224:227], a[168:171], 0\n"
    "v_mfma_f32_16x16x32_bf16 a[92:95], a[228:231], a[172:175], a[92:95]\n"
    "s_waitcnt lgkmcnt(4)\n"
    "v_mfma_f32_16x16x32_bf16 a[96:99], a[176:179], a[144:147], 0\n"
    "ds_read_b64_tr_b16 a[224:225], v248 offset:2048\n"
    "v_mfma_f32_16x16x32_bf16 a[96:99], a[180:183], a[148:151], a[96:99]\n"
    "ds_read_b64_tr_b16 a[226:227], v248 offset:11264\n"
    "v_mfma_f32_16x16x32_bf16 a[100:103], a[176:179], a[152:155], 0\n"
    "ds_read_b64_tr_b16 a[228:229], v248 offset:20480\n"
    "v_mfma_f32_16x16x32_bf16 a[100:103], a[180:183], a[156:159], a[100:103]\n"
    "ds_read_b64_tr_b16 a[230:231], v248 offset:29696\n"
    "v_mfma_f32_16x16x32_bf16 a[104:107], a[176:179], a[160:163], 0\n"
    "s_add_u32 s86, s79, 262144\n"
    "s_mov_b64 exec, s[32:33]\n"
    "buffer_store_dwordx4 v[88:91], v246, s[48:51], s86 offen offset:3072 sc1\n"
    "s_mov_b64 exec, -1\n"
    "v_mfma_f32_16x16x32_bf16 a[104:107], a[180:183], a[164:167], a[104:107]\n"
    "v_mfma_f32_16x16x32_bf16 a[108:111], a[176:179], a[168:171], 0\n"
    "v_add_u32 v248, 4294860800, v248\n"
    "v_add_u32 v249, 4294860800, v249\n"
    "v_mfma_f32_16x16x32_bf16 a[108:111], a[180:183], a[172:175], a[108:111]\n"
    "s_waitcnt lgkmcnt(4)\n"
    "v_add_u32 v250, 4294860800, v250\n"
    "v_mfma_f32_16x16x32_bf16 a[112:115], a[184:187], a[144:147], 0\n"
    "v_mfma_f32_16x16x32_bf16 a[112:115], a[188:191], a[148:151], a[112:115]\n"
    "s_waitcnt lgkmcnt(0)\n"
    "s_barrier\n"
    "v_mfma_f32_16x16x32_bf16 a[116:119], a[184:187], a[152:155], 0\n"
    "ds_read_b64_tr_b16 a[176:177], v248 offset:0\n"
    "v_mfma_f32_16x16x32_bf16 a[116:119], a[188:191], a[156:159], a[116:119]\n"
    "s_add_u32 s86, s79, 393216\n"
    "ds_read_b64_tr_b16 a[178:179], v248 offset:9216\n"
    "v_mfma_f32_16x16x32_bf16 a[120:123], a[184:187], a[160:163], 0\n"
    "ds_read_b64_tr_b16 a[180:181], v248 offset:18432\n"
    "ds_read_b128 a[192:195], v250 offset:0\n"
    "v_mfma_f32_16x16x32_bf16 a[120:123], a[188:191], a[164:167], a[120:123]\n"
    "ds_read_b64_tr_b16 a[182:183], v248 offset:27648\n"
    "ds_read_b128 a[196:199], v250 offset:1024\n"
    "v_mfma_f32_16x16x32_bf16 a[124:127], a[184:187], a[168:171], 0\n"
    "ds_read_b128 a[200:203], v250 offset:2048\n"
    "v_mfma_f32_16x16x32_bf16 a[124:127], a[188:191], a[172:175], a[124:127]\n"
    "ds_read_b128 a[204:207], v250 offset:3072\n"
    "v_mfma_f32_16x16x32_bf16 a[128:131], a[224:227], a[144:147], 0\n"
    "ds_read_b128 a[208:211], v250 offset:4096\n"
    "ds_read_b128 a[212:215], v250 offset:5120\n"
    "v_mfma_f32_16x16x32_bf16 a[128:131], a[228:231], a[148:151], a[128:131]\n"
    "ds_read_b128 a[216:219], v250 offset:6144\n"
    "ds_read_b128 a[220:223], v250 offset:7168\n"
    "v_mfma_f32_16x16x32_bf16 a[132:135], a[224:227], a[152:155], 0\n"
    "ds_read_b64_tr_b16 a[184:185], v249 offset:0\n"
    "v_mfma_f32_16x16x32_bf16 a[132:135], a[228:231], a[156:159], a[132:135]\n"
    "ds_read_b64_tr_b16 a[186:187], v249 offset:9216\n"
    "v_mfma_f32_16x16x32_bf16 a[136:139], a[224:227], a[160:163], 0\n"
    "ds_read_b64_tr_b16 a[188:189], v249 offset:18432\n"
    "v_mfma_f32_16x16x32_bf16 a[136:139], a[228:231], a[164:167], a[136:139]\n"
    "ds_read_b64_tr_b16 a[190:191], v249 offset:27648\n"
    "v_mfma_f32_16x16x32_bf16 a[140:143], a[224:227], a[168:171], 0\n"
    "s_mov_b64 exec, s[32:33]\n"
    "buffer_store_dwordx4 v[120:123], v246, s[48:51], s86 offen offset:3072 sc1\n"
    "s_mov_b64 exec, -1\n"
    "v_mfma_f32_16x16x32_bf16 a[140:143], a[228:231], a[172:175], a[140:143]\n"
    "s_waitcnt lgkmcnt(4)\n"
    "v_mfma_f32_16x16x32_bf16 a[0:3], a[176:179], a[192:195], a[0:3]\n"
    "ds_read_b64_tr_b16 a[224:225], v248 offset:512\n"
    "v_lshrrev_b32 v178, 1, v253\n"
    "v_mfma_f32_16x16x32_bf16 a[0:3], a[180:183], a[196:199], a[0:3]\n"
    "ds_read_b64_tr_b16 a[226:227], v248 offset:9728\n"
    "v_and_b32 v178, 0, v178\n"
    "v_mfma_f32_16x16x32_bf16 a[4:7], a[176:179], a[200:203], a[4:7]\n"
    "s_xor_b32 s78, s69, 1\n"
    "ds_read_b64_tr_b16 a[228:229], v248 offset:18944\n"
    "v_xor_b32 v178, s78, v178\n"
    "v_mfma_f32_16x16x32_bf16 a[4:7], a[180:183], a[204:207], a[4:7]\n"
    "ds_read_b64_tr_b16 a[230:231], v248 offset:28160\n"
    "v_lshlrev_b32 v178, 2, v178\n"
    "v_mfma_f32_16x16x32_bf16 a[8:11], a[176:179], a[208:211], a[8:11]\n"
    "s_add_u32 m0, s83, 122880\n"
    "v_and_b32 v179, 1, v253\n"
    "v_add_u32 v178, v178, v179\n"
    "v_mfma_f32_16x16x32_bf16 a[8:11], a[180:183], a[212:215], a[8:11]\n"
    "s_add_u32 s72, s9, 73728\n"
    "buffer_load_dwordx4 v247, s[24:27], s72 offen lds\n"
    "v_mfma_f32_16x16x32_bf16 a[12:15], a[176:179], a[216:219], a[12:15]\n"
    "s_add_u32 m0, s83, 123904\n"
    "s_lshr_b32 s78, s69, 1\n"
    "s_and_b32 s78, s78, 2\n"
    "v_mfma_f32_16x16x32_bf16 a[12:15], a[180:183], a[220:223], a[12:15]\n"
    "s_waitcnt lgkmcnt(4)\n"
    "s_add_u32 s72, s9, 73856\n"
    "buffer_load_dwordx4 v247, s[24:27], s72 offen lds\n"
    "v_mfma_f32_16x16x32_bf16 a[16:19], a[184:187], a[192:195], a[16:19]\n"
    "s_lshl_b32 s78, s78, 7\n"
    "s_add_u32 s78, s78, 32768\n"
    "ds_read_b64_tr_b16 a[176:177], v249 offset:512\n"
    "v_mfma_f32_16x16x32_bf16 a[16:19], a[188:191], a[196:199], a[16:19]\n"
    "ds_read_b64_tr_b16 a[178:179], v249 offset:9728\n"
    "v_lshlrev_b32 v178, 7, v178\n"
    "v_mfma_f32_16x16x32_bf16 a[20:23], a[184:187], a[200:203], a[20:23]\n"
    "ds_read_b64_tr_b16 a[180:181], v249 offset:18944\n"
    "v_mfma_f32_16x16x32_bf16 a[20:23], a[188:191], a[204:207], a[20:23]\n"
    "ds_read_b64_tr_b16 a[182:183], v249 offset:28160\n"
    "v_mfma_f32_16x16x32_bf16 a[24:27], a[184:187], a[208:211], a[24:27]\n"
    "s_add_u32 m0, s83, 124928\n"
    "v_mfma_f32_16x16x32_bf16 a[24:27], a[188:191], a[212:215], a[24:27]\n"
    "s_add_u32 s72, s9, 73984\n"
    "buffer_load_dwordx4 v247, s[24:27], s72 offen lds\n"
    "v_mfma_f32_16x16x32_bf16 a[28:31], a[184:187], a[216:219], a[28:31]\n"
    "s_add_u32 m0, s83, 125952\n"
    "v_mfma_f32_16x16x32_bf16 a[28:31], a[188:191], a[220:223], a[28:31]\n"
    "s_waitcnt lgkmcnt(4)\n"
    "s_add_u32 s72, s9, 74112\n"
    "buffer_load_dwordx4 v247, s[24:27], s72 offen lds\n"
    "v_mfma_f32_16x16x32_bf16 a[32:35], a[224:227], a[192:195], a[32:35]\n"
    "ds_read_b64_tr_b16 a[184:185], v248 offset:1024\n"
    "v_mfma_f32_16x16x32_bf16 a[32:35], a[228:231], a[196:199], a[32:35]\n"
    "ds_read_b64_tr_b16 a[186:187], v248 offset:10240\n"
    "v_mfma_f32_16x16x32_bf16 a[36:39], a[224:227], a[200:203], a[36:39]\n"
    "ds_read_b64_tr_b16 a[188:189], v248 offset:19456\n"
    "v_mfma_f32_16x16x32_bf16 a[36:39], a[228:231], a[204:207], a[36:39]\n"
    "ds_read_b64_tr_b16 a[190:191], v248 offset:28672\n"
    "v_mfma_f32_16x16x32_bf16 a[40:43], a[224:227], a[208:211], a[40:43]\n"
    "s_add_u32 m0, s66, 122880\n"
    "v_mfma_f32_16x16x32_bf16 a[40:43], a[228:231], a[212:215], a[40:43]\n"
    "s_add_u32 s72, s9, 73728\n"
    "s_mov_b32 exec_hi, 0\n"
    "buffer_load_dwordx4 v245, s[24:27], s72 offen lds\n"
    "s_mov_b32 exec_hi, -1\n"
    "v_mfma_f32_16x16x32_bf16 a[44:47], a[224:227], a[216:219], a[44:47]\n"
    "s_add_u32 m0, s83, 127488\n"
    "v_mfma_f32_16x16x32_bf16 a[44:47], a[228:231], a[220:223], a[44:47]\n"
    "s_waitcnt lgkmcnt(4)\n"
    "s_add_u32 s72, s9, 82944\n"
    "buffer_load_dwordx4 v247, s[24:27], s72 offen lds\n"
    "s_cmp_eq_u32 s32, 0\n"
    "s_cbranch_scc1 L_opub3\n"
    "s_waitcnt vmcnt(6)\n"
    "s_add_u32 s73, s84, 32768\n"
    "v_mov_b32 v176, s74\n"
    "s_mov_b64 exec, 1\n"
    "buffer_store_dword v176, off, s[52:55], s73 sc1\n"
    "s_mov_b64 exec, -1\n"
    "L_opub3:\n"
    "v_mfma_f32_16x16x32_bf16 a[48:51], a[176:179], a[192:195], a[48:51]\n"
    "ds_read_b64_tr_b16 a[224:225], v249 offset:1024\n"
    "v_mfma_f32_16x16x32_bf16 a[48:51], a[180:183], a[196:199], a[48:51]\n"
    "ds_read_b64_tr_b16 a[226:227], v249 offset:10240\n"
    "v_mfma_f32_16x16x32_bf16 a[52:55], a[176:179], a[200:203], a[52:55]\n"
    "ds_read_b64_tr_b16 a[228:229], v249 offset:19456\n"
    "v_mfma_f32_16x16x32_bf16 a[52:55], a[180:183], a[204:207], a[52:55]\n"
    "ds_read_b64_tr_b16 a[230:231], v249 offset:28672\n"
    "v_mfma_f32_16x16x32_bf16 a[56:59], a[176:179], a[208:211], a[56:59]\n"
    "s_add_u32 m0, s83, 128512\n"
    "v_mfma_f32_16x16x32_bf16 a[56:59], a[180:183], a[212:215], a[56:59]\n"
    "s_add_u32 s72, s9, 83072\n"
    "buffer_load_dwordx4 v247, s[24:27], s72 offen lds\n"
    "v_mfma_f32_16x16x32_bf16 a[60:63], a[176:179], a[216:219], a[60:63]\n"
    "s_add_u32 m0, s83, 129536\n"
    "v_mfma_f32_16x16x32_bf16 a[60:63], a[180:183], a[220:223], a[60:63]\n"
    "s_waitcnt lgkmcnt(4)\n"
    "s_add_u32 s72, s9, 83200\n"
    "buffer_load_dwordx4 v247, s[24:27], s72 offen lds\n"
    "v_mfma_f32_16x16x32_bf16 a[64:67], a[184:187], a[192:195], a[64:67]\n"
    "ds_read_b64_tr_b16 a[176:177], v248 offset:1536\n"
    "v_mfma_f32_16x16x32_bf16 a[64:67], a[188:191], a[196:199], a[64:67]\n"
    "ds_read_b64_tr_b16 a[178:179], v248 offset:10752\n"
    "v_mfma_f32_16x16x32_bf16 a[68:71], a[184:187], a[200:203], a[68:71]\n"
    "ds_read_b64_tr_b16 a[180:181], v248 offset:19968\n"
    "v_mfma_f32_16x16x32_bf16 a[68:71], a[188:191], a[204:207], a[68:71]\n"
    "ds_read_b64_tr_b16 a[182:183], v248 offset:29184\n"
    "v_mfma_f32_16x16x32_bf16 a[72:75], a[184:187], a[208:211], a[72:75]\n"
    "s_add_u32 m0, s83, 130560\n"
    "v_mfma_f32_16x16x32_bf16 a[72:75], a[188:191], a[212:215], a[72:75]\n"
    "s_add_u32 s72, s9, 83328\n"
    "buffer_load_dwordx4 v247, s[24:27], s72 offen lds\n"
    "v_mfma_f32_16x16x32_bf16 a[76:79], a[184:187], a[216:219], a[76:79]\n"
    "s_add_u32 m0, s66, 127488\n"
    "v_mfma_f32_16x16x32_bf16 a[76:79], a[188:191], a[220:223], a[76:79]\n"
    "s_waitcnt lgkmcnt(4)\n"
    "s_add_u32 s72, s9, 82944\n"
    "s_mov_b32 exec_hi, 0\n"
    "buffer_load_dwordx4 v245, s[24:27], s72 offen lds\n"
    "s_mov_b32 exec_hi, -1\n"
    "v_mfma_f32_16x16x32_bf16 a[80:83], a[224:227], a[192:195], a[80:83]\n"
    "ds_read_b64_tr_b16 a[184:185], v249 offset:1536\n"
    "v_mfma_f32_16x16x32_bf16 a[80:83], a[228:231], a[196:199], a[80:83]\n"
    "ds_read_b64_tr_b16 a[186:187], v249 offset:10752\n"
    "v_mfma_f32_16x16x32_bf16 a[84:87], a[224:227], a[200:203], a[84:87]\n"
    "ds_read_b64_tr_b16 a[188:189], v249 offset:19968\n"
    "v_mfma_f32_16x16x32_bf16 a[84:87], a[228:231], a[204:207], a[84:87]\n"
    "ds_read_b64_tr_b16 a[190:191], v249 offset:29184\n"
    "v_mfma_f32_16x16x32_bf16 a[88:91], a[224:227], a[208:211], a[88:91]\n"
    "v_mfma_f32_16x16x32_bf16 a[88:91], a[228:231], a[212:215], a[88:91]\n"
    "v_mfma_f32_16x16x32_bf16 a[92:95], a[224:227], a[216:219], a[92:95]\n"
    "v_mfma_f32_16x16x32_bf16 a[92:95], a[228:231], a[220:223], a[92:95]\n"
    "s_waitcnt lgkmcnt(4)\n"
    "v_mfma_f32_16x16x32_bf16 a[96:99], a[176:179], a[192:195], a[96:99]\n"
    "ds_read_b64_tr_b16 a[224:225], v248 offset:2048\n"
    "v_mfma_f32_16x16x32_bf16 a[96:99], a[180:183], a[196:199], a[96:99]\n"
    "ds_read_b64_tr_b16 a[226:227], v248 offset:11264\n"
    "v_mfma_f32_16x16x32_bf16 a[100:103], a[176:179], a[200:203], a[100:103]\n"
    "ds_read_b64_tr_b16 a[228:229], v248 offset:20480\n"
    "v_mfma_f32_16x16x32_bf16 a[100:103], a[180:183], a[204:207], a[100:103]\n"
    "ds_read_b64_tr_b16 a[230:231], v248 offset:29696\n"
    "v_mfma_f32_16x16x32_bf16 a[104:107], a[176:179], a[208:211], a[104:107]\n"
    "v_mfma_f32_16x16x32_bf16 a[104:107], a[180:183], a[212:215], a[104:107]\n"
    "v_mfma_f32_16x16x32_bf16 a[108:111], a[176:179], a[216:219], a[108:111]\n"
    "v_add_u32 v248, 53248, v248\n"
    "v_add_u32 v249, 53248, v249\n"
    "v_mfma_f32_16x16x32_bf16 a[108:111], a[180:183], a[220:223], a[108:111]\n"
    "s_waitcnt lgkmcnt(4)\n"
    "v_add_u32 v250, 53248, v250\n"
    "v_mfma_f32_16x16x32_bf16 a[112:115], a[184:187], a[192:195], a[112:115]\n"
    "v_mfma_f32_16x16x32_bf16 a[112:115], a[188:191], a[196:199], a[112:115]\n"
    "v_mfma_f32_16x16x32_bf16 a[116:119], a[184:187], a[200:203], a[116:119]\n"
    "v_mfma_f32_16x16x32_bf16 a[116:119], a[188:191], a[204:207], a[116:119]\n"
    "v_mfma_f32_16x16x32_bf16 a[120:123], a[184:187], a[208:211], a[120:123]\n"
    "buffer_load_dword v179, v178, s[52:55], s78 offen sc1\n"
    "v_mfma_f32_16x16x32_bf16 a[120:123], a[188:191], a[212:215], a[120:123]\n"
    "v_mfma_f32_16x16x32_bf16 a[124:127], a[184:187], a[216:219], a[124:127]\n"
    "v_mfma_f32_16x16x32_bf16 a[124:127], a[188:191], a[220:223], a[124:127]\n"
    "s_waitcnt lgkmcnt(0)\n"
    "v_mfma_f32_16x16x32_bf16 a[128:131], a[224:227], a[192:195], a[128:131]\n"
    "v_mfma_f32_16x16x32_bf16 a[128:131], a[228:231], a[196:199], a[128:131]\n"
    "v_mfma_f32_16x16x32_bf16 a[132:135], a[224:227], a[200:203], a[132:135]\n"
    "v_mfma_f32_16x16x32_bf16 a[132:135], a[228:231], a[204:207], a[132:135]\n"
    "v_mfma_f32_16x16x32_bf16 a[136:139], a[224:227], a[208:211], a[136:139]\n"
    "v_mfma_f32_16x16x32_bf16 a[136:139], a[228:231], a[212:215], a[136:139]\n"
    "v_mfma_f32_16x16x32_bf16 a[140:143], a[224:227], a[216:219], a[140:143]\n"
    "v_mfma_f32_16x16x32_bf16 a[140:143], a[228:231], a[220:223], a[140:143]\n"
    "s_waitcnt vmcnt(0)\n"
    "v_cmp_ne_u32 vcc, s74, v179\n"
    "s_cbranch_vccz L_spun_o1\n"
    "s_mov_b32 s87, 0x40000\n"
    "L_spin_o1:\n"
    "buffer_load_dword v179, v178, s[52:55], s78 offen sc1\n"
    "s_waitcnt vmcnt(0)\n"
    "v_cmp_ne_u32 vcc, s74, v179\n"
    "s_cbranch_vccz L_spun_o1\n"
    "s_sleep 1\n"
    "s_sub_u32 s87, s87, 1\n"
    "s_cbranch_scc0 L_spin_o1\n"
    "L_spun_o1:\n"
    "s_add_u32 m0, s82, 53248\n"
    "s_add_u32 s72, s8, 0\n"
    "buffer_load_dwordx4 v246, s[20:23], s72 offen sc1 lds\n"
    "buffer_load_dwordx4 v246, s[20:23], s72 offen offset:1024 sc1 lds\n"
    "s_add_u32 m0, s82, 55296\n"
    "s_add_u32 s72, s72, s13\n"
    "buffer_load_dwordx4 v246, s[20:23], s72 offen sc1 lds\n"
    "buffer_load_dwordx4 v246, s[20:23], s72 offen offset:1024 sc1 lds\n"
    "s_add_u32 m0, s82, 106496\n"
    "s_add_u32 s72, s8, 2048\n"
    "buffer_load_dwordx4 v246, s[20:23], s72 offen sc1 lds\n"
    "buffer_load_dwordx4 v246, s[20:23], s72 offen offset:1024 sc1 lds\n"
    "s_add_u32 m0, s82, 108544\n"
    "s_add_u32 s72, s72, s13\n"
    "buffer_load_dwordx4 v246, s[20:23], s72 offen sc1 lds\n"
    "buffer_load_dwordx4 v246, s[20:23], s72 offen offset:1024 sc1 lds\n"
    "s_sub_u32 s4, 3, s10\n"
    "s_xor_b32 s4, s4, s11\n"
    "s_add_u32 s4, s4, s12\n"
    "s_lshl_b32 s8, s4, 12\n"
    "s_add_u32 s8, s8, s80\n"
    "s_mul_i32 s9, s4, 147456\n"
    "s_add_u32 s9, s9, s81\n"
    "v_lshrrev_b32 v178, 1, v253\n"
    "v_and_b32 v178, 1, v178\n"
    "s_xor_b32 s78, s69, 2\n"
    "v_xor_b32 v178, s78, v178\n"
    "v_lshlrev_b32 v178, 2, v178\n"
    "v_and_b32 v179, 1, v253\n"
    "v_add_u32 v178, v178, v179\n"
    "s_lshr_b32 s78, s69, 1\n"
    "s_and_b32 s78, s78, 2\n"
    "s_lshl_b32 s78, s78, 7\n"
    "s_add_u32 s78, s78, 32768\n"
    "v_lshlrev_b32 v178, 7, v178\n"
    "buffer_load_dword v179, v178, s[52:55], s78 offen sc1\n"
    "s_waitcnt vmcnt(5)\n"
    "s_barrier\n"
    "ds_read_b64_tr_b16 a[176:177], v248 offset:0\n"
    "ds_read_b64_tr_b16 a[178:179], v248 offset:9216\n"
    "ds_read_b64_tr_b16 a[180:181], v248 offset:18432\n"
    "ds_read_b64_tr_b16 a[182:183], v248 offset:27648\n"
    "ds_read_b128 a[144:147], v250 offset:0\n"
    "ds_read_b128 a[148:151], v250 offset:1024\n"
    "ds_read_b128 a[152:155], v250 offset:2048\n"
    "ds_read_b128 a[156:159], v250 offset:3072\n"
    "ds_read_b128 a[160:163], v250 offset:4096\n"
    "ds_read_b128 a[164:167], v250 offset:5120\n"
    "ds_read_b128 a[168:171], v250 offset:6144\n"
    "ds_read_b128 a[172:175], v250 offset:7168\n"
    "ds_read_b64_tr_b16 a[184:185], v249 offset:0\n"
    "ds_read_b64_tr_b16 a[186:187], v249 offset:9216\n"
    "ds_read_b64_tr_b16 a[188:189], v249 offset:18432\n"
    "ds_read_b64_tr_b16 a[190:191], v249 offset:27648\n"
    "s_waitcnt lgkmcnt(4)\n"
    "v_mfma_f32_16x16x32_bf16 a[0:3], a[176:179], a[144:147], a[0:3]\n"
    "s_add_u32 s86, s79, 0\n"
    "ds_read_b64_tr_b16 a[224:225], v248 offset:512\n"
    "v_mfma_f32_16x16x32_bf16 a[0:3], a[180:183], a[148:151], a[0:3]\n"
    "ds_read_b64_tr_b16 a[226:227], v248 offset:9728\n"
    "v_mfma_f32_16x16x32_bf16 a[4:7], a[176:179], a[152:155], a[4:7]\n"
    "ds_read_b64_tr_b16 a[228:229], v248 offset:18944\n"
    "v_mfma_f32_16x16x32_bf16 a[4:7], a[180:183], a[156:159], a[4:7]\n"
    "ds_read_b64_tr_b16 a[230:231], v248 offset:28160\n"
    "v_mfma_f32_16x16x32_bf16 a[8:11], a[176:179], a[160:163], a[8:11]\n"
    "s_add_u32 m0, s83, 16384\n"
    "s_not_b64 exec, s[32:33]\n"
    "buffer_store_dwordx4 v[0:3], v246, s[48:51], s86 offen offset:0 sc1\n"
    "s_mov_b64 exec, -1\n"
    "v_mfma_f32_16x16x32_bf16 a[8:11], a[180:183], a[164:167], a[8:11]\n"
    "s_add_u32 s72, s9, 0\n"
    "buffer_load_dwordx4 v247, s[24:27], s72 offen lds\n"
    "v_mfma_f32_16x16x32_bf16 a[12:15], a[176:179], a[168:171], a[12:15]\n"
    "s_add_u32 m0, s83, 17408\n"
    "s_not_b64 exec, s[32:33]\n"
    "buffer_store_dwordx4 v[8:11], v246, s[48:51], s86 offen offset:1024 sc1\n"
    "s_mov_b64 exec, -1\n"
    "v_mfma_f32_16x16x32_bf16 a[12:15], a[180:183], a[172:175], a[12:15]\n"
    "s_waitcnt lgkmcnt(4)\n"
    "s_add_u32 s72, s9, 128\n"
    "buffer_load_dwordx4 v247, s[24:27], s72 offen lds\n"
    "v_mfma_f32_16x16x32_bf16 a[16:19], a[184:187], a[144:147], a[16:19]\n"
    "ds_read_b64_tr_b16 a[176:177], v249 offset:512\n"
    "v_mfma_f32_16x16x32_bf16 a[16:19], a[188:191], a[148:151], a[16:19]\n"
    "ds_read_b64_tr_b16 a[178:179], v249 offset:9728\n"
    "v_mfma_f32_16x16x32_bf16 a[20:23], a[184:187], a[152:155], a[20:23]\n"
    "ds_read_b64_tr_b16 a[180:181], v249 offset:18944\n"
    "v_mfma_f32_16x16x32_bf16 a[20:23], a[188:191], a[156:159], a[20:23]\n"
    "ds_read_b64_tr_b16 a[182:183], v249 offset:28160\n"
    "v_mfma_f32_16x16x32_bf16 a[24:27], a[184:187], a[160:163], a[24:27]\n"
    "s_add_u32 m0, s83, 18432\n"
    "s_not_b64 exec, s[32:33]\n"
    "buffer_store_dwordx4 v[16:19], v246, s[48:51], s86 offen offset:2048 sc1\n"
    "s_mov_b64 exec, -1\n"
    "v_mfma_f32_16x16x32_bf16 a[24:27], a[188:191], a[164:167], a[24:27]\n"
    "s_add_u32 s72, s9, 256\n"
    "buffer_load_dwordx4 v247, s[24:27], s72 offen lds\n"
    "v_mfma_f32_16x16x32_bf16 a[28:31], a[184:187], a[168:171], a[28:31]\n"
    "s_add_u32 m0, s83, 19456\n"
    "v_mfma_f32_16x16x32_bf16 a[28:31], a[188:191], a[172:175], a[28:31]\n"
    "s_waitcnt lgkmcnt(4)\n"
    "s_add_u32 s72, s9, 384\n"
    "buffer_load_dwordx4 v247, s[24:27], s72 offen lds\n"
    "v_mfma_f32_16x16x32_bf16 a[32:35], a[224:227], a[144:147], a[32:35]\n"
    "ds_read_b64_tr_b16 a[184:185], v248 offset:1024\n"
    "v_mfma_f32_16x16x32_bf16 a[32:35], a[228:231], a[148:151], a[32:35]\n"
    "ds_read_b64_tr_b16 a[186:187], v248 offset:10240\n"
    "v_mfma_f32_16x16x32_bf16 a[36:39], a[224:227], a[152:155], a[36:39]\n"
    "ds_read_b64_tr_b16 a[188:189], v248 offset:19456\n"
    "v_mfma_f32_16x16x32_bf16 a[36:39], a[228:231], a[156:159], a[36:39]\n"
    "ds_read_b64_tr_b16 a[190:191], v248 offset:28672\n"
    "v_mfma_f32_16x16x32_bf16 a[40:43], a[224:227], a[160:163], a[40:43]\n"
    "s_add_u32 m0, s66, 16384\n"
    "s_not_b64 exec, s[32:33]\n"
    "buffer_store_dwordx4 v[24:27], v246, s[48:51], s86 offen offset:3072 sc1\n"
    "s_mov_b64 exec, -1\n"
    "v_mfma_f32_16x16x32_bf16 a[40:43], a[228:231], a[164:167], a[40:43]\n"
    "s_add_u32 s72, s9, 0\n"
    "s_mov_b32 exec_hi, 0\n"
    "buffer_load_dwordx4 v245, s[24:27], s72 offen lds\n"
    "s_mov_b32 exec_hi, -1\n"
    "v_mfma_f32_16x16x32_bf16 a[44:47], a[224:227], a[168:171], a[44:47]\n"
    "s_add_u32 m0, s83, 20992\n"
    "v_mfma_f32_16x16x32_bf16 a[44:47], a[228:231], a[172:175], a[44:47]\n"
    "s_waitcnt lgkmcnt(4)\n"
    "s_add_u32 s72, s9, 9216\n"
    "buffer_load_dwordx4 v247, s[24:27], s72 offen lds\n"
    "v_mfma_f32_16x16x32_bf16 a[48:51], a[176:179], a[144:147], a[48:51]\n"
    "s_add_u32 s86, s79, 131072\n"
    "ds_read_b64_tr_b16 a[224:225], v249 offset:1024\n"
    "v_mfma_f32_16x16x32_bf16 a[48:51], a[180:183], a[148:151], a[48:51]\n"
    "ds_read_b64_tr_b16 a[226:227], v249 offset:10240\n"
    "v_mfma_f32_16x16x32_bf16 a[52:55], a[176:179], a[152:155], a[52:55]\n"
    "ds_read_b64_tr_b16 a[228:229], v249 offset:19456\n"
    "v_mfma_f32_16x16x32_bf16 a[52:55], a[180:183], a[156:159], a[52:55]\n"
    "ds_read_b64_tr_b16 a[230:231], v249 offset:28672\n"
    "v_mfma_f32_16x16x32_bf16 a[56:59], a[176:179], a[160:163], a[56:59]\n"
    "s_add_u32 m0, s83, 22016\n"
    "s_not_b64 exec, s[32:33]\n"
    "buffer_store_dwordx4 v[32:35], v246, s[48:51], s86 offen offset:0 sc1\n"
    "s_mov_b64 exec, -1\n"
    "v_mfma_f32_16x16x32_bf16 a[56:59], a[180:183], a[164:167], a[56:59]\n"
    "s_add_u32 s72, s9, 9344\n"
    "buffer_load_dwordx4 v247, s[24:27], s72 offen lds\n"
    "v_mfma_f32_16x16x32_bf16 a[60:63], a[176:179], a[168:171], a[60:63]\n"
    "s_add_u32 m0, s83, 23040\n"
    "s_not_b64 exec, s[32:33]\n"
    "buffer_store_dwordx4 v[40:43], v246, s[48:51], s86 offen offset:1024 sc1\n"
    "s_mov_b64 exec, -1\n"
    "v_mfma_f32_16x16x32_bf16 a[60:63], a[180:183], a[172:175], a[60:63]\n"
    "s_waitcnt lgkmcnt(4)\n"
    "s_add_u32 s72, s9, 9472\n"
    "buffer_load_dwordx4 v247, s[24:27], s72 offen lds\n"
    "v_mfma_f32_16x16x32_bf16 a[64:67], a[184:187], a[144:147], a[64:67]\n"
    "ds_read_b64_tr_b16 a[176:177], v248 offset:1536\n"
    "v_mfma_f32_16x16x32_bf16 a[64:67], a[188:191], a[148:151], a[64:67]\n"
    "ds_read_b64_tr_b16 a[178:179], v248 offset:10752\n"
    "v_mfma_f32_16x16x32_bf16 a[68:71], a[184:187], a[152:155], a[68:71]\n"
    "ds_read_b64_tr_b16 a[180:181], v248 offset:19968\n"
    "v_mfma_f32_16x16x32_bf16 a[68:71], a[188:191], a[156:159], a[68:71]\n"
    "ds_read_b64_tr_b16 a[182:183], v248 offset:29184\n"
    "v_mfma_f32_16x16x32_bf16 a[72:75], a[184:187], a[160:163], a[72:75]\n"
    "s_add_u32 m0, s83, 24064\n"
    "s_not_b64 exec, s[32:33]\n"
    "buffer_store_dwordx4 v[48:51], v246, s[48:51], s86 offen offset:2048 sc1\n"
    "s_mov_b64 exec, -1\n"
    "v_mfma_f32_16x16x32_bf16 a[72:75], a[188:191], a[164:167], a[72:75]\n"
    "s_add_u32 s72, s9, 9600\n"
    "buffer_load_dwordx4 v247, s[24:27], s72 offen lds\n"
    "v_mfma_f32_16x16x32_bf16 a[76:79], a[184:187], a[168:171], a[76:79]\n"
    "s_add_u32 m0, s66, 20992\n"
    "v_mfma_f32_16x16x32_bf16 a[76:79], a[188:191], a[172:175], a[76:79]\n"
    "s_waitcnt lgkmcnt(4)\n"
    "s_add_u32 s72, s9, 9216\n"
    "s_mov_b32 exec_hi, 0\n"
    "buffer_load_dwordx4 v245, s[24:27], s72 offen lds\n"
    "s_mov_b32 exec_hi, -1\n"
    "v_mfma_f32_16x16x32_bf16 a[80:83], a[224:227], a[144:147], a[80:83]\n"
    "ds_read_b64_tr_b16 a[184:185], v249 offset:1536\n"
    "v_mfma_f32_16x16x32_bf16 a[80:83], a[228:231], a[148:151], a[80:83]\n"
    "ds_read_b64_tr_b16 a[186:187], v249 offset:10752\n"
    "s_waitcnt vmcnt(17)\n"
    "v_cmp_ne_u32 vcc, s74, v179\n"
    "s_cbranch_vccz L_spun_o2\n"
    "s_mov_b32 s87, 0x40000\n"
    "L_spin_o2:\n"
    "buffer_load_dword v179, v178, s[52:55], s78 offen sc1\n"
    "s_waitcnt vmcnt(0)\n"
    "v_cmp_ne_u32 vcc, s74, v179\n"
    "s_cbranch_vccz L_spun_o2\n"
    "s_sleep 1\n"
    "s_sub_u32 s87, s87, 1\n"
    "s_cbranch_scc0 L_spin_o2\n"
    "L_spun_o2:\n"
    "v_mfma_f32_16x16x32_bf16 a[84:87], a[224:227], a[152:155], a[84:87]\n"
    "ds_read_b64_tr_b16 a[188:189], v249 offset:19968\n"
    "v_mfma_f32_16x16x32_bf16 a[84:87], a[228:231], a[156:159], a[84:87]\n"
    "ds_read_b64_tr_b16 a[190:191], v249 offset:29184\n"
    "v_mfma_f32_16x16x32_bf16 a[88:91], a[224:227], a[160:163], a[88:91]\n"
    "s_add_u32 m0, s82, 0\n"
    "s_not_b64 exec, s[32:33]\n"
    "buffer_store_dwordx4 v[56:59], v246, s[48:51], s86 offen offset:3072 sc1\n"
    "s_mov_b64 exec, -1\n"
    "v_mfma_f32_16x16x32_bf16 a[88:91], a[228:231], a[164:167], a[88:91]\n"
    "s_add_u32 s72, s8, 0\n"
    "buffer_load_dwordx4 v246, s[20:23], s72 offen sc1 lds\n"
    "v_mfma_f32_16x16x32_bf16 a[92:95], a[224:227], a[168:171], a[92:95]\n"
    "v_mfma_f32_16x16x32_bf16 a[92:95], a[228:231], a[172:175], a[92:95]\n"
    "s_waitcnt lgkmcnt(4)\n"
    "buffer_load_dwordx4 v246, s[20:23], s72 offen offset:1024 sc1 lds\n"
    "v_mfma_f32_16x16x32_bf16 a[96:99], a[176:179], a[144:147], a[96:99]\n"
    "s_add_u32 s86, s79, 262144\n"
    "ds_read_b64_tr_b16 a[224:225], v248 offset:2048\n"
    "v_mfma_f32_16x16x32_bf16 a[96:99], a[180:183], a[148:151], a[96:99]\n"
    "ds_read_b64_tr_b16 a[226:227], v248 offset:11264\n"
    "v_mfma_f32_16x16x32_bf16 a[100:103], a[176:179], a[152:155], a[100:103]\n"
    "ds_read_b64_tr_b16 a[228:229], v248 offset:20480\n"
    "v_mfma_f32_16x16x32_bf16 a[100:103], a[180:183], a[156:159], a[100:103]\n"
    "ds_read_b64_tr_b16 a[230:231], v248 offset:29696\n"
    "v_mfma_f32_16x16x32_bf16 a[104:107], a[176:179], a[160:163], a[104:107]\n"
    "s_add_u32 m0, s82, 2048\n"
    "s_not_b64 exec, s[32:33]\n"
    "buffer_store_dwordx4 v[64:67], v246, s[48:51], s86 offen offset:0 sc1\n"
    "s_mov_b64 exec, -1\n"
    "v_mfma_f32_16x16x32_bf16 a[104:107], a[180:183], a[164:167], a[104:107]\n"
    "s_add_u32 s72, s72, s13\n"
    "buffer_load_dwordx4 v246, s[20:23], s72 offen sc1 lds\n"
    "v_mfma_f32_16x16x32_bf16 a[108:111], a[176:179], a[168:171], a[108:111]\n"
    "v_add_u32 v248, 53248, v248\n"
    "v_add_u32 v249, 53248, v249\n"
    "v_mfma_f32_16x16x32_bf16 a[108:111], a[180:183], a[172:175], a[108:111]\n"
    "s_waitcnt lgkmcnt(4)\n"
    "v_add_u32 v250, 53248, v250\n"
    "v_mfma_f32_16x16x32_bf16 a[112:115], a[184:187], a[144:147], a[112:115]\n"
    "buffer_load_dwordx4 v246, s[20:23], s72 offen offset:1024 sc1 lds\n"
    "v_mfma_f32_16x16x32_bf16 a[112:115], a[188:191], a[148:151], a[112:115]\n"
    "s_waitcnt vmcnt(24) lgkmcnt(0)\n"
    "s_barrier\n"
    "v_mfma_f32_16x16x32_bf16 a[116:119], a[184:187], a[152:155], a[116:119]\n"
    "ds_read_b64_tr_b16 a[176:177], v248 offset:0\n"
    "v_mfma_f32_16x16x32_bf16 a[116:119], a[188:191], a[156:159], a[116:119]\n"
    "ds_read_b64_tr_b16 a[178:179], v248 offset:9216\n"
    "v_mfma_f32_16x16x32_bf16 a[120:123], a[184:187], a[160:163], a[120:123]\n"
    "ds_read_b64_tr_b16 a[180:181], v248 offset:18432\n"
    "ds_read_b128 a[192:195], v250 offset:0\n"
    "v_mfma_f32_16x16x32_bf16 a[120:123], a[188:191], a[164:167], a[120:123]\n"
    "ds_read_b64_tr_b16 a[182:183], v248 offset:27648\n"
    "ds_read_b128 a[196:199], v250 offset:1024\n"
    "v_mfma_f32_16x16x32_bf16 a[124:127], a[184:187], a[168:171], a[124:127]\n"
    "ds_read_b128 a[200:203], v250 offset:2048\n"
    "v_mfma_f32_16x16x32_bf16 a[124:127], a[188:191], a[172:175], a[124:127]\n"
    "ds_read_b128 a[204:207], v250 offset:3072\n"
    "v_mfma_f32_16x16x32_bf16 a[128:131], a[224:227], a[144:147], a[128:131]\n"
    "ds_read_b128 a[208:211], v250 offset:4096\n"
    "ds_read_b128 a[212:215], v250 offset:5120\n"
    "v_mfma_f32_16x16x32_bf16 a[128:131], a[228:231], a[148:151], a[128:131]\n"
    "ds_read_b128 a[216:219], v250 offset:6144\n"
    "ds_read_b128 a[220:223], v250 offset:7168\n"
    "v_mfma_f32_16x16x32_bf16 a[132:135], a[224:227], a[152:155], a[132:135]\n"
    "ds_read_b64_tr_b16 a[184:185], v249 offset:0\n"
    "v_mfma_f32_16x16x32_bf16 a[132:135], a[228:231], a[156:159], a[132:135]\n"
    "ds_read_b64_tr_b16 a[186:187], v249 offset:9216\n"
    "v_mfma_f32_16x16x32_bf16 a[136:139], a[224:227], a[160:163], a[136:139]\n"
    "ds_read_b64_tr_b16 a[188:189], v249 offset:18432\n"
    "v_mfma_f32_16x16x32_bf16 a[136:139], a[228:231], a[164:167], a[136:139]\n"
    "ds_read_b64_tr_b16 a[190:191], v249 offset:27648\n"
    "v_mfma_f32_16x16x32_bf16 a[140:143], a[224:227], a[168:171], a[140:143]\n"
    "s_not_b64 exec, s[32:33]\n"
    "buffer_store_dwordx4 v[72:75], v246, s[48:51], s86 offen offset:1024 sc1\n"
    "s_mov_b64 exec, -1\n"
    "v_mfma_f32_16x16x32_bf16 a[140:143], a[228:231], a[172:175], a[140:143]\n"
    "s_waitcnt lgkmcnt(4)\n"
    "s_not_b64 exec, s[32:33]\n"
    "buffer_store_dwordx4 v[80:83], v246, s[48:51], s86 offen offset:2048 sc1\n"
    "s_mov_b64 exec, -1\n"
    "v_mfma_f32_16x16x32_bf16 a[0:3], a[176:179], a[192:195], a[0:3]\n"
    "ds_read_b64_tr_b16 a[224:225], v248 offset:512\n"
    "v_mfma_f32_16x16x32_bf16 a[0:3], a[180:183], a[196:199], a[0:3]\n"
    "ds_read_b64_tr_b16 a[226:227], v248 offset:9728\n"
    "v_mfma_f32_16x16x32_bf16 a[4:7], a[176:179], a[200:203], a[4:7]\n"
    "ds_read_b64_tr_b16 a[228:229], v248 offset:18944\n"
    "v_mfma_f32_16x16x32_bf16 a[4:7], a[180:183], a[204:207], a[4:7]\n"
    "ds_read_b64_tr_b16 a[230:231], v248 offset:28160\n"
    "v_mfma_f32_16x16x32_bf16 a[8:11], a[176:179], a[208:211], a[8:11]\n"
    "s_add_u32 m0, s82, 53248\n"
    "s_not_b64 exec, s[32:33]\n"
    "buffer_store_dwordx4 v[88:91], v246, s[48:51], s86 offen offset:3072 sc1\n"
    "s_mov_b64 exec, -1\n"
    "v_mfma_f32_16x16x32_bf16 a[8:11], a[180:183], a[212:215], a[8:11]\n"
    "s_add_u32 s72, s8, 2048\n"
    "buffer_load_dwordx4 v246, s[20:23], s72 offen sc1 lds\n"
    "v_mfma_f32_16x16x32_bf16 a[12:15], a[176:179], a[216:219], a[12:15]\n"
    "s_add_u32 s86, s79, 393216\n"
    "s_not_b64 exec, s[32:33]\n"
    "buffer_store_dwordx4 v[96:99], v246, s[48:51], s86 offen offset:0 sc1\n"
    "s_mov_b64 exec, -1\n"
    "v_mfma_f32_16x16x32_bf16 a[12:15], a[180:183], a[220:223], a[12:15]\n"
    "s_waitcnt lgkmcnt(4)\n"
    "buffer_load_dwordx4 v246, s[20:23], s72 offen offset:1024 sc1 lds\n"
    "v_mfma_f32_16x16x32_bf16 a[16:19], a[184:187], a[192:195], a[16:19]\n"
    "ds_read_b64_tr_b16 a[176:177], v249 offset:512\n"
    "v_mfma_f32_16x16x32_bf16 a[16:19], a[188:191], a[196:199], a[16:19]\n"
    "ds_read_b64_tr_b16 a[178:179], v249 offset:9728\n"
    "v_mfma_f32_16x16x32_bf16 a[20:23], a[184:187], a[200:203], a[20:23]\n"
    "ds_read_b64_tr_b16 a[180:181], v249 offset:18944\n"
    "v_mfma_f32_16x16x32_bf16 a[20:23], a[188:191], a[204:207], a[20:23]\n"
    "ds_read_b64_tr_b16 a[182:183], v249 offset:28160\n"
    "v_mfma_f32_16x16x32_bf16 a[24:27], a[184:187], a[208:211], a[24:27]\n"
    "s_add_u32 m0, s82, 55296\n"
    "s_not_b64 exec, s[32:33]\n"
    "buffer_store_dwordx4 v[104:107], v246, s[48:51], s86 offen offset:1024 sc1\n"
    "s_mov_b64 exec, -1\n"
    "v_mfma_f32_16x16x32_bf16 a[24:27], a[188:191], a[212:215], a[24:27]\n"
    "s_add_u32 s72, s72, s13\n"
    "buffer_load_dwordx4 v246, s[20:23], s72 offen sc1 lds\n"
    "v_mfma_f32_16x16x32_bf16 a[28:31], a[184:187], a[216:219], a[28:31]\n"
    "s_not_b64 exec, s[32:33]\n"
    "buffer_store_dwordx4 v[112:115], v246, s[48:51], s86 offen offset:2048 sc1\n"
    "s_mov_b64 exec, -1\n"
    "v_mfma_f32_16x16x32_bf16 a[28:31], a[188:191], a[220:223], a[28:31]\n"
    "s_waitcnt lgkmcnt(4)\n"
    "buffer_load_dwordx4 v246, s[20:23], s72 offen offset:1024 sc1 lds\n"
    "v_mfma_f32_16x16x32_bf16 a[32:35], a[224:227], a[192:195], a[32:35]\n"
    "ds_read_b64_tr_b16 a[184:185], v248 offset:1024\n"
    "v_mfma_f32_16x16x32_bf16 a[32:35], a[228:231], a[196:199], a[32:35]\n"
    "ds_read_b64_tr_b16 a[186:187], v248 offset:10240\n"
    "v_mfma_f32_16x16x32_bf16 a[36:39], a[224:227], a[200:203], a[36:39]\n"
    "ds_read_b64_tr_b16 a[188:189], v248 offset:19456\n"
    "v_mfma_f32_16x16x32_bf16 a[36:39], a[228:231], a[204:207], a[36:39]\n"
    "ds_read_b64_tr_b16 a[190:191], v248 offset:28672\n"
    "v_mfma_f32_16x16x32_bf16 a[40:43], a[224:227], a[208:211], a[40:43]\n"
    "s_add_u32 m0, s83, 69632\n"
    "s_not_b64 exec, s[32:33]\n"
    "buffer_store_dwordx4 v[120:123], v246, s[48:51], s86 offen offset:3072 sc1\n"
    "s_mov_b64 exec, -1\n"
    "v_mfma_f32_16x16x32_bf16 a[40:43], a[228:231], a[212:215], a[40:43]\n"
    "s_add_u32 s72, s9, 73728\n"
    "buffer_load_dwordx4 v247, s[24:27], s72 offen lds\n"
    "v_mfma_f32_16x16x32_bf16 a[44:47], a[224:227], a[216:219], a[44:47]\n"
    "s_add_u32 m0, s83, 70656\n"
    "v_mfma_f32_16x16x32_bf16 a[44:47], a[228:231], a[220:223], a[44:47]\n"
    "s_waitcnt lgkmcnt(4)\n"
    "s_add_u32 s72, s9, 73856\n"
    "buffer_load_dwordx4 v247, s[24:27], s72 offen lds\n"
    "v_mfma_f32_16x16x32_bf16 a[48:51], a[176:179], a[192:195], a[48:51]\n"
    "ds_read_b64_tr_b16 a[224:225], v249 offset:1024\n"
    "v_mfma_f32_16x16x32_bf16 a[48:51], a[180:183], a[196:199], a[48:51]\n"
    "ds_read_b64_tr_b16 a[226:227], v249 offset:10240\n"
    "v_mfma_f32_16x16x32_bf16 a[52:55], a[176:179], a[200:203], a[52:55]\n"
    "ds_read_b64_tr_b16 a[228:229], v249 offset:19456\n"
    "v_mfma_f32_16x16x32_bf16 a[52:55], a[180:183], a[204:207], a[52:55]\n"
    "ds_read_b64_tr_b16 a[230:231], v249 offset:28672\n"
    "v_mfma_f32_16x16x32_bf16 a[56:59], a[176:179], a[208:211], a[56:59]\n"
    "s_add_u32 m0, s83, 71680\n"
    "v_mfma_f32_16x16x32_bf16 a[56:59], a[180:183], a[212:215], a[56:59]\n"
    "s_add_u32 s72, s9, 73984\n"
    "buffer_load_dwordx4 v247, s[24:27], s72 offen lds\n"
    "v_mfma_f32_16x16x32_bf16 a[60:63], a[176:179], a[216:219], a[60:63]\n"
    "s_add_u32 m0, s83, 72704\n"
    "v_mfma_f32_16x16x32_bf16 a[60:63], a[180:183], a[220:223], a[60:63]\n"
    "s_waitcnt lgkmcnt(4)\n"
    "s_add_u32 s72, s9, 74112\n"
    "buffer_load_dwordx4 v247, s[24:27], s72 offen lds\n"
    "v_mfma_f32_16x16x32_bf16 a[64:67], a[184:187], a[192:195], a[64:67]\n"
    "ds_read_b64_tr_b16 a[176:177], v248 offset:1536\n"
    "v_mfma_f32_16x16x32_bf16 a[64:67], a[188:191], a[196:199], a[64:67]\n"
    "ds_read_b64_tr_b16 a[178:179], v248 offset:10752\n"
    "v_mfma_f32_16x16x32_bf16 a[68:71], a[184:187], a[200:203], a[68:71]\n"
    "ds_read_b64_tr_b16 a[180:181], v248 offset:19968\n"
    "v_mfma_f32_16x16x32_bf16 a[68:71], a[188:191], a[204:207], a[68:71]\n"
    "ds_read_b64_tr_b16 a[182:183], v248 offset:29184\n"
    "v_mfma_f32_16x16x32_bf16 a[72:75], a[184:187], a[208:211], a[72:75]\n"
    "s_add_u32 m0, s66, 69632\n"
    "v_mfma_f32_16x16x32_bf16 a[72:75], a[188:191], a[212:215], a[72:75]\n"
    "s_add_u32 s72, s9, 73728\n"
    "s_mov_b32 exec_hi, 0\n"
    "buffer_load_dwordx4 v245, s[24:27], s72 offen lds\n"
    "s_mov_b32 exec_hi, -1\n"
    "v_mfma_f32_16x16x32_bf16 a[76:79], a[184:187], a[216:219], a[76:79]\n"
    "s_add_u32 m0, s83, 74240\n"
    "v_mfma_f32_16x16x32_bf16 a[76:79], a[188:191], a[220:223], a[76:79]\n"
    "s_waitcnt lgkmcnt(4)\n"
    "s_add_u32 s72, s9, 82944\n"
    "buffer_load_dwordx4 v247, s[24:27], s72 offen lds\n"
    "v_mfma_f32_16x16x32_bf16 a[80:83], a[224:227], a[192:195], a[80:83]\n"
    "ds_read_b64_tr_b16 a[184:185], v249 offset:1536\n"
    "v_mfma_f32_16x16x32_bf16 a[80:83], a[228:231], a[196:199], a[80:83]\n"
    "ds_read_b64_tr_b16 a[186:187], v249 offset:10752\n"
    "v_mfma_f32_16x16x32_bf16 a[84:87], a[224:227], a[200:203], a[84:87]\n"
    "ds_read_b64_tr_b16 a[188:189], v249 offset:19968\n"
    "v_mfma_f32_16x16x32_bf16 a[84:87], a[228:231], a[204:207], a[84:87]\n"
    "ds_read_b64_tr_b16 a[190:191], v249 offset:29184\n"
    "v_mfma_f32_16x16x32_bf16 a[88:91], a[224:227], a[208:211], a[88:91]\n"
    "s_add_u32 m0, s83, 75264\n"
    "v_mfma_f32_16x16x32_bf16 a[88:91], a[228:231], a[212:215], a[88:91]\n"
    "s_add_u32 s72, s9, 83072\n"
    "buffer_load_dwordx4 v247, s[24:27], s72 offen lds\n"
    "v_mfma_f32_16x16x32_bf16 a[92:95], a[224:227], a[216:219], a[92:95]\n"
    "s_add_u32 m0, s83, 76288\n"
    "v_mfma_f32_16x16x32_bf16 a[92:95], a[228:231], a[220:223], a[92:95]\n"
    "s_waitcnt lgkmcnt(4)\n"
    "s_add_u32 s72, s9, 83200\n"
    "buffer_load_dwordx4 v247, s[24:27], s72 offen lds\n"
    "v_mfma_f32_16x16x32_bf16 a[96:99], a[176:179], a[192:195], a[96:99]\n"
    "ds_read_b64_tr_b16 a[224:225], v248 offset:2048\n"
    "v_mfma_f32_16x16x32_bf16 a[96:99], a[180:183], a[196:199], a[96:99]\n"
    "ds_read_b64_tr_b16 a[226:227], v248 offset:11264\n"
    "v_mfma_f32_16x16x32_bf16 a[100:103], a[176:179], a[200:203], a[100:103]\n"
    "ds_read_b64_tr_b16 a[228:229], v248 offset:20480\n"
    "v_mfma_f32_16x16x32_bf16 a[100:103], a[180:183], a[204:207], a[100:103]\n"
    "ds_read_b64_tr_b16 a[230:231], v248 offset:29696\n"
    "v_mfma_f32_16x16x32_bf16 a[104:107], a[176:179], a[208:211], a[104:107]\n"
    "s_add_u32 m0, s83, 77312\n"
    "v_mfma_f32_16x16x32_bf16 a[104:107], a[180:183], a[212:215], a[104:107]\n"
    "s_add_u32 s72, s9, 83328\n"
    "buffer_load_dwordx4 v247, s[24:27], s72 offen lds\n"
    "v_mfma_f32_16x16x32_bf16 a[108:111], a[176:179], a[216:219], a[108:111]\n"
    "v_add_u32 v248, 4294860800, v248\n"
    "v_add_u32 v249, 4294860800, v249\n"
    "v_mfma_f32_16x16x32_bf16 a[108:111], a[180:183], a[220:223], a[108:111]\n"
    "s_waitcnt lgkmcnt(4)\n"
    "s_add_u32 m0, s66, 74240\n"
    "v_add_u32 v250, 4294860800, v250\n"
    "v_mfma_f32_16x16x32_bf16 a[112:115], a[184:187], a[192:195], a[112:115]\n"
    "s_add_u32 s72, s9, 82944\n"
    "s_mov_b32 exec_hi, 0\n"
    "buffer_load_dwordx4 v245, s[24:27], s72 offen lds\n"
    "s_mov_b32 exec_hi, -1\n"
    "v_mfma_f32_16x16x32_bf16 a[112:115], a[188:191], a[196:199], a[112:115]\n"
    "s_waitcnt vmcnt(21) lgkmcnt(0)\n"
    "s_barrier\n"
    "v_mfma_f32_16x16x32_bf16 a[116:119], a[184:187], a[200:203], a[116:119]\n"
    "ds_read_b64_tr_b16 a[176:177], v248 offset:0\n"
    "v_mfma_f32_16x16x32_bf16 a[116:119], a[188:191], a[204:207], a[116:119]\n"
    "ds_read_b64_tr_b16 a[178:179], v248 offset:9216\n"
    "v_mfma_f32_16x16x32_bf16 a[120:123], a[184:187], a[208:211], a[120:123]\n"
    "s_sub_u32 s4, 4, s10\n"
    "ds_read_b64_tr_b16 a[180:181], v248 offset:18432\n"
    "ds_read_b128 a[144:147], v250 offset:0\n"
    "v_mfma_f32_16x16x32_bf16 a[120:123], a[188:191], a[212:215], a[120:123]\n"
    "s_xor_b32 s4, s4, s11\n"
    "ds_read_b64_tr_b16 a[182:183], v248 offset:27648\n"
    "ds_read_b128 a[148:151], v250 offset:1024\n"
    "v_mfma_f32_16x16x32_bf16 a[124:127], a[184:187], a[216:219], a[124:127]\n"
    "s_add_u32 s4, s4, s12\n"
    "ds_read_b128 a[152:155], v250 offset:2048\n"
    "v_mfma_f32_16x16x32_bf16 a[124:127], a[188:191], a[220:223], a[124:127]\n"
    "s_lshl_b32 s8, s4, 12\n"
    "ds_read_b128 a[156:159], v250 offset:3072\n"
    "v_mfma_f32_16x16x32_bf16 a[128:131], a[224:227], a[192:195], a[128:131]\n"
    "s_add_u32 s8, s8, s80\n"
    "ds_read_b128 a[160:163], v250 offset:4096\n"
    "ds_read_b128 a[164:167], v250 offset:5120\n"
    "v_mfma_f32_16x16x32_bf16 a[128:131], a[228:231], a[196:199], a[128:131]\n"
    "s_mul_i32 s9, s4, 147456\n"
    "ds_read_b128 a[168:171], v250 offset:6144\n"
    "ds_read_b128 a[172:175], v250 offset:7168\n"
    "v_mfma_f32_16x16x32_bf16 a[132:135], a[224:227], a[200:203], a[132:135]\n"
    "s_add_u32 s9, s9, s81\n"
    "ds_read_b64_tr_b16 a[184:185], v249 offset:0\n"
    "v_mfma_f32_16x16x32_bf16 a[132:135], a[228:231], a[204:207], a[132:135]\n"
    "ds_read_b64_tr_b16 a[186:187], v249 offset:9216\n"
    "v_mfma_f32_16x16x32_bf16 a[136:139], a[224:227], a[208:211], a[136:139]\n"
    "ds_read_b64_tr_b16 a[188:189], v249 offset:18432\n"
    "v_mfma_f32_16x16x32_bf16 a[136:139], a[228:231], a[212:215], a[136:139]\n"
    "ds_read_b64_tr_b16 a[190:191], v249 offset:27648\n"
    "s_cmp_lg_u32 s32, 0\n"
    "s_cbranch_scc1 L_opub4\n"
    "s_waitcnt vmcnt(10)\n"
    "s_add_u32 s73, s84, 32768\n"
    "v_mov_b32 v176, s74\n"
    "s_mov_b64 exec, 1\n"
    "buffer_store_dword v176, off, s[52:55], s73 sc1\n"
    "s_mov_b64 exec, -1\n"
    "L_opub4:\n"
    "v_mfma_f32_16x16x32_bf16 a[140:143], a[224:227], a[216:219], a[140:143]\n"
    "v_mfma_f32_16x16x32_bf16 a[140:143], a[228:231], a[220:223], a[140:143]\n"
    "s_branch L_common\n"
    "L_side_ev:\n"
    "s_cmp_lt_u32 s71, 7\n"
    "s_cbranch_scc1 L_ev1\n"
    "s_cmp_lg_u32 s71, 15\n"
    "s_cbranch_scc1 L_ev0a\n"
    "s_add_u32 s73, s7, 20480\n"
    "v_mov_b32 v192, s74\n"
    "s_mov_b64 exec, 1\n"
    "buffer_store_dword v192, off, s[52:55], s73 sc1\n"
    "s_mov_b64 exec, -1\n"
    "s_branch L_back_ev\n"
    "L_ev0a:\n"
    "s_cmp_lg_u32 s71, 8\n"
    "s_cbranch_scc1 L_ev0b\n"
    "v_and_b32 v192, 7, v253\n"
    "v_lshlrev_b32 v192, 7, v192\n"
    "s_and_b32 s5, s7, 0xc00\n"
    "s_add_u32 s5, s5, 20480\n"
    "buffer_load_dword v193, v192, s[52:55], s5 offen sc1\n"
    "s_branch L_back_ev\n"
    "L_ev0b:\n"
    "s_waitcnt vmcnt(16)\n"
    "v_cmp_ne_u32 vcc, s74, v193\n"
    "s_cbranch_vccz L_back_ev\n"
    "s_mov_b32 s87, 0x40000\n"
    "L_spin_h:\n"
    "buffer_load_dword v193, v192, s[52:55], s5 offen sc1\n"
    "s_waitcnt vmcnt(0)\n"
    "v_cmp_ne_u32 vcc, s74, v193\n"
    "s_cbranch_vccz L_spun_h\n"
    "s_sleep 1\n"
    "s_sub_u32 s87, s87, 1\n"
    "s_cbranch_scc0 L_spin_h\n"
    "L_spun_h:\n"
    "s_branch L_back_ev\n"
    "L_ev1:\n"
    "s_cmp_eq_u32 s71, 1\n"
    "s_cbranch_scc1 L_ev2\n"
    "s_lshl_b32 s73, s70, 1\n"
    "s_add_u32 s73, s73, s75\n"
    "s_lshl_b32 s73, s73, 2\n"
    "s_add_u32 s73, s73, s68\n"
    "s_lshl_b32 s73, s73, 13\n"
    "s_sub_u32 s72, 5, s71\n"
    "s_lshl_b32 s72, s72, 19\n"
    "s_add_u32 s73, s73, s72\n"
    "s_sub_u32 s72, 9, s10\n"
    "s_mul_i32 s72, s72, 147456\n"
    "s_mul_i32 s86, s12, 147456\n"
    "s_cmp_lt_u32 s73, s72\n"
    "s_cselect_b32 s86, s86, 1073741824\n"
    "s_add_u32 s86, s86, s73\n"
    "v_lshlrev_b32 v200, 7, v253\n"
    "buffer_load_dword v201, v200, s[24:27], s86 offen\n"
    "s_branch L_back_ev\n"
    "L_ev2:\n"
    "s_getpc_b64 s[86:87]\n"
    "L_pc1:\n"
    "s_lshr_b32 s72, %1, 3\n"
    "s_lshl_b32 s72, s72, 2\n"
    "s_add_u32 s72, s72, s68\n"
    "s_lshl_b32 s72, s72, 9\n"
    "s_min_u32 s72, s72, L_safe-L_tail-512\n"
    "s_add_u32 s72, s72, L_tail-L_pc1\n"
    "s_ashr_i32 s73, s72, 31\n"
    "s_add_u32 s86, s86, s72\n"
    "s_addc_u32 s87, s87, s73\n"
    "v_lshlrev_b32 v200, 3, v253\n"
    "global_load_dwordx2 v[202:203], v200, s[86:87]\n"
    "s_branch L_back_ev\n"
    "L_safe:\n"
    "s_waitcnt vmcnt(0) lgkmcnt(0)\n"
    "v_and_b32 v170, 15, v253\n"
    "v_lshrrev_b32 v171, 4, v253\n"
    "v_lshlrev_b32 v168, 8, v170\n"
    "v_lshl_add_u32 v168, v171, 4, v168\n"
    "v_lshlrev_b32 v169, 10, v171\n"
    "v_lshl_add_u32 v169, v170, 1, v169\n"
    "v_mov_b32 v160, 0xff7fffff\n"
    "v_mov_b32 v172, 0\n"
    "v_mov_b32 v161, 0xff7fffff\n"
    "v_mov_b32 v173, 0\n"
    "v_mov_b32 v162, 0xff7fffff\n"
    "v_mov_b32 v174, 0\n"
    "v_mov_b32 v163, 0xff7fffff\n"
    "v_mov_b32 v175, 0\n"
    "s_nop 1\n"
    "v_mfma_f32_32x32x16_bf16 v[0:15], v[172:175], v[172:175], 0\n"
    "v_mfma_f32_32x32x16_bf16 v[16:31], v[172:175], v[172:175], 0\n"
    "v_mfma_f32_32x32x16_bf16 v[32:47], v[172:175], v[172:175], 0\n"
    "v_mfma_f32_32x32x16_bf16 v[48:63], v[172:175], v[172:175], 0\n"
    "v_mfma_f32_32x32x16_bf16 v[64:79], v[172:175], v[172:175], 0\n"
    "v_mfma_f32_32x32x16_bf16 v[80:95], v[172:175], v[172:175], 0\n"
    "v_mfma_f32_32x32x16_bf16 v[96:111], v[172:175], v[172:175], 0\n"
    "v_mfma_f32_32x32x16_bf16 v[112:127], v[172:175], v[172:175], 0\n"
    "v_mfma_f32_32x32x16_bf16 v[176:191], v[172:175], v[172:175], 0\n"
    "s_mov_b32 s86, 0\n"
    "L_safe_loop:\n"
    "s_add_u32 s87, s86, 0\n"
    "buffer_load_dwordx4 a[64:67], v168, s[36:39], s87 offen offset:0\n"
    "buffer_load_dwordx4 a[68:71], v168, s[36:39], s87 offen offset:64\n"
    "buffer_load_dwordx4 a[72:75], v168, s[36:39], s87 offen offset:128\n"
    "buffer_load_dwordx4 a[76:79], v168, s[36:39], s87 offen offset:192\n"
    "s_add_u32 s87, s86, 4096\n"
    "buffer_load_dwordx4 a[80:83], v168, s[36:39], s87 offen offset:0\n"
    "buffer_load_dwordx4 a[84:87], v168, s[36:39], s87 offen offset:64\n"
    "buffer_load_dwordx4 a[88:91], v168, s[36:39], s87 offen offset:128\n"
    "buffer_load_dwordx4 a[92:95], v168, s[36:39], s87 offen offset:192\n"
    "s_add_u32 s87, s86, 8192\n"
    "buffer_load_dwordx4 a[96:99], v168, s[36:39], s87 offen offset:0\n"
    "buffer_load_dwordx4 a[100:103], v168, s[36:39], s87 offen offset:64\n"
    "buffer_load_dwordx4 a[104:107], v168, s[36:39], s87 offen offset:128\n"
    "buffer_load_dwordx4 a[108:111], v168, s[36:39], s87 offen offset:192\n"
    "s_add_u32 s87, s86, 12288\n"
    "buffer_load_dwordx4 a[112:115], v168, s[36:39], s87 offen offset:0\n"
    "buffer_load_dwordx4 a[116:119], v168, s[36:39], s87 offen offset:64\n"
    "buffer_load_dwordx4 a[120:123], v168, s[36:39], s87 offen offset:128\n"
    "buffer_load_dwordx4 a[124:127], v168, s[36:39], s87 offen offset:192\n"
    "s_add_u32 s87, s86, 0\n"
    "buffer_load_ushort v128, v169, s[40:43], s87 offen offset:0\n"
    "buffer_load_ushort v129, v169, s[40:43], s87 offen offset:256\n"
    "buffer_load_ushort v130, v169, s[40:43], s87 offen offset:512\n"
    "buffer_load_ushort v131, v169, s[40:43], s87 offen offset:768\n"
    "s_add_u32 s87, s86, 4096\n"
    "buffer_load_ushort v132, v169, s[40:43], s87 offen offset:0\n"
    "buffer_load_ushort v133, v169, s[40:43], s87 offen offset:256\n"
    "buffer_load_ushort v134, v169, s[40:43], s87 offen offset:512\n"
    "buffer_load_ushort v135, v169, s[40:43], s87 offen offset:768\n"
    "s_waitcnt vmcnt(0)\n"
    "v_lshl_or_b32 v128, v129, 16, v128\n"
    "v_accvgpr_write_b32 a192, v128\n"
    "v_lshl_or_b32 v130, v131, 16, v130\n"
    "v_accvgpr_write_b32 a193, v130\n"
    "v_lshl_or_b32 v132, v133, 16, v132\n"
    "v_accvgpr_write_b32 a194, v132\n"
    "v_lshl_or_b32 v134, v135, 16, v134\n"
    "v_accvgpr_write_b32 a195, v134\n"
    "s_add_u32 s87, s86, 8192\n"
    "buffer_load_ushort v128, v169, s[40:43], s87 offen offset:0\n"
    "buffer_load_ushort v129, v169, s[40:43], s87 offen offset:256\n"
    "buffer_load_ushort v130, v169, s[40:43], s87 offen offset:512\n"
    "buffer_load_ushort v131, v169, s[40:43], s87 offen offset:768\n"
    "s_add_u32 s87, s86, 12288\n"
    "buffer_load_ushort v132, v169, s[40:43], s87 offen offset:0\n"
    "buffer_load_ushort v133, v169, s[40:43], s87 offen offset:256\n"
    "buffer_load_ushort v134, v169, s[40:43], s87 offen offset:512\n"
    "buffer_load_ushort v135, v169, s[40:43], s87 offen offset:768\n"
    "s_waitcnt vmcnt(0)\n"
    "v_lshl_or_b32 v128, v129, 16, v128\n"
    "v_accvgpr_write_b32 a196, v128\n"
    "v_lshl_or_b32 v130, v131, 16, v130\n"
    "v_accvgpr_write_b32 a197, v130\n"
    "v_lshl_or_b32 v132, v133, 16, v132\n"
    "v_accvgpr_write_b32 a198, v132\n"
    "v_lshl_or_b32 v134, v135, 16, v134\n"
    "v_accvgpr_write_b32 a199, v134\n"
    "s_add_u32 s87, s86, 0\n"
    "buffer_load_ushort v128, v169, s[40:43], s87 offen offset:32\n"
    "buffer_load_ushort v129, v169, s[40:43], s87 offen offset:288\n"
    "buffer_load_ushort v130, v169, s[40:43], s87 offen offset:544\n"
    "buffer_load_ushort v131, v169, s[40:43], s87 offen offset:800\n"
    "s_add_u32 s87, s86, 4096\n"
    "buffer_load_ushort v132, v169, s[40:43], s87 offen offset:32\n"
    "buffer_load_ushort v133, v169, s[40:43], s87 offen offset:288\n"
    "buffer_load_ushort v134, v169, s[40:43], s87 offen offset:544\n"
    "buffer_load_ushort v135, v169, s[40:43], s87 offen offset:800\n"
    "s_waitcnt vmcnt(0)\n"
    "v_lshl_or_b32 v128, v129, 16, v128\n"
    "v_accvgpr_write_b32 a200, v128\n"
    "v_lshl_or_b32 v130, v131, 16, v130\n"
    "v_accvgpr_write_b32 a201, v130\n"
    "v_lshl_or_b32 v132, v133, 16, v132\n"
    "v_accvgpr_write_b32 a202, v132\n"
    "v_lshl_or_b32 v134, v135, 16, v134\n"
    "v_accvgpr_write_b32 a203, v134\n"
    "s_add_u32 s87, s86, 8192\n"
    "buffer_load_ushort v128, v169, s[40:43], s87 offen offset:32\n"
    "buffer_load_ushort v129, v169, s[40:43], s87 offen offset:288\n"
    "buffer_load_ushort v130, v169, s[40:43], s87 offen offset:544\n"
    "buffer_load_ushort v131, v169, s[40:43], s87 offen offset:800\n"
    "s_add_u32 s87, s86, 12288\n"
    "buffer_load_ushort v132, v169, s[40:43], s87 offen offset:32\n"
    "buffer_load_ushort v133, v169, s[40:43], s87 offen offset:288\n"
    "buffer_load_ushort v134, v169, s[40:43], s87 offen offset:544\n"
    "buffer_load_ushort v135, v169, s[40:43], s87 offen offset:800\n"
    "s_waitcnt vmcnt(0)\n"
    "v_lshl_or_b32 v128, v129, 16, v128\n"
    "v_accvgpr_write_b32 a204, v128\n"
    "v_lshl_or_b32 v130, v131, 16, v130\n"
    "v_accvgpr_write_b32 a205, v130\n"
    "v_lshl_or_b32 v132, v133, 16, v132\n"
    "v_accvgpr_write_b32 a206, v132\n"
    "v_lshl_or_b32 v134, v135, 16, v134\n"
    "v_accvgpr_write_b32 a207, v134\n"
    "s_add_u32 s87, s86, 0\n"
    "buffer_load_ushort v128, v169, s[40:43], s87 offen offset:64\n"
    "buffer_load_ushort v129, v169, s[40:43], s87 offen offset:320\n"
    "buffer_load_ushort v130, v169, s[40:43], s87 offen offset:576\n"
    "buffer_load_ushort v131, v169, s[40:43], s87 offen offset:832\n"
    "s_add_u32 s87, s86, 4096\n"
    "buffer_load_ushort v132, v169, s[40:43], s87 offen offset:64\n"
    "buffer_load_ushort v133, v169, s[40:43], s87 offen offset:320\n"
    "buffer_load_ushort v134, v169, s[40:43], s87 offen offset:576\n"
    "buffer_load_ushort v135, v169, s[40:43], s87 offen offset:832\n"
    "s_waitcnt vmcnt(0)\n"
    "v_lshl_or_b32 v128, v129, 16, v128\n"
    "v_accvgpr_write_b32 a208, v128\n"
    "v_lshl_or_b32 v130, v131, 16, v130\n"
    "v_accvgpr_write_b32 a209, v130\n"
    "v_lshl_or_b32 v132, v133, 16, v132\n"
    "v_accvgpr_write_b32 a210, v132\n"
    "v_lshl_or_b32 v134, v135, 16, v134\n"
    "v_accvgpr_write_b32 a211, v134\n"
    "s_add_u32 s87, s86, 8192\n"
    "buffer_load_ushort v128, v169, s[40:43], s87 offen offset:64\n"
    "buffer_load_ushort v129, v169, s[40:43], s87 offen offset:320\n"
    "buffer_load_ushort v130, v169, s[40:43], s87 offen offset:576\n"
    "buffer_load_ushort v131, v169, s[40:43], s87 offen offset:832\n"
    "s_add_u32 s87, s86, 12288\n"
    "buffer_load_ushort v132, v169, s[40:43], s87 offen offset:64\n"
    "buffer_load_ushort v133, v169, s[40:43], s87 offen offset:320\n"
    "buffer_load_ushort v134, v169, s[40:43], s87 offen offset:576\n"
    "buffer_load_ushort v135, v169, s[40:43], s87 offen offset:832\n"
    "s_waitcnt vmcnt(0)\n"
    "v_lshl_or_b32 v128, v129, 16, v128\n"
    "v_accvgpr_write_b32 a212, v128\n"
    "v_lshl_or_b32 v130, v131, 16, v130\n"
    "v_accvgpr_write_b32 a213, v130\n"
    "v_lshl_or_b32 v132, v133, 16, v132\n"
    "v_accvgpr_write_b32 a214, v132\n"
    "v_lshl_or_b32 v134, v135, 16, v134\n"
    "v_accvgpr_write_b32 a215, v134\n"
    "s_add_u32 s87, s86, 0\n"
    "buffer_load_ushort v128, v169, s[40:43], s87 offen offset:96\n"
    "buffer_load_ushort v129, v169, s[40:43], s87 offen offset:352\n"
    "buffer_load_ushort v130, v169, s[40:43], s87 offen offset:608\n"
    "buffer_load_ushort v131, v169, s[40:43], s87 offen offset:864\n"
    "s_add_u32 s87, s86, 4096\n"
    "buffer_load_ushort v132, v169, s[40:43], s87 offen offset:96\n"
    "buffer_load_ushort v133, v169, s[40:43], s87 offen offset:352\n"
    "buffer_load_ushort v134, v169, s[40:43], s87 offen offset:608\n"
    "buffer_load_ushort v135, v169, s[40:43], s87 offen offset:864\n"
    "s_waitcnt vmcnt(0)\n"
    "v_lshl_or_b32 v128, v129, 16, v128\n"
    "v_accvgpr_write_b32 a216, v128\n"
    "v_lshl_or_b32 v130, v131, 16, v130\n"
    "v_accvgpr_write_b32 a217, v130\n"
    "v_lshl_or_b32 v132, v133, 16, v132\n"
    "v_accvgpr_write_b32 a218, v132\n"
    "v_lshl_or_b32 v134, v135, 16, v134\n"
    "v_accvgpr_write_b32 a219, v134\n"
    "s_add_u32 s87, s86, 8192\n"
    "buffer_load_ushort v128, v169, s[40:43], s87 offen offset:96\n"
    "buffer_load_ushort v129, v169, s[40:43], s87 offen offset:352\n"
    "buffer_load_ushort v130, v169, s[40:43], s87 offen offset:608\n"
    "buffer_load_ushort v131, v169, s[40:43], s87 offen offset:864\n"
    "s_add_u32 s87, s86, 12288\n"
    "buffer_load_ushort v132, v169, s[40:43], s87 offen offset:96\n"
    "buffer_load_ushort v133, v169, s[40:43], s87 offen offset:352\n"
    "buffer_load_ushort v134, v169, s[40:43], s87 offen offset:608\n"
    "buffer_load_ushort v135, v169, s[40:43], s87 offen offset:864\n"
    "s_waitcnt vmcnt(0)\n"
    "v_lshl_or_b32 v128, v129, 16, v128\n"
    "v_accvgpr_write_b32 a220, v128\n"
    "v_lshl_or_b32 v130, v131, 16, v130\n"
    "v_accvgpr_write_b32 a221, v130\n"
    "v_lshl_or_b32 v132, v133, 16, v132\n"
    "v_accvgpr_write_b32 a222, v132\n"
    "v_lshl_or_b32 v134, v135, 16, v134\n"
    "v_accvgpr_write_b32 a223, v134\n"
    "s_add_u32 s87, s86, 0\n"
    "buffer_load_ushort v128, v169, s[40:43], s87 offen offset:128\n"
    "buffer_load_ushort v129, v169, s[40:43], s87 offen offset:384\n"
    "buffer_load_ushort v130, v169, s[40:43], s87 offen offset:640\n"
    "buffer_load_ushort v131, v169, s[40:43], s87 offen offset:896\n"
    "s_add_u32 s87, s86, 4096\n"
    "buffer_load_ushort v132, v169, s[40:43], s87 offen offset:128\n"
    "buffer_load_ushort v133, v169, s[40:43], s87 offen offset:384\n"
    "buffer_load_ushort v134, v169, s[40:43], s87 offen offset:640\n"
    "buffer_load_ushort v135, v169, s[40:43], s87 offen offset:896\n"
    "s_waitcnt vmcnt(0)\n"
    "v_lshl_or_b32 v128, v129, 16, v128\n"
    "v_accvgpr_write_b32 a224, v128\n"
    "v_lshl_or_b32 v130, v131, 16, v130\n"
    "v_accvgpr_write_b32 a225, v130\n"
    "v_lshl_or_b32 v132, v133, 16, v132\n"
    "v_accvgpr_write_b32 a226, v132\n"
    "v_lshl_or_b32 v134, v135, 16, v134\n"
    "v_accvgpr_write_b32 a227, v134\n"
    "s_add_u32 s87, s86, 8192\n"
    "buffer_load_ushort v128, v169, s[40:43], s87 offen offset:128\n"
    "buffer_load_ushort v129, v169, s[40:43], s87 offen offset:384\n"
    "buffer_load_ushort v130, v169, s[40:43], s87 offen offset:640\n"
    "buffer_load_ushort v131, v169, s[40:43], s87 offen offset:896\n"
    "s_add_u32 s87, s86, 12288\n"
    "buffer_load_ushort v132, v169, s[40:43], s87 offen offset:128\n"
    "buffer_load_ushort v133, v169, s[40:43], s87 offen offset:384\n"
    "buffer_load_ushort v134, v169, s[40:43], s87 offen offset:640\n"
    "buffer_load_ushort v135, v169, s[40:43], s87 offen offset:896\n"
    "s_waitcnt vmcnt(0)\n"
    "v_lshl_or_b32 v128, v129, 16, v128\n"
    "v_accvgpr_write_b32 a228, v128\n"
    "v_lshl_or_b32 v130, v131, 16, v130\n"
    "v_accvgpr_write_b32 a229, v130\n"
    "v_lshl_or_b32 v132, v133, 16, v132\n"
    "v_accvgpr_write_b32 a230, v132\n"
    "v_lshl_or_b32 v134, v135, 16, v134\n"
    "v_accvgpr_write_b32 a231, v134\n"
    "s_add_u32 s87, s86, 0\n"
    "buffer_load_ushort v128, v169, s[40:43], s87 offen offset:160\n"
    "buffer_load_ushort v129, v169, s[40:43], s87 offen offset:416\n"
    "buffer_load_ushort v130, v169, s[40:43], s87 offen offset:672\n"
    "buffer_load_ushort v131, v169, s[40:43], s87 offen offset:928\n"
    "s_add_u32 s87, s86, 4096\n"
    "buffer_load_ushort v132, v169, s[40:43], s87 offen offset:160\n"
    "buffer_load_ushort v133, v169, s[40:43], s87 offen offset:416\n"
    "buffer_load_ushort v134, v169, s[40:43], s87 offen offset:672\n"
    "buffer_load_ushort v135, v169, s[40:43], s87 offen offset:928\n"
    "s_waitcnt vmcnt(0)\n"
    "v_lshl_or_b32 v128, v129, 16, v128\n"
    "v_accvgpr_write_b32 a232, v128\n"
    "v_lshl_or_b32 v130, v131, 16, v130\n"
    "v_accvgpr_write_b32 a233, v130\n"
    "v_lshl_or_b32 v132, v133, 16, v132\n"
    "v_accvgpr_write_b32 a234, v132\n"
    "v_lshl_or_b32 v134, v135, 16, v134\n"
    "v_accvgpr_write_b32 a235, v134\n"
    "s_add_u32 s87, s86, 8192\n"
    "buffer_load_ushort v128, v169, s[40:43], s87 offen offset:160\n"
    "buffer_load_ushort v129, v169, s[40:43], s87 offen offset:416\n"
    "buffer_load_ushort v130, v169, s[40:43], s87 offen offset:672\n"
    "buffer_load_ushort v131, v169, s[40:43], s87 offen offset:928\n"
    "s_add_u32 s87, s86, 12288\n"
    "buffer_load_ushort v132, v169, s[40:43], s87 offen offset:160\n"
    "buffer_load_ushort v133, v169, s[40:43], s87 offen offset:416\n"
    "buffer_load_ushort v134, v169, s[40:43], s87 offen offset:672\n"
    "buffer_load_ushort v135, v169, s[40:43], s87 offen offset:928\n"
    "s_waitcnt vmcnt(0)\n"
    "v_lshl_or_b32 v128, v129, 16, v128\n"
    "v_accvgpr_write_b32 a236, v128\n"
    "v_lshl_or_b32 v130, v131, 16, v130\n"
    "v_accvgpr_write_b32 a237, v130\n"
    "v_lshl_or_b32 v132, v133, 16, v132\n"
    "v_accvgpr_write_b32 a238, v132\n"
    "v_lshl_or_b32 v134, v135, 16, v134\n"
    "v_accvgpr_write_b32 a239, v134\n"
    "s_add_u32 s87, s86, 0\n"
    "buffer_load_ushort v128, v169, s[40:43], s87 offen offset:192\n"
    "buffer_load_ushort v129, v169, s[40:43], s87 offen offset:448\n"
    "buffer_load_ushort v130, v169, s[40:43], s87 offen offset:704\n"
    "buffer_load_ushort v131, v169, s[40:43], s87 offen offset:960\n"
    "s_add_u32 s87, s86, 4096\n"
    "buffer_load_ushort v132, v169, s[40:43], s87 offen offset:192\n"
    "buffer_load_ushort v133, v169, s[40:43], s87 offen offset:448\n"
    "buffer_load_ushort v134, v169, s[40:43], s87 offen offset:704\n"
    "buffer_load_ushort v135, v169, s[40:43], s87 offen offset:960\n"
    "s_waitcnt vmcnt(0)\n"
    "v_lshl_or_b32 v128, v129, 16, v128\n"
    "v_accvgpr_write_b32 a240, v128\n"
    "v_lshl_or_b32 v130, v131, 16, v130\n"
    "v_accvgpr_write_b32 a241, v130\n"
    "v_lshl_or_b32 v132, v133, 16, v132\n"
    "v_accvgpr_write_b32 a242, v132\n"
    "v_lshl_or_b32 v134, v135, 16, v134\n"
    "v_accvgpr_write_b32 a243, v134\n"
    "s_add_u32 s87, s86, 8192\n"
    "buffer_load_ushort v128, v169, s[40:43], s87 offen offset:192\n"
    "buffer_load_ushort v129, v169, s[40:43], s87 offen offset:448\n"
    "buffer_load_ushort v130, v169, s[40:43], s87 offen offset:704\n"
    "buffer_load_ushort v131, v169, s[40:43], s87 offen offset:960\n"
    "s_add_u32 s87, s86, 12288\n"
    "buffer_load_ushort v132, v169, s[40:43], s87 offen offset:192\n"
    "buffer_load_ushort v133, v169, s[40:43], s87 offen offset:448\n"
    "buffer_load_ushort v134, v169, s[40:43], s87 offen offset:704\n"
    "buffer_load_ushort v135, v169, s[40:43], s87 offen offset:960\n"
    "s_waitcnt vmcnt(0)\n"
    "v_lshl_or_b32 v128, v129, 16, v128\n"
    "v_accvgpr_write_b32 a244, v128\n"
    "v_lshl_or_b32 v130, v131, 16, v130\n"
    "v_accvgpr_write_b32 a245, v130\n"
    "v_lshl_or_b32 v132, v133, 16, v132\n"
    "v_accvgpr_write_b32 a246, v132\n"
    "v_lshl_or_b32 v134, v135, 16, v134\n"
    "v_accvgpr_write_b32 a247, v134\n"
    "s_add_u32 s87, s86, 0\n"
    "buffer_load_ushort v128, v169, s[40:43], s87 offen offset:224\n"
    "buffer_load_ushort v129, v169, s[40:43], s87 offen offset:480\n"
    "buffer_load_ushort v130, v169, s[40:43], s87 offen offset:736\n"
    "buffer_load_ushort v131, v169, s[40:43], s87 offen offset:992\n"
    "s_add_u32 s87, s86, 4096\n"
    "buffer_load_ushort v132, v169, s[40:43], s87 offen offset:224\n"
    "buffer_load_ushort v133, v169, s[40:43], s87 offen offset:480\n"
    "buffer_load_ushort v134, v169, s[40:43], s87 offen offset:736\n"
    "buffer_load_ushort v135, v169, s[40:43], s87 offen offset:992\n"
    "s_waitcnt vmcnt(0)\n"
    "v_lshl_or_b32 v128, v129, 16, v128\n"
    "v_accvgpr_write_b32 a248, v128\n"
    "v_lshl_or_b32 v130, v131, 16, v130\n"
    "v_accvgpr_write_b32 a249, v130\n"
    "v_lshl_or_b32 v132, v133, 16, v132\n"
    "v_accvgpr_write_b32 a250, v132\n"
    "v_lshl_or_b32 v134, v135, 16, v134\n"
    "v_accvgpr_write_b32 a251, v134\n"
    "s_add_u32 s87, s86, 8192\n"
    "buffer_load_ushort v128, v169, s[40:43], s87 offen offset:224\n"
    "buffer_load_ushort v129, v169, s[40:43], s87 offen offset:480\n"
    "buffer_load_ushort v130, v169, s[40:43], s87 offen offset:736\n"
    "buffer_load_ushort v131, v169, s[40:43], s87 offen offset:992\n"
    "s_add_u32 s87, s86, 12288\n"
    "buffer_load_ushort v132, v169, s[40:43], s87 offen offset:224\n"
    "buffer_load_ushort v133, v169, s[40:43], s87 offen offset:480\n"
    "buffer_load_ushort v134, v169, s[40:43], s87 offen offset:736\n"
    "buffer_load_ushort v135, v169, s[40:43], s87 offen offset:992\n"
    "s_waitcnt vmcnt(0)\n"
    "v_lshl_or_b32 v128, v129, 16, v128\n"
    "v_accvgpr_write_b32 a252, v128\n"
    "v_lshl_or_b32 v130, v131, 16, v130\n"
    "v_accvgpr_write_b32 a253, v130\n"
    "v_lshl_or_b32 v132, v133, 16, v132\n"
    "v_accvgpr_write_b32 a254, v132\n"
    "v_lshl_or_b32 v134, v135, 16, v134\n"
    "v_accvgpr_write_b32 a255, v134\n"
    "v_mfma_f32_16x16x32_bf16 v[128:131], a[64:67], a[0:3], 0\n"
    "v_mfma_f32_16x16x32_bf16 v[128:131], a[68:71], a[4:7], v[128:131]\n"
    "v_mfma_f32_16x16x32_bf16 v[128:131], a[72:75], a[8:11], v[128:131]\n"
    "v_mfma_f32_16x16x32_bf16 v[128:131], a[76:79], a[12:15], v[128:131]\n"
    "v_mfma_f32_16x16x32_bf16 v[132:135], a[80:83], a[0:3], 0\n"
    "v_mfma_f32_16x16x32_bf16 v[132:135], a[84:87], a[4:7], v[132:135]\n"
    "v_mfma_f32_16x16x32_bf16 v[132:135], a[88:91], a[8:11], v[132:135]\n"
    "v_mfma_f32_16x16x32_bf16 v[132:135], a[92:95], a[12:15], v[132:135]\n"
    "v_mfma_f32_16x16x32_bf16 v[136:139], a[96:99], a[0:3], 0\n"
    "v_mfma_f32_16x16x32_bf16 v[136:139], a[100:103], a[4:7], v[136:139]\n"
    "v_mfma_f32_16x16x32_bf16 v[136:139], a[104:107], a[8:11], v[136:139]\n"
    "v_mfma_f32_16x16x32_bf16 v[136:139], a[108:111], a[12:15], v[136:139]\n"
    "v_mfma_f32_16x16x32_bf16 v[140:143], a[112:115], a[0:3], 0\n"
    "v_mfma_f32_16x16x32_bf16 v[140:143], a[116:119], a[4:7], v[140:143]\n"
    "v_mfma_f32_16x16x32_bf16 v[140:143], a[120:123], a[8:11], v[140:143]\n"
    "v_mfma_f32_16x16x32_bf16 v[140:143], a[124:127], a[12:15], v[140:143]\n"
    "s_nop 7\n"
    "v_max3_f32 v170, v128, v129, v160\n"
    "v_max3_f32 v170, v170, v130, v131\n"
    "v_max3_f32 v170, v170, v132, v133\n"
    "v_max3_f32 v170, v170, v134, v135\n"
    "v_max3_f32 v170, v170, v136, v137\n"
    "v_max3_f32 v170, v170, v138, v139\n"
    "v_max3_f32 v170, v170, v140, v141\n"
    "v_max3_f32 v170, v170, v142, v143\n"
    "v_mov_b32 v171, v170\n"
    "s_nop 1\n"
    "v_permlane16_swap_b32 v170, v171\n"
    "s_nop 1\n"
    "v_max_f32 v170, v170, v171\n"
    "v_mov_b32 v171, v170\n"
    "s_nop 1\n"
    "v_permlane32_swap_b32 v170, v171\n"
    "s_nop 1\n"
    "v_max_f32 v170, v170, v171\n"
    "v_sub_f32 v164, v160, v170\n"
    "v_exp_f32 v164, v164\n"
    "v_mov_b32 v160, v170\n"
    "v_sub_f32 v128, v128, v170\n"
    "v_exp_f32 v128, v128\n"
    "v_sub_f32 v129, v129, v170\n"
    "v_exp_f32 v129, v129\n"
    "v_sub_f32 v130, v130, v170\n"
    "v_exp_f32 v130, v130\n"
    "v_sub_f32 v131, v131, v170\n"
    "v_exp_f32 v131, v131\n"
    "v_sub_f32 v132, v132, v170\n"
    "v_exp_f32 v132, v132\n"
    "v_sub_f32 v133, v133, v170\n"
    "v_exp_f32 v133, v133\n"
    "v_sub_f32 v134, v134, v170\n"
    "v_exp_f32 v134, v134\n"
    "v_sub_f32 v135, v135, v170\n"
    "v_exp_f32 v135, v135\n"
    "v_sub_f32 v136, v136, v170\n"
    "v_exp_f32 v136, v136\n"
    "v_sub_f32 v137, v137, v170\n"
    "v_exp_f32 v137, v137\n"
    "v_sub_f32 v138, v138, v170\n"
    "v_exp_f32 v138, v138\n"
    "v_sub_f32 v139, v139, v170\n"
    "v_exp_f32 v139, v139\n"
    "v_sub_f32 v140, v140, v170\n"
    "v_exp_f32 v140, v140\n"
    "v_sub_f32 v141, v141, v170\n"
    "v_exp_f32 v141, v141\n"
    "v_sub_f32 v142, v142, v170\n"
    "v_exp_f32 v142, v142\n"
    "v_sub_f32 v143, v143, v170\n"
    "v_exp_f32 v143, v143\n"
    "v_mul_f32 v176, v176, v164\n"
    "v_mul_f32 v177, v177, v164\n"
    "v_mul_f32 v178, v178, v164\n"
    "v_mul_f32 v179, v179, v164\n"
    "v_mul_f32 v0, v0, v164\n"
    "v_mul_f32 v1, v1, v164\n"
    "v_mul_f32 v2, v2, v164\n"
    "v_mul_f32 v3, v3, v164\n"
    "v_mul_f32 v4, v4, v164\n"
    "v_mul_f32 v5, v5, v164\n"
    "v_mul_f32 v6, v6, v164\n"
    "v_mul_f32 v7, v7, v164\n"
    "v_mul_f32 v8, v8, v164\n"
    "v_mul_f32 v9, v9, v164\n"
    "v_mul_f32 v10, v10, v164\n"
    "v_mul_f32 v11, v11, v164\n"
    "v_mul_f32 v12, v12, v164\n"
    "v_mul_f32 v13, v13, v164\n"
    "v_mul_f32 v14, v14, v164\n"
    "v_mul_f32 v15, v15, v164\n"
    "v_mul_f32 v16, v16, v164\n"
    "v_mul_f32 v17, v17, v164\n"
    "v_mul_f32 v18, v18, v164\n"
    "v_mul_f32 v19, v19, v164\n"
    "v_mul_f32 v20, v20, v164\n"
    "v_mul_f32 v21, v21, v164\n"
    "v_mul_f32 v22, v22, v164\n"
    "v_mul_f32 v23, v23, v164\n"
    "v_mul_f32 v24, v24, v164\n"
    "v_mul_f32 v25, v25, v164\n"
    "v_mul_f32 v26, v26, v164\n"
    "v_mul_f32 v27, v27, v164\n"
    "v_mul_f32 v28, v28, v164\n"
    "v_mul_f32 v29, v29, v164\n"
    "v_mul_f32 v30, v30, v164\n"
    "v_mul_f32 v31, v31, v164\n"
    "v_cvt_pk_bf16_f32 v144, v128, v129\n"
    "v_cvt_pk_bf16_f32 v145, v130, v131\n"
    "v_cvt_pk_bf16_f32 v146, v132, v133\n"
    "v_cvt_pk_bf16_f32 v147, v134, v135\n"
    "v_cvt_pk_bf16_f32 v148, v136, v137\n"
    "v_cvt_pk_bf16_f32 v149, v138, v139\n"
    "v_cvt_pk_bf16_f32 v150, v140, v141\n"
    "v_cvt_pk_bf16_f32 v151, v142, v143\n"
    "s_nop 1\n"
    "v_mfma_f32_16x16x32_bf16 v[0:3], a[192:195], v[144:147], v[0:3]\n"
    "v_mfma_f32_16x16x32_bf16 v[0:3], a[196:199], v[148:151], v[0:3]\n"
    "v_mfma_f32_16x16x32_bf16 v[4:7], a[200:203], v[144:147], v[4:7]\n"
    "v_mfma_f32_16x16x32_bf16 v[4:7], a[204:207], v[148:151], v[4:7]\n"
    "v_mfma_f32_16x16x32_bf16 v[8:11], a[208:211], v[144:147], v[8:11]\n"
    "v_mfma_f32_16x16x32_bf16 v[8:11], a[212:215], v[148:151], v[8:11]\n"
    "v_mfma_f32_16x16x32_bf16 v[12:15], a[216:219], v[144:147], v[12:15]\n"
    "v_mfma_f32_16x16x32_bf16 v[12:15], a[220:223], v[148:151], v[12:15]\n"
    "v_mfma_f32_16x16x32_bf16 v[16:19], a[224:227], v[144:147], v[16:19]\n"
    "v_mfma_f32_16x16x32_bf16 v[16:19], a[228:231], v[148:151], v[16:19]\n"
    "v_mfma_f32_16x16x32_bf16 v[20:23], a[232:235], v[144:147], v[20:23]\n"
    "v_mfma_f32_16x16x32_bf16 v[20:23], a[236:239], v[148:151], v[20:23]\n"
    "v_mfma_f32_16x16x32_bf16 v[24:27], a[240:243], v[144:147], v[24:27]\n"
    "v_mfma_f32_16x16x32_bf16 v[24:27], a[244:247], v[148:151], v[24:27]\n"
    "v_mfma_f32_16x16x32_bf16 v[28:31], a[248:251], v[144:147], v[28:31]\n"
    "v_mfma_f32_16x16x32_bf16 v[28:31], a[252:255], v[148:151], v[28:31]\n"
    "v_mfma_f32_16x16x32_bf16 v[176:179], v[208:211], v[144:147], v[176:179]\n"
    "v_mfma_f32_16x16x32_bf16 v[176:179], v[208:211], v[148:151], v[176:179]\n"
    "s_nop 7\n"
    "v_mfma_f32_16x16x32_bf16 v[128:131], a[64:67], a[16:19], 0\n"
    "v_mfma_f32_16x16x32_bf16 v[128:131], a[68:71], a[20:23], v[128:131]\n"
    "v_mfma_f32_16x16x32_bf16 v[128:131], a[72:75], a[24:27], v[128:131]\n"
    "v_mfma_f32_16x16x32_bf16 v[128:131], a[76:79], a[28:31], v[128:131]\n"
    "v_mfma_f32_16x16x32_bf16 v[132:135], a[80:83], a[16:19], 0\n"
    "v_mfma_f32_16x16x32_bf16 v[132:135], a[84:87], a[20:23], v[132:135]\n"
    "v_mfma_f32_16x16x32_bf16 v[132:135], a[88:91], a[24:27], v[132:135]\n"
    "v_mfma_f32_16x16x32_bf16 v[132:135], a[92:95], a[28:31], v[132:135]\n"
    "v_mfma_f32_16x16x32_bf16 v[136:139], a[96:99], a[16:19], 0\n"
    "v_mfma_f32_16x16x32_bf16 v[136:139], a[100:103], a[20:23], v[136:139]\n"
    "v_mfma_f32_16x16x32_bf16 v[136:139], a[104:107], a[24:27], v[136:139]\n"
    "v_mfma_f32_16x16x32_bf16 v[136:139], a[108:111], a[28:31], v[136:139]\n"
    "v_mfma_f32_16x16x32_bf16 v[140:143], a[112:115], a[16:19], 0\n"
    "v_mfma_f32_16x16x32_bf16 v[140:143], a[116:119], a[20:23], v[140:143]\n"
    "v_mfma_f32_16x16x32_bf16 v[140:143], a[120:123], a[24:27], v[140:143]\n"
    "v_mfma_f32_16x16x32_bf16 v[140:143], a[124:127], a[28:31], v[140:143]\n"
    "s_nop 7\n"
    "v_max3_f32 v170, v128, v129, v161\n"
    "v_max3_f32 v170, v170, v130, v131\n"
    "v_max3_f32 v170, v170, v132, v133\n"
    "v_max3_f32 v170, v170, v134, v135\n"
    "v_max3_f32 v170, v170, v136, v137\n"
    "v_max3_f32 v170, v170, v138, v139\n"
    "v_max3_f32 v170, v170, v140, v141\n"
    "v_max3_f32 v170, v170, v142, v143\n"
    "v_mov_b32 v171, v170\n"
    "s_nop 1\n"
    "v_permlane16_swap_b32 v170, v171\n"
    "s_nop 1\n"
    "v_max_f32 v170, v170, v171\n"
    "v_mov_b32 v171, v170\n"
    "s_nop 1\n"
    "v_permlane32_swap_b32 v170, v171\n"
    "s_nop 1\n"
    "v_max_f32 v170, v170, v171\n"
    "v_sub_f32 v164, v161, v170\n"
    "v_exp_f32 v164, v164\n"
    "v_mov_b32 v161, v170\n"
    "v_sub_f32 v128, v128, v170\n"
    "v_exp_f32 v128, v128\n"
    "v_sub_f32 v129, v129, v170\n"
    "v_exp_f32 v129, v129\n"
    "v_sub_f32 v130, v130, v170\n"
    "v_exp_f32 v130, v130\n"
    "v_sub_f32 v131, v131, v170\n"
    "v_exp_f32 v131, v131\n"
    "v_sub_f32 v132, v132, v170\n"
    "v_exp_f32 v132, v132\n"
    "v_sub_f32 v133, v133, v170\n"
    "v_exp_f32 v133, v133\n"
    "v_sub_f32 v134, v134, v170\n"
    "v_exp_f32 v134, v134\n"
    "v_sub_f32 v135, v135, v170\n"
    "v_exp_f32 v135, v135\n"
    "v_sub_f32 v136, v136, v170\n"
    "v_exp_f32 v136, v136\n"
    "v_sub_f32 v137, v137, v170\n"
    "v_exp_f32 v137, v137\n"
    "v_sub_f32 v138, v138, v170\n"
    "v_exp_f32 v138, v138\n"
    "v_sub_f32 v139, v139, v170\n"
    "v_exp_f32 v139, v139\n"
    "v_sub_f32 v140, v140, v170\n"
    "v_exp_f32 v140, v140\n"
    "v_sub_f32 v141, v141, v170\n"
    "v_exp_f32 v141, v141\n"
    "v_sub_f32 v142, v142, v170\n"
    "v_exp_f32 v142, v142\n"
    "v_sub_f32 v143, v143, v170\n"
    "v_exp_f32 v143, v143\n"
    "v_mul_f32 v180, v180, v164\n"
    "v_mul_f32 v181, v181, v164\n"
    "v_mul_f32 v182, v182, v164\n"
    "v_mul_f32 v183, v183, v164\n"
    "v_mul_f32 v32, v32, v164\n"
    "v_mul_f32 v33, v33, v164\n"
    "v_mul_f32 v34, v34, v164\n"
    "v_mul_f32 v35, v35, v164\n"
    "v_mul_f32 v36, v36, v164\n"
    "v_mul_f32 v37, v37, v164\n"
    "v_mul_f32 v38, v38, v164\n"
    "v_mul_f32 v39, v39, v164\n"
    "v_mul_f32 v40, v40, v164\n"
    "v_mul_f32 v41, v41, v164\n"
    "v_mul_f32 v42, v42, v164\n"
    "v_mul_f32 v43, v43, v164\n"
    "v_mul_f32 v44, v44, v164\n"
    "v_mul_f32 v45, v45, v164\n"
    "v_mul_f32 v46, v46, v164\n"
    "v_mul_f32 v47, v47, v164\n"
    "v_mul_f32 v48, v48, v164\n"
    "v_mul_f32 v49, v49, v164\n"
    "v_mul_f32 v50, v50, v164\n"
    "v_mul_f32 v51, v51, v164\n"
    "v_mul_f32 v52, v52, v164\n"
    "v_mul_f32 v53, v53, v164\n"
    "v_mul_f32 v54, v54, v164\n"
    "v_mul_f32 v55, v55, v164\n"
    "v_mul_f32 v56, v56, v164\n"
    "v_mul_f32 v57, v57, v164\n"
    "v_mul_f32 v58, v58, v164\n"
    "v_mul_f32 v59, v59, v164\n"
    "v_mul_f32 v60, v60, v164\n"
    "v_mul_f32 v61, v61, v164\n"
    "v_mul_f32 v62, v62, v164\n"
    "v_mul_f32 v63, v63, v164\n"
    "v_cvt_pk_bf16_f32 v144, v128, v129\n"
    "v_cvt_pk_bf16_f32 v145, v130, v131\n"
    "v_cvt_pk_bf16_f32 v146, v132, v133\n"
    "v_cvt_pk_bf16_f32 v147, v134, v135\n"
    "v_cvt_pk_bf16_f32 v148, v136, v137\n"
    "v_cvt_pk_bf16_f32 v149, v138, v139\n"
    "v_cvt_pk_bf16_f32 v150, v140, v141\n"
    "v_cvt_pk_bf16_f32 v151, v142, v143\n"
    "s_nop 1\n"
    "v_mfma_f32_16x16x32_bf16 v[32:35], a[192:195], v[144:147], v[32:35]\n"
    "v_mfma_f32_16x16x32_bf16 v[32:35], a[196:199], v[148:151], v[32:35]\n"
    "v_mfma_f32_16x16x32_bf16 v[36:39], a[200:203], v[144:147], v[36:39]\n"
    "v_mfma_f32_16x16x32_bf16 v[36:39], a[204:207], v[148:151], v[36:39]\n"
    "v_mfma_f32_16x16x32_bf16 v[40:43], a[208:211], v[144:147], v[40:43]\n"
    "v_mfma_f32_16x16x32_bf16 v[40:43], a[212:215], v[148:151], v[40:43]\n"
    "v_mfma_f32_16x16x32_bf16 v[44:47], a[216:219], v[144:147], v[44:47]\n"
    "v_mfma_f32_16x16x32_bf16 v[44:47], a[220:223], v[148:151], v[44:47]\n"
    "v_mfma_f32_16x16x32_bf16 v[48:51], a[224:227], v[144:147], v[48:51]\n"
    "v_mfma_f32_16x16x32_bf16 v[48:51], a[228:231], v[148:151], v[48:51]\n"
    "v_mfma_f32_16x16x32_bf16 v[52:55], a[232:235], v[144:147], v[52:55]\n"
    "v_mfma_f32_16x16x32_bf16 v[52:55], a[236:239], v[148:151], v[52:55]\n"
    "v_mfma_f32_16x16x32_bf16 v[56:59], a[240:243], v[144:147], v[56:59]\n"
    "v_mfma_f32_16x16x32_bf16 v[56:59], a[244:247], v[148:151], v[56:59]\n"
    "v_mfma_f32_16x16x32_bf16 v[60:63], a[248:251], v[144:147], v[60:63]\n"
    "v_mfma_f32_16x16x32_bf16 v[60:63], a[252:255], v[148:151], v[60:63]\n"
    "v_mfma_f32_16x16x32_bf16 v[180:183], v[208:211], v[144:147], v[180:183]\n"
    "v_mfma_f32_16x16x32_bf16 v[180:183], v[208:211], v[148:151], v[180:183]\n"
    "s_nop 7\n"
    "v_mfma_f32_16x16x32_bf16 v[128:131], a[64:67], a[32:35], 0\n"
    "v_mfma_f32_16x16x32_bf16 v[128:131], a[68:71], a[36:39], v[128:131]\n"
    "v_mfma_f32_16x16x32_bf16 v[128:131], a[72:75], a[40:43], v[128:131]\n"
    "v_mfma_f32_16x16x32_bf16 v[128:131], a[76:79], a[44:47], v[128:131]\n"
    "v_mfma_f32_16x16x32_bf16 v[132:135], a[80:83], a[32:35], 0\n"
    "v_mfma_f32_16x16x32_bf16 v[132:135], a[84:87], a[36:39], v[132:135]\n"
    "v_mfma_f32_16x16x32_bf16 v[132:135], a[88:91], a[40:43], v[132:135]\n"
    "v_mfma_f32_16x16x32_bf16 v[132:135], a[92:95], a[44:47], v[132:135]\n"
    "v_mfma_f32_16x16x32_bf16 v[136:139], a[96:99], a[32:35], 0\n"
    "v_mfma_f32_16x16x32_bf16 v[136:139], a[100:103], a[36:39], v[136:139]\n"
    "v_mfma_f32_16x16x32_bf16 v[136:139], a[104:107], a[40:43], v[136:139]\n"
    "v_mfma_f32_16x16x32_bf16 v[136:139], a[108:111], a[44:47], v[136:139]\n"
    "v_mfma_f32_16x16x32_bf16 v[140:143], a[112:115], a[32:35], 0\n"
    "v_mfma_f32_16x16x32_bf16 v[140:143], a[116:119], a[36:39], v[140:143]\n"
    "v_mfma_f32_16x16x32_bf16 v[140:143], a[120:123], a[40:43], v[140:143]\n"
    "v_mfma_f32_16x16x32_bf16 v[140:143], a[124:127], a[44:47], v[140:143]\n"
    "s_nop 7\n"
    "v_max3_f32 v170, v128, v129, v162\n"
    "v_max3_f32 v170, v170, v130, v131\n"
    "v_max3_f32 v170, v170, v132, v133\n"
    "v_max3_f32 v170, v170, v134, v135\n"
    "v_max3_f32 v170, v170, v136, v137\n"
    "v_max3_f32 v170, v170, v138, v139\n"
    "v_max3_f32 v170, v170, v140, v141\n"
    "v_max3_f32 v170, v170, v142, v143\n"
    "v_mov_b32 v171, v170\n"
    "s_nop 1\n"
    "v_permlane16_swap_b32 v170, v171\n"
    "s_nop 1\n"
    "v_max_f32 v170, v170, v171\n"
    "v_mov_b32 v171, v170\n"
    "s_nop 1\n"
    "v_permlane32_swap_b32 v170, v171\n"
    "s_nop 1\n"
    "v_max_f32 v170, v170, v171\n"
    "v_sub_f32 v164, v162, v170\n"
    "v_exp_f32 v164, v164\n"
    "v_mov_b32 v162, v170\n"
    "v_sub_f32 v128, v128, v170\n"
    "v_exp_f32 v128, v128\n"
    "v_sub_f32 v129, v129, v170\n"
    "v_exp_f32 v129, v129\n"
    "v_sub_f32 v130, v130, v170\n"
    "v_exp_f32 v130, v130\n"
    "v_sub_f32 v131, v131, v170\n"
    "v_exp_f32 v131, v131\n"
    "v_sub_f32 v132, v132, v170\n"
    "v_exp_f32 v132, v132\n"
    "v_sub_f32 v133, v133, v170\n"
    "v_exp_f32 v133, v133\n"
    "v_sub_f32 v134, v134, v170\n"
    "v_exp_f32 v134, v134\n"
    "v_sub_f32 v135, v135, v170\n"
    "v_exp_f32 v135, v135\n"
    "v_sub_f32 v136, v136, v170\n"
    "v_exp_f32 v136, v136\n"
    "v_sub_f32 v137, v137, v170\n"
    "v_exp_f32 v137, v137\n"
    "v_sub_f32 v138, v138, v170\n"
    "v_exp_f32 v138, v138\n"
    "v_sub_f32 v139, v139, v170\n"
    "v_exp_f32 v139, v139\n"
    "v_sub_f32 v140, v140, v170\n"
    "v_exp_f32 v140, v140\n"
    "v_sub_f32 v141, v141, v170\n"
    "v_exp_f32 v141, v141\n"
    "v_sub_f32 v142, v142, v170\n"
    "v_exp_f32 v142, v142\n"
    "v_sub_f32 v143, v143, v170\n"
    "v_exp_f32 v143, v143\n"
    "v_mul_f32 v184, v184, v164\n"
    "v_mul_f32 v185, v185, v164\n"
    "v_mul_f32 v186, v186, v164\n"
    "v_mul_f32 v187, v187, v164\n"
    "v_mul_f32 v64, v64, v164\n"
    "v_mul_f32 v65, v65, v164\n"
    "v_mul_f32 v66, v66, v164\n"
    "v_mul_f32 v67, v67, v164\n"
    "v_mul_f32 v68, v68, v164\n"
    "v_mul_f32 v69, v69, v164\n"
    "v_mul_f32 v70, v70, v164\n"
    "v_mul_f32 v71, v71, v164\n"
    "v_mul_f32 v72, v72, v164\n"
    "v_mul_f32 v73, v73, v164\n"
    "v_mul_f32 v74, v74, v164\n"
    "v_mul_f32 v75, v75, v164\n"
    "v_mul_f32 v76, v76, v164\n"
    "v_mul_f32 v77, v77, v164\n"
    "v_mul_f32 v78, v78, v164\n"
    "v_mul_f32 v79, v79, v164\n"
    "v_mul_f32 v80, v80, v164\n"
    "v_mul_f32 v81, v81, v164\n"
    "v_mul_f32 v82, v82, v164\n"
    "v_mul_f32 v83, v83, v164\n"
    "v_mul_f32 v84, v84, v164\n"
    "v_mul_f32 v85, v85, v164\n"
    "v_mul_f32 v86, v86, v164\n"
    "v_mul_f32 v87, v87, v164\n"
    "v_mul_f32 v88, v88, v164\n"
    "v_mul_f32 v89, v89, v164\n"
    "v_mul_f32 v90, v90, v164\n"
    "v_mul_f32 v91, v91, v164\n"
    "v_mul_f32 v92, v92, v164\n"
    "v_mul_f32 v93, v93, v164\n"
    "v_mul_f32 v94, v94, v164\n"
    "v_mul_f32 v95, v95, v164\n"
    "v_cvt_pk_bf16_f32 v144, v128, v129\n"
    "v_cvt_pk_bf16_f32 v145, v130, v131\n"
    "v_cvt_pk_bf16_f32 v146, v132, v133\n"
    "v_cvt_pk_bf16_f32 v147, v134, v135\n"
    "v_cvt_pk_bf16_f32 v148, v136, v137\n"
    "v_cvt_pk_bf16_f32 v149, v138, v139\n"
    "v_cvt_pk_bf16_f32 v150, v140, v141\n"
    "v_cvt_pk_bf16_f32 v151, v142, v143\n"
    "s_nop 1\n"
    "v_mfma_f32_16x16x32_bf16 v[64:67], a[192:195], v[144:147], v[64:67]\n"
    "v_mfma_f32_16x16x32_bf16 v[64:67], a[196:199], v[148:151], v[64:67]\n"
    "v_mfma_f32_16x16x32_bf16 v[68:71], a[200:203], v[144:147], v[68:71]\n"
    "v_mfma_f32_16x16x32_bf16 v[68:71], a[204:207], v[148:151], v[68:71]\n"
    "v_mfma_f32_16x16x32_bf16 v[72:75], a[208:211], v[144:147], v[72:75]\n"
    "v_mfma_f32_16x16x32_bf16 v[72:75], a[212:215], v[148:151], v[72:75]\n"
    "v_mfma_f32_16x16x32_bf16 v[76:79], a[216:219], v[144:147], v[76:79]\n"
    "v_mfma_f32_16x16x32_bf16 v[76:79], a[220:223], v[148:151], v[76:79]\n"
    "v_mfma_f32_16x16x32_bf16 v[80:83], a[224:227], v[144:147], v[80:83]\n"
    "v_mfma_f32_16x16x32_bf16 v[80:83], a[228:231], v[148:151], v[80:83]\n"
    "v_mfma_f32_16x16x32_bf16 v[84:87], a[232:235], v[144:147], v[84:87]\n"
    "v_mfma_f32_16x16x32_bf16 v[84:87], a[236:239], v[148:151], v[84:87]\n"
    "v_mfma_f32_16x16x32_bf16 v[88:91], a[240:243], v[144:147], v[88:91]\n"
    "v_mfma_f32_16x16x32_bf16 v[88:91], a[244:247], v[148:151], v[88:91]\n"
    "v_mfma_f32_16x16x32_bf16 v[92:95], a[248:251], v[144:147], v[92:95]\n"
    "v_mfma_f32_16x16x32_bf16 v[92:95], a[252:255], v[148:151], v[92:95]\n"
    "v_mfma_f32_16x16x32_bf16 v[184:187], v[208:211], v[144:147], v[184:187]\n"
    "v_mfma_f32_16x16x32_bf16 v[184:187], v[208:211], v[148:151], v[184:187]\n"
    "s_nop 7\n"
    "v_mfma_f32_16x16x32_bf16 v[128:131], a[64:67], a[48:51], 0\n"
    "v_mfma_f32_16x16x32_bf16 v[128:131], a[68:71], a[52:55], v[128:131]\n"
    "v_mfma_f32_16x16x32_bf16 v[128:131], a[72:75], a[56:59], v[128:131]\n"
    "v_mfma_f32_16x16x32_bf16 v[128:131], a[76:79], a[60:63], v[128:131]\n"
    "v_mfma_f32_16x16x32_bf16 v[132:135], a[80:83], a[48:51], 0\n"
    "v_mfma_f32_16x16x32_bf16 v[132:135], a[84:87], a[52:55], v[132:135]\n"
    "v_mfma_f32_16x16x32_bf16 v[132:135], a[88:91], a[56:59], v[132:135]\n"
    "v_mfma_f32_16x16x32_bf16 v[132:135], a[92:95], a[60:63], v[132:135]\n"
    "v_mfma_f32_16x16x32_bf16 v[136:139], a[96:99], a[48:51], 0\n"
    "v_mfma_f32_16x16x32_bf16 v[136:139], a[100:103], a[52:55], v[136:139]\n"
    "v_mfma_f32_16x16x32_bf16 v[136:139], a[104:107], a[56:59], v[136:139]\n"
    "v_mfma_f32_16x16x32_bf16 v[136:139], a[108:111], a[60:63], v[136:139]\n"
    "v_mfma_f32_16x16x32_bf16 v[140:143], a[112:115], a[48:51], 0\n"
    "v_mfma_f32_16x16x32_bf16 v[140:143], a[116:119], a[52:55], v[140:143]\n"
    "v_mfma_f32_16x16x32_bf16 v[140:143], a[120:123], a[56:59], v[140:143]\n"
    "v_mfma_f32_16x16x32_bf16 v[140:143], a[124:127], a[60:63], v[140:143]\n"
    "s_nop 7\n"
    "v_max3_f32 v170, v128, v129, v163\n"
    "v_max3_f32 v170, v170, v130, v131\n"
    "v_max3_f32 v170, v170, v132, v133\n"
    "v_max3_f32 v170, v170, v134, v135\n"
    "v_max3_f32 v170, v170, v136, v137\n"
    "v_max3_f32 v170, v170, v138, v139\n"
    "v_max3_f32 v170, v170, v140, v141\n"
    "v_max3_f32 v170, v170, v142, v143\n"
    "v_mov_b32 v171, v170\n"
    "s_nop 1\n"
    "v_permlane16_swap_b32 v170, v171\n"
    "s_nop 1\n"
    "v_max_f32 v170, v170, v171\n"
    "v_mov_b32 v171, v170\n"
    "s_nop 1\n"
    "v_permlane32_swap_b32 v170, v171\n"
    "s_nop 1\n"
    "v_max_f32 v170, v170, v171\n"
    "v_sub_f32 v164, v163, v170\n"
    "v_exp_f32 v164, v164\n"
    "v_mov_b32 v163, v170\n"
    "v_sub_f32 v128, v128, v170\n"
    "v_exp_f32 v128, v128\n"
    "v_sub_f32 v129, v129, v170\n"
    "v_exp_f32 v129, v129\n"
    "v_sub_f32 v130, v130, v170\n"
    "v_exp_f32 v130, v130\n"
    "v_sub_f32 v131, v131, v170\n"
    "v_exp_f32 v131, v131\n"
    "v_sub_f32 v132, v132, v170\n"
    "v_exp_f32 v132, v132\n"
    "v_sub_f32 v133, v133, v170\n"
    "v_exp_f32 v133, v133\n"
    "v_sub_f32 v134, v134, v170\n"
    "v_exp_f32 v134, v134\n"
    "v_sub_f32 v135, v135, v170\n"
    "v_exp_f32 v135, v135\n"
    "v_sub_f32 v136, v136, v170\n"
    "v_exp_f32 v136, v136\n"
    "v_sub_f32 v137, v137, v170\n"
    "v_exp_f32 v137, v137\n"
    "v_sub_f32 v138, v138, v170\n"
    "v_exp_f32 v138, v138\n"
    "v_sub_f32 v139, v139, v170\n"
    "v_exp_f32 v139, v139\n"
    "v_sub_f32 v140, v140, v170\n"
    "v_exp_f32 v140, v140\n"
    "v_sub_f32 v141, v141, v170\n"
    "v_exp_f32 v141, v141\n"
    "v_sub_f32 v142, v142, v170\n"
    "v_exp_f32 v142, v142\n"
    "v_sub_f32 v143, v143, v170\n"
    "v_exp_f32 v143, v143\n"
    "v_mul_f32 v188, v188, v164\n"
    "v_mul_f32 v189, v189, v164\n"
    "v_mul_f32 v190, v190, v164\n"
    "v_mul_f32 v191, v191, v164\n"
    "v_mul_f32 v96, v96, v164\n"
    "v_mul_f32 v97, v97, v164\n"
    "v_mul_f32 v98, v98, v164\n"
    "v_mul_f32 v99, v99, v164\n"
    "v_mul_f32 v100, v100, v164\n"
    "v_mul_f32 v101, v101, v164\n"
    "v_mul_f32 v102, v102, v164\n"
    "v_mul_f32 v103, v103, v164\n"
    "v_mul_f32 v104, v104, v164\n"
    "v_mul_f32 v105, v105, v164\n"
    "v_mul_f32 v106, v106, v164\n"
    "v_mul_f32 v107, v107, v164\n"
    "v_mul_f32 v108, v108, v164\n"
    "v_mul_f32 v109, v109, v164\n"
    "v_mul_f32 v110, v110, v164\n"
    "v_mul_f32 v111, v111, v164\n"
    "v_mul_f32 v112, v112, v164\n"
    "v_mul_f32 v113, v113, v164\n"
    "v_mul_f32 v114, v114, v164\n"
    "v_mul_f32 v115, v115, v164\n"
    "v_mul_f32 v116, v116, v164\n"
    "v_mul_f32 v117, v117, v164\n"
    "v_mul_f32 v118, v118, v164\n"
    "v_mul_f32 v119, v119, v164\n"
    "v_mul_f32 v120, v120, v164\n"
    "v_mul_f32 v121, v121, v164\n"
    "v_mul_f32 v122, v122, v164\n"
    "v_mul_f32 v123, v123, v164\n"
    "v_mul_f32 v124, v124, v164\n"
    "v_mul_f32 v125, v125, v164\n"
    "v_mul_f32 v126, v126, v164\n"
    "v_mul_f32 v127, v127, v164\n"
    "v_cvt_pk_bf16_f32 v144, v128, v129\n"
    "v_cvt_pk_bf16_f32 v145, v130, v131\n"
    "v_cvt_pk_bf16_f32 v146, v132, v133\n"
    "v_cvt_pk_bf16_f32 v147, v134, v135\n"
    "v_cvt_pk_bf16_f32 v148, v136, v137\n"
    "v_cvt_pk_bf16_f32 v149, v138, v139\n"
    "v_cvt_pk_bf16_f32 v150, v140, v141\n"
    "v_cvt_pk_bf16_f32 v151, v142, v143\n"
    "s_nop 1\n"
    "v_mfma_f32_16x16x32_bf16 v[96:99], a[192:195], v[144:147], v[96:99]\n"
    "v_mfma_f32_16x16x32_bf16 v[96:99], a[196:199], v[148:151], v[96:99]\n"
    "v_mfma_f32_16x16x32_bf16 v[100:103], a[200:203], v[144:147], v[100:103]\n"
    "v_mfma_f32_16x16x32_bf16 v[100:103], a[204:207], v[148:151], v[100:103]\n"
    "v_mfma_f32_16x16x32_bf16 v[104:107], a[208:211], v[144:147], v[104:107]\n"
    "v_mfma_f32_16x16x32_bf16 v[104:107], a[212:215], v[148:151], v[104:107]\n"
    "v_mfma_f32_16x16x32_bf16 v[108:111], a[216:219], v[144:147], v[108:111]\n"
    "v_mfma_f32_16x16x32_bf16 v[108:111], a[220:223], v[148:151], v[108:111]\n"
    "v_mfma_f32_16x16x32_bf16 v[112:115], a[224:227], v[144:147], v[112:115]\n"
    "v_mfma_f32_16x16x32_bf16 v[112:115], a[228:231], v[148:151], v[112:115]\n"
    "v_mfma_f32_16x16x32_bf16 v[116:119], a[232:235], v[144:147], v[116:119]\n"
    "v_mfma_f32_16x16x32_bf16 v[116:119], a[236:239], v[148:151], v[116:119]\n"
    "v_mfma_f32_16x16x32_bf16 v[120:123], a[240:243], v[144:147], v[120:123]\n"
    "v_mfma_f32_16x16x32_bf16 v[120:123], a[244:247], v[148:151], v[120:123]\n"
    "v_mfma_f32_16x16x32_bf16 v[124:127], a[248:251], v[144:147], v[124:127]\n"
    "v_mfma_f32_16x16x32_bf16 v[124:127], a[252:255], v[148:151], v[124:127]\n"
    "v_mfma_f32_16x16x32_bf16 v[188:191], v[208:211], v[144:147], v[188:191]\n"
    "v_mfma_f32_16x16x32_bf16 v[188:191], v[208:211], v[148:151], v[188:191]\n"
    "s_nop 7\n"
    "s_add_u32 s86, s86, 16384\n"
    "s_cmp_lt_u32 s86, 524288\n"
    "s_cbranch_scc1 L_safe_loop\n"
    "s_branch L_safe_done\n"
    "L_end:\n"
    :: "s"(__builtin_amdgcn_kernarg_segment_ptr()), "s"(blockIdx.x), "s"(__builtin_amdgcn_readfirstlane(threadIdx.x >> 6))
    : "v0","v1","v2","v3","v4","v5","v6","v7","v8","v9","v10","v11","v12","v13","v14","v15","v16","v17","v18","v19","v20","v21","v22","v23","v24","v25","v26","v27","v28","v29","v30","v31","v32","v33","v34","v35","v36","v37","v38","v39","v40","v41","v42","v43","v44","v45","v46","v47","v48","v49","v50","v51","v52","v53","v54","v55","v56","v57","v58","v59","v60","v61","v62","v63","v64","v65","v66","v67","v68","v69","v70","v71","v72","v73","v74","v75","v76","v77","v78","v79","v80","v81","v82","v83","v84","v85","v86","v87","v88","v89","v90","v91","v92","v93","v94","v95","v96","v97","v98","v99","v100","v101","v102","v103","v104","v105","v106","v107","v108","v109","v110","v111","v112","v113","v114","v115","v116","v117","v118","v119","v120","v121","v122","v123","v124","v125","v126","v127","v128","v129","v130","v131","v132","v133","v134","v135","v136","v137","v138","v139","v140","v141","v142","v143","v144","v145","v146","v147","v148","v149","v150","v151","v152","v153","v154","v155","v156","v157","v158","v159","v160","v161","v162","v163","v164","v165","v166","v167","v168","v169","v170","v171","v172","v173","v174","v175","v176","v177","v178","v179","v180","v181","v182","v183","v184","v185","v186","v187","v188","v189","v190","v191","v192","v193","v194","v195","v196","v197","v198","v199","v200","v201","v202","v203","v204","v205","v206","v207","v208","v209","v210","v211","v212","v213","v214","v215","v216","v217","v218","v219","v220","v221","v222","v223","v224","v225","v226","v227","v228","v229","v230","v231","v232","v233","v234","v235","v236","v237","v238","v239","v240","v241","v242","v243","v244","v245","v246","v247","v248","v249","v250","v251","v252","v253","v254","v255","a0","a1","a2","a3","a4","a5","a6","a7","a8","a9","a10","a11","a12","a13","a14","a15","a16","a17","a18","a19","a20","a21","a22","a23","a24","a25","a26","a27","a28","a29","a30","a31","a32","a33","a34","a35","a36","a37","a38","a39","a40","a41","a42","a43","a44","a45","a46","a47","a48","a49","a50","a51","a52","a53","a54","a55","a56","a57","a58","a59","a60","a61","a62","a63","a64","a65","a66","a67","a68","a69","a70","a71","a72","a73","a74","a75","a76","a77","a78","a79","a80","a81","a82","a83","a84","a85","a86","a87","a88","a89","a90","a91","a92","a93","a94","a95","a96","a97","a98","a99","a100","a101","a102","a103","a104","a105","a106","a107","a108","a109","a110","a111","a112","a113","a114","a115","a116","a117","a118","a119","a120","a121","a122","a123","a124","a125","a126","a127","a128","a129","a130","a131","a132","a133","a134","a135","a136","a137","a138","a139","a140","a141","a142","a143","a144","a145","a146","a147","a148","a149","a150","a151","a152","a153","a154","a155","a156","a157","a158","a159","a160","a161","a162","a163","a164","a165","a166","a167","a168","a169","a170","a171","a172","a173","a174","a175","a176","a177","a178","a179","a180","a181","a182","a183","a184","a185","a186","a187","a188","a189","a190","a191","a192","a193","a194","a195","a196","a197","a198","a199","a200","a201","a202","a203","a204","a205","a206","a207","a208","a209","a210","a211","a212","a213","a214","a215","a216","a217","a218","a219","a220","a221","a222","a223","a224","a225","a226","a227","a228","a229","a230","a231","a232","a233","a234","a235","a236","a237","a238","a239","a240","a241","a242","a243","a244","a245","a246","a247","a248","a249","a250","a251","a252","a253","a254","a255","s4","s5","s6","s7","s8","s9","s10","s11","s12","s13","s14","s15","s16","s17","s18","s19","s20","s21","s22","s23","s24","s25","s26","s27","s28","s29","s30","s31","s36","s37","s38","s39","s40","s41","s42","s43","s44","s45","s46","s47","s48","s49","s50","s51","s52","s53","s54","s55","s56","s57","s58","s59","s60","s61","s62","s63","s64","s65","s66","s67","s68","s69","s70","s71","s72","s73","s74","s75","s76","s77","s78","s79","s80","s81","s82","s83","s84","s85","s86","s87","s88","s89","s90","s91","s92","s93","s94","s95","s96","s97","s98","s99","vcc","scc","memory");
}

extern "C" void kernel_launch(void* const* d_in, const int* in_sizes, int n_in, void* d_out, int out_size, void* d_ws, size_t ws_size, hipStream_t stream) {
  (void)hipFuncSetAttribute((const void*)k_block, hipFuncAttributeMaxDynamicSharedMemorySize, 160 * 1024);
  k_block<<<256, 256, 160 * 1024, stream>>>(d_in[0], d_in[3], d_in[6], d_in[7], d_in[1], d_in[2], d_in[4], d_in[8], d_out, d_ws);
}
```
